```python
import torch

def _softmax(x, axis=-1):
    return torch.softmax(x, dim=axis)
def _gelu(x):
    return torch.nn.functional.gelu(x, approximate="tanh")
def _silu(x):
    return torch.nn.functional.silu(x)
def _sigmoid(x):
    return torch.sigmoid(x)
def _layernorm(x, axis=-1, eps=1e-5):
    m = x.mean(dim=axis, keepdim=True)
    v = x.var(dim=axis, keepdim=True, unbiased=False)
    return (x - m) / torch.sqrt(v + eps)
def _scatter_add(tgt, idx, src, axis=0):
    return tgt.clone().index_add_(axis, idx, src)
def _segment_sum(src, seg, n):
    out = torch.zeros((n,) + src.shape[1:], dtype=src.dtype)
    return out.index_add_(0, seg, src)
def _pad(x, pw):
    flat = tuple(v for pair in reversed(pw) for v in pair)
    return torch.nn.functional.pad(x, flat)

kernel_name = 'concat_heads__ssm_conv_h3__ssm_conv_s4__prompt'

def setup_inputs():

    _w = (lambda a: (lambda t: t.to(torch.bfloat16) if t.is_floating_point() else t)(torch.as_tensor(a)))
    def _A_setup():
        B, S, D = 1, 2048, 640
        ks = 4
        return {
            "q": _w((lambda *s: torch.randn(*s, dtype=torch.bfloat16) * 0.05)(B, S, D)),
            "k": _w((lambda *s: torch.randn(*s, dtype=torch.bfloat16) * 0.2)(B, S, D)),
            "v": _w((lambda *s: torch.randn(*s, dtype=torch.bfloat16) * 0.2)(B, S, D)),
            "shift_k": _w((lambda *s: torch.randn(*s, dtype=torch.bfloat16) * 0.2)(D, ks) * 0.5),
            "ssm_k": _w((lambda *s: torch.randn(*s, dtype=torch.bfloat16) * 0.8)(D, S) * (1.0 / S ** 0.5)),
            "ssm_D": _w((lambda *s: torch.randn(*s, dtype=torch.bfloat16) * 0.2)(D)),
        }
    _A_in = _A_setup()

    def _B_setup():
        B, L, D, N = 1, 2048, 640, 128
        A_re = -(lambda _x: torch.exp(torch.as_tensor(_x)))((lambda *s: torch.randn(*s, dtype=torch.bfloat16) * 3.2)(D, N))
        A_im = torch.pi * (lambda *s: torch.randn(*s, dtype=torch.bfloat16) * 0.05)(D, N)
        B_re = (lambda *s: torch.randn(*s, dtype=torch.bfloat16) * 0.1)(D, N)
        B_im = (lambda *s: torch.randn(*s, dtype=torch.bfloat16) * 0.1)(D, N)
        C_re = (lambda *s: torch.randn(*s, dtype=torch.bfloat16) * 0.1)(D, N)
        C_im = (lambda *s: torch.randn(*s, dtype=torch.bfloat16) * 0.1)(D, N)
        log_dt = (lambda *s: torch.randn(*s, dtype=torch.bfloat16) * 0.05)(D) * 0.5 - 4.0
        Dskip = (lambda *s: torch.randn(*s, dtype=torch.bfloat16) * 0.05)(D)
        return {
            "u": _w((lambda *s: torch.randn(*s, dtype=torch.bfloat16) * 0.05)(B, L, D)),
            "A_re": _w(A_re),
            "A_im": _w(A_im),
            "B_re": _w(B_re),
            "B_im": _w(B_im),
            "C_re": _w(C_re),
            "C_im": _w(C_im),
            "log_dt": _w(log_dt),
            "Dskip": _w(Dskip),
        }
    _B_in = _B_setup()

    return {
        "A_q": _A_in["q"],
        "A_k": _A_in["k"],
        "A_v": _A_in["v"],
        "A_shift_k": _A_in["shift_k"],
        "A_ssm_k": _A_in["ssm_k"],
        "A_ssm_D": _A_in["ssm_D"],
        "B_u": _B_in["u"],
        "B_A_re": _B_in["A_re"],
        "B_A_im": _B_in["A_im"],
        "B_B_re": _B_in["B_re"],
        "B_B_im": _B_in["B_im"],
        "B_C_re": _B_in["C_re"],
        "B_C_im": _B_in["C_im"],
        "B_log_dt": _B_in["log_dt"],
        "B_Dskip": _B_in["Dskip"],
    }

def reference(A_q, A_k, A_v, A_shift_k, A_ssm_k, A_ssm_D, B_u, B_A_re, B_A_im, B_B_re, B_B_im, B_C_re, B_C_im, B_log_dt, B_Dskip):
    def _A_ref(q, k, v, shift_k, ssm_k, ssm_D):
        B, S, D = q.shape
        ks = shift_k.shape[-1]
        k_pad = _pad(k, ((0, 0), (ks - 1, 0), (0, 0)))
        k_shift = torch.zeros_like(k)
        for t in range(ks):
            k_shift = k_shift + k_pad[:, t:t + S, :] * shift_k[:, ks - 1 - t][None, None, :]
        kv = k_shift * v
        L = 2 * S
        kv_pad = _pad(kv, ((0, 0), (0, S), (0, 0)))
        kf_pad = _pad(ssm_k, ((0, 0), (0, S)))
        kv_f = (lambda x, axes=(-2,-1): torch.fft.rfft2(x.float(), dim=axes))(kv_pad[:, None, :, :], axes=(1, 2))
        kf_f = (lambda x, axes=(-2,-1): torch.fft.rfft2(x.float(), dim=axes))(kf_pad[:, None, :], axes=(1, 2))
        kf_f = (lambda x, *d: x.permute(*d))(kf_f, 1, 2, 0)
        y_f = kv_f * kf_f[None, :, :, :]
        y = (lambda x, s, axes=(-2,-1): torch.fft.irfft2(x, s=s, dim=axes).to(torch.bfloat16))(y_f, (1, L), axes=(1, 2))[:, 0, :S, :]
        y = y + kv * ssm_D[None, None, :]
        return q * y

    def _B_ref(u, A_re, A_im, B_re, B_im, C_re, C_im, log_dt, Dskip):
        Bsz, L, D = u.shape
        dt = torch.exp(log_dt)[:, None]
        A = A_re + 1j * A_im
        Bc = B_re + 1j * B_im
        Cc = C_re + 1j * C_im
        logdA = dt * A
        dB = dt * Bc
        l = (lambda x, dt: x.to(dt))(torch.arange(L), A_re.dtype)
        P = torch.exp(logdA[:, :, None] * l[None, None, :])
        K = torch.einsum("dn,dnl,dn->dl", Cc, P, dB).real
        L2 = 2 * L
        Kp = _pad(K, ((0, 0), (0, L)))
        up = (lambda x, *d: x.permute(*d))(u, 0, 2, 1)
        up = _pad(up, ((0, 0), (0, 0), (0, L)))
        Kf = (lambda x, axes=(-2,-1): torch.fft.rfft2(x.float(), dim=axes))(Kp[:, None, :], axes=(-2, -1))
        Uf = (lambda x, axes=(-2,-1): torch.fft.rfft2(x.float(), dim=axes))(up[:, :, None, :], axes=(-2, -1))
        Yf = Uf * Kf[None, :, :, :]
        y = (lambda x, s, axes=(-2,-1): torch.fft.irfft2(x, s=s, dim=axes).to(torch.bfloat16))(Yf, (1, L2), axes=(-2, -1))[:, :, 0, :L]
        y = (lambda x, *d: x.permute(*d))(y, 0, 2, 1) + u * Dskip[None, None, :]
        return y

    a = _A_ref(A_q, A_k, A_v, A_shift_k, A_ssm_k, A_ssm_D)
    b = _B_ref(B_u, B_A_re, B_A_im, B_B_re, B_B_im, B_C_re, B_C_im, B_log_dt, B_Dskip)
    return torch.concatenate([a, b], axis=-1)
```

```cpp
#include <hip/hip_runtime.h>
#include <hip/hip_bf16.h>
#include <stdint.h>

typedef __attribute__((ext_vector_type(4))) short s16x4;
typedef __attribute__((ext_vector_type(8))) short s16x8;
typedef __attribute__((ext_vector_type(8))) _Float16 f16x8;
typedef __attribute__((ext_vector_type(8))) __bf16 bf16x8;
typedef __attribute__((ext_vector_type(2))) __bf16 bf16x2;
typedef __attribute__((ext_vector_type(2))) _Float16 f16x2;
typedef __attribute__((ext_vector_type(2))) float f32x2;
typedef __attribute__((ext_vector_type(4))) float f32x4;
typedef __attribute__((ext_vector_type(2))) unsigned int u32x2;
typedef __attribute__((ext_vector_type(4))) unsigned int u32x4;
typedef unsigned int u32;
typedef unsigned short u16;
#define LDSP __attribute__((address_space(3)))
#define DEV __device__ __forceinline__

constexpr int SEQ = 2048;
constexpr int DCH = 640;
constexpr int NST = 128;

constexpr int XS = 4272;
constexpr int XPADB = 160;
constexpr int XIMG = 0;
constexpr int KREG = 34304;
constexpr int KRS = 8192;
constexpr int KPADB = 96;
constexpr int TBOFF = 4224;
constexpr int RAW0 = KREG + 8 * KRS;
constexpr int A_KRS = 4224;
constexpr int A_RAWK = KREG + 8 * A_KRS;
constexpr int A_RAWV = A_RAWK + 64 + 32768;
constexpr int LDS_BYTES = A_RAWV + 32768;

DEV float bf2f(u16 v) { return __uint_as_float(((u32)v) << 16); }
DEV float bflo(u32 v) { return __uint_as_float(v << 16); }
DEV float bfhi(u32 v) { return __uint_as_float(v & 0xffff0000u); }
DEV u32 pkbf(float a, float b) {
  f32x2 t = {a, b};
  bf16x2 r = __builtin_convertvector(t, bf16x2);
  return __builtin_bit_cast(u32, r);
}
DEV u32 pkh(float a, float b) {
  f32x2 t = {a, b};
  f16x2 r = __builtin_convertvector(t, f16x2);
  return __builtin_bit_cast(u32, r);
}
DEV float rbf(float a) { return bflo(pkbf(a, a)); }

DEV s16x4 trread(const char* lds, u32 off) {
  return __builtin_amdgcn_ds_read_tr16_b64_v4i16((LDSP s16x4*)(LDSP char*)(lds + off));
}
DEV bf16x8 frag(s16x4 a, s16x4 b) {
  s16x8 f = __builtin_shufflevector(a, b, 0, 1, 2, 3, 4, 5, 6, 7);
  return __builtin_bit_cast(bf16x8, f);
}
DEV float ror1(float v) {
  int r = __builtin_amdgcn_update_dpp(0, __float_as_int(v), 0x13C, 0xf, 0xf, false);
  return __int_as_float(r);
}
DEV void wavefence() { __builtin_amdgcn_fence(__ATOMIC_ACQ_REL, "wavefront"); __builtin_amdgcn_wave_barrier(); }

struct cf { float r, i; };
DEV cf cmul(cf a, cf b) {
  cf o;
  o.r = fmaf(a.r, b.r, -(a.i * b.i));
  o.i = fmaf(a.r, b.i, a.i * b.r);
  return o;
}
DEV cf cexp_pow2(float zr, float zi, float x) {
  float mag = __builtin_amdgcn_exp2f((zr * x) * 1.44269504088896340736f);
  float th = zi * x;
  const float C_HI = 0.15915494f;
  const float C_LO = 6.4206382e-09f;
  float hi = th * C_HI;
  float lo = fmaf(th, C_HI, -hi) + th * C_LO;
  float f = (hi - rintf(hi)) + lo;
  cf o;
  o.r = mag * __builtin_amdgcn_cosf(f);
  o.i = mag * __builtin_amdgcn_sinf(f);
  return o;
}

struct Params {
  const u16 *q, *k, *v, *shift_k, *ssm_k, *ssm_D;
  const u16 *u, *A_re, *A_im, *B_re, *B_im, *C_re, *C_im, *log_dt, *Dskip;
  u16* out;
};

DEV void conv_channel(char* sm, u32 ximg, u32 kimg, u32 yimg, int w, int lane, float dsk) {
  const int g = lane >> 4, i = lane & 15, q = i >> 2, pp = i & 3;
  bf16x8 kf[16];
  {
    const u32 kb = kimg + 8 * (4 * pp + 8 * g + q);
#pragma unroll
    for (int d = 0; d < 16; ++d) kf[d] = frag(trread(sm, kb + 256 * d), trread(sm, kb + 256 * d + 32));
  }
  f32x4 acc[32];
#pragma unroll
  for (int p = 0; p < 32; ++p) acc[p] = f32x4{0.f, 0.f, 0.f, 0.f};
  const u32 xb = ximg + 224 + 8 * (pp - 8 * g - q);
  float c3 = 0.f, c2 = 0.f, c1 = 0.f;
  const bool l63 = (lane == 63);
#pragma unroll
  for (int b = 0; b < 32; ++b) {
    bf16x8 xf = frag(trread(sm, xb + 128 * b + 32), trread(sm, xb + 128 * b));
#pragma unroll
    for (int d = 0; d < 16; ++d)
      if (b + 2 * d < 32) acc[b + 2 * d] = __builtin_amdgcn_mfma_f32_16x16x32_bf16(kf[d], xf, acc[b + 2 * d], 0, 0, 0);
    float d0 = acc[b][0], d1 = acc[b][1], d2 = acc[b][2], d3 = acc[b][3];
    float u2 = d2 + ror1(l63 ? c3 : d3);
    float u1 = d1 + ror1(l63 ? c2 : u2);
    float y = d0 + ror1(l63 ? c1 : u1);
    c3 = d3; c2 = u2; c1 = u1;
    const int t = 64 * b + lane;
    float x = bf2f(*(const u16*)(sm + ximg + XPADB + 2 * t));
    float y2 = rbf(y) + rbf(x * dsk);
    *(u16*)(sm + yimg + 16 * t + 2 * w) = (u16)pkbf(y2, y2);
  }
}

__global__ __launch_bounds__(512, 2) void fused_kernel(Params P) {
  extern __shared__ __attribute__((aligned(16))) char sm[];
  const int tid = threadIdx.x;
  const int w = __builtin_amdgcn_readfirstlane(tid >> 6);
  const int lane = tid & 63;
  const int wg = blockIdx.x;
  const int xcd = wg & 7, idx = wg >> 3;
  const bool partA = idx < 10;
  const int group = xcd * 10 + (partA ? idx : idx - 10);
  const int ch0 = group * 8;
  const int ch = ch0 + w;
  const u32 ximg = XIMG + w * XS;

  if (lane < 10) *(u32x4*)(sm + ximg + 16 * lane) = u32x4{0, 0, 0, 0};

  if (partA) {
    const u32 kimg = KREG + w * A_KRS;
    {
      u32x4 rk[4], rv[4], kk[4];
#pragma unroll
      for (int j = 0; j < 4; ++j) {
        const int t = tid + 512 * j;
        rk[j] = *(const u32x4*)(P.k + (size_t)t * DCH + ch0);
        rv[j] = *(const u32x4*)(P.v + (size_t)t * DCH + ch0);
        kk[j] = *(const u32x4*)(P.ssm_k + (size_t)ch * SEQ + 8 * (lane + 64 * j));
      }
      if (lane < 6) *(u32x4*)(sm + kimg + 16 * lane) = u32x4{0, 0, 0, 0};
      if (tid < 4) *(u32x4*)(sm + A_RAWK + 16 * tid) = u32x4{0, 0, 0, 0};
#pragma unroll
      for (int j = 0; j < 4; ++j) {
        const int t = tid + 512 * j;
        *(u32x4*)(sm + A_RAWK + 64 + 16 * t) = rk[j];
        *(u32x4*)(sm + A_RAWV + 16 * t) = rv[j];
        *(u32x4*)(sm + kimg + KPADB + 16 * (lane + 64 * j)) = kk[j];
      }
    }
    __syncthreads();
    {
      const int c = lane & 7;
      u32x2 sw = *(const u32x2*)(P.shift_k + (size_t)(ch0 + c) * 4);
      const float w0 = bflo(sw.x), w1 = bfhi(sw.x), w2 = bflo(sw.y), w3 = bfhi(sw.y);
      const int g = lane >> 4, i = lane & 15, q = i >> 2, pq = i & 3;
      const u32 ro = 16 * (8 * g + 4 * (pq >> 1) + q) + 8 * (pq & 1);
#pragma unroll 2
      for (int it = 0; it < 8; ++it) {
        const int R0 = 256 * it + 32 * w;
        s16x4 kp = trread(sm, A_RAWK + 16 * R0 + ro);
        s16x4 kc = trread(sm, A_RAWK + 64 + 16 * R0 + ro);
        s16x4 vc = trread(sm, A_RAWV + 16 * R0 + ro);
        float kx[8], kv[4];
#pragma unroll
        for (int j = 0; j < 4; ++j) { kx[j] = bf2f((u16)kp[j]); kx[4 + j] = bf2f((u16)kc[j]); }
#pragma unroll
        for (int j = 0; j < 4; ++j) {
          float s = rbf(kx[j + 1] * w3);
          s = rbf(s + rbf(kx[j + 2] * w2));
          s = rbf(s + rbf(kx[j + 3] * w1));
          s = rbf(s + rbf(kx[j + 4] * w0));
          kv[j] = s * bf2f((u16)vc[j]);
        }
        const int t0 = R0 + 4 * (lane >> 3);
        *(u32x2*)(sm + XIMG + c * XS + XPADB + 2 * t0) = u32x2{pkbf(kv[0], kv[1]), pkbf(kv[2], kv[3])};
      }
    }
    __syncthreads();
    conv_channel(sm, ximg, kimg, A_RAWV, w, lane, bf2f(P.ssm_D[ch]));
    __syncthreads();
#pragma unroll
    for (int j = 0; j < 4; ++j) {
      const int t = tid + 512 * j;
      u32x4 qq = *(const u32x4*)(P.q + (size_t)t * DCH + ch0);
      u32x4 yy = *(const u32x4*)(sm + A_RAWV + 16 * t);
      u32x4 o;
#pragma unroll
      for (int e = 0; e < 4; ++e) {
        u32 a = qq[e], b = yy[e];
        o[e] = pkbf(bflo(a) * bflo(b), bfhi(a) * bfhi(b));
      }
      *(u32x4*)(P.out + (size_t)t * (2 * DCH) + ch0) = o;
    }
  } else {
    const u32 kreg = KREG + w * KRS;
    u32x4 ru[4];
#pragma unroll
    for (int j = 0; j < 4; ++j) ru[j] = *(const u32x4*)(P.u + (size_t)(tid + 512 * j) * DCH + ch0);

    const float dt = rbf(expf(bf2f(P.log_dt[ch])));
    float zr[2], zi[2];
    cf W[2];
    float mx = 0.f;
#pragma unroll
    for (int ps = 0; ps < 2; ++ps) {
      const size_t o = (size_t)ch * NST + 64 * ps + lane;
      zr[ps] = dt * bf2f(P.A_re[o]);
      zi[ps] = dt * bf2f(P.A_im[o]);
      cf dB = {dt * bf2f(P.B_re[o]), dt * bf2f(P.B_im[o])};
      cf C = {bf2f(P.C_re[o]), bf2f(P.C_im[o])};
      W[ps] = cmul(C, dB);
      mx = fmaxf(mx, fmaxf(fabsf(W[ps].r), fabsf(W[ps].i)));
    }
#pragma unroll
    for (int s = 1; s < 64; s <<= 1) mx = fmaxf(mx, __shfl_xor(mx, s));
    int ex = (int)((__float_as_uint(mx) >> 23) & 0xff);
    ex = min(max(ex, 30), 240);
    const float sY = __uint_as_float((u32)(267 - ex) << 23);
    const float sOut = __uint_as_float((u32)(ex - 13) << 23);
    const float SX = 16384.f;

    f32x4 ak[3] = {f32x4{0, 0, 0, 0}, f32x4{0, 0, 0, 0}, f32x4{0, 0, 0, 0}};
    const u32 wb = kreg + ((lane >> 2) << 4) + (lane & 3) * 4;
    const int fr = lane & 15, fg = lane >> 4;
#define STG(row, val_r, val_i) \
    *(u32*)(sm + ((wb ^ (((row) & 15) << 4)) + (row) * 256)) = pkh((val_r), (val_i))
#pragma unroll
    for (int ps = 0; ps < 2; ++ps) {
      const cf E1 = cexp_pow2(zr[ps], zi[ps], 1.f);
      const cf E8 = cexp_pow2(zr[ps], zi[ps], 8.f);
      const cf E128 = cexp_pow2(zr[ps], zi[ps], 128.f);
      const cf E1024 = cexp_pow2(zr[ps], zi[ps], 1024.f);
      const cf E2 = cmul(E1, E1);
      const cf E4 = cmul(E2, E2);
      wavefence();
      {
        cf v = {W[ps].r * sY, W[ps].i * sY};
#pragma unroll
        for (int b = 0; b < 16; ++b) { STG(b, v.r, -v.i); if (b < 15) v = cmul(v, E8); }
      }
      cf Pm = {SX, 0.f};
#pragma unroll
      for (int m = 0; m < 2; ++m) {
        cf v = Pm;
#pragma unroll
        for (int r = 0; r < 8; ++r) { STG(16 + 8 * m + r, v.r, v.i); if (r < 7) v = cmul(v, E1); }
        Pm = cmul(Pm, E128);
      }
      wavefence();
      f16x8 yf[4];
#pragma unroll
      for (int s = 0; s < 4; ++s) {
        yf[s] = *(const f16x8*)(sm + kreg + fr * 256 + (((4 * s + fg) ^ fr) << 4));
        f16x8 xf = *(const f16x8*)(sm + kreg + (16 + fr) * 256 + (((4 * s + fg) ^ fr) << 4));
        ak[0] = __builtin_amdgcn_mfma_f32_16x16x32_f16(xf, yf[s], ak[0], 0, 0, 0);
      }
      wavefence();
#pragma unroll
      for (int m = 0; m < 2; ++m) {
        cf v = Pm;
#pragma unroll
        for (int r = 0; r < 4; ++r) { STG(4 * m + r, v.r, v.i); if (r < 3) v = cmul(v, E2); }
        Pm = cmul(Pm, E128);
      }
#pragma unroll
      for (int m = 0; m < 4; ++m) {
        STG(8 + 2 * m, Pm.r, Pm.i);
        cf v = cmul(Pm, E4);
        STG(9 + 2 * m, v.r, v.i);
        if (m < 3) Pm = cmul(Pm, E128);
      }
      Pm = cf{E1024.r * SX, E1024.i * SX};
#pragma unroll
      for (int m = 0; m < 9; ++m) { STG(16 + m, Pm.r, Pm.i); if (m < 8) Pm = cmul(Pm, E128); }
      wavefence();
#pragma unroll
      for (int s = 0; s < 4; ++s) {
        f16x8 x1 = *(const f16x8*)(sm + kreg + fr * 256 + (((4 * s + fg) ^ fr) << 4));
        f16x8 x2 = *(const f16x8*)(sm + kreg + (16 + fr) * 256 + (((4 * s + fg) ^ fr) << 4));
        ak[1] = __builtin_amdgcn_mfma_f32_16x16x32_f16(x1, yf[s], ak[1], 0, 0, 0);
        ak[2] = __builtin_amdgcn_mfma_f32_16x16x32_f16(x2, yf[s], ak[2], 0, 0, 0);
      }
    }
#undef STG
    wavefence();
    {
      const float so = sOut * (1.f / 16384.f);
      const int b = lane & 15, p = lane >> 4;
      if (lane < 6) *(u32x4*)(sm + kreg + 16 * lane) = u32x4{0, 0, 0, 0};
      {
        float a0 = ak[0][0] * so, a1 = ak[0][1] * so, a2 = ak[0][2] * so, a3 = ak[0][3] * so;
        *(u32x2*)(sm + kreg + KPADB + 2 * (128 * (p >> 1) + 8 * b + 4 * (p & 1))) = u32x2{pkbf(a0, a1), pkbf(a2, a3)};
      }
      {
        float a0 = ak[1][0] * so, a1 = ak[1][1] * so, a2 = ak[1][2] * so, a3 = ak[1][3] * so;
        int i01, i23;
        if (p < 2) { i01 = 64 * p + 4 * b; i23 = i01 + 2; }
        else { i01 = 128 + 64 * (p - 2) + 2 * b; i23 = i01 + 32; }
        *(u32x2*)(sm + kreg + TBOFF + 4 * i01) = u32x2{pkbf(a0, a0), pkbf(a1, a1)};
        *(u32x2*)(sm + kreg + TBOFF + 4 * i23) = u32x2{pkbf(a2, a2), pkbf(a3, a3)};
#pragma unroll
        for (int j = 0; j < 4; ++j) {
          float a = ak[2][j] * so;
          int ii = 256 + 16 * (4 * p + j) + b;
          *(u32*)(sm + kreg + TBOFF + 4 * ii) = pkbf(a, a);
        }
      }
      wavefence();
      const u32* TB = (const u32*)(sm + kreg + TBOFF);
      if (lane < 32) {
        const int i0 = 4 * lane;
        u32 g0 = TB[i0], g1 = TB[i0 + 1], g2 = TB[i0 + 2], g3 = TB[i0 + 3], g4 = TB[i0 + 4];
        u32x4 o = {g0, (g1 & 0xffffu) | (g2 & 0xffff0000u), g2, (g3 & 0xffffu) | (g4 & 0xffff0000u)};
        *(u32x4*)(sm + kreg + KPADB + 16 * (32 + lane)) = o;
      }
      {
        const int i0 = 128 + 2 * lane;
        u32 g0 = TB[i0], g1 = TB[i0 + 1], g2 = TB[i0 + 2];
        u32x4 o = {g0, (g0 & 0xffffu) | (g1 & 0xffff0000u), g1, g2};
        *(u32x4*)(sm + kreg + KPADB + 16 * (64 + lane)) = o;
      }
#pragma unroll
      for (int h = 0; h < 2; ++h) {
        const int i0 = 256 + 64 * h + lane;
        u32 g0 = TB[i0], g1 = TB[i0 + 1];
        u32 mid = (lane & 1) ? g1 : ((g0 & 0xffffu) | (g1 & 0xffff0000u));
        u32x4 o = {g0, g0, mid, g1};
        *(u32x4*)(sm + kreg + KPADB + 16 * (128 + 64 * h + lane)) = o;
      }
    }
#pragma unroll
    for (int j = 0; j < 4; ++j) *(u32x4*)(sm + RAW0 + 16 * (tid + 512 * j)) = ru[j];
    __syncthreads();
    {
      const int c = lane & 7;
      const int g = lane >> 4, i = lane & 15, q = i >> 2, pq = i & 3;
      const u32 ro = 16 * (8 * g + 4 * (pq >> 1) + q) + 8 * (pq & 1);
#pragma unroll
      for (int it = 0; it < 8; ++it) {
        const int R0 = 256 * it + 32 * w;
        s16x4 uc = trread(sm, RAW0 + 16 * R0 + ro);
        const int t0 = R0 + 4 * (lane >> 3);
        *(s16x4*)(sm + XIMG + c * XS + XPADB + 2 * t0) = uc;
      }
    }
    __syncthreads();
    conv_channel(sm, ximg, kreg, RAW0, w, lane, bf2f(P.Dskip[ch]));
    __syncthreads();
#pragma unroll
    for (int j = 0; j < 4; ++j) {
      const int t = tid + 512 * j;
      u32x4 yy = *(const u32x4*)(sm + RAW0 + 16 * t);
      *(u32x4*)(P.out + (size_t)t * (2 * DCH) + DCH + ch0) = yy;
    }
  }
}

extern "C" void kernel_launch(void* const* d_in, const int* in_sizes, int n_in,
                              void* d_out, int out_size, void* d_ws, size_t ws_size,
                              hipStream_t stream) {
  Params P;
  P.q = (const u16*)d_in[0];
  P.k = (const u16*)d_in[1];
  P.v = (const u16*)d_in[2];
  P.shift_k = (const u16*)d_in[3];
  P.ssm_k = (const u16*)d_in[4];
  P.ssm_D = (const u16*)d_in[5];
  P.u = (const u16*)d_in[6];
  P.A_re = (const u16*)d_in[7];
  P.A_im = (const u16*)d_in[8];
  P.B_re = (const u16*)d_in[9];
  P.B_im = (const u16*)d_in[10];
  P.C_re = (const u16*)d_in[11];
  P.C_im = (const u16*)d_in[12];
  P.log_dt = (const u16*)d_in[13];
  P.Dskip = (const u16*)d_in[14];
  P.out = (u16*)d_out;
  (void)hipFuncSetAttribute((const void*)fused_kernel, hipFuncAttributeMaxDynamicSharedMemorySize, LDS_BYTES);
  fused_kernel<<<160, 512, LDS_BYTES, stream>>>(P);
}
```

```cpp
#include <hip/hip_runtime.h>
#include <hip/hip_bf16.h>
#include <stdint.h>

typedef __attribute__((ext_vector_type(4))) short s16x4;
typedef __attribute__((ext_vector_type(8))) short s16x8;
typedef __attribute__((ext_vector_type(8))) _Float16 f16x8;
typedef __attribute__((ext_vector_type(8))) __bf16 bf16x8;
typedef __attribute__((ext_vector_type(2))) __bf16 bf16x2;
typedef __attribute__((ext_vector_type(2))) _Float16 f16x2;
typedef __attribute__((ext_vector_type(2))) float f32x2;
typedef __attribute__((ext_vector_type(4))) float f32x4;
typedef __attribute__((ext_vector_type(2))) unsigned int u32x2;
typedef __attribute__((ext_vector_type(4))) unsigned int u32x4;
typedef unsigned int u32;
typedef unsigned short u16;
#define LDSP __attribute__((address_space(3)))
#define DEV __device__ __forceinline__

constexpr int SEQ = 2048;
constexpr int DCH = 640;
constexpr int NST = 128;

constexpr int XS = 4272;
constexpr int XPADB = 160;
constexpr int XIMG = 0;
constexpr int YIMG = 34304;
constexpr int YS = 1088;
constexpr int ZROW = 43008;
constexpr int SHK = 43024;
constexpr int KREG = 44288;
constexpr int KRS_A = 4224;
constexpr int KRS_B = 8192;
constexpr int KPADB = 96;
constexpr int TBOFF = 4224;
constexpr int RINGK = 81920;
constexpr int RINGV = 98304;
constexpr int RINGQ = 114688;
constexpr int RAWU = 114688;
constexpr int LDS_BYTES = RAWU + 32768;

DEV float bf2f(u16 v) { return __uint_as_float(((u32)v) << 16); }
DEV float bflo(u32 v) { return __uint_as_float(v << 16); }
DEV float bfhi(u32 v) { return __uint_as_float(v & 0xffff0000u); }
DEV u32 pkbf(float a, float b) {
  f32x2 t = {a, b};
  bf16x2 r = __builtin_convertvector(t, bf16x2);
  return __builtin_bit_cast(u32, r);
}
DEV u32 pkh(float a, float b) {
  f32x2 t = {a, b};
  f16x2 r = __builtin_convertvector(t, f16x2);
  return __builtin_bit_cast(u32, r);
}
DEV float rbf(float a) { return bflo(pkbf(a, a)); }
DEV float dot2(u32 a, u32 b, float c) {
  return __builtin_amdgcn_fdot2_f32_bf16(__builtin_bit_cast(bf16x2, a), __builtin_bit_cast(bf16x2, b), c, false);
}
typedef LDSP char* lptr;
DEV u32x2 trread(lptr p) {
  s16x4 r = __builtin_amdgcn_ds_read_tr16_b64_v4i16((LDSP s16x4*)p);
  return __builtin_bit_cast(u32x2, r);
}
DEV bf16x8 frag(u32x2 a, u32x2 b) {
  u32x4 f = {a.x, a.y, b.x, b.y};
  return __builtin_bit_cast(bf16x8, f);
}
DEV float ror1(float v) {
  int r = __builtin_amdgcn_update_dpp(0, __float_as_int(v), 0x13C, 0xf, 0xf, false);
  return __int_as_float(r);
}
DEV void wavefence() { __builtin_amdgcn_fence(__ATOMIC_ACQ_REL, "wavefront"); __builtin_amdgcn_wave_barrier(); }
DEV void wgbarrier() {
  asm volatile("s_waitcnt lgkmcnt(0)" ::: "memory");
  __builtin_amdgcn_s_barrier();
  asm volatile("" ::: "memory");
}
DEV void dma16(const void* base, u32 voff, u32 ldsaddr) {
  u32 keep;
  asm volatile("s_mov_b32 %0, m0\n\ts_mov_b32 m0, %3\n\ts_nop 0\n\tglobal_load_lds_dwordx4 %1, %2\n\ts_mov_b32 m0, %0"
               : "=&s"(keep) : "v"(voff), "s"(base), "s"(ldsaddr) : "memory");
}
#define VMCNT(n) asm volatile("s_waitcnt vmcnt(" #n ")" ::: "memory")

struct cf { float r, i; };
DEV cf cmul(cf a, cf b) {
  cf o;
  o.r = fmaf(a.r, b.r, -(a.i * b.i));
  o.i = fmaf(a.r, b.i, a.i * b.r);
  return o;
}
DEV cf cexp_pow2(float zr, float zi, float x) {
  float mag = __builtin_amdgcn_exp2f((zr * x) * 1.44269504088896340736f);
  float th = zi * x;
  const float C_HI = 0.15915494f;
  const float C_LO = 6.4206382e-09f;
  float hi = th * C_HI;
  float lo = fmaf(th, C_HI, -hi) + th * C_LO;
  float f = (hi - rintf(hi)) + lo;
  cf o;
  o.r = mag * __builtin_amdgcn_cosf(f);
  o.i = mag * __builtin_amdgcn_sinf(f);
  return o;
}

#define STAMP(n)

struct Params {
  const u16 *q, *k, *v, *shift_k, *ssm_k, *ssm_D;
  const u16 *u, *A_re, *A_im, *B_re, *B_im, *C_re, *C_im, *log_dt, *Dskip;
  u16* out;
  unsigned long long* ws;
};

DEV float ld_bf_uniform(const u16* p, int idx) {
  u32 d = *(const u32*)(p + (idx & ~1));
  return (idx & 1) ? bfhi(d) : bflo(d);
}

__global__ __launch_bounds__(512, 2) void fused_kernel(Params P) {
  extern __shared__ __attribute__((aligned(16))) char smem[];
  const lptr sm = (lptr)smem;
  const u32 smb = (u32)(uintptr_t)sm;
  const int tid = threadIdx.x;
  const int w = __builtin_amdgcn_readfirstlane(tid >> 6);
  const int lane = tid & 63;
  const int wg = blockIdx.x;
  const int xcd = wg & 7, idx = wg >> 3;
  const bool partA = idx < 10;
  const int group = xcd * 10 + (partA ? idx : idx - 10);
  const int ch0 = group * 8;
  const int ch = ch0 + w;
  const int g = lane >> 4, li = lane & 15, lq = li >> 2, lp = li & 3;
  STAMP(0);

  const u32 rowoff = (u32)lane * (DCH * 2) + (u32)ch0 * 2;
  const u32 kimg = KREG + w * (partA ? KRS_A : KRS_B);
  const int srow = 8 * g + 4 * (lp >> 1);
  const int shalf = 8 * (lp & 1);
  const int c8 = lane & 7;
  const u32 xw = XIMG + c8 * XS + XPADB + 8 * (lane >> 3) + 64 * w;

  if (lane < 10) *(LDSP u32x4*)(sm + XIMG + w * XS + 16 * lane) = u32x4{0, 0, 0, 0};

  u32 W32 = 0, W10 = 0;
  u32 ka1 = 0, ka2 = 0, ka3 = 0, va1 = 0, va2 = 0;

  if (partA) {
    const u16* src = (w < 4) ? P.k : P.v;
    const u32 ring = (w < 4) ? RINGK : RINGV;
    const int wq = w & 3;
#pragma unroll
    for (int j = 0; j < 4; ++j) dma16(P.ssm_k, (u32)ch * (SEQ * 2) + 1024 * j + 16 * lane, smb + kimg + KPADB + 1024 * j);
    if (w == 0) { if (lane < 4) dma16(P.shift_k, (u32)ch0 * 8 + 16 * lane, smb + SHK); }
    dma16(src, rowoff + (u32)(0 * 256 + 64 * wq) * (DCH * 2), smb + ring + 0 * 4096 + 1024 * wq);
    dma16(src, rowoff + (u32)(1 * 256 + 64 * wq) * (DCH * 2), smb + ring + 1 * 4096 + 1024 * wq);
    dma16(P.q, rowoff + (u32)(64 * w) * (DCH * 2), smb + RINGQ + 1024 * w);
    dma16(src, rowoff + (u32)(2 * 256 + 64 * wq) * (DCH * 2), smb + ring + 2 * 4096 + 1024 * wq);
    if (lane < 6) *(LDSP u32x4*)(sm + kimg + 16 * lane) = u32x4{0, 0, 0, 0};
    if (tid < 4) *(LDSP u32x4*)(sm + RINGK + 16384 - 64 + 16 * tid) = u32x4{0, 0, 0, 0};
    if (tid == 4) *(LDSP u32x4*)(sm + ZROW) = u32x4{0, 0, 0, 0};
    const float dsk = ld_bf_uniform(P.ssm_D, ch);
    {
      const int r1 = lq - 3;
      const int r2 = (lq < 2) ? lq + 1 : lq - 4;
      ka1 = 16 * (32 * w + srow + r1) + shalf;
      ka2 = 16 * (32 * w + srow + r2) + shalf;
      ka3 = 16 * (32 * w + srow + lq) + shalf;
      va1 = 16 * (32 * w + srow + (lq >> 1)) + shalf;
      va2 = va1 + 32;
    }
    VMCNT(3);
    STAMP(1);
    if (lane == 0) {
      LDSP u16* k0 = (LDSP u16*)(sm + kimg + KPADB);
      *k0 = (u16)pkbf(bf2f(*k0) + dsk, 0.f);
    }
    wgbarrier();
    STAMP(2);
    u32x2 sw = *(LDSP u32x2*)(sm + SHK + 8 * c8);
    W32 = (sw.y >> 16) | (sw.y << 16);
    W10 = (sw.x >> 16) | (sw.x << 16);
  } else {
    const u32 kreg = kimg;
    const float dt = rbf(expf(ld_bf_uniform(P.log_dt, ch)));
    const float dsk = ld_bf_uniform(P.Dskip, ch);
    float zr[2], zi[2];
    cf W[2];
    float mx = 0.f;
#pragma unroll
    for (int ps = 0; ps < 2; ++ps) {
      const size_t o = (size_t)ch * NST + 64 * ps + lane;
      zr[ps] = dt * bf2f(P.A_re[o]);
      zi[ps] = dt * bf2f(P.A_im[o]);
      cf dB = {dt * bf2f(P.B_re[o]), dt * bf2f(P.B_im[o])};
      cf C = {bf2f(P.C_re[o]), bf2f(P.C_im[o])};
      W[ps] = cmul(C, dB);
      mx = fmaxf(mx, fmaxf(fabsf(W[ps].r), fabsf(W[ps].i)));
    }
    asm volatile("" :: "v"(mx), "v"(zr[0]), "v"(zr[1]), "v"(zi[0]), "v"(zi[1]) : "memory");
    STAMP(1);
#pragma unroll
    for (int j = 0; j < 4; ++j) dma16(P.u, rowoff + (u32)(512 * j + 64 * w) * (DCH * 2), smb + RAWU + 8192 * j + 1024 * w);
#pragma unroll
    for (int s = 1; s < 64; s <<= 1) mx = fmaxf(mx, __shfl_xor(mx, s));
    int ex = (int)((__float_as_uint(mx) >> 23) & 0xff);
    ex = min(max(ex, 30), 240);
    const float sY = __uint_as_float((u32)(267 - ex) << 23);
    const float sOut = __uint_as_float((u32)(ex - 13) << 23);
    const float SX = 16384.f;

    f32x4 ak[3] = {f32x4{0, 0, 0, 0}, f32x4{0, 0, 0, 0}, f32x4{0, 0, 0, 0}};
    const u32 wb = kreg + ((lane >> 2) << 4) + (lane & 3) * 4;
    const u32 rb = kreg + li * 256;
#define STG(row, val_r, val_i) \
    *(LDSP u32*)(sm + ((wb ^ (((row) & 15) << 4)) + (row) * 256)) = pkh((val_r), (val_i))
#pragma unroll 1
    for (int ps = 0; ps < 2; ++ps) {
      const float zzr = ps ? zr[1] : zr[0], zzi = ps ? zi[1] : zi[0];
      const float wr = ps ? W[1].r : W[0].r, wi = ps ? W[1].i : W[0].i;
      const cf E1 = cexp_pow2(zzr, zzi, 1.f);
      const cf E8 = cexp_pow2(zzr, zzi, 8.f);
      const cf E128 = cexp_pow2(zzr, zzi, 128.f);
      const cf E1024 = cexp_pow2(zzr, zzi, 1024.f);
      const cf E2 = cmul(E1, E1);
      const cf E4 = cmul(E2, E2);
      wavefence();
      {
        cf v = {wr * sY, wi * sY};
#pragma unroll
        for (int b = 0; b < 16; ++b) { STG(b, v.r, -v.i); if (b < 15) v = cmul(v, E8); }
      }
      cf Pm = {SX, 0.f};
#pragma unroll
      for (int m = 0; m < 2; ++m) {
        cf v = Pm;
#pragma unroll
        for (int r = 0; r < 8; ++r) { STG(16 + 8 * m + r, v.r, v.i); if (r < 7) v = cmul(v, E1); }
        Pm = cmul(Pm, E128);
      }
      wavefence();
      f16x8 yf[4];
#pragma unroll
      for (int s = 0; s < 4; ++s) {
        yf[s] = *(const LDSP f16x8*)(sm + rb + (((4 * s + g) ^ li) << 4));
        f16x8 xf = *(const LDSP f16x8*)(sm + rb + 4096 + (((4 * s + g) ^ li) << 4));
        ak[0] = __builtin_amdgcn_mfma_f32_16x16x32_f16(xf, yf[s], ak[0], 0, 0, 0);
      }
      wavefence();
#pragma unroll
      for (int m = 0; m < 2; ++m) {
        cf v = Pm;
#pragma unroll
        for (int r = 0; r < 4; ++r) { STG(4 * m + r, v.r, v.i); if (r < 3) v = cmul(v, E2); }
        Pm = cmul(Pm, E128);
      }
#pragma unroll
      for (int m = 0; m < 4; ++m) {
        STG(8 + 2 * m, Pm.r, Pm.i);
        cf v = cmul(Pm, E4);
        STG(9 + 2 * m, v.r, v.i);
        if (m < 3) Pm = cmul(Pm, E128);
      }
      Pm = cf{E1024.r * SX, E1024.i * SX};
#pragma unroll
      for (int m = 0; m < 9; ++m) { STG(16 + m, Pm.r, Pm.i); if (m < 8) Pm = cmul(Pm, E128); }
      wavefence();
#pragma unroll
      for (int s = 0; s < 4; ++s) {
        f16x8 x1 = *(const LDSP f16x8*)(sm + rb + (((4 * s + g) ^ li) << 4));
        f16x8 x2 = *(const LDSP f16x8*)(sm + rb + 4096 + (((4 * s + g) ^ li) << 4));
        ak[1] = __builtin_amdgcn_mfma_f32_16x16x32_f16(x1, yf[s], ak[1], 0, 0, 0);
        ak[2] = __builtin_amdgcn_mfma_f32_16x16x32_f16(x2, yf[s], ak[2], 0, 0, 0);
      }
    }
#undef STG
    wavefence();
    STAMP(2);
    {
      const float so = sOut * (1.f / 16384.f);
      const int b = li, p = g;
      if (lane < 6) *(LDSP u32x4*)(sm + kreg + 16 * lane) = u32x4{0, 0, 0, 0};
      {
        float a0 = fmaf(ak[0][0], so, lane == 0 ? dsk : 0.f), a1 = ak[0][1] * so, a2 = ak[0][2] * so, a3 = ak[0][3] * so;
        *(LDSP u32x2*)(sm + kreg + KPADB + 2 * (128 * (p >> 1) + 8 * b + 4 * (p & 1))) = u32x2{pkbf(a0, a1), pkbf(a2, a3)};
      }
      {
        float a0 = ak[1][0] * so, a1 = ak[1][1] * so, a2 = ak[1][2] * so, a3 = ak[1][3] * so;
        int i01, i23;
        if (p < 2) { i01 = 64 * p + 4 * b; i23 = i01 + 2; }
        else { i01 = 128 + 64 * (p - 2) + 2 * b; i23 = i01 + 32; }
        *(LDSP u32x2*)(sm + kreg + TBOFF + 4 * i01) = u32x2{pkbf(a0, a0), pkbf(a1, a1)};
        *(LDSP u32x2*)(sm + kreg + TBOFF + 4 * i23) = u32x2{pkbf(a2, a2), pkbf(a3, a3)};
#pragma unroll
        for (int j = 0; j < 4; ++j) {
          float a = ak[2][j] * so;
          int ii = 256 + 16 * (4 * p + j) + b;
          *(LDSP u32*)(sm + kreg + TBOFF + 4 * ii) = pkbf(a, a);
        }
      }
      wavefence();
      const LDSP u32* TB = (const LDSP u32*)(sm + kreg + TBOFF);
      {
        const int i0 = 4 * (lane & 31);
        u32 g0 = TB[i0], g1 = TB[i0 + 1], g2 = TB[i0 + 2], g3 = TB[i0 + 3], g4 = TB[i0 + 4];
        u32x4 o = {g0, (g1 & 0xffffu) | (g2 & 0xffff0000u), g2, (g3 & 0xffffu) | (g4 & 0xffff0000u)};
        *(LDSP u32x4*)(sm + kreg + KPADB + 16 * (32 + (lane & 31))) = o;
      }
      {
        const int i0 = 128 + 2 * lane;
        u32 g0 = TB[i0], g1 = TB[i0 + 1], g2 = TB[i0 + 2];
        u32x4 o = {g0, (g0 & 0xffffu) | (g1 & 0xffff0000u), g1, g2};
        *(LDSP u32x4*)(sm + kreg + KPADB + 16 * (64 + lane)) = o;
      }
#pragma unroll
      for (int h = 0; h < 2; ++h) {
        const int i0 = 256 + 64 * h + lane;
        u32 g0 = TB[i0], g1 = TB[i0 + 1];
        u32 mid = (lane & 1) ? g1 : ((g0 & 0xffffu) | (g1 & 0xffff0000u));
        u32x4 o = {g0, g0, mid, g1};
        *(LDSP u32x4*)(sm + kreg + KPADB + 16 * (128 + 64 * h + lane)) = o;
      }
    }
    STAMP(3);
    VMCNT(0);
    wgbarrier();
    {
      const u32 ua = RAWU + 16 * (32 * w + srow + lq) + shalf;
#pragma unroll
      for (int it = 0; it < 8; ++it) {
        u32x2 uc = trread(sm + ua + 4096 * it);
        *(LDSP u32x2*)(sm + xw + 512 * it) = uc;
      }
    }
  }

  auto make_kv = [&](int blk) {
    const u32 so = (u32)(blk & 3) * 4096;
    u32x2 r1 = trread(sm + RINGK + ((so + ka1) & 16383));
    u32x2 r2 = trread(sm + RINGK + ((so + ka2) & 16383));
    u32x2 r3 = trread(sm + RINGK + (so + ka3));
    u32x2 v1 = trread(sm + ((lq & 1) ? (RINGV + so + va1) : (u32)ZROW));
    u32x2 v2 = trread(sm + ((lq & 1) ? (RINGV + so + va2) : (u32)ZROW));
    float s0 = dot2(r1.y, W10, dot2(r1.x, W32, 0.f));
    float s1 = dot2(r3.x, W10, dot2(r2.y, W32, 0.f));
    float s2 = dot2(r2.x, W10, dot2(r1.y, W32, 0.f));
    float s3 = dot2(r3.y, W10, dot2(r3.x, W32, 0.f));
    u32 o0 = pkbf(s0 * __uint_as_float(v1.x), s1 * __uint_as_float(v1.y));
    u32 o1 = pkbf(s2 * __uint_as_float(v2.x), s3 * __uint_as_float(v2.y));
    *(LDSP u32x2*)(sm + xw + 512 * blk) = u32x2{o0, o1};
  };

  if (partA) { make_kv(0); VMCNT(1); }
  wavefence();
  bf16x8 kf[16];
  {
    const u32 kb = kimg + 8 * (4 * lp + 8 * g + lq);
#pragma unroll
    for (int d = 0; d < 16; ++d) kf[d] = frag(trread(sm + kb + 256 * d), trread(sm + kb + 256 * d + 32));
  }
  wgbarrier();
  STAMP(4);

  f32x4 Re[17], Ro[17];
#pragma unroll
  for (int d = 0; d < 17; ++d) { Re[d] = f32x4{0.f, 0.f, 0.f, 0.f}; Ro[d] = f32x4{0.f, 0.f, 0.f, 0.f}; }
  u32 xb = XIMG + w * XS + 224 + 8 * (lp - 8 * g - lq);
  float c3 = 0.f, c2 = 0.f, c1 = 0.f;
  const bool l63 = (lane == 63);
  u32 qe = 0, qo = 0;
  const u32 yrd = YIMG + lq * YS + 2 * (64 * (w & 3) + 16 * g + 4 * lp);
  u16* const outp = P.out + (partA ? 0 : DCH) + ch0 + (size_t)(64 * (w & 3) + lane) * (2 * DCH);

  auto store_block = [&](int blk) {
    u32x2 a = trread(sm + yrd + 512 * (blk & 1));
    u32x2 b = trread(sm + yrd + 512 * (blk & 1) + 4 * YS);
    *(u32x4*)(outp + (size_t)blk * 256 * (2 * DCH)) = u32x4{a.x, a.y, b.x, b.y};
  };

#pragma unroll 1
  for (int r = 0; r < 8; ++r) {
    int nvm = 0;
    if (partA) {
      if (!(r & 1) && r < 6) { dma16(P.q, rowoff + (u32)(256 * (r + 2) + 64 * w) * (DCH * 2), smb + RINGQ + ((r + 2) & 3) * 4096 + 1024 * w); ++nvm; }
      if (r < 5) {
        const u16* src = (w < 4) ? P.k : P.v;
        const u32 ring = (w < 4) ? RINGK : RINGV;
        dma16(src, rowoff + (u32)(256 * (r + 3) + 64 * (w & 3)) * (DCH * 2), smb + ring + ((r + 3) & 3) * 4096 + 1024 * (w & 3)); ++nvm;
      }
    }
    if (r >= 1 && (w >> 2) == ((r - 1) & 1)) store_block(r - 1);
    if (r >= 2 || (r == 1 && w < 4)) ++nvm;
    if (partA && r < 7) make_kv(r + 1);
#pragma unroll 1
    for (int h = 0; h < 2; ++h) {
      int n = 16 - (2 * r + h);
      asm volatile("" : "+s"(n));
      bf16x8 xe = frag(trread(sm + xb + 32), trread(sm + xb));
      bf16x8 xo = frag(trread(sm + xb + 160), trread(sm + xb + 128));
      xb += 256;
      const u32 tl = (u32)((2 * r + h) * 128 + lane);
      if (partA) {
        const u32 qa = smb + RINGQ + ((tl * 16) & 16383) + 2 * w;
        asm volatile("ds_read_u16_d16_hi %0, %2\n\tds_read_u16_d16_hi %1, %2 offset:1024" : "+v"(qe), "+v"(qo) : "v"(qa) : "memory");
      }
#define CH(d) Re[d] = __builtin_amdgcn_mfma_f32_16x16x32_bf16(kf[d], xe, Re[d + 1], 0, 0, 0); \
              Ro[d] = __builtin_amdgcn_mfma_f32_16x16x32_bf16(kf[d], xo, Ro[d + 1], 0, 0, 0); \
              if (n == d + 1) break;
      do {
        CH(0) CH(1) CH(2) CH(3) CH(4) CH(5) CH(6) CH(7) CH(8) CH(9) CH(10) CH(11) CH(12) CH(13) CH(14) CH(15)
      } while (0);
#undef CH
      float e0 = Re[0][0], e1 = Re[0][1], e2 = Re[0][2], e3 = Re[0][3];
      float o0 = Ro[0][0], o1 = Ro[0][1], o2 = Ro[0][2], o3 = Ro[0][3];
      float eu2 = e2 + ror1(l63 ? c3 : e3);
      float ou2 = o2 + ror1(l63 ? e3 : o3);
      float eu1 = e1 + ror1(l63 ? c2 : eu2);
      float ou1 = o1 + ror1(l63 ? eu2 : ou2);
      float ye = e0 + ror1(l63 ? c1 : eu1);
      float yo = o0 + ror1(l63 ? eu1 : ou1);
      c3 = o3; c2 = ou2; c1 = ou1;
      asm volatile("" : "+v"(ye));
      asm volatile("" : "+v"(yo));
      if (partA) {
        asm volatile("s_waitcnt lgkmcnt(0)" : "+v"(qe), "+v"(qo) :: "memory");
        ye *= __uint_as_float(qe);
        yo *= __uint_as_float(qo);
      }
      const u32 pk = pkbf(ye, yo);
      LDSP u16* yp = (LDSP u16*)(sm + YIMG + w * YS + ((tl * 2) & 1023));
      yp[0] = (u16)pk;
      yp[64] = (u16)(pk >> 16);
    }
    if (partA) {
      if (nvm >= 3) VMCNT(3); else if (nvm == 2) VMCNT(2); else if (nvm == 1) VMCNT(1); else VMCNT(0);
    }
    wgbarrier();
  }
  if (w >= 4) store_block(7);
}

extern "C" void kernel_launch(void* const* d_in, const int* in_sizes, int n_in,
                              void* d_out, int out_size, void* d_ws, size_t ws_size,
                              hipStream_t stream) {
  Params P;
  P.q = (const u16*)d_in[0];
  P.k = (const u16*)d_in[1];
  P.v = (const u16*)d_in[2];
  P.shift_k = (const u16*)d_in[3];
  P.ssm_k = (const u16*)d_in[4];
  P.ssm_D = (const u16*)d_in[5];
  P.u = (const u16*)d_in[6];
  P.A_re = (const u16*)d_in[7];
  P.A_im = (const u16*)d_in[8];
  P.B_re = (const u16*)d_in[9];
  P.B_im = (const u16*)d_in[10];
  P.C_re = (const u16*)d_in[11];
  P.C_im = (const u16*)d_in[12];
  P.log_dt = (const u16*)d_in[13];
  P.Dskip = (const u16*)d_in[14];
  P.out = (u16*)d_out;
  P.ws = (unsigned long long*)d_ws;
  (void)hipFuncSetAttribute((const void*)fused_kernel, hipFuncAttributeMaxDynamicSharedMemorySize, LDS_BYTES);
  fused_kernel<<<160, 512, LDS_BYTES, stream>>>(P);
}
```

```cpp
#include <hip/hip_runtime.h>
#include <hip/hip_bf16.h>
#include <stdint.h>

typedef __attribute__((ext_vector_type(4))) short s16x4;
typedef __attribute__((ext_vector_type(8))) _Float16 f16x8;
typedef __attribute__((ext_vector_type(8))) __bf16 bf16x8;
typedef __attribute__((ext_vector_type(2))) __bf16 bf16x2;
typedef __attribute__((ext_vector_type(2))) _Float16 f16x2;
typedef __attribute__((ext_vector_type(2))) float f32x2;
typedef __attribute__((ext_vector_type(4))) float f32x4;
typedef __attribute__((ext_vector_type(2))) unsigned int u32x2;
typedef __attribute__((ext_vector_type(4))) unsigned int u32x4;
typedef unsigned int u32;
typedef unsigned short u16;
#define LDSP __attribute__((address_space(3)))
#define DEV __device__ __forceinline__

constexpr int SEQ = 2048;
constexpr int DCH = 640;
constexpr int NST = 128;

constexpr int XS = 4272;
constexpr int XPADB = 160;
constexpr int XIMG = 0;
constexpr int YIMG = 34304;
constexpr int YS = 2112;
constexpr int ZROW = 51200;
constexpr int SHK = 51264;
constexpr int KIMG = 51456;
constexpr int KS = 4224;
constexpr int KPADB = 96;
constexpr int RINGK = 98304;
constexpr int RINGV = 114688;
constexpr int RINGQ = 131072;
constexpr int STS = 8704;
constexpr int STG = 17152;
constexpr int B_XIMG = 0;
constexpr int B_YIMG = 86784;
constexpr int B_KIMG = 95232;
constexpr int RAWU = 114688;
constexpr int LDS_BYTES = 147456;

DEV float bf2f(u16 v) { return __uint_as_float(((u32)v) << 16); }
DEV float bflo(u32 v) { return __uint_as_float(v << 16); }
DEV float bfhi(u32 v) { return __uint_as_float(v & 0xffff0000u); }
DEV u32 pkbf(float a, float b) {
  f32x2 t = {a, b};
  bf16x2 r = __builtin_convertvector(t, bf16x2);
  return __builtin_bit_cast(u32, r);
}
DEV u32 pkh(float a, float b) {
  f32x2 t = {a, b};
  f16x2 r = __builtin_convertvector(t, f16x2);
  return __builtin_bit_cast(u32, r);
}
DEV float rbf(float a) { return bflo(pkbf(a, a)); }
DEV float dot2(u32 a, u32 b, float c) {
  return __builtin_amdgcn_fdot2_f32_bf16(__builtin_bit_cast(bf16x2, a), __builtin_bit_cast(bf16x2, b), c, false);
}
typedef LDSP char* lptr;
DEV u32x2 trread(lptr p) {
  s16x4 r = __builtin_amdgcn_ds_read_tr16_b64_v4i16((LDSP s16x4*)p);
  return __builtin_bit_cast(u32x2, r);
}
DEV bf16x8 frag(u32x2 a, u32x2 b) {
  u32x4 f = {a.x, a.y, b.x, b.y};
  return __builtin_bit_cast(bf16x8, f);
}
DEV float ror1(float v) {
  int r = __builtin_amdgcn_update_dpp(0, __float_as_int(v), 0x13C, 0xf, 0xf, false);
  return __int_as_float(r);
}
DEV void wavefence() { __builtin_amdgcn_fence(__ATOMIC_ACQ_REL, "wavefront"); __builtin_amdgcn_wave_barrier(); }
DEV void wgbarrier() {
  asm volatile("s_waitcnt lgkmcnt(0)" ::: "memory");
  __builtin_amdgcn_s_barrier();
  asm volatile("" ::: "memory");
}
DEV void dma16(const void* base, u32 voff, u32 ldsaddr) {
  u32 keep;
  asm volatile("s_mov_b32 %0, m0\n\ts_mov_b32 m0, %3\n\ts_nop 0\n\tglobal_load_lds_dwordx4 %1, %2\n\ts_mov_b32 m0, %0"
               : "=&s"(keep) : "v"(voff), "s"(base), "s"(ldsaddr) : "memory");
}
template <int N> DEV void vmcnt() { asm volatile("s_waitcnt vmcnt(%0)" :: "i"(N) : "memory"); }
#define SCHED0() __builtin_amdgcn_sched_barrier(0)

struct cf { float r, i; };
DEV cf cmul(cf a, cf b) {
  cf o;
  o.r = fmaf(a.r, b.r, -(a.i * b.i));
  o.i = fmaf(a.r, b.i, a.i * b.r);
  return o;
}
DEV cf cexp_pow2(float zr, float zi, float x) {
  float mag = __builtin_amdgcn_exp2f((zr * x) * 1.44269504088896340736f);
  float th = zi * x;
  const float C_HI = 0.15915494f;
  const float C_LO = 6.4206382e-09f;
  float hi = th * C_HI;
  float lo = fmaf(th, C_HI, -hi) + th * C_LO;
  float f = (hi - rintf(hi)) + lo;
  cf o;
  o.r = mag * __builtin_amdgcn_cosf(f);
  o.i = mag * __builtin_amdgcn_sinf(f);
  return o;
}

#define STAMP(n)
#define STV_ARG
#define STV_PASS

struct Params {
  const u16 *q, *k, *v, *shift_k, *ssm_k, *ssm_D;
  const u16 *u, *A_re, *A_im, *B_re, *B_im, *C_re, *C_im, *log_dt, *Dskip;
  u16* out;
  unsigned long long* ws;
};

DEV float ld_bf_uniform(const u16* p, int idx) {
  u32 d = *(const u32*)(p + (idx & ~1));
  return (idx & 1) ? bfhi(d) : bflo(d);
}

struct AState {
  const u16* kvsrc;
  const u16* q;
  u16* outp;
  u32 rowoff;
  u32 kvring;
  u32 qring;
  u32 kvrow, qrow;
  u32 ka1, ka2, ka3, ka1w, ka2w, va, vinc, xw, W32, W10, qa, yrd;
};

#define MFMA(a, b, c) __builtin_amdgcn_mfma_f32_16x16x32_bf16((a), (b), (c), 0, 0, 0)

struct KvTmp { u32x2 r1, r2, r3, v1, v2; float s0, s1, s2, s3; };
DEV void kv_read(lptr sm, AState& S, KvTmp& T, int blk) {
  const int so = (blk & 3) * 4096;
  T.r1 = trread(sm + (so ? S.ka1 + so : S.ka1w));
  T.r2 = trread(sm + (so ? S.ka2 + so : S.ka2w));
  T.r3 = trread(sm + S.ka3 + so);
  T.v1 = trread(sm + S.va);
  T.v2 = trread(sm + S.va + 32);
  S.va = ((blk & 3) == 3) ? S.va - 3 * S.vinc : S.va + S.vinc;
}
DEV void kv_a(AState& S, KvTmp& T) {
  T.s0 = dot2(T.r1.y, S.W10, dot2(T.r1.x, S.W32, 0.f));
  T.s2 = dot2(T.r2.x, S.W10, dot2(T.r1.y, S.W32, 0.f));
}
DEV void kv_b(AState& S, KvTmp& T) {
  T.s1 = dot2(T.r3.x, S.W10, dot2(T.r2.y, S.W32, 0.f));
  T.s3 = dot2(T.r3.y, S.W10, dot2(T.r3.x, S.W32, 0.f));
}
DEV void kv_c(lptr sm, AState& S, KvTmp& T, int blk) {
  u32 o0 = pkbf(T.s0 * __uint_as_float(T.v1.x), T.s1 * __uint_as_float(T.v1.y));
  u32 o1 = pkbf(T.s2 * __uint_as_float(T.v2.x), T.s3 * __uint_as_float(T.v2.y));
  *(LDSP u32x2*)(sm + S.xw + 512 * blk) = u32x2{o0, o1};
}

template <bool PA>
DEV void conv_stream(lptr sm, u32 kb, u32 xb, u32 yw, bool l63, AState& S STV_ARG) {
  bf16x8 kf[16], xe[16], xo[16];
  f32x4 ae[16], ao[16];
  const f32x4 zero = {0.f, 0.f, 0.f, 0.f};
#pragma unroll
  for (int d = 0; d < 8; ++d) kf[d] = frag(trread(sm + kb + 256 * d), trread(sm + kb + 256 * d + 32));
  xe[0] = frag(trread(sm + xb + 32), trread(sm + xb));
  xo[0] = frag(trread(sm + xb + 160), trread(sm + xb + 128));
  float c3 = 0.f, c2 = 0.f, c1 = 0.f;
  u32 qe = 0, qo = 0;
  KvTmp T;
  u32x2 sa, sb;
  SCHED0();
#pragma unroll
  for (int s = 0; s < 16; ++s) {
    const int n1 = (16 - s) < 8 ? (16 - s) : 8;
    const int n2 = (s >= 7 && s < 15) ? (s - 6) : 0;
    const int nU = n1 + n2;
    const bool odd = s & 1;
    const int r = s >> 1;
    float eu2, ou2, eu1, ou1, ye, yo;
#pragma unroll
    for (int k = 0; k < 10; ++k) {
      if (k < n1) {
        const int p = s + k;
        const bool first = (s == 0) || (k == 7);
        ae[p] = MFMA(kf[k], xe[s], first ? zero : ae[p]);
        ao[p] = MFMA(kf[k], xo[s], first ? zero : ao[p]);
      } else if (k < nU) {
        const int d = 8 + (k - n1);
        ae[s + 1] = MFMA(kf[d], xe[s + 1 - d], ae[s + 1]);
        ao[s + 1] = MFMA(kf[d], xo[s + 1 - d], ao[s + 1]);
      }
      if (k == 0) {
        if (PA) asm volatile("ds_read_u16_d16_hi %0, %2 offset:%3\n\tds_read_u16_d16_hi %1, %2 offset:%4"
                             : "+v"(qe), "+v"(qo) : "v"(S.qa), "i"((s * 2048) & 16383), "i"(((s * 2048) & 16383) + 1024) : "memory");
        if (!odd) {
          xe[s + 1] = frag(trread(sm + xb + 256 * (s + 1) + 32), trread(sm + xb + 256 * (s + 1)));
          xo[s + 1] = frag(trread(sm + xb + 256 * (s + 1) + 160), trread(sm + xb + 256 * (s + 1) + 128));
        }
        if (!PA && odd && s < 15) {
          xe[s + 1] = frag(trread(sm + xb + 256 * (s + 1) + 32), trread(sm + xb + 256 * (s + 1)));
          xo[s + 1] = frag(trread(sm + xb + 256 * (s + 1) + 160), trread(sm + xb + 256 * (s + 1) + 128));
        }
        if (s >= 6 && s < 14) kf[s + 2] = frag(trread(sm + kb + 256 * (s + 2)), trread(sm + kb + 256 * (s + 2) + 32));
      }
      if (k == 1) {
        float e2 = ae[s][2], e3 = ae[s][3], o2 = ao[s][2], o3 = ao[s][3];
        eu2 = e2 + ror1(l63 ? c3 : e3);
        ou2 = o2 + ror1(l63 ? e3 : o3);
        c3 = o3;
        if (PA && !odd && r < 7) kv_read(sm, S, T, r + 1);
      }
      if (k == 2) {
        float e1 = ae[s][1], o1 = ao[s][1];
        eu1 = e1 + ror1(l63 ? c2 : eu2);
        ou1 = o1 + ror1(l63 ? eu2 : ou2);
        c2 = ou2;
      }
      if (k == 3) {
        float e0 = ae[s][0], o0 = ao[s][0];
        ye = e0 + ror1(l63 ? c1 : eu1);
        yo = o0 + ror1(l63 ? eu1 : ou1);
        c1 = ou1;
        asm volatile("" : "+v"(ye));
        asm volatile("" : "+v"(yo));
      }
      if (k == 4) {
        if (PA) {
          asm volatile("s_waitcnt lgkmcnt(0)" : "+v"(qe), "+v"(qo) :: "memory");
          ye *= __uint_as_float(qe);
          yo *= __uint_as_float(qo);
        }
        const u32 pk = pkbf(ye, yo);
        LDSP u16* yp = (LDSP u16*)(sm + yw + ((s * 256) & 2047));
        yp[0] = (u16)pk;
        yp[64] = (u16)(pk >> 16);
        if (PA && odd) {
          if (r == 0) vmcnt<2>();
          else if (r == 1) vmcnt<1>();
          else if (r == 2 || r == 4) vmcnt<3>();
          else if (r == 3) vmcnt<2>();
          else if (r == 5) vmcnt<1>();
        }
        if (odd && (PA || (r & 1))) {
          wgbarrier();
        }
        if (PA && odd && s < 15) {
          xe[s + 1] = frag(trread(sm + xb + 256 * (s + 1) + 32), trread(sm + xb + 256 * (s + 1)));
          xo[s + 1] = frag(trread(sm + xb + 256 * (s + 1) + 160), trread(sm + xb + 256 * (s + 1) + 128));
        }
      }
      if (PA && !odd && r < 7) {
        if (k == 5) kv_a(S, T);
        if (k == 6) kv_b(S, T);
        if (k == 7) kv_c(sm, S, T, r + 1);
      }
      if (PA && odd) {
        if (k == 5) {
          if (r & 1) {
            sa = trread(sm + S.yrd + 1024 * ((r >> 1) & 1));
            sb = trread(sm + S.yrd + 1024 * ((r >> 1) & 1) + 4 * YS);
            if (r < 5) dma16(S.q, S.rowoff + (S.qrow + 256 * (r + 3)) * (DCH * 2), S.qring + ((r + 3) & 3) * 4096);
          }
        }
        if (k == 6) {
          if (r + 4 < 8) dma16(S.kvsrc, S.rowoff + (S.kvrow + 256 * (r + 4)) * (DCH * 2), S.kvring + ((r + 4) & 3) * 4096);
        }
        if (k == 7 && (r & 1)) {
          *(u32x4*)(S.outp + (size_t)(r - 1) * 256 * (2 * DCH)) = u32x4{sa.x, sa.y, sb.x, sb.y};
        }
      }
      SCHED0();
    }
  }
}

__global__ __launch_bounds__(512, 2) void fused_kernel(Params P) {
  extern __shared__ __attribute__((aligned(16))) char smem[];
  const lptr sm = (lptr)smem;
  const u32 smb = (u32)(uintptr_t)sm;
  const int tid = threadIdx.x;
  const int w = __builtin_amdgcn_readfirstlane(tid >> 6);
  const int lane = tid & 63;
  const int wg = blockIdx.x;
  const int xcd = wg & 7, idx = wg >> 3;
  const bool partA = idx < 10;
  const int g = lane >> 4, li = lane & 15, lq = li >> 2, lp = li & 3;
  STAMP(0);
  if (w == 7) {
    unsigned long long pc;
    u32 len, keep;
    asm volatile("s_getpc_b64 %0\n.Lfk_code_begin:\n\ts_mov_b32 %1, .Lfk_code_end-.Lfk_code_begin" : "=s"(pc), "=s"(len));
    const u32 off = min((u32)(lane + 64 * (idx % 3)) * 128u, len & ~3u);
    asm volatile("s_mov_b32 %0, m0\n\ts_mov_b32 m0, %3\n\ts_nop 0\n\tglobal_load_lds_dword %1, %2\n\ts_mov_b32 m0, %0"
                 : "=&s"(keep) : "v"(off), "s"(pc), "s"(smb + B_YIMG) : "memory");
  }
  const bool l63 = (lane == 63);
  AState S;

  if (partA) {
    const int ch0 = (xcd * 10 + idx) * 8;
    const int ch = ch0 + w;
    const int wq = w & 3;
    const u32 kimg = KIMG + w * KS;
    S.rowoff = (u32)lane * (DCH * 2) + (u32)ch0 * 2;
    S.kvsrc = (w < 4) ? P.k : P.v;
    S.q = P.q;
    S.kvring = smb + ((w < 4) ? RINGK : RINGV) + 1024 * wq;
    S.qring = smb + RINGQ + 1024 * w;
    S.kvrow = 64 * wq;
    S.qrow = 64 * w;
    dma16(S.kvsrc, S.rowoff + (S.kvrow) * (DCH * 2), S.kvring);
    if (w == 0) { if (lane < 4) dma16(P.shift_k, (u32)ch0 * 8 + 16 * lane, smb + SHK); }
#pragma unroll
    for (int j = 0; j < 4; ++j) dma16(P.ssm_k, (u32)ch * (SEQ * 2) + 1024 * j + 16 * lane, smb + kimg + KPADB + 1024 * j);
    dma16(S.kvsrc, S.rowoff + (S.kvrow + 256) * (DCH * 2), S.kvring + 4096);
    dma16(S.q, S.rowoff + S.qrow * (DCH * 2), S.qring);
    dma16(S.kvsrc, S.rowoff + (S.kvrow + 512) * (DCH * 2), S.kvring + 8192);
    if (lane < 10) *(LDSP u32x4*)(sm + XIMG + w * XS + 16 * lane) = u32x4{0, 0, 0, 0};
    if (lane < 6) *(LDSP u32x4*)(sm + kimg + 16 * lane) = u32x4{0, 0, 0, 0};
    if (tid < 4) *(LDSP u32x4*)(sm + RINGK + 16384 - 64 + 16 * tid) = u32x4{0, 0, 0, 0};
    if (tid >= 4 && tid < 8) *(LDSP u32x4*)(sm + ZROW + 16 * (tid - 4)) = u32x4{0, 0, 0, 0};
    const float dsk = ld_bf_uniform(P.ssm_D, ch);
    {
      const int srow = 32 * w + 8 * g + 4 * (lp >> 1);
      const int shalf = 8 * (lp & 1);
      const int r1 = lq - 3;
      const int r2 = (lq < 2) ? lq + 1 : lq - 4;
      const int a1 = 16 * (srow + r1) + shalf, a2 = 16 * (srow + r2) + shalf;
      S.ka1 = RINGK + a1; S.ka1w = RINGK + (a1 & 16383);
      S.ka2 = RINGK + a2; S.ka2w = RINGK + (a2 & 16383);
      S.ka3 = RINGK + 16 * (srow + lq) + shalf;
      S.va = (lq & 1) ? (u32)(RINGV + 16 * (srow + (lq >> 1)) + shalf) : (u32)ZROW;
      S.vinc = (lq & 1) ? 4096u : 0u;
      S.xw = XIMG + (lane & 7) * XS + XPADB + 8 * (lane >> 3) + 64 * w;
      S.qa = smb + RINGQ + 16 * lane + 2 * w;
      S.yrd = YIMG + lq * YS + 2 * (64 * w + 16 * g + 4 * lp);
      S.outp = P.out + ch0 + (size_t)(64 * w + lane) * (2 * DCH);
    }
    vmcnt<7>();
    STAMP(1);
    wgbarrier();
    {
      u32x2 sw = *(LDSP u32x2*)(sm + SHK + 8 * (lane & 7));
      S.W32 = (sw.y >> 16) | (sw.y << 16);
      S.W10 = (sw.x >> 16) | (sw.x << 16);
      KvTmp T;
      kv_read(sm, S, T, 0); kv_a(S, T); kv_b(S, T); kv_c(sm, S, T, 0);
    }
    STAMP(2);
    vmcnt<1>();
    if (lane == 0) {
      LDSP u16* k0 = (LDSP u16*)(sm + kimg + KPADB);
      *k0 = (u16)pkbf(bf2f(*k0) + dsk, 0.f);
    }
    STAMP(3);
    wgbarrier();
    dma16(S.kvsrc, S.rowoff + (S.kvrow + 768) * (DCH * 2), S.kvring + 12288);
    dma16(S.q, S.rowoff + (S.qrow + 512) * (DCH * 2), S.qring + 8192);
    STAMP(4);
    conv_stream<true>(sm, kimg + 8 * (4 * lp + 8 * g + lq), XIMG + w * XS + 224 + 8 * (lp - 8 * g - lq),
                      YIMG + w * YS + 2 * lane, l63, S STV_PASS);
  } else {
    const int ch0 = (xcd * 20 + (idx - 10)) * 4;
    const int cw = w & 3, ps = w >> 2;
    const int ch = ch0 + cw;
    const size_t o = (size_t)ch * NST + 64 * ps + lane;
    const u16 i_ar = P.A_re[o], i_ai = P.A_im[o], i_br = P.B_re[o], i_bi = P.B_im[o], i_cr = P.C_re[o], i_ci = P.C_im[o];
    const float dt = rbf(expf(ld_bf_uniform(P.log_dt, ch)));
    const float dsk = ld_bf_uniform(P.Dskip, ch);
    const float zr = dt * bf2f(i_ar), zi = dt * bf2f(i_ai);
    const cf dB = {dt * bf2f(i_br), dt * bf2f(i_bi)};
    const cf C = {bf2f(i_cr), bf2f(i_ci)};
    const cf W = cmul(C, dB);
    float mx = fmaxf(fabsf(W.r), fabsf(W.i));
    asm volatile("" :: "v"(mx), "v"(zr), "v"(zi) : "memory");
    STAMP(1);
    {
      const u32 rowoff = (u32)lane * (DCH * 2) + (u32)(ch0 & ~7) * 2;
#pragma unroll
      for (int j = 0; j < 4; ++j) dma16(P.u, rowoff + (u32)(512 * j + 64 * w) * (DCH * 2), smb + RAWU + 8192 * j + 1024 * w);
    }
#pragma unroll
    for (int s = 1; s < 64; s <<= 1) mx = fmaxf(mx, __shfl_xor(mx, s));
    int ex = (int)((__float_as_uint(mx) >> 23) & 0xff);
    ex = min(max(ex, 30), 240);
    const float sY = __uint_as_float((u32)(267 - ex) << 23);
    const float so = __uint_as_float((u32)(ex - 27) << 23);
    const float SX = 16384.f;

    f32x4 ak[3];
    const u32 stg = STG + w * STS;
    const u32 rb = stg + li * 272 + g * 16;
    const u32 m0v = smb + w * STS;
#define STGW(row, val_r, val_i) \
    asm volatile("ds_write_addtid_b32 %0 offset:%1" :: "v"(pkh((val_r), (val_i))), "i"(STG + (row) * 272) : "memory")
    {
      const cf E1 = cexp_pow2(zr, zi, 1.f);
      const cf E8 = cexp_pow2(zr, zi, 8.f);
      const cf E128 = cexp_pow2(zr, zi, 128.f);
      const cf E1024 = cexp_pow2(zr, zi, 1024.f);
      const cf E2 = cmul(E1, E1);
      const cf E4 = cmul(E2, E2);
      u32 keep;
      asm volatile("s_mov_b32 %0, m0\n\ts_mov_b32 m0, %1\n\ts_nop 0" : "=&s"(keep) : "s"(m0v) : "memory");
      cf y = {W.r * sY, W.i * sY};
      cf P1 = {E128.r * SX, E128.i * SX};
      cf v0 = {SX, 0.f}, v1 = P1;
#pragma unroll
      for (int b = 0; b < 8; ++b) {
        STGW(2 * b, y.r, -y.i); y = cmul(y, E8);
        STGW(16 + b, v0.r, v0.i); if (b < 7) v0 = cmul(v0, E1);
        STGW(2 * b + 1, y.r, -y.i); if (b < 7) y = cmul(y, E8);
        STGW(24 + b, v1.r, v1.i); if (b < 7) v1 = cmul(v1, E1);
      }
      f16x8 yf[4];
      const f32x4 zero = {0.f, 0.f, 0.f, 0.f};
#pragma unroll
      for (int s = 0; s < 4; ++s) {
        yf[s] = *(const LDSP f16x8*)(sm + rb + 64 * s);
        f16x8 xf = *(const LDSP f16x8*)(sm + rb + 16 * 272 + 64 * s);
        ak[0] = __builtin_amdgcn_mfma_f32_16x16x32_f16(xf, yf[s], s ? ak[0] : zero, 0, 0, 0);
      }
      asm volatile("" ::: "memory");
      cf Pa = cmul(P1, E128);
      cf Pb = {E1024.r * SX, E1024.i * SX};
#pragma unroll
      for (int m = 0; m < 2; ++m) {
        cf v = Pa;
#pragma unroll
        for (int r = 0; r < 4; ++r) {
          STGW(4 * m + r, v.r, v.i); if (r < 3) v = cmul(v, E2);
          if (r & 1) { STGW(16 + 2 * m + (r >> 1), Pb.r, Pb.i); Pb = cmul(Pb, E128); }
        }
        Pa = cmul(Pa, E128);
      }
#pragma unroll
      for (int m = 0; m < 4; ++m) {
        STGW(8 + 2 * m, Pa.r, Pa.i);
        cf v = cmul(Pa, E4);
        STGW(20 + m, Pb.r, Pb.i); Pb = cmul(Pb, E128);
        STGW(9 + 2 * m, v.r, v.i);
        if (m < 3) Pa = cmul(Pa, E128);
      }
      STGW(24, Pb.r, Pb.i);
      asm volatile("s_mov_b32 m0, %0" :: "s"(keep) : "memory");
#pragma unroll
      for (int s = 0; s < 4; ++s) {
        f16x8 x1 = *(const LDSP f16x8*)(sm + rb + 64 * s);
        f16x8 x2 = *(const LDSP f16x8*)(sm + rb + 16 * 272 + 64 * s);
        ak[1] = __builtin_amdgcn_mfma_f32_16x16x32_f16(x1, yf[s], s ? ak[1] : zero, 0, 0, 0);
        ak[2] = __builtin_amdgcn_mfma_f32_16x16x32_f16(x2, yf[s], s ? ak[2] : zero, 0, 0, 0);
      }
    }
#undef STGW
#pragma unroll
    for (int i = 0; i < 3; ++i) ak[i] *= so;
    STAMP(2);
    asm volatile("" ::: "memory");
    if (ps) {
#pragma unroll
      for (int i = 0; i < 3; ++i) *(LDSP f32x4*)(sm + stg + 1024 * i + 16 * lane) = ak[i];
    }
    vmcnt<0>();
    wgbarrier();
    STAMP(3);
    if (ps) {
      const u32 ua = RAWU + 16 * (512 * cw + 16 * g + 4 * lp + lq) + 2 * (ch0 & 4);
      const u32 xw = B_XIMG + (lane & 3) * XS + XPADB + 8 * (lane >> 2) + 1024 * cw;
#pragma unroll
      for (int it = 0; it < 8; ++it) *(LDSP u32x2*)(sm + xw + 128 * it) = trread(sm + ua + 1024 * it);
      if (lane < 10) *(LDSP u32x4*)(sm + B_XIMG + cw * XS + 16 * lane) = u32x4{0, 0, 0, 0};
      wgbarrier();
      const u32 yrd = B_YIMG + lq * YS + 2 * (128 * cw + 16 * g + 4 * lp);
      u16* outp = P.out + DCH + ch0 + (size_t)(128 * cw + lane) * (2 * DCH);
#pragma unroll 1
      for (int j = 0; j < 4; ++j) {
        wgbarrier();
        u32x2 a = trread(sm + yrd + 1024 * (j & 1));
        u32x2 b = trread(sm + yrd + 1024 * (j & 1) + 128);
        *(u32x2*)(outp + (size_t)j * 512 * (2 * DCH)) = a;
        *(u32x2*)(outp + ((size_t)j * 512 + 64) * (2 * DCH)) = b;
      }
    } else {
      const u32 kimg = B_KIMG + cw * KS;
      const u32 tb = stg;
#pragma unroll
      for (int i = 0; i < 3; ++i) ak[i] += *(const LDSP f32x4*)(sm + stg + 4 * STS + 1024 * i + 16 * lane);
      const int b = li, p = g;
      if (lane < 6) *(LDSP u32x4*)(sm + kimg + 16 * lane) = u32x4{0, 0, 0, 0};
      {
        float a0 = ak[0][0] + (lane == 0 ? dsk : 0.f), a1 = ak[0][1], a2 = ak[0][2], a3 = ak[0][3];
        *(LDSP u32x2*)(sm + kimg + KPADB + 2 * (128 * (p >> 1) + 8 * b + 4 * (p & 1))) = u32x2{pkbf(a0, a1), pkbf(a2, a3)};
      }
      {
        float a0 = ak[1][0], a1 = ak[1][1], a2 = ak[1][2], a3 = ak[1][3];
        int i01, i23;
        if (p < 2) { i01 = 64 * p + 4 * b; i23 = i01 + 2; }
        else { i01 = 128 + 64 * (p - 2) + 2 * b; i23 = i01 + 32; }
        *(LDSP u32x2*)(sm + tb + 4 * i01) = u32x2{pkbf(a0, a0), pkbf(a1, a1)};
        *(LDSP u32x2*)(sm + tb + 4 * i23) = u32x2{pkbf(a2, a2), pkbf(a3, a3)};
#pragma unroll
        for (int j = 0; j < 4; ++j) {
          float a = ak[2][j];
          int ii = 256 + 16 * (4 * p + j) + b;
          *(LDSP u32*)(sm + tb + 4 * ii) = pkbf(a, a);
        }
      }
      wavefence();
      const LDSP u32* TB = (const LDSP u32*)(sm + tb);
      {
        const int i0 = 4 * (lane & 31);
        u32 g0 = TB[i0], g1 = TB[i0 + 1], g2 = TB[i0 + 2], g3 = TB[i0 + 3], g4 = TB[i0 + 4];
        u32x4 ov = {g0, (g1 & 0xffffu) | (g2 & 0xffff0000u), g2, (g3 & 0xffffu) | (g4 & 0xffff0000u)};
        *(LDSP u32x4*)(sm + kimg + KPADB + 16 * (32 + (lane & 31))) = ov;
      }
      {
        const int i0 = 128 + 2 * lane;
        u32 g0 = TB[i0], g1 = TB[i0 + 1], g2 = TB[i0 + 2];
        u32x4 ov = {g0, (g0 & 0xffffu) | (g1 & 0xffff0000u), g1, g2};
        *(LDSP u32x4*)(sm + kimg + KPADB + 16 * (64 + lane)) = ov;
      }
#pragma unroll
      for (int h = 0; h < 2; ++h) {
        const int i0 = 256 + 64 * h + lane;
        u32 g0 = TB[i0], g1 = TB[i0 + 1];
        u32 mid = (lane & 1) ? g1 : ((g0 & 0xffffu) | (g1 & 0xffff0000u));
        u32x4 ov = {g0, g0, mid, g1};
        *(LDSP u32x4*)(sm + kimg + KPADB + 16 * (128 + 64 * h + lane)) = ov;
      }
      wgbarrier();
      STAMP(4);
      conv_stream<false>(sm, kimg + 8 * (4 * lp + 8 * g + lq), B_XIMG + cw * XS + 224 + 8 * (lp - 8 * g - lq),
                         B_YIMG + cw * YS + 2 * lane, l63, S STV_PASS);
    }
  }
  asm volatile(".Lfk_code_end:");
}

extern "C" void kernel_launch(void* const* d_in, const int* in_sizes, int n_in,
                              void* d_out, int out_size, void* d_ws, size_t ws_size,
                              hipStream_t stream) {
  Params P;
  P.q = (const u16*)d_in[0];
  P.k = (const u16*)d_in[1];
  P.v = (const u16*)d_in[2];
  P.shift_k = (const u16*)d_in[3];
  P.ssm_k = (const u16*)d_in[4];
  P.ssm_D = (const u16*)d_in[5];
  P.u = (const u16*)d_in[6];
  P.A_re = (const u16*)d_in[7];
  P.A_im = (const u16*)d_in[8];
  P.B_re = (const u16*)d_in[9];
  P.B_im = (const u16*)d_in[10];
  P.C_re = (const u16*)d_in[11];
  P.C_im = (const u16*)d_in[12];
  P.log_dt = (const u16*)d_in[13];
  P.Dskip = (const u16*)d_in[14];
  P.out = (u16*)d_out;
  P.ws = (unsigned long long*)d_ws;
  (void)hipFuncSetAttribute((const void*)fused_kernel, hipFuncAttributeMaxDynamicSharedMemorySize, LDS_BYTES);
  fused_kernel<<<240, 512, LDS_BYTES, stream>>>(P);
}
```

```cpp
#include <hip/hip_runtime.h>
#include <hip/hip_bf16.h>
#include <stdint.h>

typedef __attribute__((ext_vector_type(4))) short s16x4;
typedef __attribute__((ext_vector_type(8))) _Float16 f16x8;
typedef __attribute__((ext_vector_type(8))) __bf16 bf16x8;
typedef __attribute__((ext_vector_type(2))) __bf16 bf16x2;
typedef __attribute__((ext_vector_type(2))) _Float16 f16x2;
typedef __attribute__((ext_vector_type(2))) float f32x2;
typedef __attribute__((ext_vector_type(4))) float f32x4;
typedef __attribute__((ext_vector_type(2))) unsigned int u32x2;
typedef __attribute__((ext_vector_type(4))) unsigned int u32x4;
typedef unsigned int u32;
typedef unsigned short u16;
#define LDSP __attribute__((address_space(3)))
#define DEV __device__ __forceinline__

constexpr int SEQ = 2048;
constexpr int DCH = 640;
constexpr int NST = 128;

constexpr int XS = 4272;
constexpr int XPADB = 160;
constexpr int XIMG = 0;
constexpr int YIMG = 34304;
constexpr int YS = 2112;
constexpr int ZROW = 51200;
constexpr int SHK = 51264;
constexpr int KIMG = 51456;
constexpr int KS = 4224;
constexpr int KPADB = 96;
constexpr int RINGK = 98304;
constexpr int RINGV = 114688;
constexpr int RINGQ = 131072;
constexpr int STS = 8704;
constexpr int STG = 17152;
constexpr int B_XIMG = 0;
constexpr int B_YIMG = 86784;
constexpr int B_KIMG = 95232;
constexpr int RAWU = 114688;
constexpr int LDS_BYTES = 147456;

DEV float bf2f(u16 v) { return __uint_as_float(((u32)v) << 16); }
DEV float bflo(u32 v) { return __uint_as_float(v << 16); }
DEV float bfhi(u32 v) { return __uint_as_float(v & 0xffff0000u); }
DEV u32 pkbf(float a, float b) {
  f32x2 t = {a, b};
  bf16x2 r = __builtin_convertvector(t, bf16x2);
  return __builtin_bit_cast(u32, r);
}
DEV u32 pkh(float a, float b) {
  f32x2 t = {a, b};
  f16x2 r = __builtin_convertvector(t, f16x2);
  return __builtin_bit_cast(u32, r);
}
DEV float rbf(float a) { return bflo(pkbf(a, a)); }
DEV float dot2(u32 a, u32 b, float c) {
  return __builtin_amdgcn_fdot2_f32_bf16(__builtin_bit_cast(bf16x2, a), __builtin_bit_cast(bf16x2, b), c, false);
}
typedef LDSP char* lptr;
DEV u32x2 trread(lptr p) {
  s16x4 r = __builtin_amdgcn_ds_read_tr16_b64_v4i16((LDSP s16x4*)p);
  return __builtin_bit_cast(u32x2, r);
}
DEV bf16x8 frag(u32x2 a, u32x2 b) {
  u32x4 f = {a.x, a.y, b.x, b.y};
  return __builtin_bit_cast(bf16x8, f);
}
DEV float ror1(float v) {
  int r = __builtin_amdgcn_update_dpp(0, __float_as_int(v), 0x13C, 0xf, 0xf, false);
  return __int_as_float(r);
}
DEV void wavefence() { __builtin_amdgcn_fence(__ATOMIC_ACQ_REL, "wavefront"); __builtin_amdgcn_wave_barrier(); }
DEV void wgbarrier() {
  asm volatile("s_waitcnt lgkmcnt(0)" ::: "memory");
  __builtin_amdgcn_s_barrier();
  asm volatile("" ::: "memory");
}
DEV void dma16(const void* base, u32 voff, u32 ldsaddr) {
  u32 keep;
  asm volatile("s_mov_b32 %0, m0\n\ts_mov_b32 m0, %3\n\ts_nop 0\n\tglobal_load_lds_dwordx4 %1, %2\n\ts_mov_b32 m0, %0"
               : "=&s"(keep) : "v"(voff), "s"(base), "s"(ldsaddr) : "memory");
}
DEV void touch(const void* base, u32 voff, u32 ldsaddr) {
  u32 keep;
  asm volatile("s_mov_b32 %0, m0\n\ts_mov_b32 m0, %3\n\ts_nop 0\n\tglobal_load_lds_dword %1, %2\n\ts_mov_b32 m0, %0"
               : "=&s"(keep) : "v"(voff), "s"(base), "s"(ldsaddr) : "memory");
}
template <int N> DEV void vmcnt() { asm volatile("s_waitcnt vmcnt(%0)" :: "i"(N) : "memory"); }
#define SCHED0() __builtin_amdgcn_sched_barrier(0)

struct cf { float r, i; };
DEV cf cmul(cf a, cf b) {
  cf o;
  o.r = fmaf(a.r, b.r, -(a.i * b.i));
  o.i = fmaf(a.r, b.i, a.i * b.r);
  return o;
}
DEV cf cexp_pow2(float zr, float zi, float x) {
  float mag = __builtin_amdgcn_exp2f((zr * x) * 1.44269504088896340736f);
  float th = zi * x;
  const float C_HI = 0.15915494f;
  const float C_LO = 6.4206382e-09f;
  float f = fmaf(th, C_LO, fmaf(th, C_HI, -rintf(th * C_HI)));
  cf o;
  o.r = mag * __builtin_amdgcn_cosf(f);
  o.i = mag * __builtin_amdgcn_sinf(f);
  return o;
}

#define STAMP(n)
#define CSTAMP(n)
#define STV_ARG
#define STV_PASS

struct Params {
  const u16 *q, *k, *v, *shift_k, *ssm_k, *ssm_D;
  const u16 *u, *A_re, *A_im, *B_re, *B_im, *C_re, *C_im, *log_dt, *Dskip;
  u16* out;
  unsigned long long* ws;
};

DEV float ld_bf_uniform(const u16* p, int idx) {
  u32 d = *(const u32*)(p + (idx & ~1));
  return (idx & 1) ? bfhi(d) : bflo(d);
}

struct AState {
  const u16* kvsrc;
  const u16* q;
  u16* outp;
  u32 rowoff;
  u32 kvring;
  u32 qring;
  u32 kvrow, qrow;
  u32 ka1, ka2, ka3, ka1w, ka2w, va, vinc, xw, W32, W10, qa, yrd;
};

#define MFMA(a, b, c) __builtin_amdgcn_mfma_f32_16x16x32_bf16((a), (b), (c), 0, 0, 0)

struct KvTmp { u32x2 r1, r2, r3, v1, v2; float s0, s1, s2, s3; };
DEV void kv_read(lptr sm, AState& S, KvTmp& T, int blk) {
  const int so = (blk & 3) * 4096;
  T.r1 = trread(sm + (so ? S.ka1 + so : S.ka1w));
  T.r2 = trread(sm + (so ? S.ka2 + so : S.ka2w));
  T.r3 = trread(sm + S.ka3 + so);
  T.v1 = trread(sm + S.va);
  T.v2 = trread(sm + S.va + 32);
  S.va = ((blk & 3) == 3) ? S.va - 3 * S.vinc : S.va + S.vinc;
}
DEV void kv_a(AState& S, KvTmp& T) {
  T.s0 = dot2(T.r1.y, S.W10, dot2(T.r1.x, S.W32, 0.f));
  T.s2 = dot2(T.r2.x, S.W10, dot2(T.r1.y, S.W32, 0.f));
}
DEV void kv_b(AState& S, KvTmp& T) {
  T.s1 = dot2(T.r3.x, S.W10, dot2(T.r2.y, S.W32, 0.f));
  T.s3 = dot2(T.r3.y, S.W10, dot2(T.r3.x, S.W32, 0.f));
}
DEV void kv_c(lptr sm, AState& S, KvTmp& T, int blk) {
  u32 o0 = pkbf(T.s0 * __uint_as_float(T.v1.x), T.s1 * __uint_as_float(T.v1.y));
  u32 o1 = pkbf(T.s2 * __uint_as_float(T.v2.x), T.s3 * __uint_as_float(T.v2.y));
  *(LDSP u32x2*)(sm + S.xw + 512 * blk) = u32x2{o0, o1};
}

template <bool PA>
DEV void conv_stream(lptr sm, u32 kb, u32 xb, u32 yw, bool l63, AState& S STV_ARG) {
  bf16x8 kf[16], xe[16], xo[16];
  f32x4 ae[16], ao[16];
  const f32x4 zero = {0.f, 0.f, 0.f, 0.f};
#pragma unroll
  for (int d = 0; d < 8; ++d) kf[d] = frag(trread(sm + kb + 256 * d), trread(sm + kb + 256 * d + 32));
  xe[0] = frag(trread(sm + xb + 32), trread(sm + xb));
  xo[0] = frag(trread(sm + xb + 160), trread(sm + xb + 128));
  float c3 = 0.f, c2 = 0.f, c1 = 0.f;
  u32 qe = 0, qo = 0;
  KvTmp T;
  u32x2 sa, sb;
  SCHED0();
#pragma unroll
  for (int s = 0; s < 16; ++s) {
    const int n1 = (16 - s) < 8 ? (16 - s) : 8;
    const int n2 = (s >= 7 && s < 15) ? (s - 6) : 0;
    const int nU = n1 + n2;
    const bool odd = s & 1;
    const int r = s >> 1;
    float eu2, ou2, eu1, ou1, ye, yo;
#pragma unroll
    for (int k = 0; k < 10; ++k) {
      if (k < n1) {
        const int p = s + k;
        const bool first = (s == 0) || (k == 7);
        ae[p] = MFMA(kf[k], xe[s], first ? zero : ae[p]);
        ao[p] = MFMA(kf[k], xo[s], first ? zero : ao[p]);
      } else if (k < nU) {
        const int d = 8 + (k - n1);
        ae[s + 1] = MFMA(kf[d], xe[s + 1 - d], ae[s + 1]);
        ao[s + 1] = MFMA(kf[d], xo[s + 1 - d], ao[s + 1]);
      }
      if (k == 1) SCHED0();
      if (k == 0) {
        if (PA) asm volatile("ds_read_u16_d16_hi %0, %2 offset:%3\n\tds_read_u16_d16_hi %1, %2 offset:%4"
                             : "+v"(qe), "+v"(qo) : "v"(S.qa), "i"((s * 2048) & 16383), "i"(((s * 2048) & 16383) + 1024) : "memory");
        if (!odd) {
          xe[s + 1] = frag(trread(sm + xb + 256 * (s + 1) + 32), trread(sm + xb + 256 * (s + 1)));
          xo[s + 1] = frag(trread(sm + xb + 256 * (s + 1) + 160), trread(sm + xb + 256 * (s + 1) + 128));
        }
        if (!PA && odd && !(r & 1)) {
          xe[s + 1] = frag(trread(sm + xb + 256 * (s + 1) + 32), trread(sm + xb + 256 * (s + 1)));
          xo[s + 1] = frag(trread(sm + xb + 256 * (s + 1) + 160), trread(sm + xb + 256 * (s + 1) + 128));
        }
        if (s >= 6 && s < 14) kf[s + 2] = frag(trread(sm + kb + 256 * (s + 2)), trread(sm + kb + 256 * (s + 2) + 32));
      }
      if (k == 1) {
        float e2 = ae[s][2], e3 = ae[s][3], o2 = ao[s][2], o3 = ao[s][3];
        eu2 = e2 + ror1(l63 ? c3 : e3);
        ou2 = o2 + ror1(l63 ? e3 : o3);
        c3 = o3;
        if (PA && !odd && r < 7) kv_read(sm, S, T, r + 1);
      }
      if (k == 2) {
        float e1 = ae[s][1], o1 = ao[s][1];
        eu1 = e1 + ror1(l63 ? c2 : eu2);
        ou1 = o1 + ror1(l63 ? eu2 : ou2);
        c2 = ou2;
      }
      if (k == 3) {
        float e0 = ae[s][0], o0 = ao[s][0];
        ye = e0 + ror1(l63 ? c1 : eu1);
        yo = o0 + ror1(l63 ? eu1 : ou1);
        c1 = ou1;
        asm volatile("" : "+v"(ye));
        asm volatile("" : "+v"(yo));
      }
      if (k == 4) {
        if (PA) {
          asm volatile("s_waitcnt lgkmcnt(0)" : "+v"(qe), "+v"(qo) :: "memory");
          ye *= __uint_as_float(qe);
          yo *= __uint_as_float(qo);
        }
        const u32 pk = pkbf(ye, yo);
        LDSP u16* yp = (LDSP u16*)(sm + yw + ((s * 256) & 2047));
        yp[0] = (u16)pk;
        yp[64] = (u16)(pk >> 16);
        if (PA && odd) {
          if (r == 0) vmcnt<4>();
          else if (r == 1) vmcnt<1>();
          else if (r == 2 || r == 4) vmcnt<3>();
          else if (r == 3) vmcnt<2>();
          else if (r == 5) vmcnt<1>();
        }
        if (odd && (PA || (r & 1))) {
          wgbarrier();
        }
        if ((PA || (r & 1)) && odd && s < 15) {
          xe[s + 1] = frag(trread(sm + xb + 256 * (s + 1) + 32), trread(sm + xb + 256 * (s + 1)));
          xo[s + 1] = frag(trread(sm + xb + 256 * (s + 1) + 160), trread(sm + xb + 256 * (s + 1) + 128));
        }
      }
      if (PA && !odd && r < 7) {
        if (k == 5) kv_a(S, T);
        if (k == 6) kv_b(S, T);
        if (k == 7) kv_c(sm, S, T, r + 1);
      }
      if (PA && odd) {
        if (k == 5) {
          if (r & 1) {
            sa = trread(sm + S.yrd + 1024 * ((r >> 1) & 1));
            sb = trread(sm + S.yrd + 1024 * ((r >> 1) & 1) + 4 * YS);
            if (r < 5) dma16(S.q, S.rowoff + (S.qrow + 256 * (r + 3)) * (DCH * 2), S.qring + ((r + 3) & 3) * 4096);
          }
        }
        if (k == 6) {
          if (r + 4 < 8) dma16(S.kvsrc, S.rowoff + (S.kvrow + 256 * (r + 4)) * (DCH * 2), S.kvring + ((r + 4) & 3) * 4096);
        }
        if (k == 7 && (r & 1)) {
          *(u32x4*)(S.outp + (size_t)(r - 1) * 256 * (2 * DCH)) = u32x4{sa.x, sa.y, sb.x, sb.y};
        }
      }
      SCHED0();
    }
  }
}

__global__ __launch_bounds__(512, 2) void fused_kernel(Params P) {
  extern __shared__ __attribute__((aligned(16))) char smem[];
  const lptr sm = (lptr)smem;
  const u32 smb = (u32)(uintptr_t)sm;
  const int tid = threadIdx.x;
  const int w = __builtin_amdgcn_readfirstlane(tid >> 6);
  const int lane = tid & 63;
  const int wg = blockIdx.x;
  const int xcd = wg & 7, idx = wg >> 3;
  const bool partA = idx < 10;
  const int g = lane >> 4, li = lane & 15, lq = li >> 2, lp = li & 3;
  STAMP(0);
  CSTAMP(0);
  if (w == 7) {
    unsigned long long pc;
    u32 len, keep;
    asm volatile("s_getpc_b64 %0\n.Lfk_code_begin:\n\ts_mov_b32 %1, .Lfk_code_end-.Lfk_code_begin" : "=s"(pc), "=s"(len));
    const u32 off = min((u32)(lane + 64 * (idx % 3)) * 128u, len & ~3u);
    asm volatile("s_mov_b32 %0, m0\n\ts_mov_b32 m0, %3\n\ts_nop 0\n\tglobal_load_lds_dword %1, %2\n\ts_mov_b32 m0, %0"
                 : "=&s"(keep) : "v"(off), "s"(pc), "s"(smb + B_YIMG) : "memory");
  }
  const bool l63 = (lane == 63);
  const u32 pfoff = (u32)(lane >> 1) * (DCH * 2) + (u32)(((160 * xcd) >> 7) + (lane & 1)) * 128u;
  AState S;
  if (idx >= 30) {
    const int j = 160 + (idx - 30) * 8 + w;
    __builtin_amdgcn_s_sleep(32);
    touch(P.q, pfoff + (u32)(j - 112) * (32 * DCH * 2), smb + B_YIMG);
    vmcnt<0>();
    return;
  }

  if (partA) {
    const int ch0 = (xcd * 10 + idx) * 8;
    const int ch = ch0 + w;
    const int wq = w & 3;
    const u32 kimg = KIMG + w * KS;
    S.rowoff = (u32)lane * (DCH * 2) + (u32)ch0 * 2;
    S.kvsrc = (w < 4) ? P.k : P.v;
    S.q = P.q;
    S.kvring = smb + ((w < 4) ? RINGK : RINGV) + 1024 * wq;
    S.qring = smb + RINGQ + 1024 * w;
    S.kvrow = 64 * wq;
    S.qrow = 64 * w;
    {
      const int j = idx * 8 + w;
      const u16* pt; int chunk;
      if (j < 32) { pt = (j & 1) ? P.v : P.k; chunk = j >> 1; }
      else if (j < 48) { pt = P.q; chunk = j - 32; }
      else { pt = (j & 1) ? P.v : P.k; chunk = 16 + ((j - 48) >> 1); }
      touch(pt, pfoff + (u32)chunk * (32 * DCH * 2), smb + B_YIMG);
    }
#pragma unroll
    for (int j = 0; j < 2; ++j) dma16(P.ssm_k, (u32)ch * (SEQ * 2) + 1024 * j + 16 * lane, smb + kimg + KPADB + 1024 * j);
    if (w == 0) { if (lane < 4) dma16(P.shift_k, (u32)ch0 * 8 + 16 * lane, smb + SHK); }
    if (lane < 10) *(LDSP u32x4*)(sm + XIMG + w * XS + 16 * lane) = u32x4{0, 0, 0, 0};
    if (lane < 6) *(LDSP u32x4*)(sm + kimg + 16 * lane) = u32x4{0, 0, 0, 0};
    if (tid < 4) *(LDSP u32x4*)(sm + RINGK + 16384 - 64 + 16 * tid) = u32x4{0, 0, 0, 0};
    if (tid >= 4 && tid < 8) *(LDSP u32x4*)(sm + ZROW + 16 * (tid - 4)) = u32x4{0, 0, 0, 0};
    const float dsk = ld_bf_uniform(P.ssm_D, ch);
    {
      const int srow = 32 * w + 8 * g + 4 * (lp >> 1);
      const int shalf = 8 * (lp & 1);
      const int r1 = lq - 3;
      const int r2 = (lq < 2) ? lq + 1 : lq - 4;
      const int a1 = 16 * (srow + r1) + shalf, a2 = 16 * (srow + r2) + shalf;
      S.ka1 = RINGK + a1; S.ka1w = RINGK + (a1 & 16383);
      S.ka2 = RINGK + a2; S.ka2w = RINGK + (a2 & 16383);
      S.ka3 = RINGK + 16 * (srow + lq) + shalf;
      S.va = (lq & 1) ? (u32)(RINGV + 16 * (srow + (lq >> 1)) + shalf) : (u32)ZROW;
      S.vinc = (lq & 1) ? 4096u : 0u;
      S.xw = XIMG + (lane & 7) * XS + XPADB + 8 * (lane >> 3) + 64 * w;
      S.qa = smb + RINGQ + 16 * lane + 2 * w;
      S.yrd = YIMG + lq * YS + 2 * (64 * w + 16 * g + 4 * lp);
      S.outp = P.out + ch0 + (size_t)(64 * w + lane) * (2 * DCH);
    }
    vmcnt<2>();
    STAMP(9);
    dma16(S.kvsrc, S.rowoff + (S.kvrow) * (DCH * 2), S.kvring);
    __builtin_amdgcn_s_barrier();
    dma16(S.kvsrc, S.rowoff + (S.kvrow + 256) * (DCH * 2), S.kvring + 4096);
    dma16(S.q, S.rowoff + S.qrow * (DCH * 2), S.qring);
    vmcnt<2>();
    STAMP(1);
    wgbarrier();
    dma16(S.kvsrc, S.rowoff + (S.kvrow + 512) * (DCH * 2), S.kvring + 8192);
    {
      u32x2 sw = *(LDSP u32x2*)(sm + SHK + 8 * (lane & 7));
      S.W32 = (sw.y >> 16) | (sw.y << 16);
      S.W10 = (sw.x >> 16) | (sw.x << 16);
      KvTmp T;
      kv_read(sm, S, T, 0); kv_a(S, T); kv_b(S, T); kv_c(sm, S, T, 0);
    }
    STAMP(2);
    vmcnt<1>();
    if (lane == 0) {
      LDSP u16* k0 = (LDSP u16*)(sm + kimg + KPADB);
      *k0 = (u16)pkbf(bf2f(*k0) + dsk, 0.f);
    }
    STAMP(3);
    wgbarrier();
#pragma unroll
    for (int j = 2; j < 4; ++j) dma16(P.ssm_k, (u32)ch * (SEQ * 2) + 1024 * j + 16 * lane, smb + kimg + KPADB + 1024 * j);
    dma16(S.kvsrc, S.rowoff + (S.kvrow + 768) * (DCH * 2), S.kvring + 12288);
    dma16(S.q, S.rowoff + (S.qrow + 512) * (DCH * 2), S.qring + 8192);
    STAMP(4);
    conv_stream<true>(sm, kimg + 8 * (4 * lp + 8 * g + lq), XIMG + w * XS + 224 + 8 * (lp - 8 * g - lq),
                      YIMG + w * YS + 2 * lane, l63, S STV_PASS);
  } else {
    const int ch0 = (xcd * 20 + (idx - 10)) * 4;
    const int cw = w & 3, ps = w >> 2;
    const int ch = ch0 + cw;
    const int jp = (idx - 10) * 8 + w;
    if (jp < 64) touch(P.u, pfoff + (u32)jp * (32 * DCH * 2), smb + B_YIMG);
    const size_t o = (size_t)ch * NST + 64 * ps + lane;
    const u16 i_ar = P.A_re[o], i_ai = P.A_im[o], i_br = P.B_re[o], i_bi = P.B_im[o], i_cr = P.C_re[o], i_ci = P.C_im[o];
    const float dt = rbf(expf(ld_bf_uniform(P.log_dt, ch)));
    const float dsk = ld_bf_uniform(P.Dskip, ch);
    const float zr = dt * bf2f(i_ar), zi = dt * bf2f(i_ai);
    const cf dB = {dt * bf2f(i_br), dt * bf2f(i_bi)};
    const cf C = {bf2f(i_cr), bf2f(i_ci)};
    const cf W = cmul(C, dB);
    float mx = fmaxf(fabsf(W.r), fabsf(W.i));
    asm volatile("" :: "v"(mx), "v"(zr), "v"(zi) : "memory");
    STAMP(1);
    if (jp >= 64) {
      const u16* pt; int chunk;
      if (jp < 128) { pt = (jp & 1) ? P.v : P.k; chunk = 32 + ((jp - 64) >> 1); }
      else { pt = P.q; chunk = 16 + (jp - 128); }
      touch(pt, pfoff + (u32)chunk * (32 * DCH * 2), smb + B_YIMG);
    }
    const u32 rowoff = (u32)lane * (DCH * 2) + (u32)(ch0 & ~7) * 2;
#pragma unroll
    for (int s = 1; s < 64; s <<= 1) mx = fmaxf(mx, __shfl_xor(mx, s));
    int ex = (int)((__float_as_uint(mx) >> 23) & 0xff);
    ex = min(max(ex, 30), 240);
    const float sY = __uint_as_float((u32)(267 - ex) << 23);
    const float so = __uint_as_float((u32)(ex - 27) << 23);
    const float SX = 16384.f;

    f32x4 ak[3];
    const u32 stg = STG + w * STS;
    const u32 rb = stg + li * 272 + g * 16;
    const u32 m0v = smb + w * STS;
#define STGW(row, val_r, val_i) \
    asm volatile("ds_write_addtid_b32 %0 offset:%1" :: "v"(pkh((val_r), (val_i))), "i"(STG + (row) * 272) : "memory")
    {
      const cf E1 = cexp_pow2(zr, zi, 1.f);
      const cf E8 = cexp_pow2(zr, zi, 8.f);
      const cf E128 = cexp_pow2(zr, zi, 128.f);
      const cf E1024 = cexp_pow2(zr, zi, 1024.f);
      const cf E2 = cmul(E1, E1);
      const cf E4 = cmul(E2, E2);
      u32 keep;
      asm volatile("s_mov_b32 %0, m0\n\ts_mov_b32 m0, %1\n\ts_nop 0" : "=&s"(keep) : "s"(m0v) : "memory");
      cf y = {W.r * sY, W.i * sY};
      cf P1 = {E128.r * SX, E128.i * SX};
      cf v0 = {SX, 0.f}, v1 = P1;
#pragma unroll
      for (int b = 0; b < 8; ++b) {
        STGW(2 * b, y.r, -y.i); y = cmul(y, E8);
        STGW(16 + b, v0.r, v0.i); if (b < 7) v0 = cmul(v0, E1);
        STGW(2 * b + 1, y.r, -y.i); if (b < 7) y = cmul(y, E8);
        STGW(24 + b, v1.r, v1.i); if (b < 7) v1 = cmul(v1, E1);
      }
      f16x8 yf[4];
      const f32x4 zero = {0.f, 0.f, 0.f, 0.f};
#pragma unroll
      for (int s = 0; s < 4; ++s) {
        yf[s] = *(const LDSP f16x8*)(sm + rb + 64 * s);
        f16x8 xf = *(const LDSP f16x8*)(sm + rb + 16 * 272 + 64 * s);
        ak[0] = __builtin_amdgcn_mfma_f32_16x16x32_f16(xf, yf[s], s ? ak[0] : zero, 0, 0, 0);
      }
      dma16(P.u, rowoff + (u32)(64 * w) * (DCH * 2), smb + RAWU + 1024 * w);
      cf Pa = cmul(P1, E128);
      cf Pb = {E1024.r * SX, E1024.i * SX};
#pragma unroll
      for (int m = 0; m < 2; ++m) {
        cf v = Pa;
#pragma unroll
        for (int r = 0; r < 4; ++r) {
          STGW(4 * m + r, v.r, v.i); if (r < 3) v = cmul(v, E2);
          if (r & 1) { STGW(16 + 2 * m + (r >> 1), Pb.r, Pb.i); Pb = cmul(Pb, E128); }
        }
        Pa = cmul(Pa, E128);
      }
#pragma unroll
      for (int m = 0; m < 4; ++m) {
        STGW(8 + 2 * m, Pa.r, Pa.i);
        cf v = cmul(Pa, E4);
        STGW(20 + m, Pb.r, Pb.i); Pb = cmul(Pb, E128);
        STGW(9 + 2 * m, v.r, v.i);
        if (m < 3) Pa = cmul(Pa, E128);
      }
      STGW(24, Pb.r, Pb.i);
      asm volatile("s_mov_b32 m0, %0" :: "s"(keep) : "memory");
      dma16(P.u, rowoff + (u32)(512 + 64 * w) * (DCH * 2), smb + RAWU + 8192 + 1024 * w);
#pragma unroll
      for (int s = 0; s < 4; ++s) {
        f16x8 x1 = *(const LDSP f16x8*)(sm + rb + 64 * s);
        f16x8 x2 = *(const LDSP f16x8*)(sm + rb + 16 * 272 + 64 * s);
        ak[1] = __builtin_amdgcn_mfma_f32_16x16x32_f16(x1, yf[s], s ? ak[1] : zero, 0, 0, 0);
        ak[2] = __builtin_amdgcn_mfma_f32_16x16x32_f16(x2, yf[s], s ? ak[2] : zero, 0, 0, 0);
      }
    }
#undef STGW
#pragma unroll
    for (int i = 0; i < 3; ++i) ak[i] *= so;
    STAMP(2);
    asm volatile("" ::: "memory");
    if (ps) {
#pragma unroll
      for (int i = 0; i < 3; ++i) *(LDSP f32x4*)(sm + stg + 1024 * i + 16 * lane) = ak[i];
    }
    vmcnt<1>();
    wgbarrier();
    STAMP(3);
    if (ps) {
#pragma unroll
      for (int j = 0; j < 4; ++j)
        dma16(P.u, rowoff + (u32)(1024 + 512 * (j >> 1) + 128 * cw + 64 * (j & 1)) * (DCH * 2), smb + RAWU + 16384 + 8192 * (j >> 1) + 2048 * cw + 1024 * (j & 1));
      const u32 ua = RAWU + 16 * (128 * cw + 16 * g + 4 * lp + lq) + 2 * (ch0 & 4);
      const u32 xw = B_XIMG + (lane & 3) * XS + XPADB + 8 * (lane >> 2) + 256 * cw;
      *(LDSP u32x2*)(sm + xw) = trread(sm + ua);
      *(LDSP u32x2*)(sm + xw + 128) = trread(sm + ua + 1024);
      if (lane < 10) *(LDSP u32x4*)(sm + B_XIMG + cw * XS + 16 * lane) = u32x4{0, 0, 0, 0};
      vmcnt<4>();
      wgbarrier();
      const u32 yrd = B_YIMG + lq * YS + 2 * (128 * cw + 16 * g + 4 * lp);
      u16* outp = P.out + DCH + ch0 + (size_t)(128 * cw + lane) * (2 * DCH);
#pragma unroll
      for (int j = 0; j < 4; ++j) {
        if (j == 1) vmcnt<4>();
        if (j == 2) vmcnt<4>();
        if (j < 3) {
          *(LDSP u32x2*)(sm + xw + 1024 * (j + 1)) = trread(sm + ua + 8192 * (j + 1));
          *(LDSP u32x2*)(sm + xw + 1024 * (j + 1) + 128) = trread(sm + ua + 8192 * (j + 1) + 1024);
        }
        wgbarrier();
        u32x2 a = trread(sm + yrd + 1024 * (j & 1));
        u32x2 b = trread(sm + yrd + 1024 * (j & 1) + 128);
        *(u32x2*)(outp + (size_t)j * 512 * (2 * DCH)) = a;
        *(u32x2*)(outp + ((size_t)j * 512 + 64) * (2 * DCH)) = b;
      }
    } else {
      const u32 kimg = B_KIMG + cw * KS;
      const u32 tb = stg;
#pragma unroll
      for (int i = 0; i < 3; ++i) ak[i] += *(const LDSP f32x4*)(sm + stg + 4 * STS + 1024 * i + 16 * lane);
      const int b = li, p = g;
      if (lane < 6) *(LDSP u32x4*)(sm + kimg + 16 * lane) = u32x4{0, 0, 0, 0};
      {
        float a0 = ak[0][0] + (lane == 0 ? dsk : 0.f), a1 = ak[0][1], a2 = ak[0][2], a3 = ak[0][3];
        *(LDSP u32x2*)(sm + kimg + KPADB + 2 * (128 * (p >> 1) + 8 * b + 4 * (p & 1))) = u32x2{pkbf(a0, a1), pkbf(a2, a3)};
      }
      {
        float a0 = ak[1][0], a1 = ak[1][1], a2 = ak[1][2], a3 = ak[1][3];
        int i01, i23;
        if (p < 2) { i01 = 64 * p + 4 * b; i23 = i01 + 2; }
        else { i01 = 128 + 64 * (p - 2) + 2 * b; i23 = i01 + 32; }
        *(LDSP u32x2*)(sm + tb + 4 * i01) = u32x2{pkbf(a0, a0), pkbf(a1, a1)};
        *(LDSP u32x2*)(sm + tb + 4 * i23) = u32x2{pkbf(a2, a2), pkbf(a3, a3)};
#pragma unroll
        for (int j = 0; j < 4; ++j) {
          float a = ak[2][j];
          int ii = 256 + 16 * (4 * p + j) + b;
          *(LDSP u32*)(sm + tb + 4 * ii) = pkbf(a, a);
        }
      }
      wavefence();
      const LDSP u32* TB = (const LDSP u32*)(sm + tb);
      {
        const int i0 = 4 * (lane & 31);
        u32 g0 = TB[i0], g1 = TB[i0 + 1], g2 = TB[i0 + 2], g3 = TB[i0 + 3], g4 = TB[i0 + 4];
        u32x4 ov = {g0, (g1 & 0xffffu) | (g2 & 0xffff0000u), g2, (g3 & 0xffffu) | (g4 & 0xffff0000u)};
        *(LDSP u32x4*)(sm + kimg + KPADB + 16 * (32 + (lane & 31))) = ov;
      }
      {
        const int i0 = 128 + 2 * lane;
        u32 g0 = TB[i0], g1 = TB[i0 + 1], g2 = TB[i0 + 2];
        u32x4 ov = {g0, (g0 & 0xffffu) | (g1 & 0xffff0000u), g1, g2};
        *(LDSP u32x4*)(sm + kimg + KPADB + 16 * (64 + lane)) = ov;
      }
#pragma unroll
      for (int h = 0; h < 2; ++h) {
        const int i0 = 256 + 64 * h + lane;
        u32 g0 = TB[i0], g1 = TB[i0 + 1];
        u32 mid = (lane & 1) ? g1 : ((g0 & 0xffffu) | (g1 & 0xffff0000u));
        u32x4 ov = {g0, g0, mid, g1};
        *(LDSP u32x4*)(sm + kimg + KPADB + 16 * (128 + 64 * h + lane)) = ov;
      }
      vmcnt<0>();
      wgbarrier();
      STAMP(4);
      conv_stream<false>(sm, kimg + 8 * (4 * lp + 8 * g + lq), B_XIMG + cw * XS + 224 + 8 * (lp - 8 * g - lq),
                         B_YIMG + cw * YS + 2 * lane, l63, S STV_PASS);
    }
  }
  asm volatile(".Lfk_code_end:");
}

extern "C" void kernel_launch(void* const* d_in, const int* in_sizes, int n_in,
                              void* d_out, int out_size, void* d_ws, size_t ws_size,
                              hipStream_t stream) {
  Params P;
  P.q = (const u16*)d_in[0];
  P.k = (const u16*)d_in[1];
  P.v = (const u16*)d_in[2];
  P.shift_k = (const u16*)d_in[3];
  P.ssm_k = (const u16*)d_in[4];
  P.ssm_D = (const u16*)d_in[5];
  P.u = (const u16*)d_in[6];
  P.A_re = (const u16*)d_in[7];
  P.A_im = (const u16*)d_in[8];
  P.B_re = (const u16*)d_in[9];
  P.B_im = (const u16*)d_in[10];
  P.C_re = (const u16*)d_in[11];
  P.C_im = (const u16*)d_in[12];
  P.log_dt = (const u16*)d_in[13];
  P.Dskip = (const u16*)d_in[14];
  P.out = (u16*)d_out;
  P.ws = (unsigned long long*)d_ws;
  (void)hipFuncSetAttribute((const void*)fused_kernel, hipFuncAttributeMaxDynamicSharedMemorySize, LDS_BYTES);
  fused_kernel<<<256, 512, LDS_BYTES, stream>>>(P);
}
```

```cpp
#define STAMPS
#include <hip/hip_runtime.h>
#include <hip/hip_bf16.h>
#include <stdint.h>

typedef __attribute__((ext_vector_type(4))) short s16x4;
typedef __attribute__((ext_vector_type(8))) _Float16 f16x8;
typedef __attribute__((ext_vector_type(8))) __bf16 bf16x8;
typedef __attribute__((ext_vector_type(2))) __bf16 bf16x2;
typedef __attribute__((ext_vector_type(2))) _Float16 f16x2;
typedef __attribute__((ext_vector_type(2))) float f32x2;
typedef __attribute__((ext_vector_type(4))) float f32x4;
typedef __attribute__((ext_vector_type(2))) unsigned int u32x2;
typedef __attribute__((ext_vector_type(4))) unsigned int u32x4;
typedef unsigned int u32;
typedef unsigned short u16;
#define LDSP __attribute__((address_space(3)))
#define DEV __device__ __forceinline__

constexpr int SEQ = 2048;
constexpr int DCH = 640;
constexpr int NST = 128;

constexpr int XS = 4272;
constexpr int XPADB = 160;
constexpr int XIMG = 0;
constexpr int YIMG = 34304;
constexpr int YS = 2112;
constexpr int ZROW = 51200;
constexpr int SHK = 51264;
constexpr int KIMG = 51456;
constexpr int KS = 4224;
constexpr int KPADB = 96;
constexpr int RINGK = 98304;
constexpr int RINGV = 114688;
constexpr int RINGQ = 131072;
constexpr int STS = 8704;
constexpr int STG = 17152;
constexpr int B_XIMG = 0;
constexpr int B_YIMG = 86784;
constexpr int B_KIMG = 95232;
constexpr int RAWU = 114688;
constexpr int LDS_BYTES = 147456;

DEV float bf2f(u16 v) { return __uint_as_float(((u32)v) << 16); }
DEV float bflo(u32 v) { return __uint_as_float(v << 16); }
DEV float bfhi(u32 v) { return __uint_as_float(v & 0xffff0000u); }
DEV u32 pkbf(float a, float b) {
  f32x2 t = {a, b};
  bf16x2 r = __builtin_convertvector(t, bf16x2);
  return __builtin_bit_cast(u32, r);
}
DEV u32 pkh(float a, float b) {
  f32x2 t = {a, b};
  f16x2 r = __builtin_convertvector(t, f16x2);
  return __builtin_bit_cast(u32, r);
}
DEV float rbf(float a) { return bflo(pkbf(a, a)); }
DEV float dot2(u32 a, u32 b, float c) {
  return __builtin_amdgcn_fdot2_f32_bf16(__builtin_bit_cast(bf16x2, a), __builtin_bit_cast(bf16x2, b), c, false);
}
typedef LDSP char* lptr;
DEV u32x2 trread(lptr p) {
  s16x4 r = __builtin_amdgcn_ds_read_tr16_b64_v4i16((LDSP s16x4*)p);
  return __builtin_bit_cast(u32x2, r);
}
DEV bf16x8 frag(u32x2 a, u32x2 b) {
  u32x4 f = {a.x, a.y, b.x, b.y};
  return __builtin_bit_cast(bf16x8, f);
}
DEV float ror1(float v) {
  int r = __builtin_amdgcn_update_dpp(0, __float_as_int(v), 0x13C, 0xf, 0xf, false);
  return __int_as_float(r);
}
DEV void wavefence() { __builtin_amdgcn_fence(__ATOMIC_ACQ_REL, "wavefront"); __builtin_amdgcn_wave_barrier(); }
DEV void wgbarrier() {
  asm volatile("s_waitcnt lgkmcnt(0)" ::: "memory");
  __builtin_amdgcn_s_barrier();
  asm volatile("" ::: "memory");
}
DEV void dma16(const void* base, u32 voff, u32 ldsaddr) {
  u32 keep;
  asm volatile("s_mov_b32 %0, m0\n\ts_mov_b32 m0, %3\n\ts_nop 0\n\tglobal_load_lds_dwordx4 %1, %2\n\ts_mov_b32 m0, %0"
               : "=&s"(keep) : "v"(voff), "s"(base), "s"(ldsaddr) : "memory");
}
DEV void touch(const void* base, u32 voff, u32 ldsaddr) {
  u32 keep;
  asm volatile("s_mov_b32 %0, m0\n\ts_mov_b32 m0, %3\n\ts_nop 0\n\tglobal_load_lds_dword %1, %2\n\ts_mov_b32 m0, %0"
               : "=&s"(keep) : "v"(voff), "s"(base), "s"(ldsaddr) : "memory");
}
template <int N> DEV void vmcnt() { asm volatile("s_waitcnt vmcnt(%0)" :: "i"(N) : "memory"); }
#define SCHED0() __builtin_amdgcn_sched_barrier(0)

struct cf { float r, i; };
DEV cf cmul(cf a, cf b) {
  cf o;
  o.r = fmaf(a.r, b.r, -(a.i * b.i));
  o.i = fmaf(a.r, b.i, a.i * b.r);
  return o;
}
DEV cf cexp_pow2(float zr, float zi, float x) {
  float mag = __builtin_amdgcn_exp2f((zr * x) * 1.44269504088896340736f);
  float th = zi * x;
  const float C_HI = 0.15915494f;
  const float C_LO = 6.4206382e-09f;
  float f = fmaf(th, C_LO, fmaf(th, C_HI, -rintf(th * C_HI)));
  cf o;
  o.r = mag * __builtin_amdgcn_cosf(f);
  o.i = mag * __builtin_amdgcn_sinf(f);
  return o;
}

#define STAMP_TYPE(wg) (((wg) >> 3) < 10 ? 0 : 1)
#define STAMP(n) do { __builtin_amdgcn_sched_barrier(0); unsigned long long _t = __builtin_amdgcn_s_memrealtime(); __builtin_amdgcn_s_waitcnt(0xC07F); stv[n] = _t; __builtin_amdgcn_sched_barrier(0); } while (0)
#define CSTAMP(n) do { __builtin_amdgcn_sched_barrier(0); unsigned long long _t = __builtin_amdgcn_s_memtime(); __builtin_amdgcn_s_waitcnt(0xC07F); cyv[n] = _t; __builtin_amdgcn_sched_barrier(0); } while (0)
#define STV_ARG , unsigned long long (&stv)[16]
#define STV_PASS , stv

struct Params {
  const u16 *q, *k, *v, *shift_k, *ssm_k, *ssm_D;
  const u16 *u, *A_re, *A_im, *B_re, *B_im, *C_re, *C_im, *log_dt, *Dskip;
  u16* out;
  unsigned long long* ws;
};

DEV float ld_bf_uniform(const u16* p, int idx) {
  u32 d = *(const u32*)(p + (idx & ~1));
  return (idx & 1) ? bfhi(d) : bflo(d);
}

struct AState {
  const u16* kvsrc;
  const u16* q;
  u16* outp;
  u32 rowoff;
  u32 kvring;
  u32 qring;
  u32 kvrow, qrow;
  u32 ka1, ka2, ka3, ka1w, ka2w, va, vinc, xw, W32, W10, qa, yrd;
};

#define MFMA(a, b, c) __builtin_amdgcn_mfma_f32_16x16x32_bf16((a), (b), (c), 0, 0, 0)

struct KvTmp { u32x2 r1, r2, r3, v1, v2; float s0, s1, s2, s3; };
DEV void kv_read(lptr sm, AState& S, KvTmp& T, int blk) {
  const int so = (blk & 3) * 4096;
  T.r1 = trread(sm + (so ? S.ka1 + so : S.ka1w));
  T.r2 = trread(sm + (so ? S.ka2 + so : S.ka2w));
  T.r3 = trread(sm + S.ka3 + so);
  T.v1 = trread(sm + S.va);
  T.v2 = trread(sm + S.va + 32);
  S.va = ((blk & 3) == 3) ? S.va - 3 * S.vinc : S.va + S.vinc;
}
DEV void kv_a(AState& S, KvTmp& T) {
  T.s0 = dot2(T.r1.y, S.W10, dot2(T.r1.x, S.W32, 0.f));
  T.s2 = dot2(T.r2.x, S.W10, dot2(T.r1.y, S.W32, 0.f));
}
DEV void kv_b(AState& S, KvTmp& T) {
  T.s1 = dot2(T.r3.x, S.W10, dot2(T.r2.y, S.W32, 0.f));
  T.s3 = dot2(T.r3.y, S.W10, dot2(T.r3.x, S.W32, 0.f));
}
DEV void kv_c(lptr sm, AState& S, KvTmp& T, int blk) {
  u32 o0 = pkbf(T.s0 * __uint_as_float(T.v1.x), T.s1 * __uint_as_float(T.v1.y));
  u32 o1 = pkbf(T.s2 * __uint_as_float(T.v2.x), T.s3 * __uint_as_float(T.v2.y));
  *(LDSP u32x2*)(sm + S.xw + 512 * blk) = u32x2{o0, o1};
}

template <bool PA>
DEV void conv_stream(lptr sm, u32 kb, u32 xb, u32 yw, bool l63, AState& S STV_ARG) {
  bf16x8 kf[16], xe[16], xo[16];
  f32x4 ae[16], ao[16];
  const f32x4 zero = {0.f, 0.f, 0.f, 0.f};
#pragma unroll
  for (int d = 0; d < 8; ++d) kf[d] = frag(trread(sm + kb + 256 * d), trread(sm + kb + 256 * d + 32));
  xe[0] = frag(trread(sm + xb + 32), trread(sm + xb));
  xo[0] = frag(trread(sm + xb + 160), trread(sm + xb + 128));
  float c3 = 0.f, c2 = 0.f, c1 = 0.f;
  u32 qe = 0, qo = 0;
  KvTmp T;
  u32x2 sa, sb;
  SCHED0();
#pragma unroll
  for (int s = 0; s < 16; ++s) {
    const int n1 = (16 - s) < 8 ? (16 - s) : 8;
    const int n2 = (s >= 7 && s < 15) ? (s - 6) : 0;
    const int nU = n1 + n2;
    const bool odd = s & 1;
    const int r = s >> 1;
    float eu2, ou2, eu1, ou1, ye, yo;
#pragma unroll
    for (int k = 0; k < 10; ++k) {
      if (k < n1) {
        const int p = s + k;
        const bool first = (s == 0) || (k == 7);
        ae[p] = MFMA(kf[k], xe[s], first ? zero : ae[p]);
        ao[p] = MFMA(kf[k], xo[s], first ? zero : ao[p]);
      } else if (k < nU) {
        const int d = 8 + (k - n1);
        ae[s + 1] = MFMA(kf[d], xe[s + 1 - d], ae[s + 1]);
        ao[s + 1] = MFMA(kf[d], xo[s + 1 - d], ao[s + 1]);
      }
      if (k == 1) SCHED0();
      if (k == 0) {
        if (PA) asm volatile("ds_read_u16_d16_hi %0, %2 offset:%3\n\tds_read_u16_d16_hi %1, %2 offset:%4"
                             : "+v"(qe), "+v"(qo) : "v"(S.qa), "i"((s * 2048) & 16383), "i"(((s * 2048) & 16383) + 1024) : "memory");
        if (!odd) {
          xe[s + 1] = frag(trread(sm + xb + 256 * (s + 1) + 32), trread(sm + xb + 256 * (s + 1)));
          xo[s + 1] = frag(trread(sm + xb + 256 * (s + 1) + 160), trread(sm + xb + 256 * (s + 1) + 128));
        }
        if (!PA && odd && !(r & 1)) {
          xe[s + 1] = frag(trread(sm + xb + 256 * (s + 1) + 32), trread(sm + xb + 256 * (s + 1)));
          xo[s + 1] = frag(trread(sm + xb + 256 * (s + 1) + 160), trread(sm + xb + 256 * (s + 1) + 128));
        }
        if (s >= 6 && s < 14) kf[s + 2] = frag(trread(sm + kb + 256 * (s + 2)), trread(sm + kb + 256 * (s + 2) + 32));
      }
      if (k == 1) {
        float e2 = ae[s][2], e3 = ae[s][3], o2 = ao[s][2], o3 = ao[s][3];
        eu2 = e2 + ror1(l63 ? c3 : e3);
        ou2 = o2 + ror1(l63 ? e3 : o3);
        c3 = o3;
        if (PA && !odd && r < 7) kv_read(sm, S, T, r + 1);
      }
      if (k == 2) {
        float e1 = ae[s][1], o1 = ao[s][1];
        eu1 = e1 + ror1(l63 ? c2 : eu2);
        ou1 = o1 + ror1(l63 ? eu2 : ou2);
        c2 = ou2;
      }
      if (k == 3) {
        float e0 = ae[s][0], o0 = ao[s][0];
        ye = e0 + ror1(l63 ? c1 : eu1);
        yo = o0 + ror1(l63 ? eu1 : ou1);
        c1 = ou1;
        asm volatile("" : "+v"(ye));
        asm volatile("" : "+v"(yo));
      }
      if (k == 4) {
        if (PA) {
          asm volatile("s_waitcnt lgkmcnt(0)" : "+v"(qe), "+v"(qo) :: "memory");
          ye *= __uint_as_float(qe);
          yo *= __uint_as_float(qo);
        }
        const u32 pk = pkbf(ye, yo);
        LDSP u16* yp = (LDSP u16*)(sm + yw + ((s * 256) & 2047));
        yp[0] = (u16)pk;
        yp[64] = (u16)(pk >> 16);
        if (PA && odd) {
          if (r == 0) vmcnt<4>();
          else if (r == 1) vmcnt<1>();
          else if (r == 2 || r == 4) vmcnt<3>();
          else if (r == 3) vmcnt<2>();
          else if (r == 5) vmcnt<1>();
        }
        if (odd && (PA || (r & 1))) {
          wgbarrier();
          if (r == 1) STAMP(5); if (r == 3) STAMP(6); if (r == 5) STAMP(7); if (r == 7) STAMP(8);
        }
        if ((PA || (r & 1)) && odd && s < 15) {
          xe[s + 1] = frag(trread(sm + xb + 256 * (s + 1) + 32), trread(sm + xb + 256 * (s + 1)));
          xo[s + 1] = frag(trread(sm + xb + 256 * (s + 1) + 160), trread(sm + xb + 256 * (s + 1) + 128));
        }
      }
      if (PA && !odd && r < 7) {
        if (k == 5) kv_a(S, T);
        if (k == 6) kv_b(S, T);
        if (k == 7) kv_c(sm, S, T, r + 1);
      }
      if (PA && odd) {
        if (k == 5) {
          if (r & 1) {
            sa = trread(sm + S.yrd + 1024 * ((r >> 1) & 1));
            sb = trread(sm + S.yrd + 1024 * ((r >> 1) & 1) + 4 * YS);
            if (r < 5) dma16(S.q, S.rowoff + (S.qrow + 256 * (r + 3)) * (DCH * 2), S.qring + ((r + 3) & 3) * 4096);
          }
        }
        if (k == 6) {
          if (r + 4 < 8) dma16(S.kvsrc, S.rowoff + (S.kvrow + 256 * (r + 4)) * (DCH * 2), S.kvring + ((r + 4) & 3) * 4096);
        }
        if (k == 7 && (r & 1)) {
          *(u32x4*)(S.outp + (size_t)(r - 1) * 256 * (2 * DCH)) = u32x4{sa.x, sa.y, sb.x, sb.y};
        }
      }
      SCHED0();
    }
  }
}

__global__ __launch_bounds__(512, 2) void fused_kernel(Params P) {
  extern __shared__ __attribute__((aligned(16))) char smem[];
  const lptr sm = (lptr)smem;
  const u32 smb = (u32)(uintptr_t)sm;
  const int tid = threadIdx.x;
  const int w = __builtin_amdgcn_readfirstlane(tid >> 6);
  const int lane = tid & 63;
  const int wg = blockIdx.x;
  const int xcd = wg & 7, idx = wg >> 3;
  const bool partA = idx < 10;
  const int g = lane >> 4, li = lane & 15, lq = li >> 2, lp = li & 3;
  unsigned long long stv[16]; unsigned long long cyv[2];
#pragma unroll
  for (int i = 0; i < 16; ++i) stv[i] = 0;
  STAMP(0);
  CSTAMP(0);
  if (w == 7) {
    unsigned long long pc;
    u32 len, keep;
    asm volatile("s_getpc_b64 %0\n.Lfk_code_begin:\n\ts_mov_b32 %1, .Lfk_code_end-.Lfk_code_begin" : "=s"(pc), "=s"(len));
    const u32 off = min((u32)(lane + 64 * (idx % 3)) * 128u, len & ~3u);
    asm volatile("s_mov_b32 %0, m0\n\ts_mov_b32 m0, %3\n\ts_nop 0\n\tglobal_load_lds_dword %1, %2\n\ts_mov_b32 m0, %0"
                 : "=&s"(keep) : "v"(off), "s"(pc), "s"(smb + B_YIMG) : "memory");
  }
  const bool l63 = (lane == 63);
  const u32 pfoff = (u32)(lane >> 1) * (DCH * 2) + (u32)(((160 * xcd) >> 7) + (lane & 1)) * 128u;
  AState S;
  if (idx >= 30) {
    const int j = 160 + (idx - 30) * 8 + w;
    __builtin_amdgcn_s_sleep(32);
    touch(P.q, pfoff + (u32)(j - 112) * (32 * DCH * 2), smb + B_YIMG);
    vmcnt<0>();
    return;
  }

  if (partA) {
    const int ch0 = (xcd * 10 + idx) * 8;
    const int ch = ch0 + w;
    const int wq = w & 3;
    const u32 kimg = KIMG + w * KS;
    S.rowoff = (u32)lane * (DCH * 2) + (u32)ch0 * 2;
    S.kvsrc = (w < 4) ? P.k : P.v;
    S.q = P.q;
    S.kvring = smb + ((w < 4) ? RINGK : RINGV) + 1024 * wq;
    S.qring = smb + RINGQ + 1024 * w;
    S.kvrow = 64 * wq;
    S.qrow = 64 * w;
    {
      const int j = idx * 8 + w;
      const u16* pt; int chunk;
      if (j < 32) { pt = (j & 1) ? P.v : P.k; chunk = j >> 1; }
      else if (j < 48) { pt = P.q; chunk = j - 32; }
      else { pt = (j & 1) ? P.v : P.k; chunk = 16 + ((j - 48) >> 1); }
      touch(pt, pfoff + (u32)chunk * (32 * DCH * 2), smb + B_YIMG);
    }
#pragma unroll
    for (int j = 0; j < 2; ++j) dma16(P.ssm_k, (u32)ch * (SEQ * 2) + 1024 * j + 16 * lane, smb + kimg + KPADB + 1024 * j);
    if (w == 0) { if (lane < 4) dma16(P.shift_k, (u32)ch0 * 8 + 16 * lane, smb + SHK); }
    if (lane < 10) *(LDSP u32x4*)(sm + XIMG + w * XS + 16 * lane) = u32x4{0, 0, 0, 0};
    if (lane < 6) *(LDSP u32x4*)(sm + kimg + 16 * lane) = u32x4{0, 0, 0, 0};
    if (tid < 4) *(LDSP u32x4*)(sm + RINGK + 16384 - 64 + 16 * tid) = u32x4{0, 0, 0, 0};
    if (tid >= 4 && tid < 8) *(LDSP u32x4*)(sm + ZROW + 16 * (tid - 4)) = u32x4{0, 0, 0, 0};
    const float dsk = ld_bf_uniform(P.ssm_D, ch);
    {
      const int srow = 32 * w + 8 * g + 4 * (lp >> 1);
      const int shalf = 8 * (lp & 1);
      const int r1 = lq - 3;
      const int r2 = (lq < 2) ? lq + 1 : lq - 4;
      const int a1 = 16 * (srow + r1) + shalf, a2 = 16 * (srow + r2) + shalf;
      S.ka1 = RINGK + a1; S.ka1w = RINGK + (a1 & 16383);
      S.ka2 = RINGK + a2; S.ka2w = RINGK + (a2 & 16383);
      S.ka3 = RINGK + 16 * (srow + lq) + shalf;
      S.va = (lq & 1) ? (u32)(RINGV + 16 * (srow + (lq >> 1)) + shalf) : (u32)ZROW;
      S.vinc = (lq & 1) ? 4096u : 0u;
      S.xw = XIMG + (lane & 7) * XS + XPADB + 8 * (lane >> 3) + 64 * w;
      S.qa = smb + RINGQ + 16 * lane + 2 * w;
      S.yrd = YIMG + lq * YS + 2 * (64 * w + 16 * g + 4 * lp);
      S.outp = P.out + ch0 + (size_t)(64 * w + lane) * (2 * DCH);
    }
    vmcnt<2>();
    STAMP(9);
    dma16(S.kvsrc, S.rowoff + (S.kvrow) * (DCH * 2), S.kvring);
    __builtin_amdgcn_s_barrier();
    dma16(S.kvsrc, S.rowoff + (S.kvrow + 256) * (DCH * 2), S.kvring + 4096);
    dma16(S.q, S.rowoff + S.qrow * (DCH * 2), S.qring);
    vmcnt<2>();
    STAMP(1);
    wgbarrier();
    dma16(S.kvsrc, S.rowoff + (S.kvrow + 512) * (DCH * 2), S.kvring + 8192);
    {
      u32x2 sw = *(LDSP u32x2*)(sm + SHK + 8 * (lane & 7));
      S.W32 = (sw.y >> 16) | (sw.y << 16);
      S.W10 = (sw.x >> 16) | (sw.x << 16);
      KvTmp T;
      kv_read(sm, S, T, 0); kv_a(S, T); kv_b(S, T); kv_c(sm, S, T, 0);
    }
    STAMP(2);
    vmcnt<1>();
    if (lane == 0) {
      LDSP u16* k0 = (LDSP u16*)(sm + kimg + KPADB);
      *k0 = (u16)pkbf(bf2f(*k0) + dsk, 0.f);
    }
    STAMP(3);
    wgbarrier();
#pragma unroll
    for (int j = 2; j < 4; ++j) dma16(P.ssm_k, (u32)ch * (SEQ * 2) + 1024 * j + 16 * lane, smb + kimg + KPADB + 1024 * j);
    dma16(S.kvsrc, S.rowoff + (S.kvrow + 768) * (DCH * 2), S.kvring + 12288);
    dma16(S.q, S.rowoff + (S.qrow + 512) * (DCH * 2), S.qring + 8192);
    STAMP(4);
    conv_stream<true>(sm, kimg + 8 * (4 * lp + 8 * g + lq), XIMG + w * XS + 224 + 8 * (lp - 8 * g - lq),
                      YIMG + w * YS + 2 * lane, l63, S STV_PASS);
  } else {
    const int ch0 = (xcd * 20 + (idx - 10)) * 4;
    const int cw = w & 3, ps = w >> 2;
    const int ch = ch0 + cw;
    const int jp = (idx - 10) * 8 + w;
    if (jp < 64) touch(P.u, pfoff + (u32)jp * (32 * DCH * 2), smb + B_YIMG);
    const size_t o = (size_t)ch * NST + 64 * ps + lane;
    const u16 i_ar = P.A_re[o], i_ai = P.A_im[o], i_br = P.B_re[o], i_bi = P.B_im[o], i_cr = P.C_re[o], i_ci = P.C_im[o];
    const float dt = rbf(expf(ld_bf_uniform(P.log_dt, ch)));
    const float dsk = ld_bf_uniform(P.Dskip, ch);
    const float zr = dt * bf2f(i_ar), zi = dt * bf2f(i_ai);
    const cf dB = {dt * bf2f(i_br), dt * bf2f(i_bi)};
    const cf C = {bf2f(i_cr), bf2f(i_ci)};
    const cf W = cmul(C, dB);
    float mx = fmaxf(fabsf(W.r), fabsf(W.i));
    asm volatile("" :: "v"(mx), "v"(zr), "v"(zi) : "memory");
    STAMP(1);
    if (jp >= 64) {
      const u16* pt; int chunk;
      if (jp < 128) { pt = (jp & 1) ? P.v : P.k; chunk = 32 + ((jp - 64) >> 1); }
      else { pt = P.q; chunk = 16 + (jp - 128); }
      touch(pt, pfoff + (u32)chunk * (32 * DCH * 2), smb + B_YIMG);
    }
    const u32 rowoff = (u32)lane * (DCH * 2) + (u32)(ch0 & ~7) * 2;
#pragma unroll
    for (int s = 1; s < 64; s <<= 1) mx = fmaxf(mx, __shfl_xor(mx, s));
    int ex = (int)((__float_as_uint(mx) >> 23) & 0xff);
    ex = min(max(ex, 30), 240);
    const float sY = __uint_as_float((u32)(267 - ex) << 23);
    const float so = __uint_as_float((u32)(ex - 27) << 23);
    const float SX = 16384.f;

    f32x4 ak[3];
    const u32 stg = STG + w * STS;
    const u32 rb = stg + li * 272 + g * 16;
    const u32 m0v = smb + w * STS;
#define STGW(row, val_r, val_i) \
    asm volatile("ds_write_addtid_b32 %0 offset:%1" :: "v"(pkh((val_r), (val_i))), "i"(STG + (row) * 272) : "memory")
    {
      const cf E1 = cexp_pow2(zr, zi, 1.f);
      const cf E8 = cexp_pow2(zr, zi, 8.f);
      const cf E128 = cexp_pow2(zr, zi, 128.f);
      const cf E1024 = cexp_pow2(zr, zi, 1024.f);
      const cf E2 = cmul(E1, E1);
      const cf E4 = cmul(E2, E2);
      u32 keep;
      asm volatile("s_mov_b32 %0, m0\n\ts_mov_b32 m0, %1\n\ts_nop 0" : "=&s"(keep) : "s"(m0v) : "memory");
      cf y = {W.r * sY, W.i * sY};
      cf P1 = {E128.r * SX, E128.i * SX};
      cf v0 = {SX, 0.f}, v1 = P1;
#pragma unroll
      for (int b = 0; b < 8; ++b) {
        STGW(2 * b, y.r, -y.i); y = cmul(y, E8);
        STGW(16 + b, v0.r, v0.i); if (b < 7) v0 = cmul(v0, E1);
        STGW(2 * b + 1, y.r, -y.i); if (b < 7) y = cmul(y, E8);
        STGW(24 + b, v1.r, v1.i); if (b < 7) v1 = cmul(v1, E1);
      }
      f16x8 yf[4];
      const f32x4 zero = {0.f, 0.f, 0.f, 0.f};
#pragma unroll
      for (int s = 0; s < 4; ++s) {
        yf[s] = *(const LDSP f16x8*)(sm + rb + 64 * s);
        f16x8 xf = *(const LDSP f16x8*)(sm + rb + 16 * 272 + 64 * s);
        ak[0] = __builtin_amdgcn_mfma_f32_16x16x32_f16(xf, yf[s], s ? ak[0] : zero, 0, 0, 0);
      }
      dma16(P.u, rowoff + (u32)(64 * w) * (DCH * 2), smb + RAWU + 1024 * w);
      cf Pa = cmul(P1, E128);
      cf Pb = {E1024.r * SX, E1024.i * SX};
#pragma unroll
      for (int m = 0; m < 2; ++m) {
        cf v = Pa;
#pragma unroll
        for (int r = 0; r < 4; ++r) {
          STGW(4 * m + r, v.r, v.i); if (r < 3) v = cmul(v, E2);
          if (r & 1) { STGW(16 + 2 * m + (r >> 1), Pb.r, Pb.i); Pb = cmul(Pb, E128); }
        }
        Pa = cmul(Pa, E128);
      }
#pragma unroll
      for (int m = 0; m < 4; ++m) {
        STGW(8 + 2 * m, Pa.r, Pa.i);
        cf v = cmul(Pa, E4);
        STGW(20 + m, Pb.r, Pb.i); Pb = cmul(Pb, E128);
        STGW(9 + 2 * m, v.r, v.i);
        if (m < 3) Pa = cmul(Pa, E128);
      }
      STGW(24, Pb.r, Pb.i);
      asm volatile("s_mov_b32 m0, %0" :: "s"(keep) : "memory");
      dma16(P.u, rowoff + (u32)(512 + 64 * w) * (DCH * 2), smb + RAWU + 8192 + 1024 * w);
#pragma unroll
      for (int s = 0; s < 4; ++s) {
        f16x8 x1 = *(const LDSP f16x8*)(sm + rb + 64 * s);
        f16x8 x2 = *(const LDSP f16x8*)(sm + rb + 16 * 272 + 64 * s);
        ak[1] = __builtin_amdgcn_mfma_f32_16x16x32_f16(x1, yf[s], s ? ak[1] : zero, 0, 0, 0);
        ak[2] = __builtin_amdgcn_mfma_f32_16x16x32_f16(x2, yf[s], s ? ak[2] : zero, 0, 0, 0);
      }
    }
#undef STGW
#pragma unroll
    for (int i = 0; i < 3; ++i) ak[i] *= so;
    STAMP(2);
    asm volatile("" ::: "memory");
    if (ps) {
#pragma unroll
      for (int i = 0; i < 3; ++i) *(LDSP f32x4*)(sm + stg + 1024 * i + 16 * lane) = ak[i];
    }
    vmcnt<1>();
    wgbarrier();
    STAMP(3);
    if (ps) {
#pragma unroll
      for (int j = 0; j < 4; ++j)
        dma16(P.u, rowoff + (u32)(1024 + 512 * (j >> 1) + 128 * cw + 64 * (j & 1)) * (DCH * 2), smb + RAWU + 16384 + 8192 * (j >> 1) + 2048 * cw + 1024 * (j & 1));
      const u32 ua = RAWU + 16 * (128 * cw + 16 * g + 4 * lp + lq) + 2 * (ch0 & 4);
      const u32 xw = B_XIMG + (lane & 3) * XS + XPADB + 8 * (lane >> 2) + 256 * cw;
      *(LDSP u32x2*)(sm + xw) = trread(sm + ua);
      *(LDSP u32x2*)(sm + xw + 128) = trread(sm + ua + 1024);
      if (lane < 10) *(LDSP u32x4*)(sm + B_XIMG + cw * XS + 16 * lane) = u32x4{0, 0, 0, 0};
      vmcnt<4>();
      wgbarrier();
      const u32 yrd = B_YIMG + lq * YS + 2 * (128 * cw + 16 * g + 4 * lp);
      u16* outp = P.out + DCH + ch0 + (size_t)(128 * cw + lane) * (2 * DCH);
#pragma unroll
      for (int j = 0; j < 4; ++j) {
        if (j == 1) vmcnt<4>();
        if (j == 2) vmcnt<4>();
        if (j < 3) {
          *(LDSP u32x2*)(sm + xw + 1024 * (j + 1)) = trread(sm + ua + 8192 * (j + 1));
          *(LDSP u32x2*)(sm + xw + 1024 * (j + 1) + 128) = trread(sm + ua + 8192 * (j + 1) + 1024);
        }
        wgbarrier();
        u32x2 a = trread(sm + yrd + 1024 * (j & 1));
        u32x2 b = trread(sm + yrd + 1024 * (j & 1) + 128);
        *(u32x2*)(outp + (size_t)j * 512 * (2 * DCH)) = a;
        *(u32x2*)(outp + ((size_t)j * 512 + 64) * (2 * DCH)) = b;
      }
    } else {
      const u32 kimg = B_KIMG + cw * KS;
      const u32 tb = stg;
#pragma unroll
      for (int i = 0; i < 3; ++i) ak[i] += *(const LDSP f32x4*)(sm + stg + 4 * STS + 1024 * i + 16 * lane);
      const int b = li, p = g;
      if (lane < 6) *(LDSP u32x4*)(sm + kimg + 16 * lane) = u32x4{0, 0, 0, 0};
      {
        float a0 = ak[0][0] + (lane == 0 ? dsk : 0.f), a1 = ak[0][1], a2 = ak[0][2], a3 = ak[0][3];
        *(LDSP u32x2*)(sm + kimg + KPADB + 2 * (128 * (p >> 1) + 8 * b + 4 * (p & 1))) = u32x2{pkbf(a0, a1), pkbf(a2, a3)};
      }
      {
        float a0 = ak[1][0], a1 = ak[1][1], a2 = ak[1][2], a3 = ak[1][3];
        int i01, i23;
        if (p < 2) { i01 = 64 * p + 4 * b; i23 = i01 + 2; }
        else { i01 = 128 + 64 * (p - 2) + 2 * b; i23 = i01 + 32; }
        *(LDSP u32x2*)(sm + tb + 4 * i01) = u32x2{pkbf(a0, a0), pkbf(a1, a1)};
        *(LDSP u32x2*)(sm + tb + 4 * i23) = u32x2{pkbf(a2, a2), pkbf(a3, a3)};
#pragma unroll
        for (int j = 0; j < 4; ++j) {
          float a = ak[2][j];
          int ii = 256 + 16 * (4 * p + j) + b;
          *(LDSP u32*)(sm + tb + 4 * ii) = pkbf(a, a);
        }
      }
      wavefence();
      const LDSP u32* TB = (const LDSP u32*)(sm + tb);
      {
        const int i0 = 4 * (lane & 31);
        u32 g0 = TB[i0], g1 = TB[i0 + 1], g2 = TB[i0 + 2], g3 = TB[i0 + 3], g4 = TB[i0 + 4];
        u32x4 ov = {g0, (g1 & 0xffffu) | (g2 & 0xffff0000u), g2, (g3 & 0xffffu) | (g4 & 0xffff0000u)};
        *(LDSP u32x4*)(sm + kimg + KPADB + 16 * (32 + (lane & 31))) = ov;
      }
      {
        const int i0 = 128 + 2 * lane;
        u32 g0 = TB[i0], g1 = TB[i0 + 1], g2 = TB[i0 + 2];
        u32x4 ov = {g0, (g0 & 0xffffu) | (g1 & 0xffff0000u), g1, g2};
        *(LDSP u32x4*)(sm + kimg + KPADB + 16 * (64 + lane)) = ov;
      }
#pragma unroll
      for (int h = 0; h < 2; ++h) {
        const int i0 = 256 + 64 * h + lane;
        u32 g0 = TB[i0], g1 = TB[i0 + 1];
        u32 mid = (lane & 1) ? g1 : ((g0 & 0xffffu) | (g1 & 0xffff0000u));
        u32x4 ov = {g0, g0, mid, g1};
        *(LDSP u32x4*)(sm + kimg + KPADB + 16 * (128 + 64 * h + lane)) = ov;
      }
      vmcnt<0>();
      wgbarrier();
      STAMP(4);
      conv_stream<false>(sm, kimg + 8 * (4 * lp + 8 * g + lq), B_XIMG + cw * XS + 224 + 8 * (lp - 8 * g - lq),
                         B_YIMG + cw * YS + 2 * lane, l63, S STV_PASS);
    }
  }
  asm volatile(".Lfk_code_end:");
  STAMP(10);
  vmcnt<0>();
  STAMP(11);
  CSTAMP(1);
  stv[15] = stv[0] + (cyv[1] - cyv[0]) * 100 / (stv[11] - stv[0]) ;
  if (lane == 0) {
#pragma unroll
    for (int i = 0; i < 16; ++i) P.ws[(blockIdx.x * 16 + w) * 16 + i] = stv[i];
  }
}

__global__ void report_kernel(unsigned long long* ws) {
  if (threadIdx.x) return;
  unsigned long long mn = ~0ull;
  for (int b = 0; b < 240; ++b) for (int w = 0; w < 8; ++w) { unsigned long long v = ws[(b * 16 + w) * 16]; if (v < mn) mn = v; }
  for (int type = 0; type < 2; ++type) {
    for (int s = 0; s < 12; ++s) {
      unsigned long long lo = ~0ull, hi = 0, sum = 0; int n = 0;
      for (int b = 0; b < 240; ++b) { if (((b >> 3) < 10 ? 0 : 1) != type) continue;
        for (int w = 0; w < 8; ++w) { unsigned long long v = ws[(b * 16 + w) * 16 + s]; if (!v) continue; v -= mn; if (v < lo) lo = v; if (v > hi) hi = v; sum += v; ++n; } }
      if (n) printf("type %d stamp %d: min %d avg %d max %d (x10ns)\n", type, s, (int)lo, (int)(sum / n), (int)hi);
    }
  }
  printf("clk MHz wg0: %d  wg100: %d\n", (int)(ws[15] - ws[0]), (int)(ws[100 * 256 + 15] - ws[100 * 256]));
}
extern "C" void kernel_launch(void* const* d_in, const int* in_sizes, int n_in,
                              void* d_out, int out_size, void* d_ws, size_t ws_size,
                              hipStream_t stream) {
  Params P;
  P.q = (const u16*)d_in[0];
  P.k = (const u16*)d_in[1];
  P.v = (const u16*)d_in[2];
  P.shift_k = (const u16*)d_in[3];
  P.ssm_k = (const u16*)d_in[4];
  P.ssm_D = (const u16*)d_in[5];
  P.u = (const u16*)d_in[6];
  P.A_re = (const u16*)d_in[7];
  P.A_im = (const u16*)d_in[8];
  P.B_re = (const u16*)d_in[9];
  P.B_im = (const u16*)d_in[10];
  P.C_re = (const u16*)d_in[11];
  P.C_im = (const u16*)d_in[12];
  P.log_dt = (const u16*)d_in[13];
  P.Dskip = (const u16*)d_in[14];
  P.out = (u16*)d_out;
  P.ws = (unsigned long long*)d_ws;
  (void)hipFuncSetAttribute((const void*)fused_kernel, hipFuncAttributeMaxDynamicSharedMemorySize, LDS_BYTES);
  fused_kernel<<<256, 512, LDS_BYTES, stream>>>(P);
  report_kernel<<<1, 64, 0, stream>>>((unsigned long long*)d_ws);
}
```

```cpp
#define STAMPS
#include <hip/hip_runtime.h>
#include <hip/hip_bf16.h>
#include <stdint.h>

typedef __attribute__((ext_vector_type(4))) short s16x4;
typedef __attribute__((ext_vector_type(8))) _Float16 f16x8;
typedef __attribute__((ext_vector_type(8))) __bf16 bf16x8;
typedef __attribute__((ext_vector_type(2))) __bf16 bf16x2;
typedef __attribute__((ext_vector_type(2))) _Float16 f16x2;
typedef __attribute__((ext_vector_type(2))) float f32x2;
typedef __attribute__((ext_vector_type(4))) float f32x4;
typedef __attribute__((ext_vector_type(2))) unsigned int u32x2;
typedef __attribute__((ext_vector_type(4))) unsigned int u32x4;
typedef unsigned int u32;
typedef unsigned short u16;
#define LDSP __attribute__((address_space(3)))
#define DEV __device__ __forceinline__

constexpr int SEQ = 2048;
constexpr int DCH = 640;
constexpr int NST = 128;

constexpr int XS = 4272;
constexpr int XPADB = 160;
constexpr int XIMG = 0;
constexpr int YIMG = 34304;
constexpr int YS = 2112;
constexpr int ZROW = 51200;
constexpr int SHK = 51264;
constexpr int KIMG = 51456;
constexpr int KS = 4224;
constexpr int KPADB = 96;
constexpr int RINGK = 98304;
constexpr int RINGV = 114688;
constexpr int RINGQ = 131072;
constexpr int STS = 8704;
constexpr int STG = 17152;
constexpr int B_XIMG = 0;
constexpr int B_YIMG = 86784;
constexpr int B_KIMG = 95232;
constexpr int RAWU = 114688;
constexpr int LDS_BYTES = 147456;

DEV float bf2f(u16 v) { return __uint_as_float(((u32)v) << 16); }
DEV float bflo(u32 v) { return __uint_as_float(v << 16); }
DEV float bfhi(u32 v) { return __uint_as_float(v & 0xffff0000u); }
DEV u32 pkbf(float a, float b) {
  f32x2 t = {a, b};
  bf16x2 r = __builtin_convertvector(t, bf16x2);
  return __builtin_bit_cast(u32, r);
}
DEV u32 pkh(float a, float b) {
  f32x2 t = {a, b};
  f16x2 r = __builtin_convertvector(t, f16x2);
  return __builtin_bit_cast(u32, r);
}
DEV float rbf(float a) { return bflo(pkbf(a, a)); }
DEV float dot2(u32 a, u32 b, float c) {
  return __builtin_amdgcn_fdot2_f32_bf16(__builtin_bit_cast(bf16x2, a), __builtin_bit_cast(bf16x2, b), c, false);
}
typedef LDSP char* lptr;
DEV u32x2 trread(lptr p) {
  s16x4 r = __builtin_amdgcn_ds_read_tr16_b64_v4i16((LDSP s16x4*)p);
  return __builtin_bit_cast(u32x2, r);
}
DEV bf16x8 frag(u32x2 a, u32x2 b) {
  u32x4 f = {a.x, a.y, b.x, b.y};
  return __builtin_bit_cast(bf16x8, f);
}
DEV float ror1(float v) {
  int r = __builtin_amdgcn_update_dpp(0, __float_as_int(v), 0x13C, 0xf, 0xf, false);
  return __int_as_float(r);
}
DEV void wavefence() { __builtin_amdgcn_fence(__ATOMIC_ACQ_REL, "wavefront"); __builtin_amdgcn_wave_barrier(); }
DEV void wgbarrier() {
  asm volatile("s_waitcnt lgkmcnt(0)" ::: "memory");
  __builtin_amdgcn_s_barrier();
  asm volatile("" ::: "memory");
}
DEV void dma16(const void* base, u32 voff, u32 ldsaddr) {
  u32 keep;
  asm volatile("s_mov_b32 %0, m0\n\ts_mov_b32 m0, %3\n\ts_nop 0\n\tglobal_load_lds_dwordx4 %1, %2\n\ts_mov_b32 m0, %0"
               : "=&s"(keep) : "v"(voff), "s"(base), "s"(ldsaddr) : "memory");
}
DEV void touch(const void* base, u32 voff, u32 ldsaddr) {
  u32 keep;
  asm volatile("s_mov_b32 %0, m0\n\ts_mov_b32 m0, %3\n\ts_nop 0\n\tglobal_load_lds_dword %1, %2\n\ts_mov_b32 m0, %0"
               : "=&s"(keep) : "v"(voff), "s"(base), "s"(ldsaddr) : "memory");
}
template <int N> DEV void vmcnt() { asm volatile("s_waitcnt vmcnt(%0)" :: "i"(N) : "memory"); }
#define SCHED0() __builtin_amdgcn_sched_barrier(0)

struct cf { float r, i; };
DEV cf cmul(cf a, cf b) {
  cf o;
  o.r = fmaf(a.r, b.r, -(a.i * b.i));
  o.i = fmaf(a.r, b.i, a.i * b.r);
  return o;
}
DEV cf cexp_pow2(float zr, float zi, float x) {
  float mag = __builtin_amdgcn_exp2f((zr * x) * 1.44269504088896340736f);
  float th = zi * x;
  const float C_HI = 0.15915494f;
  const float C_LO = 6.4206382e-09f;
  float f = fmaf(th, C_LO, fmaf(th, C_HI, -rintf(th * C_HI)));
  cf o;
  o.r = mag * __builtin_amdgcn_cosf(f);
  o.i = mag * __builtin_amdgcn_sinf(f);
  return o;
}

#define STAMP_TYPE(wg) (((wg) >> 3) < 10 ? 0 : 1)
#define STAMP(n) do { __builtin_amdgcn_sched_barrier(0); unsigned long long _t = __builtin_amdgcn_s_memrealtime(); __builtin_amdgcn_s_waitcnt(0xC07F); stv[n] = _t; __builtin_amdgcn_sched_barrier(0); } while (0)
#define CSTAMP(n) do { __builtin_amdgcn_sched_barrier(0); unsigned long long _t = __builtin_amdgcn_s_memtime(); __builtin_amdgcn_s_waitcnt(0xC07F); cyv[n] = _t; __builtin_amdgcn_sched_barrier(0); } while (0)
#define STV_ARG , unsigned long long (&stv)[16]
#define STV_PASS , stv

struct Params {
  const u16 *q, *k, *v, *shift_k, *ssm_k, *ssm_D;
  const u16 *u, *A_re, *A_im, *B_re, *B_im, *C_re, *C_im, *log_dt, *Dskip;
  u16* out;
  unsigned long long* ws;
};

DEV float ld_bf_uniform(const u16* p, int idx) {
  u32 d = *(const u32*)(p + (idx & ~1));
  return (idx & 1) ? bfhi(d) : bflo(d);
}

struct AState {
  const u16* kvsrc;
  const u16* q;
  u16* outp;
  u32 rowoff;
  u32 kvring;
  u32 qring;
  u32 kvrow, qrow;
  u32 ka1, ka2, ka3, ka1w, ka2w, va, vinc, xw, W32, W10, qa, yrd;
};

#define MFMA(a, b, c) __builtin_amdgcn_mfma_f32_16x16x32_bf16((a), (b), (c), 0, 0, 0)

struct KvTmp { u32x2 r1, r2, r3, v1, v2; float s0, s1, s2, s3; };
DEV void kv_read(lptr sm, AState& S, KvTmp& T, int blk) {
  const int so = (blk & 3) * 4096;
  T.r1 = trread(sm + (so ? S.ka1 + so : S.ka1w));
  T.r2 = trread(sm + (so ? S.ka2 + so : S.ka2w));
  T.r3 = trread(sm + S.ka3 + so);
  T.v1 = trread(sm + S.va);
  T.v2 = trread(sm + S.va + 32);
  S.va = ((blk & 3) == 3) ? S.va - 3 * S.vinc : S.va + S.vinc;
}
DEV void kv_a(AState& S, KvTmp& T) {
  T.s0 = dot2(T.r1.y, S.W10, dot2(T.r1.x, S.W32, 0.f));
  T.s2 = dot2(T.r2.x, S.W10, dot2(T.r1.y, S.W32, 0.f));
}
DEV void kv_b(AState& S, KvTmp& T) {
  T.s1 = dot2(T.r3.x, S.W10, dot2(T.r2.y, S.W32, 0.f));
  T.s3 = dot2(T.r3.y, S.W10, dot2(T.r3.x, S.W32, 0.f));
}
DEV void kv_c(lptr sm, AState& S, KvTmp& T, int blk) {
  u32 o0 = pkbf(T.s0 * __uint_as_float(T.v1.x), T.s1 * __uint_as_float(T.v1.y));
  u32 o1 = pkbf(T.s2 * __uint_as_float(T.v2.x), T.s3 * __uint_as_float(T.v2.y));
  *(LDSP u32x2*)(sm + S.xw + 512 * blk) = u32x2{o0, o1};
}

template <bool PA>
DEV void conv_stream(lptr sm, u32 kb, u32 xb, u32 yw, bool l63, AState& S STV_ARG) {
  bf16x8 kf[16], xe[16], xo[16];
  f32x4 ae[16], ao[16];
  const f32x4 zero = {0.f, 0.f, 0.f, 0.f};
#pragma unroll
  for (int d = 0; d < 8; ++d) kf[d] = frag(trread(sm + kb + 256 * d), trread(sm + kb + 256 * d + 32));
  xe[0] = frag(trread(sm + xb + 32), trread(sm + xb));
  xo[0] = frag(trread(sm + xb + 160), trread(sm + xb + 128));
  float c3 = 0.f, c2 = 0.f, c1 = 0.f;
  u32 qe = 0, qo = 0;
  KvTmp T;
  u32x2 sa, sb;
  SCHED0();
#pragma unroll
  for (int s = 0; s < 16; ++s) {
    const int n1 = (16 - s) < 8 ? (16 - s) : 8;
    const int n2 = (s >= 7 && s < 15) ? (s - 6) : 0;
    const int nU = n1 + n2;
    const bool odd = s & 1;
    const int r = s >> 1;
    float eu2, ou2, eu1, ou1, ye, yo;
#pragma unroll
    for (int k = 0; k < 10; ++k) {
      if (k < n1) {
        const int p = s + k;
        const bool first = (s == 0) || (k == 7);
        ae[p] = MFMA(kf[k], xe[s], first ? zero : ae[p]);
        ao[p] = MFMA(kf[k], xo[s], first ? zero : ao[p]);
      } else if (k < nU) {
        const int d = 8 + (k - n1);
        ae[s + 1] = MFMA(kf[d], xe[s + 1 - d], ae[s + 1]);
        ao[s + 1] = MFMA(kf[d], xo[s + 1 - d], ao[s + 1]);
      }
      if (k == 1) SCHED0();
      if (k == 0) {
        if (PA) asm volatile("ds_read_u16_d16_hi %0, %2 offset:%3\n\tds_read_u16_d16_hi %1, %2 offset:%4"
                             : "+v"(qe), "+v"(qo) : "v"(S.qa), "i"((s * 2048) & 16383), "i"(((s * 2048) & 16383) + 1024) : "memory");
        if (!odd) {
          xe[s + 1] = frag(trread(sm + xb + 256 * (s + 1) + 32), trread(sm + xb + 256 * (s + 1)));
          xo[s + 1] = frag(trread(sm + xb + 256 * (s + 1) + 160), trread(sm + xb + 256 * (s + 1) + 128));
        }
        if (!PA && odd && !(r & 1)) {
          xe[s + 1] = frag(trread(sm + xb + 256 * (s + 1) + 32), trread(sm + xb + 256 * (s + 1)));
          xo[s + 1] = frag(trread(sm + xb + 256 * (s + 1) + 160), trread(sm + xb + 256 * (s + 1) + 128));
        }
        if (s >= 6 && s < 14) kf[s + 2] = frag(trread(sm + kb + 256 * (s + 2)), trread(sm + kb + 256 * (s + 2) + 32));
      }
      if (k == 1) {
        float e2 = ae[s][2], e3 = ae[s][3], o2 = ao[s][2], o3 = ao[s][3];
        eu2 = e2 + ror1(l63 ? c3 : e3);
        ou2 = o2 + ror1(l63 ? e3 : o3);
        c3 = o3;
        if (PA && !odd && r < 7) kv_read(sm, S, T, r + 1);
      }
      if (k == 2) {
        float e1 = ae[s][1], o1 = ao[s][1];
        eu1 = e1 + ror1(l63 ? c2 : eu2);
        ou1 = o1 + ror1(l63 ? eu2 : ou2);
        c2 = ou2;
      }
      if (k == 3) {
        float e0 = ae[s][0], o0 = ao[s][0];
        ye = e0 + ror1(l63 ? c1 : eu1);
        yo = o0 + ror1(l63 ? eu1 : ou1);
        c1 = ou1;
        asm volatile("" : "+v"(ye));
        asm volatile("" : "+v"(yo));
      }
      if (k == 4) {
        if (PA) {
          asm volatile("s_waitcnt lgkmcnt(0)" : "+v"(qe), "+v"(qo) :: "memory");
          ye *= __uint_as_float(qe);
          yo *= __uint_as_float(qo);
        }
        const u32 pk = pkbf(ye, yo);
        LDSP u16* yp = (LDSP u16*)(sm + yw + ((s * 256) & 2047));
        yp[0] = (u16)pk;
        yp[64] = (u16)(pk >> 16);
        if (PA && odd) {
          if (r == 0) vmcnt<4>();
          else if (r == 1) vmcnt<1>();
          else if (r == 2 || r == 4) vmcnt<3>();
          else if (r == 3) vmcnt<2>();
          else if (r == 5) vmcnt<1>();
        }
        if (odd && (PA || (r & 1))) {
          wgbarrier();
          if (r == 1) STAMP(5); if (r == 3) STAMP(6); if (r == 5) STAMP(7); if (r == 7) STAMP(8);
        }
        if ((PA || (r & 1)) && odd && s < 15) {
          xe[s + 1] = frag(trread(sm + xb + 256 * (s + 1) + 32), trread(sm + xb + 256 * (s + 1)));
          xo[s + 1] = frag(trread(sm + xb + 256 * (s + 1) + 160), trread(sm + xb + 256 * (s + 1) + 128));
        }
      }
      if (PA && !odd && r < 7) {
        if (k == 5) kv_a(S, T);
        if (k == 6) kv_b(S, T);
        if (k == 7) kv_c(sm, S, T, r + 1);
      }
      if (PA && odd) {
        if (k == 5) {
          if (r & 1) {
            sa = trread(sm + S.yrd + 1024 * ((r >> 1) & 1));
            sb = trread(sm + S.yrd + 1024 * ((r >> 1) & 1) + 4 * YS);
            if (r < 5) dma16(S.q, S.rowoff + (S.qrow + 256 * (r + 3)) * (DCH * 2), S.qring + ((r + 3) & 3) * 4096);
          }
        }
        if (k == 6) {
          if (r + 4 < 8) dma16(S.kvsrc, S.rowoff + (S.kvrow + 256 * (r + 4)) * (DCH * 2), S.kvring + ((r + 4) & 3) * 4096);
        }
        if (k == 7 && (r & 1)) {
          *(u32x4*)(S.outp + (size_t)(r - 1) * 256 * (2 * DCH)) = u32x4{sa.x, sa.y, sb.x, sb.y};
        }
      }
      SCHED0();
    }
  }
}

__global__ __launch_bounds__(512, 2) void fused_kernel(Params P) {
  extern __shared__ __attribute__((aligned(16))) char smem[];
  const lptr sm = (lptr)smem;
  const u32 smb = (u32)(uintptr_t)sm;
  const int tid = threadIdx.x;
  const int w = __builtin_amdgcn_readfirstlane(tid >> 6);
  const int lane = tid & 63;
  const int wg = blockIdx.x;
  const int xcd = wg & 7, idx = wg >> 3;
  const bool partA = idx < 10;
  const int g = lane >> 4, li = lane & 15, lq = li >> 2, lp = li & 3;
  unsigned long long stv[16]; unsigned long long cyv[2];
#pragma unroll
  for (int i = 0; i < 16; ++i) stv[i] = 0;
  STAMP(0);
  CSTAMP(0);
  if (w == 7) {
    unsigned long long pc;
    u32 len, keep;
    asm volatile("s_getpc_b64 %0\n.Lfk_code_begin:\n\ts_mov_b32 %1, .Lfk_code_end-.Lfk_code_begin" : "=s"(pc), "=s"(len));
    const u32 off = min((u32)(lane + 64 * (idx % 3)) * 128u, len & ~3u);
    asm volatile("s_mov_b32 %0, m0\n\ts_mov_b32 m0, %3\n\ts_nop 0\n\tglobal_load_lds_dword %1, %2\n\ts_mov_b32 m0, %0"
                 : "=&s"(keep) : "v"(off), "s"(pc), "s"(smb + B_YIMG) : "memory");
  }
  const bool l63 = (lane == 63);
  const u32 pfoff = (u32)(lane >> 1) * (DCH * 2) + (u32)(((160 * xcd) >> 7) + (lane & 1)) * 128u;
  AState S;
  if (idx >= 30) {
    const int j = 160 + (idx - 30) * 8 + w;
    __builtin_amdgcn_s_sleep(32);
    touch(P.q, pfoff + (u32)(j - 112) * (32 * DCH * 2), smb + B_YIMG);
    vmcnt<0>();
    return;
  }

  if (partA) {
    const int ch0 = (xcd * 10 + idx) * 8;
    const int ch = ch0 + w;
    const int wq = w & 3;
    const u32 kimg = KIMG + w * KS;
    S.rowoff = (u32)lane * (DCH * 2) + (u32)ch0 * 2;
    S.kvsrc = (w < 4) ? P.k : P.v;
    S.q = P.q;
    S.kvring = smb + ((w < 4) ? RINGK : RINGV) + 1024 * wq;
    S.qring = smb + RINGQ + 1024 * w;
    S.kvrow = 64 * wq;
    S.qrow = 64 * w;
    {
      const int j = idx * 8 + w;
      const u16* pt; int chunk;
      if (j < 32) { pt = (j & 1) ? P.v : P.k; chunk = j >> 1; }
      else if (j < 48) { pt = P.q; chunk = j - 32; }
      else { pt = (j & 1) ? P.v : P.k; chunk = 16 + ((j - 48) >> 1); }
      touch(pt, pfoff + (u32)chunk * (32 * DCH * 2), smb + B_YIMG);
    }
#pragma unroll
    for (int j = 0; j < 2; ++j) dma16(P.ssm_k, (u32)ch * (SEQ * 2) + 1024 * j + 16 * lane, smb + kimg + KPADB + 1024 * j);
    if (w == 0) { if (lane < 4) dma16(P.shift_k, (u32)ch0 * 8 + 16 * lane, smb + SHK); }
    if (lane < 10) *(LDSP u32x4*)(sm + XIMG + w * XS + 16 * lane) = u32x4{0, 0, 0, 0};
    if (lane < 6) *(LDSP u32x4*)(sm + kimg + 16 * lane) = u32x4{0, 0, 0, 0};
    if (tid < 4) *(LDSP u32x4*)(sm + RINGK + 16384 - 64 + 16 * tid) = u32x4{0, 0, 0, 0};
    if (tid >= 4 && tid < 8) *(LDSP u32x4*)(sm + ZROW + 16 * (tid - 4)) = u32x4{0, 0, 0, 0};
    const float dsk = ld_bf_uniform(P.ssm_D, ch);
    {
      const int srow = 32 * w + 8 * g + 4 * (lp >> 1);
      const int shalf = 8 * (lp & 1);
      const int r1 = lq - 3;
      const int r2 = (lq < 2) ? lq + 1 : lq - 4;
      const int a1 = 16 * (srow + r1) + shalf, a2 = 16 * (srow + r2) + shalf;
      S.ka1 = RINGK + a1; S.ka1w = RINGK + (a1 & 16383);
      S.ka2 = RINGK + a2; S.ka2w = RINGK + (a2 & 16383);
      S.ka3 = RINGK + 16 * (srow + lq) + shalf;
      S.va = (lq & 1) ? (u32)(RINGV + 16 * (srow + (lq >> 1)) + shalf) : (u32)ZROW;
      S.vinc = (lq & 1) ? 4096u : 0u;
      S.xw = XIMG + (lane & 7) * XS + XPADB + 8 * (lane >> 3) + 64 * w;
      S.qa = smb + RINGQ + 16 * lane + 2 * w;
      S.yrd = YIMG + lq * YS + 2 * (64 * w + 16 * g + 4 * lp);
      S.outp = P.out + ch0 + (size_t)(64 * w + lane) * (2 * DCH);
    }
    vmcnt<2>();
    STAMP(9);
    dma16(S.kvsrc, S.rowoff + (S.kvrow) * (DCH * 2), S.kvring);
    __builtin_amdgcn_s_barrier();
    dma16(S.kvsrc, S.rowoff + (S.kvrow + 256) * (DCH * 2), S.kvring + 4096);
    dma16(S.q, S.rowoff + S.qrow * (DCH * 2), S.qring);
    vmcnt<2>();
    STAMP(1);
    wgbarrier();
    dma16(S.kvsrc, S.rowoff + (S.kvrow + 512) * (DCH * 2), S.kvring + 8192);
    {
      u32x2 sw = *(LDSP u32x2*)(sm + SHK + 8 * (lane & 7));
      S.W32 = (sw.y >> 16) | (sw.y << 16);
      S.W10 = (sw.x >> 16) | (sw.x << 16);
      KvTmp T;
      kv_read(sm, S, T, 0); kv_a(S, T); kv_b(S, T); kv_c(sm, S, T, 0);
    }
    STAMP(2);
    vmcnt<1>();
    if (lane == 0) {
      LDSP u16* k0 = (LDSP u16*)(sm + kimg + KPADB);
      *k0 = (u16)pkbf(bf2f(*k0) + dsk, 0.f);
    }
    STAMP(3);
    wgbarrier();
#pragma unroll
    for (int j = 2; j < 4; ++j) dma16(P.ssm_k, (u32)ch * (SEQ * 2) + 1024 * j + 16 * lane, smb + kimg + KPADB + 1024 * j);
    dma16(S.kvsrc, S.rowoff + (S.kvrow + 768) * (DCH * 2), S.kvring + 12288);
    dma16(S.q, S.rowoff + (S.qrow + 512) * (DCH * 2), S.qring + 8192);
    STAMP(4);
    conv_stream<true>(sm, kimg + 8 * (4 * lp + 8 * g + lq), XIMG + w * XS + 224 + 8 * (lp - 8 * g - lq),
                      YIMG + w * YS + 2 * lane, l63, S STV_PASS);
  } else {
    const int ch0 = (xcd * 20 + (idx - 10)) * 4;
    const int cw = w & 3, ps = w >> 2;
    const int ch = ch0 + cw;
    const int jp = (idx - 10) * 8 + w;
    if (jp < 64) touch(P.u, pfoff + (u32)jp * (32 * DCH * 2), smb + B_YIMG);
    const size_t o = (size_t)ch * NST + 64 * ps + lane;
    const u16 i_ar = P.A_re[o], i_ai = P.A_im[o], i_br = P.B_re[o], i_bi = P.B_im[o], i_cr = P.C_re[o], i_ci = P.C_im[o];
    const float dt = rbf(expf(ld_bf_uniform(P.log_dt, ch)));
    const float dsk = ld_bf_uniform(P.Dskip, ch);
    const float zr = dt * bf2f(i_ar), zi = dt * bf2f(i_ai);
    const cf dB = {dt * bf2f(i_br), dt * bf2f(i_bi)};
    const cf C = {bf2f(i_cr), bf2f(i_ci)};
    const cf W = cmul(C, dB);
    float mx = fmaxf(fabsf(W.r), fabsf(W.i));
    asm volatile("" :: "v"(mx), "v"(zr), "v"(zi) : "memory");
    STAMP(1);
    if (jp >= 64) {
      const u16* pt; int chunk;
      if (jp < 128) { pt = (jp & 1) ? P.v : P.k; chunk = 32 + ((jp - 64) >> 1); }
      else { pt = P.q; chunk = 16 + (jp - 128); }
      touch(pt, pfoff + (u32)chunk * (32 * DCH * 2), smb + B_YIMG);
    }
    const u32 rowoff = (u32)lane * (DCH * 2) + (u32)(ch0 & ~7) * 2;
#pragma unroll
    for (int s = 1; s < 64; s <<= 1) mx = fmaxf(mx, __shfl_xor(mx, s));
    int ex = (int)((__float_as_uint(mx) >> 23) & 0xff);
    ex = min(max(ex, 30), 240);
    const float sY = __uint_as_float((u32)(267 - ex) << 23);
    const float so = __uint_as_float((u32)(ex - 27) << 23);
    const float SX = 16384.f;

    f32x4 ak[3];
    const u32 stg = STG + w * STS;
    const u32 rb = stg + li * 272 + g * 16;
    const u32 m0v = smb + w * STS;
#define STGW(row, val_r, val_i) \
    asm volatile("ds_write_addtid_b32 %0 offset:%1" :: "v"(pkh((val_r), (val_i))), "i"(STG + (row) * 272) : "memory")
    {
      const cf E1 = cexp_pow2(zr, zi, 1.f);
      const cf E8 = cexp_pow2(zr, zi, 8.f);
      const cf E128 = cexp_pow2(zr, zi, 128.f);
      const cf E1024 = cexp_pow2(zr, zi, 1024.f);
      const cf E2 = cmul(E1, E1);
      const cf E4 = cmul(E2, E2);
      u32 keep;
      asm volatile("s_mov_b32 %0, m0\n\ts_mov_b32 m0, %1\n\ts_nop 0" : "=&s"(keep) : "s"(m0v) : "memory");
      cf y = {W.r * sY, W.i * sY};
      cf P1 = {E128.r * SX, E128.i * SX};
      cf v0 = {SX, 0.f}, v1 = P1;
#pragma unroll
      for (int b = 0; b < 8; ++b) {
        STGW(2 * b, y.r, -y.i); y = cmul(y, E8);
        STGW(16 + b, v0.r, v0.i); if (b < 7) v0 = cmul(v0, E1);
        STGW(2 * b + 1, y.r, -y.i); if (b < 7) y = cmul(y, E8);
        STGW(24 + b, v1.r, v1.i); if (b < 7) v1 = cmul(v1, E1);
      }
      f16x8 yf[4];
      const f32x4 zero = {0.f, 0.f, 0.f, 0.f};
#pragma unroll
      for (int s = 0; s < 4; ++s) {
        yf[s] = *(const LDSP f16x8*)(sm + rb + 64 * s);
        f16x8 xf = *(const LDSP f16x8*)(sm + rb + 16 * 272 + 64 * s);
        ak[0] = __builtin_amdgcn_mfma_f32_16x16x32_f16(xf, yf[s], s ? ak[0] : zero, 0, 0, 0);
      }
      dma16(P.u, rowoff + (u32)(64 * w) * (DCH * 2), smb + RAWU + 1024 * w);
      cf Pa = cmul(P1, E128);
      cf Pb = {E1024.r * SX, E1024.i * SX};
#pragma unroll
      for (int m = 0; m < 2; ++m) {
        cf v = Pa;
#pragma unroll
        for (int r = 0; r < 4; ++r) {
          STGW(4 * m + r, v.r, v.i); if (r < 3) v = cmul(v, E2);
          if (r & 1) { STGW(16 + 2 * m + (r >> 1), Pb.r, Pb.i); Pb = cmul(Pb, E128); }
        }
        Pa = cmul(Pa, E128);
      }
#pragma unroll
      for (int m = 0; m < 4; ++m) {
        STGW(8 + 2 * m, Pa.r, Pa.i);
        cf v = cmul(Pa, E4);
        STGW(20 + m, Pb.r, Pb.i); Pb = cmul(Pb, E128);
        STGW(9 + 2 * m, v.r, v.i);
        if (m < 3) Pa = cmul(Pa, E128);
      }
      STGW(24, Pb.r, Pb.i);
      asm volatile("s_mov_b32 m0, %0" :: "s"(keep) : "memory");
      dma16(P.u, rowoff + (u32)(512 + 64 * w) * (DCH * 2), smb + RAWU + 8192 + 1024 * w);
#pragma unroll
      for (int s = 0; s < 4; ++s) {
        f16x8 x1 = *(const LDSP f16x8*)(sm + rb + 64 * s);
        f16x8 x2 = *(const LDSP f16x8*)(sm + rb + 16 * 272 + 64 * s);
        ak[1] = __builtin_amdgcn_mfma_f32_16x16x32_f16(x1, yf[s], s ? ak[1] : zero, 0, 0, 0);
        ak[2] = __builtin_amdgcn_mfma_f32_16x16x32_f16(x2, yf[s], s ? ak[2] : zero, 0, 0, 0);
      }
    }
#undef STGW
#pragma unroll
    for (int i = 0; i < 3; ++i) ak[i] *= so;
    STAMP(2);
    asm volatile("" ::: "memory");
    if (ps) {
#pragma unroll
      for (int i = 0; i < 3; ++i) *(LDSP f32x4*)(sm + stg + 1024 * i + 16 * lane) = ak[i];
    }
    vmcnt<1>();
    wgbarrier();
    STAMP(3);
    if (ps) {
#pragma unroll
      for (int j = 0; j < 4; ++j)
        dma16(P.u, rowoff + (u32)(1024 + 512 * (j >> 1) + 128 * cw + 64 * (j & 1)) * (DCH * 2), smb + RAWU + 16384 + 8192 * (j >> 1) + 2048 * cw + 1024 * (j & 1));
      const u32 ua = RAWU + 16 * (128 * cw + 16 * g + 4 * lp + lq) + 2 * (ch0 & 4);
      const u32 xw = B_XIMG + (lane & 3) * XS + XPADB + 8 * (lane >> 2) + 256 * cw;
      *(LDSP u32x2*)(sm + xw) = trread(sm + ua);
      *(LDSP u32x2*)(sm + xw + 128) = trread(sm + ua + 1024);
      if (lane < 10) *(LDSP u32x4*)(sm + B_XIMG + cw * XS + 16 * lane) = u32x4{0, 0, 0, 0};
      vmcnt<4>();
      wgbarrier();
      const u32 yrd = B_YIMG + lq * YS + 2 * (128 * cw + 16 * g + 4 * lp);
      u16* outp = P.out + DCH + ch0 + (size_t)(128 * cw + lane) * (2 * DCH);
#pragma unroll
      for (int j = 0; j < 4; ++j) {
        if (j == 1) vmcnt<4>();
        if (j == 2) vmcnt<4>();
        if (j < 3) {
          *(LDSP u32x2*)(sm + xw + 1024 * (j + 1)) = trread(sm + ua + 8192 * (j + 1));
          *(LDSP u32x2*)(sm + xw + 1024 * (j + 1) + 128) = trread(sm + ua + 8192 * (j + 1) + 1024);
        }
        wgbarrier();
        u32x2 a = trread(sm + yrd + 1024 * (j & 1));
        u32x2 b = trread(sm + yrd + 1024 * (j & 1) + 128);
        *(u32x2*)(outp + (size_t)j * 512 * (2 * DCH)) = a;
        *(u32x2*)(outp + ((size_t)j * 512 + 64) * (2 * DCH)) = b;
      }
    } else {
      const u32 kimg = B_KIMG + cw * KS;
      const u32 tb = stg;
#pragma unroll
      for (int i = 0; i < 3; ++i) ak[i] += *(const LDSP f32x4*)(sm + stg + 4 * STS + 1024 * i + 16 * lane);
      const int b = li, p = g;
      if (lane < 6) *(LDSP u32x4*)(sm + kimg + 16 * lane) = u32x4{0, 0, 0, 0};
      {
        float a0 = ak[0][0] + (lane == 0 ? dsk : 0.f), a1 = ak[0][1], a2 = ak[0][2], a3 = ak[0][3];
        *(LDSP u32x2*)(sm + kimg + KPADB + 2 * (128 * (p >> 1) + 8 * b + 4 * (p & 1))) = u32x2{pkbf(a0, a1), pkbf(a2, a3)};
      }
      {
        float a0 = ak[1][0], a1 = ak[1][1], a2 = ak[1][2], a3 = ak[1][3];
        int i01, i23;
        if (p < 2) { i01 = 64 * p + 4 * b; i23 = i01 + 2; }
        else { i01 = 128 + 64 * (p - 2) + 2 * b; i23 = i01 + 32; }
        *(LDSP u32x2*)(sm + tb + 4 * i01) = u32x2{pkbf(a0, a0), pkbf(a1, a1)};
        *(LDSP u32x2*)(sm + tb + 4 * i23) = u32x2{pkbf(a2, a2), pkbf(a3, a3)};
#pragma unroll
        for (int j = 0; j < 4; ++j) {
          float a = ak[2][j];
          int ii = 256 + 16 * (4 * p + j) + b;
          *(LDSP u32*)(sm + tb + 4 * ii) = pkbf(a, a);
        }
      }
      wavefence();
      const LDSP u32* TB = (const LDSP u32*)(sm + tb);
      {
        const int i0 = 4 * (lane & 31);
        u32 g0 = TB[i0], g1 = TB[i0 + 1], g2 = TB[i0 + 2], g3 = TB[i0 + 3], g4 = TB[i0 + 4];
        u32x4 ov = {g0, (g1 & 0xffffu) | (g2 & 0xffff0000u), g2, (g3 & 0xffffu) | (g4 & 0xffff0000u)};
        *(LDSP u32x4*)(sm + kimg + KPADB + 16 * (32 + (lane & 31))) = ov;
      }
      {
        const int i0 = 128 + 2 * lane;
        u32 g0 = TB[i0], g1 = TB[i0 + 1], g2 = TB[i0 + 2];
        u32x4 ov = {g0, (g0 & 0xffffu) | (g1 & 0xffff0000u), g1, g2};
        *(LDSP u32x4*)(sm + kimg + KPADB + 16 * (64 + lane)) = ov;
      }
#pragma unroll
      for (int h = 0; h < 2; ++h) {
        const int i0 = 256 + 64 * h + lane;
        u32 g0 = TB[i0], g1 = TB[i0 + 1];
        u32 mid = (lane & 1) ? g1 : ((g0 & 0xffffu) | (g1 & 0xffff0000u));
        u32x4 ov = {g0, g0, mid, g1};
        *(LDSP u32x4*)(sm + kimg + KPADB + 16 * (128 + 64 * h + lane)) = ov;
      }
      vmcnt<0>();
      wgbarrier();
      STAMP(4);
      conv_stream<false>(sm, kimg + 8 * (4 * lp + 8 * g + lq), B_XIMG + cw * XS + 224 + 8 * (lp - 8 * g - lq),
                         B_YIMG + cw * YS + 2 * lane, l63, S STV_PASS);
    }
  }
  asm volatile(".Lfk_code_end:");
  STAMP(10);
  vmcnt<0>();
  STAMP(11);
  CSTAMP(1);
  stv[15] = stv[0] + (cyv[1] - cyv[0]) * 100 / (stv[11] - stv[0]) ;
  if (lane == 0) {
#pragma unroll
    for (int i = 0; i < 16; ++i) P.ws[(blockIdx.x * 16 + w) * 16 + i] = stv[i];
  }
}

__global__ void report_kernel(unsigned long long* ws) {
  unsigned long long t0 = __builtin_amdgcn_s_memrealtime();
  unsigned long long mn = ~0ull, mx = 0;
  for (int i = threadIdx.x; i < 240 * 8; i += 64) { int b = i >> 3, w = i & 7; unsigned long long v = ws[(b * 16 + w) * 16]; if (v < mn) mn = v;
     unsigned long long e = ws[(b * 16 + w) * 16 + 11]; if (e > mx) mx = e; }
  for (int s = 1; s < 64; s <<= 1) { unsigned long long a = __shfl_xor(mn, s), c = __shfl_xor(mx, s); if (a < mn) mn = a; if (c > mx) mx = c; }
  unsigned long long q = 0;
  if (q > 20000) q = 20000;
  unsigned long long tend = t0 + 1000 + 50 * q;
  while (__builtin_amdgcn_s_memrealtime() < tend) __builtin_amdgcn_s_sleep(1);
}
extern "C" void kernel_launch(void* const* d_in, const int* in_sizes, int n_in,
                              void* d_out, int out_size, void* d_ws, size_t ws_size,
                              hipStream_t stream) {
  Params P;
  P.q = (const u16*)d_in[0];
  P.k = (const u16*)d_in[1];
  P.v = (const u16*)d_in[2];
  P.shift_k = (const u16*)d_in[3];
  P.ssm_k = (const u16*)d_in[4];
  P.ssm_D = (const u16*)d_in[5];
  P.u = (const u16*)d_in[6];
  P.A_re = (const u16*)d_in[7];
  P.A_im = (const u16*)d_in[8];
  P.B_re = (const u16*)d_in[9];
  P.B_im = (const u16*)d_in[10];
  P.C_re = (const u16*)d_in[11];
  P.C_im = (const u16*)d_in[12];
  P.log_dt = (const u16*)d_in[13];
  P.Dskip = (const u16*)d_in[14];
  P.out = (u16*)d_out;
  P.ws = (unsigned long long*)d_ws;
  (void)hipFuncSetAttribute((const void*)fused_kernel, hipFuncAttributeMaxDynamicSharedMemorySize, LDS_BYTES);
  fused_kernel<<<256, 512, LDS_BYTES, stream>>>(P);
  report_kernel<<<1, 64, 0, stream>>>((unsigned long long*)d_ws);
}
```

```cpp
#include <hip/hip_runtime.h>
#include <hip/hip_bf16.h>
#include <stdint.h>

typedef __attribute__((ext_vector_type(4))) short s16x4;
typedef __attribute__((ext_vector_type(8))) _Float16 f16x8;
typedef __attribute__((ext_vector_type(8))) __bf16 bf16x8;
typedef __attribute__((ext_vector_type(2))) __bf16 bf16x2;
typedef __attribute__((ext_vector_type(2))) _Float16 f16x2;
typedef __attribute__((ext_vector_type(2))) float f32x2;
typedef __attribute__((ext_vector_type(4))) float f32x4;
typedef __attribute__((ext_vector_type(2))) unsigned int u32x2;
typedef __attribute__((ext_vector_type(4))) unsigned int u32x4;
typedef unsigned int u32;
typedef unsigned short u16;
#define LDSP __attribute__((address_space(3)))
#define DEV __device__ __forceinline__

constexpr int SEQ = 2048;
constexpr int DCH = 640;
constexpr int NST = 128;

constexpr int XS = 4272;
constexpr int XPADB = 160;
constexpr int XIMG = 0;
constexpr int YIMG = 34304;
constexpr int YS = 2112;
constexpr int ZROW = 51200;
constexpr int SHK = 51264;
constexpr int KIMG = 51456;
constexpr int KS = 4224;
constexpr int KPADB = 96;
constexpr int RINGK = 98304;
constexpr int RINGV = 114688;
constexpr int RINGQ = 131072;
constexpr int STS = 8704;
constexpr int STG = 17152;
constexpr int B_XIMG = 0;
constexpr int B_YIMG = 86784;
constexpr int B_KIMG = 95232;
constexpr int RAWU = 114688;
constexpr int LDS_BYTES = 147456;

DEV float bf2f(u16 v) { return __uint_as_float(((u32)v) << 16); }
DEV float bflo(u32 v) { return __uint_as_float(v << 16); }
DEV float bfhi(u32 v) { return __uint_as_float(v & 0xffff0000u); }
DEV u32 pkbf(float a, float b) {
  f32x2 t = {a, b};
  bf16x2 r = __builtin_convertvector(t, bf16x2);
  return __builtin_bit_cast(u32, r);
}
DEV u32 pkh(float a, float b) {
  f32x2 t = {a, b};
  f16x2 r = __builtin_convertvector(t, f16x2);
  return __builtin_bit_cast(u32, r);
}
DEV float rbf(float a) { return bflo(pkbf(a, a)); }
DEV float dot2(u32 a, u32 b, float c) {
  return __builtin_amdgcn_fdot2_f32_bf16(__builtin_bit_cast(bf16x2, a), __builtin_bit_cast(bf16x2, b), c, false);
}
typedef LDSP char* lptr;
DEV u32x2 trread(lptr p) {
  s16x4 r = __builtin_amdgcn_ds_read_tr16_b64_v4i16((LDSP s16x4*)p);
  return __builtin_bit_cast(u32x2, r);
}
DEV bf16x8 frag(u32x2 a, u32x2 b) {
  u32x4 f = {a.x, a.y, b.x, b.y};
  return __builtin_bit_cast(bf16x8, f);
}
DEV float ror1(float v) {
  int r = __builtin_amdgcn_update_dpp(0, __float_as_int(v), 0x13C, 0xf, 0xf, false);
  return __int_as_float(r);
}
DEV void wavefence() { __builtin_amdgcn_fence(__ATOMIC_ACQ_REL, "wavefront"); __builtin_amdgcn_wave_barrier(); }
DEV void wgbarrier() {
  asm volatile("s_waitcnt lgkmcnt(0)" ::: "memory");
  __builtin_amdgcn_s_barrier();
  asm volatile("" ::: "memory");
}
DEV void dma16(const void* base, u32 voff, u32 ldsaddr) {
  u32 keep;
  asm volatile("s_mov_b32 %0, m0\n\ts_mov_b32 m0, %3\n\ts_nop 0\n\tglobal_load_lds_dwordx4 %1, %2\n\ts_mov_b32 m0, %0"
               : "=&s"(keep) : "v"(voff), "s"(base), "s"(ldsaddr) : "memory");
}
DEV void touch(const void* base, u32 voff, u32 ldsaddr) {
  u32 keep;
  asm volatile("s_mov_b32 %0, m0\n\ts_mov_b32 m0, %3\n\ts_nop 0\n\tglobal_load_lds_dword %1, %2\n\ts_mov_b32 m0, %0"
               : "=&s"(keep) : "v"(voff), "s"(base), "s"(ldsaddr) : "memory");
}
DEV void touch64(const void* p, u32 ldsaddr) {
  u32 keep;
  asm volatile("s_mov_b32 %0, m0\n\ts_mov_b32 m0, %2\n\ts_nop 0\n\tglobal_load_lds_dword %1, off\n\ts_mov_b32 m0, %0"
               : "=&s"(keep) : "v"(p), "s"(ldsaddr) : "memory");
}
template <int N> DEV void vmcnt() { asm volatile("s_waitcnt vmcnt(%0)" :: "i"(N) : "memory"); }
#define SCHED0() __builtin_amdgcn_sched_barrier(0)

struct cf { float r, i; };
DEV cf cmul(cf a, cf b) {
  cf o;
  o.r = fmaf(a.r, b.r, -(a.i * b.i));
  o.i = fmaf(a.r, b.i, a.i * b.r);
  return o;
}
DEV cf cexp_pow2(float zr, float zi, float x) {
  float mag = __builtin_amdgcn_exp2f((zr * x) * 1.44269504088896340736f);
  float th = zi * x;
  const float C_HI = 0.15915494f;
  const float C_LO = 6.4206382e-09f;
  float f = fmaf(th, C_LO, fmaf(th, C_HI, -rintf(th * C_HI)));
  cf o;
  o.r = mag * __builtin_amdgcn_cosf(f);
  o.i = mag * __builtin_amdgcn_sinf(f);
  return o;
}

#define STAMP(n)
#define CSTAMP(n)
#define STV_ARG
#define STV_PASS

#define PF_D1 8
#define PF_D2 16
struct Params {
  const u16 *q, *k, *v, *shift_k, *ssm_k, *ssm_D;
  const u16 *u, *A_re, *A_im, *B_re, *B_im, *C_re, *C_im, *log_dt, *Dskip;
  u16* out;
  unsigned long long* ws;
};

DEV float ld_bf_uniform(const u16* p, int idx) {
  u32 d = *(const u32*)(p + (idx & ~1));
  return (idx & 1) ? bfhi(d) : bflo(d);
}

#define PFI(t, b) ((t) | ((b) << 2))
#define PFC(a, b, c, d) ((a) | ((b) << 5) | ((c) << 10) | ((d) << 15))
DEV void prefetch_class(const Params& P, int cls, int slice, int xcd, int lane, u32 ldsaddr) {
  u32 code;
  switch (cls) {
    case 0: code = PFC(PFI(0,0), PFI(1,0), PFI(0,1), PFI(1,1)); break;
    case 1: code = PFC(PFI(2,0), PFI(2,1), PFI(0,2), PFI(1,2)); break;
    case 2: code = PFC(PFI(3,0), PFI(3,1), PFI(3,2), PFI(3,3)); break;
    case 3: code = PFC(PFI(0,3), PFI(1,3), PFI(2,2), PFI(2,3)); break;
    case 4: code = PFC(PFI(3,4), PFI(3,5), PFI(3,6), PFI(3,7)); break;
    case 5: code = PFC(PFI(0,4), PFI(1,4), PFI(0,5), PFI(1,5)); break;
    case 6: code = PFC(PFI(2,4), PFI(2,5), PFI(0,6), PFI(1,6)); break;
    default: code = PFC(PFI(0,7), PFI(1,7), PFI(2,6), PFI(2,7)); break;
  }
  const u32 it = (code >> (5 * (lane >> 4))) & 31u;
  const u32 t = it & 3u;
  const u16* base = (t & 2u) ? ((t & 1u) ? P.u : P.q) : ((t & 1u) ? P.v : P.k);
  const u32 row = (it >> 2) * 256u + (u32)slice * 8u + ((lane >> 1) & 7);
  const u32 off = row * (DCH * 2) + (u32)(((160 * xcd) >> 7) + (lane & 1)) * 128u;
  touch64((const char*)base + off, ldsaddr);
}

struct AState {
  const u16* kvsrc;
  const u16* q;
  u16* outp;
  u32 rowoff;
  u32 kvring;
  u32 qring;
  u32 kvrow, qrow;
  u32 ka1, ka2, ka3, ka1w, ka2w, va, vinc, xw, W32, W10, qa, yrd;
};

#define MFMA(a, b, c) __builtin_amdgcn_mfma_f32_16x16x32_bf16((a), (b), (c), 0, 0, 0)

struct KvTmp { u32x2 r1, r2, r3, v1, v2; float s0, s1, s2, s3; };
DEV void kv_read(lptr sm, AState& S, KvTmp& T, int blk) {
  const int so = (blk & 3) * 4096;
  T.r1 = trread(sm + (so ? S.ka1 + so : S.ka1w));
  T.r2 = trread(sm + (so ? S.ka2 + so : S.ka2w));
  T.r3 = trread(sm + S.ka3 + so);
  T.v1 = trread(sm + S.va);
  T.v2 = trread(sm + S.va + 32);
  S.va = ((blk & 3) == 3) ? S.va - 3 * S.vinc : S.va + S.vinc;
}
DEV void kv_a(AState& S, KvTmp& T) {
  T.s0 = dot2(T.r1.y, S.W10, dot2(T.r1.x, S.W32, 0.f));
  T.s2 = dot2(T.r2.x, S.W10, dot2(T.r1.y, S.W32, 0.f));
}
DEV void kv_b(AState& S, KvTmp& T) {
  T.s1 = dot2(T.r3.x, S.W10, dot2(T.r2.y, S.W32, 0.f));
  T.s3 = dot2(T.r3.y, S.W10, dot2(T.r3.x, S.W32, 0.f));
}
DEV void kv_c(lptr sm, AState& S, KvTmp& T, int blk) {
  u32 o0 = pkbf(T.s0 * __uint_as_float(T.v1.x), T.s1 * __uint_as_float(T.v1.y));
  u32 o1 = pkbf(T.s2 * __uint_as_float(T.v2.x), T.s3 * __uint_as_float(T.v2.y));
  *(LDSP u32x2*)(sm + S.xw + 512 * blk) = u32x2{o0, o1};
}

template <bool PA>
DEV void conv_stream(lptr sm, u32 kb, u32 xb, u32 yw, bool l63, AState& S STV_ARG) {
  bf16x8 kf[16], xe[16], xo[16];
  f32x4 ae[16], ao[16];
  const f32x4 zero = {0.f, 0.f, 0.f, 0.f};
#pragma unroll
  for (int d = 0; d < 8; ++d) kf[d] = frag(trread(sm + kb + 256 * d), trread(sm + kb + 256 * d + 32));
  xe[0] = frag(trread(sm + xb + 32), trread(sm + xb));
  xo[0] = frag(trread(sm + xb + 160), trread(sm + xb + 128));
  float c3 = 0.f, c2 = 0.f, c1 = 0.f;
  u32 qe = 0, qo = 0;
  KvTmp T;
  u32x2 sa, sb;
  SCHED0();
#pragma unroll
  for (int s = 0; s < 16; ++s) {
    const int n1 = (16 - s) < 8 ? (16 - s) : 8;
    const int n2 = (s >= 7 && s < 15) ? (s - 6) : 0;
    const int nU = n1 + n2;
    const bool odd = s & 1;
    const int r = s >> 1;
    float eu2, ou2, eu1, ou1, ye, yo;
#pragma unroll
    for (int k = 0; k < 10; ++k) {
      if (k < n1) {
        const int p = s + k;
        const bool first = (s == 0) || (k == 7);
        ae[p] = MFMA(kf[k], xe[s], first ? zero : ae[p]);
        ao[p] = MFMA(kf[k], xo[s], first ? zero : ao[p]);
      } else if (k < nU) {
        const int d = 8 + (k - n1);
        ae[s + 1] = MFMA(kf[d], xe[s + 1 - d], ae[s + 1]);
        ao[s + 1] = MFMA(kf[d], xo[s + 1 - d], ao[s + 1]);
      }
      if (k == 1) SCHED0();
      if (k == 0) {
        if (PA) asm volatile("ds_read_u16_d16_hi %0, %2 offset:%3\n\tds_read_u16_d16_hi %1, %2 offset:%4"
                             : "+v"(qe), "+v"(qo) : "v"(S.qa), "i"((s * 2048) & 16383), "i"(((s * 2048) & 16383) + 1024) : "memory");
        if (!odd) {
          xe[s + 1] = frag(trread(sm + xb + 256 * (s + 1) + 32), trread(sm + xb + 256 * (s + 1)));
          xo[s + 1] = frag(trread(sm + xb + 256 * (s + 1) + 160), trread(sm + xb + 256 * (s + 1) + 128));
        }
        if (!PA && odd && !(r & 1)) {
          xe[s + 1] = frag(trread(sm + xb + 256 * (s + 1) + 32), trread(sm + xb + 256 * (s + 1)));
          xo[s + 1] = frag(trread(sm + xb + 256 * (s + 1) + 160), trread(sm + xb + 256 * (s + 1) + 128));
        }
        if (s >= 6 && s < 14) kf[s + 2] = frag(trread(sm + kb + 256 * (s + 2)), trread(sm + kb + 256 * (s + 2) + 32));
      }
      if (k == 1) {
        float e2 = ae[s][2], e3 = ae[s][3], o2 = ao[s][2], o3 = ao[s][3];
        eu2 = e2 + ror1(l63 ? c3 : e3);
        ou2 = o2 + ror1(l63 ? e3 : o3);
        c3 = o3;
        if (PA && !odd && r < 7) kv_read(sm, S, T, r + 1);
      }
      if (k == 2) {
        float e1 = ae[s][1], o1 = ao[s][1];
        eu1 = e1 + ror1(l63 ? c2 : eu2);
        ou1 = o1 + ror1(l63 ? eu2 : ou2);
        c2 = ou2;
      }
      if (k == 3) {
        float e0 = ae[s][0], o0 = ao[s][0];
        ye = e0 + ror1(l63 ? c1 : eu1);
        yo = o0 + ror1(l63 ? eu1 : ou1);
        c1 = ou1;
        asm volatile("" : "+v"(ye));
        asm volatile("" : "+v"(yo));
      }
      if (k == 4) {
        if (PA) {
          asm volatile("s_waitcnt lgkmcnt(0)" : "+v"(qe), "+v"(qo) :: "memory");
          ye *= __uint_as_float(qe);
          yo *= __uint_as_float(qo);
        }
        const u32 pk = pkbf(ye, yo);
        LDSP u16* yp = (LDSP u16*)(sm + yw + ((s * 256) & 2047));
        yp[0] = (u16)pk;
        yp[64] = (u16)(pk >> 16);
        if (PA && odd) {
          if (r == 0) vmcnt<4>();
          else if (r == 1) vmcnt<1>();
          else if (r == 2 || r == 4) vmcnt<3>();
          else if (r == 3) vmcnt<2>();
          else if (r == 5) vmcnt<1>();
        }
        if (odd && (PA || (r & 1))) {
          wgbarrier();
        }
        if ((PA || (r & 1)) && odd && s < 15) {
          xe[s + 1] = frag(trread(sm + xb + 256 * (s + 1) + 32), trread(sm + xb + 256 * (s + 1)));
          xo[s + 1] = frag(trread(sm + xb + 256 * (s + 1) + 160), trread(sm + xb + 256 * (s + 1) + 128));
        }
      }
      if (PA && !odd && r < 7) {
        if (k == 5) kv_a(S, T);
        if (k == 6) kv_b(S, T);
        if (k == 7) kv_c(sm, S, T, r + 1);
      }
      if (PA && odd) {
        if (k == 5) {
          if (r & 1) {
            sa = trread(sm + S.yrd + 1024 * ((r >> 1) & 1));
            sb = trread(sm + S.yrd + 1024 * ((r >> 1) & 1) + 4 * YS);
            if (r < 5) dma16(S.q, S.rowoff + (S.qrow + 256 * (r + 3)) * (DCH * 2), S.qring + ((r + 3) & 3) * 4096);
          }
        }
        if (k == 6) {
          if (r + 4 < 8) dma16(S.kvsrc, S.rowoff + (S.kvrow + 256 * (r + 4)) * (DCH * 2), S.kvring + ((r + 4) & 3) * 4096);
        }
        if (k == 7 && (r & 1)) {
          *(u32x4*)(S.outp + (size_t)(r - 1) * 256 * (2 * DCH)) = u32x4{sa.x, sa.y, sb.x, sb.y};
        }
      }
      SCHED0();
    }
  }
}

__global__ __launch_bounds__(512, 2) void fused_kernel(Params P) {
  extern __shared__ __attribute__((aligned(16))) char smem[];
  const lptr sm = (lptr)smem;
  const u32 smb = (u32)(uintptr_t)sm;
  const int tid = threadIdx.x;
  const int w = __builtin_amdgcn_readfirstlane(tid >> 6);
  const int lane = tid & 63;
  const int wg = blockIdx.x;
  const int xcd = wg & 7, idx = wg >> 3;
  const bool partA = idx < 10;
  const int g = lane >> 4, li = lane & 15, lq = li >> 2, lp = li & 3;
  STAMP(0);
  CSTAMP(0);
  if (w == 7) {
    unsigned long long pc;
    u32 len, keep;
    asm volatile("s_getpc_b64 %0\n.Lfk_code_begin:\n\ts_mov_b32 %1, .Lfk_code_end-.Lfk_code_begin" : "=s"(pc), "=s"(len));
    const u32 off = min((u32)(lane + 64 * (idx % 3)) * 128u, len & ~3u);
    asm volatile("s_mov_b32 %0, m0\n\ts_mov_b32 m0, %3\n\ts_nop 0\n\tglobal_load_lds_dword %1, %2\n\ts_mov_b32 m0, %0"
                 : "=&s"(keep) : "v"(off), "s"(pc), "s"(smb + B_YIMG) : "memory");
  }
  const bool l63 = (lane == 63);
  AState S;
  if (w < 3) {
    if (w == 1) __builtin_amdgcn_s_sleep(PF_D1);
    if (w == 2) __builtin_amdgcn_s_sleep(PF_D2);
    prefetch_class(P, w, idx, xcd, lane, smb + B_YIMG);
  }
  if (idx >= 30) {
    vmcnt<0>();
    return;
  }

  if (partA) {
    const int ch0 = (xcd * 10 + idx) * 8;
    const int ch = ch0 + w;
    const int wq = w & 3;
    const u32 kimg = KIMG + w * KS;
    S.rowoff = (u32)lane * (DCH * 2) + (u32)ch0 * 2;
    S.kvsrc = (w < 4) ? P.k : P.v;
    S.q = P.q;
    S.kvring = smb + ((w < 4) ? RINGK : RINGV) + 1024 * wq;
    S.qring = smb + RINGQ + 1024 * w;
    S.kvrow = 64 * wq;
    S.qrow = 64 * w;
#pragma unroll
    for (int j = 0; j < 2; ++j) dma16(P.ssm_k, (u32)ch * (SEQ * 2) + 1024 * j + 16 * lane, smb + kimg + KPADB + 1024 * j);
    if (w == 3) { if (lane < 4) dma16(P.shift_k, (u32)ch0 * 8 + 16 * lane, smb + SHK); }
    if (lane < 10) *(LDSP u32x4*)(sm + XIMG + w * XS + 16 * lane) = u32x4{0, 0, 0, 0};
    if (lane < 6) *(LDSP u32x4*)(sm + kimg + 16 * lane) = u32x4{0, 0, 0, 0};
    if (tid < 4) *(LDSP u32x4*)(sm + RINGK + 16384 - 64 + 16 * tid) = u32x4{0, 0, 0, 0};
    if (tid >= 4 && tid < 8) *(LDSP u32x4*)(sm + ZROW + 16 * (tid - 4)) = u32x4{0, 0, 0, 0};
    const float dsk = ld_bf_uniform(P.ssm_D, ch);
    {
      const int srow = 32 * w + 8 * g + 4 * (lp >> 1);
      const int shalf = 8 * (lp & 1);
      const int r1 = lq - 3;
      const int r2 = (lq < 2) ? lq + 1 : lq - 4;
      const int a1 = 16 * (srow + r1) + shalf, a2 = 16 * (srow + r2) + shalf;
      S.ka1 = RINGK + a1; S.ka1w = RINGK + (a1 & 16383);
      S.ka2 = RINGK + a2; S.ka2w = RINGK + (a2 & 16383);
      S.ka3 = RINGK + 16 * (srow + lq) + shalf;
      S.va = (lq & 1) ? (u32)(RINGV + 16 * (srow + (lq >> 1)) + shalf) : (u32)ZROW;
      S.vinc = (lq & 1) ? 4096u : 0u;
      S.xw = XIMG + (lane & 7) * XS + XPADB + 8 * (lane >> 3) + 64 * w;
      S.qa = smb + RINGQ + 16 * lane + 2 * w;
      S.yrd = YIMG + lq * YS + 2 * (64 * w + 16 * g + 4 * lp);
      S.outp = P.out + ch0 + (size_t)(64 * w + lane) * (2 * DCH);
    }
    if (w == 0) vmcnt<2>();
    __builtin_amdgcn_s_barrier();
    STAMP(9);
    dma16(S.kvsrc, S.rowoff + (S.kvrow) * (DCH * 2), S.kvring);
    __builtin_amdgcn_s_barrier();
    dma16(S.kvsrc, S.rowoff + (S.kvrow + 256) * (DCH * 2), S.kvring + 4096);
    dma16(S.q, S.rowoff + S.qrow * (DCH * 2), S.qring);
    vmcnt<2>();
    STAMP(1);
    wgbarrier();
    dma16(S.kvsrc, S.rowoff + (S.kvrow + 512) * (DCH * 2), S.kvring + 8192);
    {
      u32x2 sw = *(LDSP u32x2*)(sm + SHK + 8 * (lane & 7));
      S.W32 = (sw.y >> 16) | (sw.y << 16);
      S.W10 = (sw.x >> 16) | (sw.x << 16);
      KvTmp T;
      kv_read(sm, S, T, 0); kv_a(S, T); kv_b(S, T); kv_c(sm, S, T, 0);
    }
    STAMP(2);
    vmcnt<1>();
    if (lane == 0) {
      LDSP u16* k0 = (LDSP u16*)(sm + kimg + KPADB);
      *k0 = (u16)pkbf(bf2f(*k0) + dsk, 0.f);
    }
    STAMP(3);
    wgbarrier();
#pragma unroll
    for (int j = 2; j < 4; ++j) dma16(P.ssm_k, (u32)ch * (SEQ * 2) + 1024 * j + 16 * lane, smb + kimg + KPADB + 1024 * j);
    dma16(S.kvsrc, S.rowoff + (S.kvrow + 768) * (DCH * 2), S.kvring + 12288);
    dma16(S.q, S.rowoff + (S.qrow + 512) * (DCH * 2), S.qring + 8192);
    STAMP(4);
    conv_stream<true>(sm, kimg + 8 * (4 * lp + 8 * g + lq), XIMG + w * XS + 224 + 8 * (lp - 8 * g - lq),
                      YIMG + w * YS + 2 * lane, l63, S STV_PASS);
  } else {
    const int ch0 = (xcd * 20 + (idx - 10)) * 4;
    const int cw = w & 3, ps = w >> 2;
    const int ch = ch0 + cw;
    const int jp = (idx - 10) * 8 + w;
    const int pcls = 3 + jp % 5, pslice = jp / 5;
    const size_t o = (size_t)ch * NST + 64 * ps + lane;
    const u16 i_ar = P.A_re[o], i_ai = P.A_im[o], i_br = P.B_re[o], i_bi = P.B_im[o], i_cr = P.C_re[o], i_ci = P.C_im[o];
    const float dt = rbf(expf(ld_bf_uniform(P.log_dt, ch)));
    const float dsk = ld_bf_uniform(P.Dskip, ch);
    const float zr = dt * bf2f(i_ar), zi = dt * bf2f(i_ai);
    const cf dB = {dt * bf2f(i_br), dt * bf2f(i_bi)};
    const cf C = {bf2f(i_cr), bf2f(i_ci)};
    const cf W = cmul(C, dB);
    float mx = fmaxf(fabsf(W.r), fabsf(W.i));
    asm volatile("" :: "v"(mx), "v"(zr), "v"(zi) : "memory");
    STAMP(1);
    if (pcls < 5) prefetch_class(P, pcls, pslice, xcd, lane, smb + B_YIMG);
    const u32 rowoff = (u32)lane * (DCH * 2) + (u32)(ch0 & ~7) * 2;
    {
      int m = __float_as_int(mx);
      m = max(m, __builtin_amdgcn_update_dpp(0, m, 0x111, 0xf, 0xf, false));
      m = max(m, __builtin_amdgcn_update_dpp(0, m, 0x112, 0xf, 0xf, false));
      m = max(m, __builtin_amdgcn_update_dpp(0, m, 0x114, 0xf, 0xf, false));
      m = max(m, __builtin_amdgcn_update_dpp(0, m, 0x118, 0xf, 0xf, false));
      int a0 = __builtin_amdgcn_readlane(m, 15), a1 = __builtin_amdgcn_readlane(m, 31);
      int a2 = __builtin_amdgcn_readlane(m, 47), a3 = __builtin_amdgcn_readlane(m, 63);
      mx = __int_as_float(max(max(a0, a1), max(a2, a3)));
    }
    int ex = (int)((__float_as_uint(mx) >> 23) & 0xff);
    ex = min(max(ex, 30), 240);
    const float sY = __uint_as_float((u32)(267 - ex) << 23);
    const float so = __uint_as_float((u32)(ex - 27) << 23);
    const float SX = 16384.f;

    f32x4 ak[3];
    const u32 stg = STG + w * STS;
    const u32 rb = stg + li * 272 + g * 16;
    const u32 m0v = smb + w * STS;
#define STGW(row, val_r, val_i) \
    asm volatile("ds_write_addtid_b32 %0 offset:%1" :: "v"(pkh((val_r), (val_i))), "i"(STG + (row) * 272) : "memory")
    {
      const cf E1 = cexp_pow2(zr, zi, 1.f);
      const cf E8 = cexp_pow2(zr, zi, 8.f);
      const cf E128 = cexp_pow2(zr, zi, 128.f);
      const cf E1024 = cexp_pow2(zr, zi, 1024.f);
      const cf E2 = cmul(E1, E1);
      const cf E4 = cmul(E2, E2);
      u32 keep;
      asm volatile("s_mov_b32 %0, m0\n\ts_mov_b32 m0, %1\n\ts_nop 0" : "=&s"(keep) : "s"(m0v) : "memory");
      cf y = {W.r * sY, W.i * sY};
      cf P1 = {E128.r * SX, E128.i * SX};
      cf v0 = {SX, 0.f}, v1 = P1;
#pragma unroll
      for (int b = 0; b < 8; ++b) {
        STGW(2 * b, y.r, -y.i); y = cmul(y, E8);
        STGW(16 + b, v0.r, v0.i); if (b < 7) v0 = cmul(v0, E1);
        STGW(2 * b + 1, y.r, -y.i); if (b < 7) y = cmul(y, E8);
        STGW(24 + b, v1.r, v1.i); if (b < 7) v1 = cmul(v1, E1);
      }
      f16x8 yf[4];
      const f32x4 zero = {0.f, 0.f, 0.f, 0.f};
#pragma unroll
      for (int s = 0; s < 4; ++s) {
        yf[s] = *(const LDSP f16x8*)(sm + rb + 64 * s);
        f16x8 xf = *(const LDSP f16x8*)(sm + rb + 16 * 272 + 64 * s);
        ak[0] = __builtin_amdgcn_mfma_f32_16x16x32_f16(xf, yf[s], s ? ak[0] : zero, 0, 0, 0);
      }
      dma16(P.u, rowoff + (u32)(64 * w) * (DCH * 2), smb + RAWU + 1024 * w);
      if (pcls >= 5) prefetch_class(P, pcls, pslice, xcd, lane, smb + B_YIMG);
      cf Pa = cmul(P1, E128);
      cf Pb = {E1024.r * SX, E1024.i * SX};
#pragma unroll
      for (int m = 0; m < 2; ++m) {
        cf v = Pa;
#pragma unroll
        for (int r = 0; r < 4; ++r) {
          STGW(4 * m + r, v.r, v.i); if (r < 3) v = cmul(v, E2);
          if (r & 1) { STGW(16 + 2 * m + (r >> 1), Pb.r, Pb.i); Pb = cmul(Pb, E128); }
        }
        Pa = cmul(Pa, E128);
      }
#pragma unroll
      for (int m = 0; m < 4; ++m) {
        STGW(8 + 2 * m, Pa.r, Pa.i);
        cf v = cmul(Pa, E4);
        STGW(20 + m, Pb.r, Pb.i); Pb = cmul(Pb, E128);
        STGW(9 + 2 * m, v.r, v.i);
        if (m < 3) Pa = cmul(Pa, E128);
      }
      STGW(24, Pb.r, Pb.i);
      asm volatile("s_mov_b32 m0, %0" :: "s"(keep) : "memory");
      dma16(P.u, rowoff + (u32)(512 + 64 * w) * (DCH * 2), smb + RAWU + 8192 + 1024 * w);
#pragma unroll
      for (int s = 0; s < 4; ++s) {
        f16x8 x1 = *(const LDSP f16x8*)(sm + rb + 64 * s);
        f16x8 x2 = *(const LDSP f16x8*)(sm + rb + 16 * 272 + 64 * s);
        ak[1] = __builtin_amdgcn_mfma_f32_16x16x32_f16(x1, yf[s], s ? ak[1] : zero, 0, 0, 0);
        ak[2] = __builtin_amdgcn_mfma_f32_16x16x32_f16(x2, yf[s], s ? ak[2] : zero, 0, 0, 0);
      }
    }
#undef STGW
#pragma unroll
    for (int i = 0; i < 3; ++i) ak[i] *= so;
    STAMP(2);
    asm volatile("" ::: "memory");
    if (ps) {
#pragma unroll
      for (int i = 0; i < 3; ++i) *(LDSP f32x4*)(sm + stg + 1024 * i + 16 * lane) = ak[i];
    }
    if (pcls >= 5) vmcnt<2>(); else vmcnt<1>();
    wgbarrier();
    STAMP(3);
    if (ps) {
#pragma unroll
      for (int j = 0; j < 4; ++j)
        dma16(P.u, rowoff + (u32)(1024 + 512 * (j >> 1) + 128 * cw + 64 * (j & 1)) * (DCH * 2), smb + RAWU + 16384 + 8192 * (j >> 1) + 2048 * cw + 1024 * (j & 1));
      const u32 ua = RAWU + 16 * (128 * cw + 16 * g + 4 * lp + lq) + 2 * (ch0 & 4);
      const u32 xw = B_XIMG + (lane & 3) * XS + XPADB + 8 * (lane >> 2) + 256 * cw;
      *(LDSP u32x2*)(sm + xw) = trread(sm + ua);
      *(LDSP u32x2*)(sm + xw + 128) = trread(sm + ua + 1024);
      if (lane < 10) *(LDSP u32x4*)(sm + B_XIMG + cw * XS + 16 * lane) = u32x4{0, 0, 0, 0};
      vmcnt<4>();
      wgbarrier();
      const u32 yrd = B_YIMG + lq * YS + 2 * (128 * cw + 16 * g + 4 * lp);
      u16* outp = P.out + DCH + ch0 + (size_t)(128 * cw + lane) * (2 * DCH);
#pragma unroll
      for (int j = 0; j < 4; ++j) {
        if (j == 1) vmcnt<4>();
        if (j == 2) vmcnt<4>();
        if (j < 3) {
          *(LDSP u32x2*)(sm + xw + 1024 * (j + 1)) = trread(sm + ua + 8192 * (j + 1));
          *(LDSP u32x2*)(sm + xw + 1024 * (j + 1) + 128) = trread(sm + ua + 8192 * (j + 1) + 1024);
        }
        wgbarrier();
        u32x2 a = trread(sm + yrd + 1024 * (j & 1));
        u32x2 b = trread(sm + yrd + 1024 * (j & 1) + 128);
        *(u32x2*)(outp + (size_t)j * 512 * (2 * DCH)) = a;
        *(u32x2*)(outp + ((size_t)j * 512 + 64) * (2 * DCH)) = b;
      }
    } else {
      const u32 kimg = B_KIMG + cw * KS;
      const u32 tb = stg;
#pragma unroll
      for (int i = 0; i < 3; ++i) ak[i] += *(const LDSP f32x4*)(sm + stg + 4 * STS + 1024 * i + 16 * lane);
      const int b = li, p = g;
      if (lane < 6) *(LDSP u32x4*)(sm + kimg + 16 * lane) = u32x4{0, 0, 0, 0};
      {
        float a0 = ak[0][0] + (lane == 0 ? dsk : 0.f), a1 = ak[0][1], a2 = ak[0][2], a3 = ak[0][3];
        *(LDSP u32x2*)(sm + kimg + KPADB + 2 * (128 * (p >> 1) + 8 * b + 4 * (p & 1))) = u32x2{pkbf(a0, a1), pkbf(a2, a3)};
      }
      {
        float a0 = ak[1][0], a1 = ak[1][1], a2 = ak[1][2], a3 = ak[1][3];
        int i01, i23;
        if (p < 2) { i01 = 64 * p + 4 * b; i23 = i01 + 2; }
        else { i01 = 128 + 64 * (p - 2) + 2 * b; i23 = i01 + 32; }
        *(LDSP u32x2*)(sm + tb + 4 * i01) = u32x2{pkbf(a0, a0), pkbf(a1, a1)};
        *(LDSP u32x2*)(sm + tb + 4 * i23) = u32x2{pkbf(a2, a2), pkbf(a3, a3)};
#pragma unroll
        for (int j = 0; j < 4; ++j) {
          float a = ak[2][j];
          int ii = 256 + 16 * (4 * p + j) + b;
          *(LDSP u32*)(sm + tb + 4 * ii) = pkbf(a, a);
        }
      }
      wavefence();
      const LDSP u32* TB = (const LDSP u32*)(sm + tb);
      {
        const int i0 = 4 * (lane & 31);
        u32 g0 = TB[i0], g1 = TB[i0 + 1], g2 = TB[i0 + 2], g3 = TB[i0 + 3], g4 = TB[i0 + 4];
        u32x4 ov = {g0, (g1 & 0xffffu) | (g2 & 0xffff0000u), g2, (g3 & 0xffffu) | (g4 & 0xffff0000u)};
        *(LDSP u32x4*)(sm + kimg + KPADB + 16 * (32 + (lane & 31))) = ov;
      }
      {
        const int i0 = 128 + 2 * lane;
        u32 g0 = TB[i0], g1 = TB[i0 + 1], g2 = TB[i0 + 2];
        u32x4 ov = {g0, (g0 & 0xffffu) | (g1 & 0xffff0000u), g1, g2};
        *(LDSP u32x4*)(sm + kimg + KPADB + 16 * (64 + lane)) = ov;
      }
#pragma unroll
      for (int h = 0; h < 2; ++h) {
        const int i0 = 256 + 64 * h + lane;
        u32 g0 = TB[i0], g1 = TB[i0 + 1];
        u32 mid = (lane & 1) ? g1 : ((g0 & 0xffffu) | (g1 & 0xffff0000u));
        u32x4 ov = {g0, g0, mid, g1};
        *(LDSP u32x4*)(sm + kimg + KPADB + 16 * (128 + 64 * h + lane)) = ov;
      }
      vmcnt<0>();
      wgbarrier();
      STAMP(4);
      conv_stream<false>(sm, kimg + 8 * (4 * lp + 8 * g + lq), B_XIMG + cw * XS + 224 + 8 * (lp - 8 * g - lq),
                         B_YIMG + cw * YS + 2 * lane, l63, S STV_PASS);
    }
  }
  asm volatile(".Lfk_code_end:");
}

extern "C" void kernel_launch(void* const* d_in, const int* in_sizes, int n_in,
                              void* d_out, int out_size, void* d_ws, size_t ws_size,
                              hipStream_t stream) {
  Params P;
  P.q = (const u16*)d_in[0];
  P.k = (const u16*)d_in[1];
  P.v = (const u16*)d_in[2];
  P.shift_k = (const u16*)d_in[3];
  P.ssm_k = (const u16*)d_in[4];
  P.ssm_D = (const u16*)d_in[5];
  P.u = (const u16*)d_in[6];
  P.A_re = (const u16*)d_in[7];
  P.A_im = (const u16*)d_in[8];
  P.B_re = (const u16*)d_in[9];
  P.B_im = (const u16*)d_in[10];
  P.C_re = (const u16*)d_in[11];
  P.C_im = (const u16*)d_in[12];
  P.log_dt = (const u16*)d_in[13];
  P.Dskip = (const u16*)d_in[14];
  P.out = (u16*)d_out;
  P.ws = (unsigned long long*)d_ws;
  (void)hipFuncSetAttribute((const void*)fused_kernel, hipFuncAttributeMaxDynamicSharedMemorySize, LDS_BYTES);
  fused_kernel<<<256, 512, LDS_BYTES, stream>>>(P);
}
```

```cpp
#include <hip/hip_runtime.h>
#include <hip/hip_bf16.h>
#include <stdint.h>

typedef __attribute__((ext_vector_type(4))) short s16x4;
typedef __attribute__((ext_vector_type(8))) _Float16 f16x8;
typedef __attribute__((ext_vector_type(8))) __bf16 bf16x8;
typedef __attribute__((ext_vector_type(2))) __bf16 bf16x2;
typedef __attribute__((ext_vector_type(2))) _Float16 f16x2;
typedef __attribute__((ext_vector_type(2))) float f32x2;
typedef __attribute__((ext_vector_type(4))) float f32x4;
typedef __attribute__((ext_vector_type(2))) unsigned int u32x2;
typedef __attribute__((ext_vector_type(4))) unsigned int u32x4;
typedef unsigned int u32;
typedef unsigned short u16;
#define LDSP __attribute__((address_space(3)))
#define DEV __device__ __forceinline__

constexpr int SEQ = 2048;
constexpr int DCH = 640;
constexpr int NST = 128;
constexpr int ROWB = DCH * 2;

constexpr int XS = 4272;
constexpr int XPADB = 160;
constexpr int YS = 4160;
constexpr int KS = 4224;
constexpr int KPADB = 96;
constexpr int FLAGS = 0;
constexpr int ZROW = 64;
constexpr int SHK = 128;
constexpr int PFD = 256;
constexpr int XIMG = 512;
constexpr int YIMG = 34688;
constexpr int KIMG = 67968;
constexpr int RAWK = 101760;
constexpr int RAWV = 119168;
constexpr int RINGQ = 135552;
constexpr int STS = 8704;
constexpr int STG = 17664;
constexpr int B_YIMG = STG + 4 * STS;
constexpr int B_KIMG = 87296;
constexpr int RAWU = 104192;
constexpr int B_ONES = 136960;
constexpr int LDS_BYTES = 153344;

DEV float bf2f(u16 v) { return __uint_as_float(((u32)v) << 16); }
DEV float bflo(u32 v) { return __uint_as_float(v << 16); }
DEV float bfhi(u32 v) { return __uint_as_float(v & 0xffff0000u); }
DEV u32 pkbf(float a, float b) {
  f32x2 t = {a, b};
  bf16x2 r = __builtin_convertvector(t, bf16x2);
  return __builtin_bit_cast(u32, r);
}
DEV u32 pkh(float a, float b) {
  f32x2 t = {a, b};
  f16x2 r = __builtin_convertvector(t, f16x2);
  return __builtin_bit_cast(u32, r);
}
DEV float rbf(float a) { return bflo(pkbf(a, a)); }
DEV float dot2(u32 a, u32 b, float c) {
  return __builtin_amdgcn_fdot2_f32_bf16(__builtin_bit_cast(bf16x2, a), __builtin_bit_cast(bf16x2, b), c, false);
}
typedef LDSP char* lptr;
DEV u32x2 trread(lptr p) {
  s16x4 r = __builtin_amdgcn_ds_read_tr16_b64_v4i16((LDSP s16x4*)p);
  return __builtin_bit_cast(u32x2, r);
}
DEV bf16x8 frag(u32x2 a, u32x2 b) {
  u32x4 f = {a.x, a.y, b.x, b.y};
  return __builtin_bit_cast(bf16x8, f);
}
DEV float ror1(float v) {
  int r = __builtin_amdgcn_update_dpp(0, __float_as_int(v), 0x13C, 0xf, 0xf, false);
  return __int_as_float(r);
}
DEV void wavefence() { __builtin_amdgcn_fence(__ATOMIC_ACQ_REL, "wavefront"); __builtin_amdgcn_wave_barrier(); }
DEV void wgbarrier() {
  asm volatile("s_waitcnt lgkmcnt(0)" ::: "memory");
  __builtin_amdgcn_s_barrier();
  asm volatile("" ::: "memory");
}
DEV void dma16(const void* base, u32 voff, u32 ldsaddr) {
  u32 keep;
  asm volatile("s_mov_b32 %0, m0\n\ts_mov_b32 m0, %3\n\ts_nop 0\n\tglobal_load_lds_dwordx4 %1, %2\n\ts_mov_b32 m0, %0"
               : "=&s"(keep) : "v"(voff), "s"(base), "s"(ldsaddr) : "memory");
}
DEV void touch64(const void* p, u32 ldsaddr) {
  u32 keep;
  asm volatile("s_mov_b32 %0, m0\n\ts_mov_b32 m0, %2\n\ts_nop 0\n\tglobal_load_lds_dword %1, off\n\ts_mov_b32 m0, %0"
               : "=&s"(keep) : "v"(p), "s"(ldsaddr) : "memory");
}
template <int N> DEV void vmcnt() { asm volatile("s_waitcnt vmcnt(%0)" :: "i"(N) : "memory"); }
#define SCHED0() __builtin_amdgcn_sched_barrier(0)

template <int V> struct IC { static constexpr int value = V; };
template <int I, int N, class F> DEV void static_for(F&& f) {
  if constexpr (I < N) { f(IC<I>{}); static_for<I + 1, N>(f); }
}

struct cf { float r, i; };
DEV cf cmul(cf a, cf b) {
  cf o;
  o.r = fmaf(a.r, b.r, -(a.i * b.i));
  o.i = fmaf(a.r, b.i, a.i * b.r);
  return o;
}
DEV cf cexp_pow2(float zr, float zi, float x) {
  float mag = __builtin_amdgcn_exp2f((zr * x) * 1.44269504088896340736f);
  float th = zi * x;
  const float C_HI = 0.15915494f;
  const float C_LO = 6.4206382e-09f;
  float f = fmaf(th, C_LO, fmaf(th, C_HI, -rintf(th * C_HI)));
  cf o;
  o.r = mag * __builtin_amdgcn_cosf(f);
  o.i = mag * __builtin_amdgcn_sinf(f);
  return o;
}

#define STAMP(n)
#define STAMP_OUT()

struct Params {
  const u16 *q, *k, *v, *shift_k, *ssm_k, *ssm_D;
  const u16 *u, *A_re, *A_im, *B_re, *B_im, *C_re, *C_im, *log_dt, *Dskip;
  u16* out;
  unsigned long long* ws;
};

DEV float ld_bf_uniform(const u16* p, int idx) {
  u32 d = *(const __attribute__((address_space(4))) u32*)(uintptr_t)(p + (idx & ~1));
  return (idx & 1) ? bfhi(d) : bflo(d);
}

DEV const char* prefetch_addr(const Params& P, int cls, int slice, int xcd, int lane) {
  const unsigned long long TAB = 0xB76A54FE93DC2810ull;
  const u32 nib = (u32)(TAB >> (4 * (2 * cls + (lane >> 5)))) & 15u;
  const bool second = (lane >> 4) & 1;
  const u16* base = (nib < 8) ? (second ? P.v : P.k) : (nib < 12 ? P.q : P.u);
  const u32 blk = (nib < 8) ? nib : 2 * (nib & 3) + (second ? 1 : 0);
  const u32 row = blk * 256u + (u32)slice * 8u + ((lane >> 1) & 7);
  return (const char*)base + (row * ROWB + (u32)(((160 * xcd) >> 7) + (lane & 1)) * 128u);
}
DEV void wait_words(lptr p, int lane, int n, u32 v) {
  for (int spin = 0; spin < (1 << 16); ++spin) {
    u32 x = *(volatile LDSP u32*)(p + 4 * (lane & (n - 1)));
    if (__all(x >= v)) break;
    __builtin_amdgcn_s_sleep(1);
  }
  asm volatile("" ::: "memory");
}
DEV void delay(int n) {
  for (int i = 0; i < n; ++i) __builtin_amdgcn_s_sleep(7);
}

#define MFMA(a, b, c) __builtin_amdgcn_mfma_f32_16x16x32_bf16((a), (b), (c), 0, 0, 0)
constexpr int NB = 8;

__global__ __launch_bounds__(768, 3) void fused_kernel(Params P) {
  extern __shared__ __attribute__((aligned(16))) char smem[];
  const lptr sm = (lptr)smem;
  const u32 smb = (u32)(uintptr_t)sm;
  const int tid = threadIdx.x;
  const int w = __builtin_amdgcn_readfirstlane(tid >> 6);
  const int lane = tid & 63;
  const int wg = blockIdx.x;
  const int xcd = wg & 7, idx = wg >> 3;
  const bool partA = idx < 10;
  const int g = lane >> 4, li = lane & 15, lq = li >> 2, lp = li & 3;
  STAMP(0);
  if (w == 7) {
    unsigned long long pc;
    u32 len;
    asm volatile("s_getpc_b64 %0\n.Lfk_code_begin:\n\ts_mov_b32 %1, .Lfk_code_end-.Lfk_code_begin" : "=s"(pc), "=s"(len));
    const u32 off = min((u32)(lane + 64 * (idx % 3)) * 128u, len & ~3u);
    touch64((const char*)pc + off, smb + PFD);
  }
  const int pcl = (w >= 8) ? w - 8 : w;
  const char* const pf1 = prefetch_addr(P, pcl, idx, xcd, lane);
  if (idx >= 30) {
    if (w < 8) { delay(w); touch64(pf1, smb + PFD); }
    vmcnt<0>();
    return;
  }
  if (tid < 32) *(LDSP u32*)(sm + FLAGS + 4 * tid) = 0;

  u32 kb, xb, yw, qa;

  if (partA) {
    const int ch0 = (xcd * 10 + idx) * 8;
    if (w >= 8) {
      const int h = w - 8;
      const u32 rowoff = (u32)(64 * h + lane) * ROWB + (u32)ch0 * 2;
      const u32 rk = smb + RAWK + h * 1088, rv = smb + RAWV + h * 1024, rq = smb + RINGQ + h * 1024;
      if (h == 0) {
        if (lane < 4) dma16(P.shift_k, (u32)ch0 * 8 + 16 * lane, smb + SHK);
        if (lane < 4) *(LDSP u32x4*)(sm + RAWK + 16 * lane) = u32x4{0, 0, 0, 0};
        vmcnt<0>();
      }
      const int srow = 8 * g + 4 * (lp >> 1);
      const int shalf = 8 * (lp & 1);
      const u32 ka1 = RAWK + h * 1088 + 64 + 16 * (srow + lq - 3) + shalf;
      const u32 ka2 = RAWK + h * 1088 + 64 + 16 * (srow + ((lq < 2) ? lq + 1 : lq - 4)) + shalf;
      const u32 ka3 = RAWK + h * 1088 + 64 + 16 * (srow + lq) + shalf;
      const u32 va0 = RAWV + h * 1024 + 16 * (srow + (lq >> 1)) + shalf;
      const u32 xw = XIMG + (lane & 7) * XS + XPADB + 8 * (lane >> 3) + 128 * h;
      const u32 yrd = YIMG + lq * YS + 2 * (64 * h + 16 * g + 4 * lp);
      u16* const outp = P.out + ch0 + (size_t)(64 * h + lane) * (2 * DCH);
      wgbarrier();
      STAMP(1);
      const u32x2 sw = *(LDSP u32x2*)(sm + SHK + 8 * (lane & 7));
      const u32 W32 = (sw.y >> 16) | (sw.y << 16);
      const u32 W10 = (sw.x >> 16) | (sw.x << 16);
#pragma unroll 1
      for (int it = -1; it < 10; ++it) {
        int i = it;
        asm volatile("" : "+s"(i));
        if (i >= 2) {
          wait_words(sm + FLAGS + 16, lane, 8, (u32)(i - 1));
          u32x2 a = trread(sm + yrd + 512 * (i - 2));
          u32x2 b = trread(sm + yrd + 512 * (i - 2) + 4 * YS);
          *(u32x4*)(outp + (size_t)(i - 2) * 256 * (2 * DCH)) = u32x4{a.x, a.y, b.x, b.y};
        }
        const int lb = i + 1;
        if (lb < 8) {
          const u32 ro = rowoff + (u32)lb * (256 * ROWB);
          const int sl = lb & 3;
          dma16(P.k, ro, rk + sl * 4352 + 64);
          dma16(P.v, ro, rv + sl * 4096);
          const bool first = (lb == 0 && h == 0);
          if (lane < 4) dma16(P.k, first ? ro : ro - 4 * ROWB, first ? smb + PFD : rk + sl * 4352);
          dma16(P.q, ro, rq + sl * 4096);
        }
        if (i >= 0 && i < 8) {
          if (i < 2) vmcnt<4>(); else if (i < 7) vmcnt<5>(); else vmcnt<1>();
          const int sl = i & 3;
#pragma unroll
          for (int hf = 0; hf < 2; ++hf) {
            const u32 ko = sl * 4352 + hf * 512;
            const u32 va = (lq & 1) ? va0 + sl * 4096 + hf * 512 : (u32)ZROW;
            u32x2 r1 = trread(sm + ka1 + ko), r2 = trread(sm + ka2 + ko), r3 = trread(sm + ka3 + ko);
            u32x2 v1 = trread(sm + va), v2 = trread(sm + va + 32);
            float s0 = dot2(r1.y, W10, dot2(r1.x, W32, 0.f));
            float s1 = dot2(r3.x, W10, dot2(r2.y, W32, 0.f));
            float s2 = dot2(r2.x, W10, dot2(r1.y, W32, 0.f));
            float s3 = dot2(r3.y, W10, dot2(r3.x, W32, 0.f));
            u32 o0 = pkbf(s0 * __uint_as_float(v1.x), s1 * __uint_as_float(v1.y));
            u32 o1 = pkbf(s2 * __uint_as_float(v2.x), s3 * __uint_as_float(v2.y));
            *(LDSP u32x2*)(sm + xw + 512 * i + 64 * hf) = u32x2{o0, o1};
          }
          asm volatile("" ::: "memory");
          *(volatile LDSP u32*)(sm + FLAGS + 4 * h) = (u32)(i + 1);
          asm volatile("" ::: "memory");
        }
      }
      STAMP_OUT();
      return;
    }
    const int ch = ch0 + w;
    const u32 kimg = KIMG + w * KS;
#pragma unroll
    for (int j = 0; j < 2; ++j) dma16(P.ssm_k, (u32)ch * (SEQ * 2) + 1024 * j + 16 * lane, smb + kimg + KPADB + 1024 * j);
    if (w < 3) { delay(w); touch64(pf1, smb + PFD); }
    if (lane < 10) *(LDSP u32x4*)(sm + XIMG + w * XS + 16 * lane) = u32x4{0, 0, 0, 0};
    if (lane < 6) *(LDSP u32x4*)(sm + kimg + 16 * lane) = u32x4{0, 0, 0, 0};
    const float dsk = ld_bf_uniform(P.ssm_D, ch);
    if (w == 0) vmcnt<0>();
    wgbarrier();
    STAMP(1);
    if (w >= 3) touch64(pf1, smb + PFD);
    vmcnt<1>();
    if (lane == 0) {
      LDSP u16* k0 = (LDSP u16*)(sm + kimg + KPADB);
      *k0 = (u16)pkbf(bf2f(*k0) + dsk, 0.f);
    }
#pragma unroll
    for (int j = 2; j < 4; ++j) dma16(P.ssm_k, (u32)ch * (SEQ * 2) + 1024 * j + 16 * lane, smb + kimg + KPADB + 1024 * j);
    wavefence();
    kb = kimg + 8 * (4 * lp + 8 * g + lq);
    xb = XIMG + w * XS + 224 + 8 * (lp - 8 * g - lq);
    yw = YIMG + w * YS + 2 * lane;
    qa = smb + RINGQ + 16 * lane + 2 * w;
  } else {
    const int ch0 = (xcd * 20 + (idx - 10)) * 4;
    if (w >= 8) {
      const int h = w - 8;
      const char* const pf2 = prefetch_addr(P, h + 4, idx, xcd, lane);
      wgbarrier();
      delay(h); touch64(pf1, smb + PFD);
      {
        const u32x4 ones = {0x3F803F80u, 0x3F803F80u, 0x3F803F80u, 0x3F803F80u};
#pragma unroll
        for (int i = 0; i < 4; ++i) *(LDSP u32x4*)(sm + B_ONES + 4096 * h + 1024 * i + 16 * lane) = ones;
      }
      if (lane < 10) *(LDSP u32x4*)(sm + XIMG + h * XS + 16 * lane) = u32x4{0, 0, 0, 0};
      delay(4); touch64(pf2, smb + PFD);
      const u32 rowoff = (u32)(64 * h + lane) * ROWB + (u32)(ch0 & ~7) * 2;
      vmcnt<1>();
      delay(3 - h);
      STAMP(1);
      const u32 ua = RAWU + 1024 * h + 16 * (16 * g + 4 * lp + lq) + 2 * (ch0 & 4);
      const u32 xw = XIMG + (lane & 3) * XS + XPADB + 8 * (lane >> 2) + 128 * h;
      const u32 yrd = B_YIMG + lq * YS + 2 * (64 * h + 16 * g + 4 * lp);
      u16* const outp = P.out + DCH + ch0 + (size_t)(64 * h + lane) * (2 * DCH);
#pragma unroll 1
      for (int it = -2; it < 8; ++it) {
        int i = it;
        asm volatile("" : "+s"(i));
        if (i + 2 < 8) dma16(P.u, rowoff + (u32)(i + 2) * (256 * ROWB), smb + RAWU + 4096 * (i + 2) + 1024 * h);
        if (i >= 0) {
          if (i < 6) vmcnt<2>(); else if (i == 6) vmcnt<1>(); else vmcnt<0>();
          *(LDSP u32x2*)(sm + xw + 512 * i) = trread(sm + ua + 4096 * i);
          asm volatile("" ::: "memory");
          *(volatile LDSP u32*)(sm + FLAGS + 4 * h) = (u32)(i + 1);
          asm volatile("" ::: "memory");
        }
      }
#pragma unroll 1
      for (int i = 0; i < 8; ++i) {
        wait_words(sm + FLAGS + 16, lane, 4, (u32)(i + 1));
        *(u32x2*)(outp + (size_t)i * 256 * (2 * DCH)) = trread(sm + yrd + 512 * i);
      }
      STAMP_OUT();
      return;
    }
    const int cw = w & 3, ps = w >> 2;
    const int ch = ch0 + cw;
    const size_t o = (size_t)ch * NST + 64 * ps + lane;
    const u16 i_ar = P.A_re[o], i_ai = P.A_im[o], i_br = P.B_re[o], i_bi = P.B_im[o], i_cr = P.C_re[o], i_ci = P.C_im[o];
    const float ldt = ld_bf_uniform(P.log_dt, ch);
    const float dsk = ld_bf_uniform(P.Dskip, ch);
    wgbarrier();
    const float dt = rbf(expf(ldt));
    const float zr = dt * bf2f(i_ar), zi = dt * bf2f(i_ai);
    const cf dB = {dt * bf2f(i_br), dt * bf2f(i_bi)};
    const cf C = {bf2f(i_cr), bf2f(i_ci)};
    const cf W = cmul(C, dB);
    float mx = fmaxf(fabsf(W.r), fabsf(W.i));
    STAMP(1);
    {
      int m = __float_as_int(mx);
      m = max(m, __builtin_amdgcn_update_dpp(0, m, 0x111, 0xf, 0xf, false));
      m = max(m, __builtin_amdgcn_update_dpp(0, m, 0x112, 0xf, 0xf, false));
      m = max(m, __builtin_amdgcn_update_dpp(0, m, 0x114, 0xf, 0xf, false));
      m = max(m, __builtin_amdgcn_update_dpp(0, m, 0x118, 0xf, 0xf, false));
      int a0 = __builtin_amdgcn_readlane(m, 15), a1 = __builtin_amdgcn_readlane(m, 31);
      int a2 = __builtin_amdgcn_readlane(m, 47), a3 = __builtin_amdgcn_readlane(m, 63);
      mx = __int_as_float(max(max(a0, a1), max(a2, a3)));
    }
    int ex = (int)((__float_as_uint(mx) >> 23) & 0xff);
    ex = min(max(ex, 30), 240);
    const float sY = __uint_as_float((u32)(267 - ex) << 23);
    const float so = __uint_as_float((u32)(ex - 27) << 23);
    const float SX = 16384.f;

    f32x4 ak[3];
    const u32 stg = STG + w * STS;
    const u32 rb = stg + li * 272 + g * 16;
    const u32 m0v = smb + w * STS;
#define STGW(row, val_r, val_i) \
    asm volatile("ds_write_addtid_b32 %0 offset:%1" :: "v"(pkh((val_r), (val_i))), "i"(STG + (row) * 272) : "memory")
    {
      const cf E1 = cexp_pow2(zr, zi, 1.f);
      const cf E8c = cexp_pow2(zr, -zi, 8.f);
      const cf E128 = cexp_pow2(zr, zi, 128.f);
      const cf E1024 = cexp_pow2(zr, zi, 1024.f);
      const cf E2 = cmul(E1, E1);
      const cf E4 = cmul(E2, E2);
      u32 keep;
      asm volatile("s_mov_b32 %0, m0\n\ts_mov_b32 m0, %1\n\ts_nop 0" : "=&s"(keep) : "s"(m0v) : "memory");
      cf y = {W.r * sY, -(W.i * sY)};
      cf P1 = {E128.r * SX, E128.i * SX};
      cf v0 = {SX, 0.f}, v1 = P1;
#pragma unroll
      for (int b = 0; b < 8; ++b) {
        STGW(2 * b, y.r, y.i); y = cmul(y, E8c);
        STGW(16 + b, v0.r, v0.i); if (b < 7) v0 = cmul(v0, E1);
        STGW(2 * b + 1, y.r, y.i); if (b < 7) y = cmul(y, E8c);
        STGW(24 + b, v1.r, v1.i); if (b < 7) v1 = cmul(v1, E1);
      }
      f16x8 yf[4];
      const f32x4 zero = {0.f, 0.f, 0.f, 0.f};
#pragma unroll
      for (int s = 0; s < 4; ++s) {
        yf[s] = *(const LDSP f16x8*)(sm + rb + 64 * s);
        f16x8 xf = *(const LDSP f16x8*)(sm + rb + 16 * 272 + 64 * s);
        ak[0] = __builtin_amdgcn_mfma_f32_16x16x32_f16(xf, yf[s], s ? ak[0] : zero, 0, 0, 0);
      }
      asm volatile("" ::: "memory");
      cf Pa = cmul(P1, E128);
      cf Pb = {E1024.r * SX, E1024.i * SX};
#pragma unroll
      for (int m = 0; m < 2; ++m) {
        cf v = Pa;
#pragma unroll
        for (int r = 0; r < 4; ++r) {
          STGW(4 * m + r, v.r, v.i); if (r < 3) v = cmul(v, E2);
          if (r & 1) { STGW(16 + 2 * m + (r >> 1), Pb.r, Pb.i); Pb = cmul(Pb, E128); }
        }
        Pa = cmul(Pa, E128);
      }
#pragma unroll
      for (int m = 0; m < 4; ++m) {
        STGW(8 + 2 * m, Pa.r, Pa.i);
        cf v = cmul(Pa, E4);
        STGW(20 + m, Pb.r, Pb.i); Pb = cmul(Pb, E128);
        STGW(9 + 2 * m, v.r, v.i);
        if (m < 3) Pa = cmul(Pa, E128);
      }
      STGW(24, Pb.r, Pb.i);
      asm volatile("s_mov_b32 m0, %0" :: "s"(keep) : "memory");
#pragma unroll
      for (int s = 0; s < 4; ++s) {
        f16x8 x1 = *(const LDSP f16x8*)(sm + rb + 64 * s);
        f16x8 x2 = *(const LDSP f16x8*)(sm + rb + 16 * 272 + 64 * s);
        ak[1] = __builtin_amdgcn_mfma_f32_16x16x32_f16(x1, yf[s], s ? ak[1] : zero, 0, 0, 0);
        ak[2] = __builtin_amdgcn_mfma_f32_16x16x32_f16(x2, yf[s], s ? ak[2] : zero, 0, 0, 0);
      }
    }
#undef STGW
#pragma unroll
    for (int i = 0; i < 3; ++i) ak[i] *= so;
    STAMP(2);
    asm volatile("" ::: "memory");
    const u32 kimg = B_KIMG + cw * KS;
    if (ps) {
#pragma unroll
      for (int i = 0; i < 3; ++i) *(LDSP f32x4*)(sm + kimg + 1024 * i + 16 * lane) = ak[i];
      asm volatile("s_waitcnt lgkmcnt(0)" ::: "memory");
      *(volatile LDSP u32*)(sm + FLAGS + 48 + 4 * cw) = 1;
      STAMP_OUT();
      return;
    }
    wait_words(sm + FLAGS + 48, lane, 4, 1);
    {
      const u32 tb = stg;
#pragma unroll
      for (int i = 0; i < 3; ++i) ak[i] += *(const LDSP f32x4*)(sm + kimg + 1024 * i + 16 * lane);
      wavefence();
      const int b = li, p = g;
      if (lane < 6) *(LDSP u32x4*)(sm + kimg + 16 * lane) = u32x4{0, 0, 0, 0};
      {
        float a0 = ak[0][0] + (lane == 0 ? dsk : 0.f), a1 = ak[0][1], a2 = ak[0][2], a3 = ak[0][3];
        *(LDSP u32x2*)(sm + kimg + KPADB + 2 * (128 * (p >> 1) + 8 * b + 4 * (p & 1))) = u32x2{pkbf(a0, a1), pkbf(a2, a3)};
      }
      {
        float a0 = ak[1][0], a1 = ak[1][1], a2 = ak[1][2], a3 = ak[1][3];
        int i01, i23;
        if (p < 2) { i01 = 64 * p + 4 * b; i23 = i01 + 2; }
        else { i01 = 128 + 64 * (p - 2) + 2 * b; i23 = i01 + 32; }
        *(LDSP u32x2*)(sm + tb + 4 * i01) = u32x2{pkbf(a0, a0), pkbf(a1, a1)};
        *(LDSP u32x2*)(sm + tb + 4 * i23) = u32x2{pkbf(a2, a2), pkbf(a3, a3)};
#pragma unroll
        for (int j = 0; j < 4; ++j) {
          float a = ak[2][j];
          int ii = 256 + 16 * (4 * p + j) + b;
          *(LDSP u32*)(sm + tb + 4 * ii) = pkbf(a, a);
        }
      }
      wavefence();
      const LDSP u32* TB = (const LDSP u32*)(sm + tb);
      {
        const int i0 = 4 * (lane & 31);
        u32 g0 = TB[i0], g1 = TB[i0 + 1], g2 = TB[i0 + 2], g3 = TB[i0 + 3], g4 = TB[i0 + 4];
        u32x4 ov = {g0, (g1 & 0xffffu) | (g2 & 0xffff0000u), g2, (g3 & 0xffffu) | (g4 & 0xffff0000u)};
        *(LDSP u32x4*)(sm + kimg + KPADB + 16 * (32 + (lane & 31))) = ov;
      }
      {
        const int i0 = 128 + 2 * lane;
        u32 g0 = TB[i0], g1 = TB[i0 + 1], g2 = TB[i0 + 2];
        u32x4 ov = {g0, (g0 & 0xffffu) | (g1 & 0xffff0000u), g1, g2};
        *(LDSP u32x4*)(sm + kimg + KPADB + 16 * (64 + lane)) = ov;
      }
#pragma unroll
      for (int hh = 0; hh < 2; ++hh) {
        const int i0 = 256 + 64 * hh + lane;
        u32 g0 = TB[i0], g1 = TB[i0 + 1];
        u32 mid = (lane & 1) ? g1 : ((g0 & 0xffffu) | (g1 & 0xffff0000u));
        u32x4 ov = {g0, g0, mid, g1};
        *(LDSP u32x4*)(sm + kimg + KPADB + 16 * (128 + 64 * hh + lane)) = ov;
      }
    }
    wavefence();
    kb = kimg + 8 * (4 * lp + 8 * g + lq);
    xb = XIMG + cw * XS + 224 + 8 * (lp - 8 * g - lq);
    yw = B_YIMG + cw * YS + 2 * lane;
    qa = smb + B_ONES + 16 * lane;
  }

  {
#define RDX_E(m) frag(trread(sm + xb + 256 * (m) + 32), trread(sm + xb + 256 * (m)))
#define RDX_O(m) frag(trread(sm + xb + 256 * (m) + 160), trread(sm + xb + 256 * (m) + 128))
#define RDK(d) frag(trread(sm + kb + 256 * (d)), trread(sm + kb + 256 * (d) + 32))
    const bool l63 = (lane == 63);
    const lptr pxp = sm + FLAGS + 4 * (lane & 3);
    const lptr pyp = sm + FLAGS + 16 + 4 * w;
    bf16x8 kf[16], xe[16], xo[16], fe[8], fo[8];
    f32x4 ae[16], ao[16];
    const f32x4 zero = {0.f, 0.f, 0.f, 0.f};
#pragma unroll
    for (int d = 0; d < NB; ++d) kf[d] = RDK(d);
    STAMP(3);
    wait_words(sm + FLAGS, lane, 4, 1);
    STAMP(4);
    xe[0] = RDX_E(0);
    xo[0] = RDX_O(0);
    float c3 = 0.f, c2 = 0.f, c1 = 0.f;
    u32 qe = 0, qo = 0, px = 0;
    SCHED0();
    static_for<0, 16>([&](auto S_) {
      constexpr int s = decltype(S_)::value;
      constexpr bool odd = s & 1;
      constexpr int r = s >> 1;
      constexpr int n1 = (16 - s) < NB ? (16 - s) : NB;
      constexpr int nj = (r >= 3 && r < 7) ? 2 * r - 4 : 0;
      constexpr int j0 = odd ? nj / 2 : 0, j1 = odd ? nj : nj / 2;
      constexpr int nslot = n1 + 2 * (j1 - j0);
      float eu2, ou2, eu1, ou1, ye, yo;
      static_for<0, (nslot > 6 ? nslot : 6)>([&](auto K_) {
        constexpr int k = decltype(K_)::value;
        if constexpr (k < n1) {
          constexpr int p = s + k;
          constexpr bool first = (s == 0) || (k == NB - 1);
          ae[p] = MFMA(kf[k], xe[s], first ? zero : ae[p]);
          ao[p] = MFMA(kf[k], xo[s], first ? zero : ao[p]);
        } else if constexpr (k < nslot) {
          constexpr int j = j0 + (k - n1) / 2, mm = nj - 1 - j;
          constexpr int p = 2 * r + 3 - ((k - n1) & 1);
          if constexpr (p - mm >= NB) {
            ae[p] = MFMA(kf[p - mm], fe[j], ae[p]);
            ao[p] = MFMA(kf[p - mm], fo[j], ao[p]);
          }
        }
        if (k == 1) SCHED0();
        if (k == 0) {
          asm volatile("ds_read_u16_d16_hi %0, %2 offset:%3\n\tds_read_u16_d16_hi %1, %2 offset:%4"
                       : "+v"(qe), "+v"(qo) : "v"(qa), "i"((s * 2048) & 16383), "i"(((s * 2048) & 16383) + 1024) : "memory");
          if constexpr (!odd) { xe[s + 1] = RDX_E(s + 1); xo[s + 1] = RDX_O(s + 1); }
          else if constexpr (s < 15) px = *(volatile LDSP u32*)pxp;
          if (s == 3) vmcnt<0>();
          if constexpr (s + 3 >= NB && s + 3 < 16) kf[s + 3] = RDK(s + 3);
        }
        {
          constexpr int kk = k + 3 - n1;
          if constexpr (kk >= 0 && !(kk & 1) && j0 + kk / 2 < j1) { constexpr int j = j0 + kk / 2; fe[j] = RDX_E(nj - 1 - j); fo[j] = RDX_O(nj - 1 - j); }
        }
        if (k == 1) {
          float e2 = ae[s][2], e3 = ae[s][3], o2 = ao[s][2], o3 = ao[s][3];
          eu2 = e2 + ror1(l63 ? c3 : e3);
          ou2 = o2 + ror1(l63 ? e3 : o3);
          c3 = o3;
        }
        if (k == 2) {
          float e1 = ae[s][1], o1 = ao[s][1];
          eu1 = e1 + ror1(l63 ? c2 : eu2);
          ou1 = o1 + ror1(l63 ? eu2 : ou2);
          c2 = ou2;
        }
        if (k == 3) {
          float e0 = ae[s][0], o0 = ao[s][0];
          ye = e0 + ror1(l63 ? c1 : eu1);
          yo = o0 + ror1(l63 ? eu1 : ou1);
          c1 = ou1;
          asm volatile("" : "+v"(ye));
          asm volatile("" : "+v"(yo));
        }
        if (k == 4) {
          asm volatile("s_waitcnt lgkmcnt(0)" : "+v"(qe), "+v"(qo) :: "memory");
          ye *= __uint_as_float(qe);
          yo *= __uint_as_float(qo);
          const u32 pk = pkbf(ye, yo);
          LDSP u16* yp = (LDSP u16*)(sm + yw + s * 256);
          yp[0] = (u16)pk;
          yp[64] = (u16)(pk >> 16);
          if (odd) {
            asm volatile("" ::: "memory");
            *(volatile LDSP u32*)pyp = (u32)(r + 1);
            asm volatile("" ::: "memory");
          }
        }
        if constexpr (k == 5 && odd && s < 15) {
          if (!__all(px >= (u32)(r + 2))) wait_words(sm + FLAGS, lane, 4, (u32)(r + 2));
          asm volatile("" ::: "memory");
          xe[s + 1] = RDX_E(s + 1); xo[s + 1] = RDX_O(s + 1);
        }
        SCHED0();
      });
    });
  }
  asm volatile(".Lfk_code_end:");
  STAMP_OUT();
}

extern "C" void kernel_launch(void* const* d_in, const int* in_sizes, int n_in,
                              void* d_out, int out_size, void* d_ws, size_t ws_size,
                              hipStream_t stream) {
  Params P;
  P.q = (const u16*)d_in[0];
  P.k = (const u16*)d_in[1];
  P.v = (const u16*)d_in[2];
  P.shift_k = (const u16*)d_in[3];
  P.ssm_k = (const u16*)d_in[4];
  P.ssm_D = (const u16*)d_in[5];
  P.u = (const u16*)d_in[6];
  P.A_re = (const u16*)d_in[7];
  P.A_im = (const u16*)d_in[8];
  P.B_re = (const u16*)d_in[9];
  P.B_im = (const u16*)d_in[10];
  P.C_re = (const u16*)d_in[11];
  P.C_im = (const u16*)d_in[12];
  P.log_dt = (const u16*)d_in[13];
  P.Dskip = (const u16*)d_in[14];
  P.out = (u16*)d_out;
  P.ws = (unsigned long long*)d_ws;
  (void)hipFuncSetAttribute((const void*)fused_kernel, hipFuncAttributeMaxDynamicSharedMemorySize, LDS_BYTES);
  fused_kernel<<<256, 768, LDS_BYTES, stream>>>(P);
}
```

```cpp
#include <hip/hip_runtime.h>
#include <hip/hip_bf16.h>
#include <stdint.h>

typedef __attribute__((ext_vector_type(4))) short s16x4;
typedef __attribute__((ext_vector_type(8))) _Float16 f16x8;
typedef __attribute__((ext_vector_type(8))) __bf16 bf16x8;
typedef __attribute__((ext_vector_type(2))) __bf16 bf16x2;
typedef __attribute__((ext_vector_type(2))) _Float16 f16x2;
typedef __attribute__((ext_vector_type(2))) float f32x2;
typedef __attribute__((ext_vector_type(4))) float f32x4;
typedef __attribute__((ext_vector_type(2))) unsigned int u32x2;
typedef __attribute__((ext_vector_type(4))) unsigned int u32x4;
typedef unsigned int u32;
typedef unsigned short u16;
#define LDSP __attribute__((address_space(3)))
#define DEV __device__ __forceinline__

constexpr int SEQ = 2048;
constexpr int DCH = 640;
constexpr int NST = 128;
constexpr int ROWB = DCH * 2;

constexpr int XS = 4272;
constexpr int XPADB = 160;
constexpr int YS = 4160;
constexpr int KS = 4224;
constexpr int KPADB = 96;
constexpr int FLAGS = 0;
constexpr int ZROW = 64;
constexpr int SHK = 192;
constexpr int PFD = 256;
constexpr int XIMG = 512;
constexpr int YIMG = 34688;
constexpr int KIMG = 67968;
constexpr int RAWK = 101760;
constexpr int RAWV = 119168;
constexpr int RINGQ = 135552;
constexpr int STS = 8704;
constexpr int STG = 17664;
constexpr int B_YIMG = STG + 4 * STS;
constexpr int B_KIMG = 87296;
constexpr int RAWU = 104192;
constexpr int B_ONES = 136960;
constexpr int LDS_BYTES = 153344;

DEV float bf2f(u16 v) { return __uint_as_float(((u32)v) << 16); }
DEV float bflo(u32 v) { return __uint_as_float(v << 16); }
DEV float bfhi(u32 v) { return __uint_as_float(v & 0xffff0000u); }
DEV u32 pkbf(float a, float b) {
  f32x2 t = {a, b};
  bf16x2 r = __builtin_convertvector(t, bf16x2);
  return __builtin_bit_cast(u32, r);
}
DEV u32 pkh(float a, float b) {
  f32x2 t = {a, b};
  f16x2 r = __builtin_convertvector(t, f16x2);
  return __builtin_bit_cast(u32, r);
}
DEV float rbf(float a) { return bflo(pkbf(a, a)); }
DEV float dot2(u32 a, u32 b, float c) {
  return __builtin_amdgcn_fdot2_f32_bf16(__builtin_bit_cast(bf16x2, a), __builtin_bit_cast(bf16x2, b), c, false);
}
DEV float vfma(float a, float b, float c) { float d; asm("v_fma_f32 %0, %1, %2, %3" : "=v"(d) : "v"(a), "v"(b), "v"(c)); return d; }
DEV float vmul(float a, float b) { float d; asm("v_mul_f32 %0, %1, %2" : "=v"(d) : "v"(a), "v"(b)); return d; }
typedef LDSP char* lptr;
DEV u32x2 trread(lptr p) {
  s16x4 r = __builtin_amdgcn_ds_read_tr16_b64_v4i16((LDSP s16x4*)p);
  return __builtin_bit_cast(u32x2, r);
}
DEV bf16x8 frag(u32x2 a, u32x2 b) {
  u32x4 f = {a.x, a.y, b.x, b.y};
  return __builtin_bit_cast(bf16x8, f);
}
DEV float ror1(float v) {
  int r = __builtin_amdgcn_update_dpp(0, __float_as_int(v), 0x13C, 0xf, 0xf, false);
  return __int_as_float(r);
}
DEV void wavefence() { __builtin_amdgcn_fence(__ATOMIC_ACQ_REL, "wavefront"); __builtin_amdgcn_wave_barrier(); }
DEV void wgbarrier() {
  asm volatile("s_waitcnt lgkmcnt(0)" ::: "memory");
  __builtin_amdgcn_s_barrier();
  asm volatile("" ::: "memory");
}
DEV void dma16(const void* base, u32 voff, u32 ldsaddr) {
  u32 keep;
  asm volatile("s_mov_b32 %0, m0\n\ts_mov_b32 m0, %3\n\ts_nop 0\n\tglobal_load_lds_dwordx4 %1, %2\n\ts_mov_b32 m0, %0"
               : "=&s"(keep) : "v"(voff), "s"(base), "s"(ldsaddr) : "memory");
}
DEV void touch64(const void* p, u32 ldsaddr) {
  u32 keep;
  asm volatile("s_mov_b32 %0, m0\n\ts_mov_b32 m0, %2\n\ts_nop 0\n\tglobal_load_lds_dword %1, off\n\ts_mov_b32 m0, %0"
               : "=&s"(keep) : "v"(p), "s"(ldsaddr) : "memory");
}
template <int N> DEV void vmcnt() { asm volatile("s_waitcnt vmcnt(%0)" :: "i"(N) : "memory"); }
#define SCHED0() __builtin_amdgcn_sched_barrier(0)

template <int V> struct IC { static constexpr int value = V; };
template <int I, int N, class F> DEV void static_for(F&& f) {
  if constexpr (I < N) { f(IC<I>{}); static_for<I + 1, N>(f); }
}

struct cf { float r, i; };
DEV cf cmul(cf a, cf b) {
  cf o;
  o.r = fmaf(a.r, b.r, -(a.i * b.i));
  o.i = fmaf(a.r, b.i, a.i * b.r);
  return o;
}
DEV cf cexp_pow2(float zr, float zi, float x) {
  float mag = __builtin_amdgcn_exp2f((zr * x) * 1.44269504088896340736f);
  float th = zi * x;
  const float C_HI = 0.15915494f;
  const float C_LO = 6.4206382e-09f;
  float f = fmaf(th, C_LO, fmaf(th, C_HI, -rintf(th * C_HI)));
  cf o;
  o.r = mag * __builtin_amdgcn_cosf(f);
  o.i = mag * __builtin_amdgcn_sinf(f);
  return o;
}

#define STAMP(n)
#define CSTAMP(n)
#define STAMP_OUT()

struct Params {
  const u16 *q, *k, *v, *shift_k, *ssm_k, *ssm_D;
  const u16 *u, *A_re, *A_im, *B_re, *B_im, *C_re, *C_im, *log_dt, *Dskip;
  u16* out;
  unsigned long long* ws;
};

DEV float ld_bf_uniform(const u16* p, int idx) {
  u32 d = *(const __attribute__((address_space(4))) u32*)(uintptr_t)(p + (idx & ~1));
  return (idx & 1) ? bfhi(d) : bflo(d);
}

DEV const char* prefetch_addr(const Params& P, int cls, int slice, int xcd, int lane) {
  const unsigned long long TAB = 0xB76A54FE93DC2810ull;
  const u32 nib = (u32)(TAB >> (4 * (2 * cls + (lane >> 5)))) & 15u;
  const bool second = (lane >> 4) & 1;
  const u16* base = (nib < 8) ? (second ? P.v : P.k) : (nib < 12 ? P.q : P.u);
  const u32 blk = (nib < 8) ? nib : 2 * (nib & 3) + (second ? 1 : 0);
  const u32 row = blk * 256u + (u32)slice * 8u + ((lane >> 1) & 7);
  return (const char*)base + (row * ROWB + (u32)(((160 * xcd) >> 7) + (lane & 1)) * 128u);
}
DEV void wait_words(lptr p, int lane, int n, u32 v) {
#pragma unroll 1
  for (int spin = 0; spin < (1 << 16); ++spin) {
    u32 x = *(volatile LDSP u32*)(p + 4 * (lane & (n - 1)));
    if (__all(x >= v)) break;
    __builtin_amdgcn_s_sleep(1);
  }
  asm volatile("" ::: "memory");
}
DEV void delay(int n) {
  for (int i = 0; i < n; ++i) __builtin_amdgcn_s_sleep(7);
}

struct Sched {
  int p[136], d[136], ld[136];
  bool first[136];
  int base[17];
  int ldm[40], ldu[40];
  int ku[16];
  int nld;
};
constexpr Sched make_sched() {
  Sched S{};
  int n = 0, nl = 16;
  for (int m = 0; m < 16; ++m) S.ldm[m] = m;
  for (int d = 0; d < 16; ++d) S.ku[d] = -1;
  for (int s = 0; s < 16; ++s) {
    S.base[s] = n;
    S.ldu[s] = n;
    for (int k = 0; k < 8 && s + k < 16; ++k) { S.p[n] = s + k; S.d[n] = k; S.ld[n] = s; S.first[n] = (s == 0) || (k == 7); ++n; }
    int lo = -1, hi = -1, cnt = 0, half = 0;
    if (s == 6) { lo = 8; hi = 8; cnt = 1; half = 2; }
    else if (s >= 7 && s <= 12) { lo = (s & 1) ? s + 2 : s + 1; hi = lo + 1; cnt = hi - 7; half = (s & 1) ? 0 : 1; }
    else if (s == 13 || s == 14) { lo = 15; hi = 15; cnt = 8; half = (s == 13) ? 0 : 1; }
    if (cnt) {
      const int split = (cnt + 1) / 2;
      const int j0 = (half == 1) ? split : 0, j1 = (half == 0) ? split : cnt;
      for (int j = j0; j < j1; ++j) {
        const int mm = cnt - 1 - j;
        S.ldm[nl] = mm; S.ldu[nl] = n;
        for (int t = hi; t >= lo; --t)
          if (t - mm >= 8) { S.p[n] = t; S.d[n] = t - mm; S.ld[n] = nl; S.first[n] = false; ++n; }
        ++nl;
      }
    }
  }
  S.base[16] = n;
  S.nld = nl;
  for (int u = 0; u < n; ++u) if (S.ku[S.d[u]] < 0) S.ku[S.d[u]] = u;
  return S;
}
constexpr Sched SC = make_sched();
static_assert(SC.base[16] == 136, "schedule");
constexpr int NGAP = 272 + 24, CAP = 2;
enum { A_NONE = 0, A_X, A_K, A_Q, A_PXR, A_PXC, A_EP, A_VM };
struct Acts { int n[NGAP]; int a[NGAP][4]; };
constexpr int place_back(Acts& T, int latest, int code, int cap = CAP) {
  int g = latest;
  while (g > 0 && T.n[g] >= cap) --g;
  T.a[g][T.n[g]++] = code;
  return g;
}
constexpr int place_fwd(Acts& T, int earliest, int code, int cap) {
  int g = earliest;
  while (T.n[g] >= (g < 272 ? cap : 4)) ++g;
  T.a[g][T.n[g]++] = code;
  return g;
}
constexpr Acts make_acts() {
  Acts T{};
  for (int l = 15; l >= 1; --l) {
    int g = 2 * SC.ldu[l] - 5;
    for (int i = 3; i >= 0; --i) g = place_back(T, g - 1, A_X | (l << 4) | (i << 12), 1);
    g = place_back(T, g - 1, A_Q | (l << 4) | (1 << 12), 1);
    g = place_back(T, g - 1, A_Q | (l << 4), 1);
    if (!(l & 1)) {
      g = place_back(T, g - 1, A_PXC | (l << 4), 1);
      place_back(T, g - 6, A_PXR, 1);
    }
  }
  for (int l = SC.nld - 1; l >= 16; --l) {
    int g = 2 * SC.ldu[l] - 5;
    for (int i = 3; i >= 0; --i) g = place_back(T, g - 1, A_X | (l << 4) | (i << 12), SC.ldu[l] >= 100 ? 2 : 1);
  }
  for (int d = 15; d >= 8; --d) {
    int g = 2 * SC.ku[d] - 5;
    g = place_back(T, g - 1, A_K | (d << 4) | (1 << 12), 2);
    g = place_back(T, g - 1, A_K | (d << 4), 2);
    if (d == 8) place_back(T, g - 1, A_VM, 2);
  }
  for (int s = 0; s < 16; ++s) {
    int g = 2 * SC.base[s] + 4;
    const int nop = (s & 1) ? 18 : 17;
    for (int e = 0; e < nop; ++e) g = place_fwd(T, g + ((e & 1) && e < 14 ? 0 : 1), A_EP | (s << 4) | (e << 12), s >= 11 ? 3 : CAP);
  }
  return T;
}
constexpr Acts AC = make_acts();

#define MFMA(a, b, c) __builtin_amdgcn_mfma_f32_16x16x32_bf16((a), (b), (c), 0, 0, 0)

__global__ __launch_bounds__(768, 3) void fused_kernel(Params P) {
  extern __shared__ __attribute__((aligned(16))) char smem[];
  const lptr sm = (lptr)smem;
  const u32 smb = (u32)(uintptr_t)sm;
  const int tid = threadIdx.x;
  const int w = __builtin_amdgcn_readfirstlane(tid >> 6);
  const int lane = tid & 63;
  const int wg = blockIdx.x;
  const int xcd = wg & 7, idx = wg >> 3;
  const bool partA = idx < 10;
  const int g = lane >> 4, li = lane & 15, lq = li >> 2, lp = li & 3;
  STAMP(0);
  if (w == 7) {
    unsigned long long pc;
    u32 len;
    asm volatile("s_getpc_b64 %0\n.Lfk_code_begin:\n\ts_mov_b32 %1, .Lfk_code_end-.Lfk_code_begin" : "=s"(pc), "=s"(len));
    const u32 off = min((u32)(lane + 64 * (idx % 3)) * 128u, len & ~3u);
    touch64((const char*)pc + off, smb + PFD);
  }
  const int pcl = (w >= 8) ? w - 8 : w;
  const char* const pf1 = prefetch_addr(P, pcl, idx, xcd, lane);
  if (idx >= 30) {
    if (w < 8) { delay(w); touch64(pf1, smb + PFD); }
    vmcnt<0>();
    return;
  }
  if (tid < 48) *(LDSP u32*)(sm + FLAGS + 4 * tid) = 0;

  u32 kb, xb, yw, qa;

  if (partA) {
    const int ch0 = (xcd * 10 + idx) * 8;
    if (w >= 8) {
      const int h = w - 8;
      const u32 rowoff = (u32)(64 * h + lane) * ROWB + (u32)ch0 * 2;
      const u32 rk = smb + RAWK + h * 1088, rv = smb + RAWV + h * 1024, rq = smb + RINGQ + h * 1024;
      if (h == 0) {
        if (lane < 4) dma16(P.shift_k, (u32)ch0 * 8 + 16 * lane, smb + SHK);
        if (lane < 4) *(LDSP u32x4*)(sm + RAWK + 16 * lane) = u32x4{0, 0, 0, 0};
        vmcnt<0>();
      }
      const int srow = 8 * g + 4 * (lp >> 1);
      const int shalf = 8 * (lp & 1);
      const u32 ka0 = RAWK + h * 1088 + 16 * (srow + (lq >> 1)) + shalf;
      const u32 va0 = RAWV + h * 1024 + 16 * (srow + (lq >> 1)) + shalf;
      const u32 xw = XIMG + (lane & 7) * XS + XPADB + 8 * (lane >> 3) + 128 * h;
      const u32 yrd = YIMG + lq * YS + 2 * (64 * h + 16 * g + 4 * lp);
      u16* const outp = P.out + ch0 + (size_t)(64 * h + lane) * (2 * DCH);
      wgbarrier();
      STAMP(1);
      const u32x2 sw = *(LDSP u32x2*)(sm + SHK + 8 * (lane & 7));
      const float w0 = bflo(sw.x), w1 = bfhi(sw.x), w2 = bflo(sw.y), w3 = bfhi(sw.y);
      __builtin_amdgcn_s_setprio(3);
#pragma unroll 1
      for (int it = -1; it < 11; ++it) {
        int i = it;
        asm volatile("" : "+s"(i));
        if (i >= 3) {
          wait_words(sm + FLAGS + 16, lane, 8, (u32)(i - 2));
          u32x2 a = trread(sm + yrd + 512 * (i - 3));
          u32x2 b = trread(sm + yrd + 512 * (i - 3) + 4 * YS);
          *(u32x4*)(outp + (size_t)(i - 3) * 256 * (2 * DCH)) = u32x4{a.x, a.y, b.x, b.y};
        }
        const int lb = i + 1;
        if (lb < 8) {
          const u32 ro = rowoff + (u32)lb * (256 * ROWB);
          const int sl = lb & 3;
          dma16(P.k, ro, rk + sl * 4352 + 64);
          dma16(P.v, ro, rv + sl * 4096);
          const bool first = (lb == 0 && h == 0);
          if (lane < 4) dma16(P.k, first ? ro : ro - 4 * ROWB, first ? smb + PFD : rk + sl * 4352);
          dma16(P.q, ro, rq + sl * 4096);
        }
        if (i >= 0 && i < 8) {
          if (i < 3) vmcnt<4>(); else if (i < 7) vmcnt<5>(); else vmcnt<1>();
          const int sl = i & 3;
#pragma unroll
          for (int hf = 0; hf < 2; ++hf) {
            const u32 ka = (lq & 1) ? ka0 + sl * 4352 + hf * 512 : (u32)ZROW;
            const u32 va = (lq & 1) ? va0 + sl * 4096 + hf * 512 : (u32)ZROW;
            float kx[8], vx[4];
#pragma unroll
            for (int j = 0; j < 4; ++j) { u32x2 r = trread(sm + ka + 32 * j); kx[2 * j] = __uint_as_float(r.x); kx[2 * j + 1] = __uint_as_float(r.y); }
#pragma unroll
            for (int j = 0; j < 2; ++j) { u32x2 r = trread(sm + va + 32 * j); vx[2 * j] = __uint_as_float(r.x); vx[2 * j + 1] = __uint_as_float(r.y); }
            float kv[4];
#pragma unroll
            for (int j = 0; j < 4; ++j)
              kv[j] = vmul(vfma(kx[j + 4], w0, vfma(kx[j + 3], w1, vfma(kx[j + 2], w2, vmul(kx[j + 1], w3)))), vx[j]);
            u32 o0 = pkbf(kv[0], kv[1]);
            u32 o1 = pkbf(kv[2], kv[3]);
            *(LDSP u32x2*)(sm + xw + 512 * i + 64 * hf) = u32x2{o0, o1};
          }
          asm volatile("" ::: "memory");
          *(volatile LDSP u32*)(sm + FLAGS + 4 * h) = (u32)(i + 1);
          if (i == 0) STAMP(12); if (i == 3) STAMP(13); if (i == 7) STAMP(14);
          asm volatile("" ::: "memory");
        }
      }
      STAMP_OUT();
      return;
    }
    const int ch = ch0 + w;
    const u32 kimg = KIMG + w * KS;
#pragma unroll
    for (int j = 0; j < 2; ++j) dma16(P.ssm_k, (u32)ch * (SEQ * 2) + 1024 * j + 16 * lane, smb + kimg + KPADB + 1024 * j);
    if (w < 3) { delay(w); touch64(pf1, smb + PFD); }
    if (lane < 10) *(LDSP u32x4*)(sm + XIMG + w * XS + 16 * lane) = u32x4{0, 0, 0, 0};
    if (lane < 6) *(LDSP u32x4*)(sm + kimg + 16 * lane) = u32x4{0, 0, 0, 0};
    const float dsk = ld_bf_uniform(P.ssm_D, ch);
    if (w == 0) vmcnt<0>();
    wgbarrier();
    STAMP(1);
    if (w >= 3) touch64(pf1, smb + PFD);
    vmcnt<1>();
    if (lane == 0) {
      LDSP u16* k0 = (LDSP u16*)(sm + kimg + KPADB);
      *k0 = (u16)pkbf(bf2f(*k0) + dsk, 0.f);
    }
#pragma unroll
    for (int j = 2; j < 4; ++j) dma16(P.ssm_k, (u32)ch * (SEQ * 2) + 1024 * j + 16 * lane, smb + kimg + KPADB + 1024 * j);
    wavefence();
    kb = kimg + 8 * (4 * lp + 8 * g + lq);
    xb = XIMG + w * XS + 224 + 8 * (lp - 8 * g - lq);
    yw = YIMG + w * YS + 2 * lane;
    qa = smb + RINGQ + 16 * lane + 2 * w;
  } else {
    const int ch0 = (xcd * 20 + (idx - 10)) * 4;
    if (w >= 8) {
      const int h = w - 8;
      const char* const pf2 = prefetch_addr(P, h + 4, idx, xcd, lane);
      wgbarrier();
      delay(h); touch64(pf1, smb + PFD);
      {
        const u32x4 ones = {0x3F803F80u, 0x3F803F80u, 0x3F803F80u, 0x3F803F80u};
#pragma unroll
        for (int i = 0; i < 4; ++i) *(LDSP u32x4*)(sm + B_ONES + 4096 * h + 1024 * i + 16 * lane) = ones;
      }
      if (lane < 10) *(LDSP u32x4*)(sm + XIMG + h * XS + 16 * lane) = u32x4{0, 0, 0, 0};
      delay(4); touch64(pf2, smb + PFD);
      const u32 rowoff = (u32)(64 * h + lane) * ROWB + (u32)(ch0 & ~7) * 2;
      vmcnt<1>();
      delay(3 - h);
      STAMP(1);
      const u32 ua = RAWU + 1024 * h + 16 * (16 * g + 4 * lp + lq) + 2 * (ch0 & 4);
      const u32 xw = XIMG + (lane & 3) * XS + XPADB + 8 * (lane >> 2) + 128 * h;
      const u32 yrd = B_YIMG + lq * YS + 2 * (64 * h + 16 * g + 4 * lp);
      u16* const outp = P.out + DCH + ch0 + (size_t)(64 * h + lane) * (2 * DCH);
#pragma unroll 1
      for (int it = -2; it < 8; ++it) {
        int i = it;
        asm volatile("" : "+s"(i));
        if (i + 2 < 8) dma16(P.u, rowoff + (u32)(i + 2) * (256 * ROWB), smb + RAWU + 4096 * (i + 2) + 1024 * h);
        if (i >= 0) {
          if (i < 6) vmcnt<2>(); else if (i == 6) vmcnt<1>(); else vmcnt<0>();
          *(LDSP u32x2*)(sm + xw + 512 * i) = trread(sm + ua + 4096 * i);
          asm volatile("" ::: "memory");
          *(volatile LDSP u32*)(sm + FLAGS + 4 * h) = (u32)(i + 1);
          if (i == 0) STAMP(12); if (i == 3) STAMP(13); if (i == 7) STAMP(14);
          asm volatile("" ::: "memory");
        }
      }
#pragma unroll 1
      for (int i = 0; i < 8; ++i) {
        wait_words(sm + FLAGS + 16, lane, 4, (u32)(i + 1));
        *(u32x2*)(outp + (size_t)i * 256 * (2 * DCH)) = trread(sm + yrd + 512 * i);
      }
      STAMP_OUT();
      return;
    }
    const int cw = w & 3, ps = w >> 2;
    const int ch = ch0 + cw;
    const size_t o = (size_t)ch * NST + 64 * ps + lane;
    const u16 i_ar = P.A_re[o], i_ai = P.A_im[o], i_br = P.B_re[o], i_bi = P.B_im[o], i_cr = P.C_re[o], i_ci = P.C_im[o];
    const float ldt = ld_bf_uniform(P.log_dt, ch);
    const float dsk = ld_bf_uniform(P.Dskip, ch);
    wgbarrier();
    const float dt = rbf(expf(ldt));
    const float zr = dt * bf2f(i_ar), zi = dt * bf2f(i_ai);
    const cf dB = {dt * bf2f(i_br), dt * bf2f(i_bi)};
    const cf C = {bf2f(i_cr), bf2f(i_ci)};
    const cf W = cmul(C, dB);
    float mx = fmaxf(fabsf(W.r), fabsf(W.i));
    STAMP(1);
    {
      int m = __float_as_int(mx);
      m = max(m, __builtin_amdgcn_update_dpp(0, m, 0x111, 0xf, 0xf, false));
      m = max(m, __builtin_amdgcn_update_dpp(0, m, 0x112, 0xf, 0xf, false));
      m = max(m, __builtin_amdgcn_update_dpp(0, m, 0x114, 0xf, 0xf, false));
      m = max(m, __builtin_amdgcn_update_dpp(0, m, 0x118, 0xf, 0xf, false));
      int a0 = __builtin_amdgcn_readlane(m, 15), a1 = __builtin_amdgcn_readlane(m, 31);
      int a2 = __builtin_amdgcn_readlane(m, 47), a3 = __builtin_amdgcn_readlane(m, 63);
      mx = __int_as_float(max(max(a0, a1), max(a2, a3)));
    }
    int ex = (int)((__float_as_uint(mx) >> 23) & 0xff);
    ex = min(max(ex, 30), 240);
    const float sY = __uint_as_float((u32)(267 - ex) << 23);
    const float so = __uint_as_float((u32)(ex - 27) << 23);
    const float SX = 16384.f;

    f32x4 ak[3];
    const u32 stg = STG + w * STS;
    const u32 rb = stg + li * 272 + g * 16;
    const u32 m0v = smb + w * STS;
#define STGW(row, val_r, val_i) \
    asm volatile("ds_write_addtid_b32 %0 offset:%1" :: "v"(pkh((val_r), (val_i))), "i"(STG + (row) * 272) : "memory")
    {
      const cf E1 = cexp_pow2(zr, zi, 1.f);
      const cf E8c = cexp_pow2(zr, -zi, 8.f);
      const cf E128 = cexp_pow2(zr, zi, 128.f);
      const cf E1024 = cexp_pow2(zr, zi, 1024.f);
      const cf E2 = cmul(E1, E1);
      const cf E4 = cmul(E2, E2);
      u32 keep;
      asm volatile("s_mov_b32 %0, m0\n\ts_mov_b32 m0, %1\n\ts_nop 0" : "=&s"(keep) : "s"(m0v) : "memory");
      cf y = {W.r * sY, -(W.i * sY)};
      cf P1 = {E128.r * SX, E128.i * SX};
      cf v0 = {SX, 0.f}, v1 = P1;
#pragma unroll
      for (int b = 0; b < 8; ++b) {
        STGW(2 * b, y.r, y.i); y = cmul(y, E8c);
        STGW(16 + b, v0.r, v0.i); if (b < 7) v0 = cmul(v0, E1);
        STGW(2 * b + 1, y.r, y.i); if (b < 7) y = cmul(y, E8c);
        STGW(24 + b, v1.r, v1.i); if (b < 7) v1 = cmul(v1, E1);
      }
      f16x8 yf[4];
      const f32x4 zero = {0.f, 0.f, 0.f, 0.f};
#pragma unroll
      for (int s = 0; s < 4; ++s) {
        yf[s] = *(const LDSP f16x8*)(sm + rb + 64 * s);
        f16x8 xf = *(const LDSP f16x8*)(sm + rb + 16 * 272 + 64 * s);
        ak[0] = __builtin_amdgcn_mfma_f32_16x16x32_f16(xf, yf[s], s ? ak[0] : zero, 0, 0, 0);
      }
      asm volatile("" ::: "memory");
      cf Pa = cmul(P1, E128);
      cf Pb = {E1024.r * SX, E1024.i * SX};
#pragma unroll
      for (int m = 0; m < 2; ++m) {
        cf v = Pa;
#pragma unroll
        for (int r = 0; r < 4; ++r) {
          STGW(4 * m + r, v.r, v.i); if (r < 3) v = cmul(v, E2);
          if (r & 1) { STGW(16 + 2 * m + (r >> 1), Pb.r, Pb.i); Pb = cmul(Pb, E128); }
        }
        Pa = cmul(Pa, E128);
      }
#pragma unroll
      for (int m = 0; m < 4; ++m) {
        STGW(8 + 2 * m, Pa.r, Pa.i);
        cf v = cmul(Pa, E4);
        STGW(20 + m, Pb.r, Pb.i); Pb = cmul(Pb, E128);
        STGW(9 + 2 * m, v.r, v.i);
        if (m < 3) Pa = cmul(Pa, E128);
      }
      STGW(24, Pb.r, Pb.i);
      asm volatile("s_mov_b32 m0, %0" :: "s"(keep) : "memory");
#pragma unroll
      for (int s = 0; s < 4; ++s) {
        f16x8 x1 = *(const LDSP f16x8*)(sm + rb + 64 * s);
        f16x8 x2 = *(const LDSP f16x8*)(sm + rb + 16 * 272 + 64 * s);
        ak[1] = __builtin_amdgcn_mfma_f32_16x16x32_f16(x1, yf[s], s ? ak[1] : zero, 0, 0, 0);
        ak[2] = __builtin_amdgcn_mfma_f32_16x16x32_f16(x2, yf[s], s ? ak[2] : zero, 0, 0, 0);
      }
    }
#undef STGW
#pragma unroll
    for (int i = 0; i < 3; ++i) ak[i] *= so;
    STAMP(2);
    asm volatile("" ::: "memory");
    const u32 kimg = B_KIMG + cw * KS;
    if (ps) {
#pragma unroll
      for (int i = 0; i < 3; ++i) *(LDSP f32x4*)(sm + kimg + 1024 * i + 16 * lane) = ak[i];
      asm volatile("s_waitcnt lgkmcnt(0)" ::: "memory");
      *(volatile LDSP u32*)(sm + FLAGS + 48 + 4 * cw) = 1;
      STAMP_OUT();
      return;
    }
    wait_words(sm + FLAGS + 48, lane, 4, 1);
    {
      const u32 tb = stg;
#pragma unroll
      for (int i = 0; i < 3; ++i) ak[i] += *(const LDSP f32x4*)(sm + kimg + 1024 * i + 16 * lane);
      wavefence();
      const int b = li, p = g;
      if (lane < 6) *(LDSP u32x4*)(sm + kimg + 16 * lane) = u32x4{0, 0, 0, 0};
      {
        float a0 = ak[0][0] + (lane == 0 ? dsk : 0.f), a1 = ak[0][1], a2 = ak[0][2], a3 = ak[0][3];
        *(LDSP u32x2*)(sm + kimg + KPADB + 2 * (128 * (p >> 1) + 8 * b + 4 * (p & 1))) = u32x2{pkbf(a0, a1), pkbf(a2, a3)};
      }
      {
        float a0 = ak[1][0], a1 = ak[1][1], a2 = ak[1][2], a3 = ak[1][3];
        int i01, i23;
        if (p < 2) { i01 = 64 * p + 4 * b; i23 = i01 + 2; }
        else { i01 = 128 + 64 * (p - 2) + 2 * b; i23 = i01 + 32; }
        *(LDSP u32x2*)(sm + tb + 4 * i01) = u32x2{pkbf(a0, a0), pkbf(a1, a1)};
        *(LDSP u32x2*)(sm + tb + 4 * i23) = u32x2{pkbf(a2, a2), pkbf(a3, a3)};
#pragma unroll
        for (int j = 0; j < 4; ++j) {
          float a = ak[2][j];
          int ii = 256 + 16 * (4 * p + j) + b;
          *(LDSP u32*)(sm + tb + 4 * ii) = pkbf(a, a);
        }
      }
      wavefence();
      const LDSP u32* TB = (const LDSP u32*)(sm + tb);
      {
        const int i0 = 4 * (lane & 31);
        u32 g0 = TB[i0], g1 = TB[i0 + 1], g2 = TB[i0 + 2], g3 = TB[i0 + 3], g4 = TB[i0 + 4];
        u32x4 ov = {g0, (g1 & 0xffffu) | (g2 & 0xffff0000u), g2, (g3 & 0xffffu) | (g4 & 0xffff0000u)};
        *(LDSP u32x4*)(sm + kimg + KPADB + 16 * (32 + (lane & 31))) = ov;
      }
      {
        const int i0 = 128 + 2 * lane;
        u32 g0 = TB[i0], g1 = TB[i0 + 1], g2 = TB[i0 + 2];
        u32x4 ov = {g0, (g0 & 0xffffu) | (g1 & 0xffff0000u), g1, g2};
        *(LDSP u32x4*)(sm + kimg + KPADB + 16 * (64 + lane)) = ov;
      }
#pragma unroll
      for (int hh = 0; hh < 2; ++hh) {
        const int i0 = 256 + 64 * hh + lane;
        u32 g0 = TB[i0], g1 = TB[i0 + 1];
        u32 mid = (lane & 1) ? g1 : ((g0 & 0xffffu) | (g1 & 0xffff0000u));
        u32x4 ov = {g0, g0, mid, g1};
        *(LDSP u32x4*)(sm + kimg + KPADB + 16 * (128 + 64 * hh + lane)) = ov;
      }
    }
    wavefence();
    kb = kimg + 8 * (4 * lp + 8 * g + lq);
    xb = XIMG + cw * XS + 224 + 8 * (lp - 8 * g - lq);
    yw = B_YIMG + cw * YS + 2 * lane;
    qa = smb + B_ONES + 16 * lane;
  }

  {
    const bool l63 = (lane == 63);
    const lptr pxp = sm + FLAGS + 4 * (lane & 3);
    const lptr pyp = sm + FLAGS + 16 + 4 * w;
    u32x2 kr[16][2], xr[40][4];
    f32x4 ac[16][2];
    const f32x4 zero = {0.f, 0.f, 0.f, 0.f};
    float ts[16][2], u2[16][2], u1[16][2], yy[16][2];
    u32 pk[16];
    u32 qv[3][2] = {{0, 0}, {0, 0}, {0, 0}}, px = 0;
#define RDQ(s_, i_) asm volatile("ds_read_u16_d16_hi %0, %1 offset:%2" : "+v"(qv[(s_) % 3][i_]) : "v"(qa), "i"((((s_) * 2048) & 16383) + 1024 * (i_)) : "memory")
#define RDXP(l_, i_) xr[l_][i_] = trread(sm + xb + 256 * SC.ldm[l_] + ((i_) >> 1) * 128 + (((i_) & 1) ? 0 : 32))
#pragma unroll
    for (int d = 0; d < 8; ++d) { kr[d][0] = trread(sm + kb + 256 * d); kr[d][1] = trread(sm + kb + 256 * d + 32); }
    STAMP(3);
    wait_words(sm + FLAGS, lane, 4, 1);
    STAMP(4);
    CSTAMP(0);
    RDQ(0, 0); RDQ(0, 1);
    RDXP(0, 0); RDXP(0, 1); RDXP(0, 2); RDXP(0, 3);
    SCHED0();
    static_for<0, NGAP>([&](auto G_) {
      constexpr int G = decltype(G_)::value;
      if constexpr (G < 272) {
        constexpr int u = G >> 1, par = G & 1;
        constexpr int p = SC.p[u], d = SC.d[u], l = SC.ld[u];
        ac[p][par] = MFMA(frag(kr[d][0], kr[d][1]), frag(xr[l][2 * par], xr[l][2 * par + 1]), SC.first[u] ? zero : ac[p][par]);
        SCHED0();
      }
      static_for<0, AC.n[G]>([&](auto I_) {
        constexpr int code = AC.a[G][decltype(I_)::value];
        constexpr int kind = code & 15, a0 = (code >> 4) & 255, a1 = code >> 12;
        if constexpr (kind == A_X) RDXP(a0, a1);
        if constexpr (kind == A_K) kr[a0][a1] = trread(sm + kb + 256 * a0 + 32 * a1);
        if constexpr (kind == A_Q) RDQ(a0, a1);
        if constexpr (kind == A_PXR) px = *(volatile LDSP u32*)pxp;
        if constexpr (kind == A_VM) vmcnt<0>();
        if constexpr (kind == A_PXC) {
          if (__builtin_expect(!__all(px >= (u32)(a0 / 2 + 1)), 0)) wait_words(sm + FLAGS, lane, 4, (u32)(a0 / 2 + 1));
          asm volatile("" ::: "memory");
        }
        if constexpr (kind == A_EP) {
          constexpr int s = a0, e = a1 >> 1, o = a1 & 1;
          if constexpr (a1 < 14) {
            if constexpr (e == 0) {
              float cur = ac[s][o][3], prev = 0.f;
              if constexpr (o) prev = ac[s][0][3]; else if constexpr (s > 0) prev = ac[s - 1][1][3];
              ts[s][o] = l63 ? prev : cur;
            }
            if constexpr (e == 1) { float a = ac[s][o][2]; u2[s][o] = a + ror1(ts[s][o]); }
            if constexpr (e == 2) {
              float prev = 0.f;
              if constexpr (o) prev = u2[s][0]; else if constexpr (s > 0) prev = u2[s - 1][1];
              ts[s][o] = l63 ? prev : u2[s][o];
            }
            if constexpr (e == 3) { float a = ac[s][o][1]; u1[s][o] = a + ror1(ts[s][o]); }
            if constexpr (e == 4) {
              float prev = 0.f;
              if constexpr (o) prev = u1[s][0]; else if constexpr (s > 0) prev = u1[s - 1][1];
              ts[s][o] = l63 ? prev : u1[s][o];
            }
            if constexpr (e == 5) { float a = ac[s][o][0]; yy[s][o] = a + ror1(ts[s][o]); asm volatile("" : "+v"(yy[s][o])); }
            if constexpr (e == 6) {
              asm volatile("" : "+v"(qv[s % 3][o]));
              yy[s][o] *= __uint_as_float(qv[s % 3][o]);
              asm volatile("" : "+v"(yy[s][o]));
            }
          }
          if constexpr (a1 == 14) pk[s] = pkbf(yy[s][0], yy[s][1]);
          if constexpr (a1 == 15) *(LDSP u16*)(sm + yw + s * 256) = (u16)pk[s];
          if constexpr (a1 == 16) *(LDSP u16*)(sm + yw + s * 256 + 128) = (u16)(pk[s] >> 16);
          if constexpr (a1 == 17) {
            asm volatile("" ::: "memory");
            *(volatile LDSP u32*)pyp = (u32)(s / 2 + 1);
            asm volatile("" ::: "memory");
          }
        }
        SCHED0();
      });
    });
  }
  asm volatile(".Lfk_code_end:");
  STAMP_OUT();
}

extern "C" void kernel_launch(void* const* d_in, const int* in_sizes, int n_in,
                              void* d_out, int out_size, void* d_ws, size_t ws_size,
                              hipStream_t stream) {
  Params P;
  P.q = (const u16*)d_in[0];
  P.k = (const u16*)d_in[1];
  P.v = (const u16*)d_in[2];
  P.shift_k = (const u16*)d_in[3];
  P.ssm_k = (const u16*)d_in[4];
  P.ssm_D = (const u16*)d_in[5];
  P.u = (const u16*)d_in[6];
  P.A_re = (const u16*)d_in[7];
  P.A_im = (const u16*)d_in[8];
  P.B_re = (const u16*)d_in[9];
  P.B_im = (const u16*)d_in[10];
  P.C_re = (const u16*)d_in[11];
  P.C_im = (const u16*)d_in[12];
  P.log_dt = (const u16*)d_in[13];
  P.Dskip = (const u16*)d_in[14];
  P.out = (u16*)d_out;
  P.ws = (unsigned long long*)d_ws;
  (void)hipFuncSetAttribute((const void*)fused_kernel, hipFuncAttributeMaxDynamicSharedMemorySize, LDS_BYTES);
  fused_kernel<<<256, 768, LDS_BYTES, stream>>>(P);
}
```

```cpp
#include <hip/hip_runtime.h>
#include <hip/hip_bf16.h>
#include <stdint.h>

typedef __attribute__((ext_vector_type(4))) short s16x4;
typedef __attribute__((ext_vector_type(8))) _Float16 f16x8;
typedef __attribute__((ext_vector_type(8))) __bf16 bf16x8;
typedef __attribute__((ext_vector_type(2))) __bf16 bf16x2;
typedef __attribute__((ext_vector_type(2))) _Float16 f16x2;
typedef __attribute__((ext_vector_type(2))) float f32x2;
typedef __attribute__((ext_vector_type(4))) float f32x4;
typedef __attribute__((ext_vector_type(2))) unsigned int u32x2;
typedef __attribute__((ext_vector_type(4))) unsigned int u32x4;
typedef unsigned int u32;
typedef unsigned short u16;
#define LDSP __attribute__((address_space(3)))
#define DEV __device__ __forceinline__

constexpr int SEQ = 2048;
constexpr int DCH = 640;
constexpr int NST = 128;
constexpr int ROWB = DCH * 2;

constexpr int XS = 4272;
constexpr int XPADB = 160;
constexpr int YS = 4160;
constexpr int KS = 4224;
constexpr int KPADB = 96;
constexpr int FLAGS = 0;
constexpr int ZROW = 64;
constexpr int FLAG2 = 192;
constexpr int PFD = 256;
constexpr int SHK = 512;
constexpr int XIMG = 1024;
constexpr int YIMG = 35200;
constexpr int KIMG = 68480;
constexpr int RAWK = 102272;
constexpr int RAWV = 119168;
constexpr int RAWQ = 135552;
constexpr int STS = 8704;
constexpr int STG = 18176;
constexpr int B_KIMG = 87808;
constexpr int B_YIMG = 104704;
constexpr int RAWU = 121344;
constexpr int PRAWK = RAWU + 16384;
constexpr int PRAWV = PRAWK + 8704;
constexpr int PRAWQ = PRAWV + 8192;
constexpr int PSTG = XIMG + 2208;
constexpr int LDS_BYTES = 162816;

constexpr size_t WS_EPOCH = 0;
constexpr size_t WS_FLAG = 4096;
constexpr size_t WS_EXX = 1 << 20;
constexpr size_t WS_EXQ = 3 << 20;
constexpr size_t WS_STAMP = 8 << 20;

DEV float bf2f(u16 v) { return __uint_as_float(((u32)v) << 16); }
DEV float bflo(u32 v) { return __uint_as_float(v << 16); }
DEV float bfhi(u32 v) { return __uint_as_float(v & 0xffff0000u); }
DEV u32 pkbf(float a, float b) {
  f32x2 t = {a, b};
  bf16x2 r = __builtin_convertvector(t, bf16x2);
  return __builtin_bit_cast(u32, r);
}
DEV u32 pkh(float a, float b) {
  f32x2 t = {a, b};
  f16x2 r = __builtin_convertvector(t, f16x2);
  return __builtin_bit_cast(u32, r);
}
DEV float rbf(float a) { return bflo(pkbf(a, a)); }
DEV float dot2(u32 a, u32 b, float c) {
  return __builtin_amdgcn_fdot2_f32_bf16(__builtin_bit_cast(bf16x2, a), __builtin_bit_cast(bf16x2, b), c, false);
}
DEV float vfma(float a, float b, float c) { float d; asm("v_fma_f32 %0, %1, %2, %3" : "=v"(d) : "v"(a), "v"(b), "v"(c)); return d; }
DEV float vmul(float a, float b) { float d; asm("v_mul_f32 %0, %1, %2" : "=v"(d) : "v"(a), "v"(b)); return d; }
typedef __attribute__((address_space(1))) unsigned long long gu64;
#define RLX_AGENT __ATOMIC_RELAXED, __HIP_MEMORY_SCOPE_AGENT
typedef LDSP char* lptr;
DEV u32x2 trread(lptr p) {
  s16x4 r = __builtin_amdgcn_ds_read_tr16_b64_v4i16((LDSP s16x4*)p);
  return __builtin_bit_cast(u32x2, r);
}
DEV bf16x8 frag(u32x2 a, u32x2 b) {
  u32x4 f = {a.x, a.y, b.x, b.y};
  return __builtin_bit_cast(bf16x8, f);
}
DEV float ror1(float v) {
  int r = __builtin_amdgcn_update_dpp(0, __float_as_int(v), 0x13C, 0xf, 0xf, false);
  return __int_as_float(r);
}
DEV void wavefence() { __builtin_amdgcn_fence(__ATOMIC_ACQ_REL, "wavefront"); __builtin_amdgcn_wave_barrier(); }
DEV void wgbarrier() {
  asm volatile("s_waitcnt lgkmcnt(0)" ::: "memory");
  __builtin_amdgcn_s_barrier();
  asm volatile("" ::: "memory");
}
DEV void dma16(const void* base, u32 voff, u32 ldsaddr) {
  u32 keep;
  ldsaddr = __builtin_amdgcn_readfirstlane(ldsaddr);
  asm volatile("s_mov_b32 %0, m0\n\ts_mov_b32 m0, %3\n\ts_nop 0\n\tglobal_load_lds_dwordx4 %1, %2\n\ts_mov_b32 m0, %0"
               : "=&s"(keep) : "v"(voff), "s"(base), "s"(ldsaddr) : "memory");
}
DEV void touch64(const void* p, u32 ldsaddr) {
  u32 keep;
  asm volatile("s_mov_b32 %0, m0\n\ts_mov_b32 m0, %2\n\ts_nop 0\n\tglobal_load_lds_dword %1, off\n\ts_mov_b32 m0, %0"
               : "=&s"(keep) : "v"(p), "s"(ldsaddr) : "memory");
}
template <int N> DEV void vmcnt() { asm volatile("s_waitcnt vmcnt(%0)" :: "i"(N) : "memory"); }
#define SCHED0() __builtin_amdgcn_sched_barrier(0)

template <int V> struct IC { static constexpr int value = V; };
template <int I, int N, class F> DEV void static_for(F&& f) {
  if constexpr (I < N) { f(IC<I>{}); static_for<I + 1, N>(f); }
}

struct cf { float r, i; };
DEV cf cmul(cf a, cf b) {
  cf o;
  o.r = fmaf(a.r, b.r, -(a.i * b.i));
  o.i = fmaf(a.r, b.i, a.i * b.r);
  return o;
}
DEV cf cexp_pow2(float zr, float zi, float x) {
  float mag = __builtin_amdgcn_exp2f((zr * x) * 1.44269504088896340736f);
  float th = zi * x;
  const float C_HI = 0.15915494f;
  const float C_LO = 6.4206382e-09f;
  float f = fmaf(th, C_LO, fmaf(th, C_HI, -rintf(th * C_HI)));
  cf o;
  o.r = mag * __builtin_amdgcn_cosf(f);
  o.i = mag * __builtin_amdgcn_sinf(f);
  return o;
}

#define STAMP(n)
#define CSTAMP(n)
#define STAMP_OUT()

struct Params {
  const u16 *q, *k, *v, *shift_k, *ssm_k, *ssm_D;
  const u16 *u, *A_re, *A_im, *B_re, *B_im, *C_re, *C_im, *log_dt, *Dskip;
  u16* out;
  unsigned long long* ws;
  char* ex;
};

DEV float ld_bf_uniform(const u16* p, int idx) {
  u32 d = *(const __attribute__((address_space(4))) u32*)(uintptr_t)(p + (idx & ~1));
  return (idx & 1) ? bfhi(d) : bflo(d);
}

DEV const char* prefetch_addr(const Params& P, int cls, int slice, int xcd, int lane) {
  asm volatile("" : "+v"(lane));
  const unsigned long long TAB = 0xFE93DC2810ull;
  const u32 nib = (u32)(TAB >> (4 * (2 * cls + (lane >> 5)))) & 15u;
  const bool second = (lane >> 4) & 1;
  const u16* base = (nib < 8) ? (second ? P.v : P.k) : (nib < 12 ? P.q : P.u);
  const u32 blk = (nib < 8) ? nib : 2 * (nib & 3) + (second ? 1 : 0);
  const u32 row = blk * 256u + (u32)slice * 8u + ((lane >> 1) & 7);
  return (const char*)base + (row * ROWB + (u32)(((160 * xcd) >> 7) + (lane & 1)) * 128u);
}
DEV void wait_words(lptr p, int lane, int n, u32 v) {
#pragma unroll 1
  for (int spin = 0; spin < (1 << 16); ++spin) {
    u32 x = *(volatile LDSP u32*)(p + 4 * (lane & (n - 1)));
    if (__all(x >= v)) break;
    __builtin_amdgcn_s_sleep(1);
  }
  asm volatile("" ::: "memory");
}
#define PFS 12
DEV void delay(int n) {
  for (int i = 0; i < n; ++i) __builtin_amdgcn_s_sleep(PFS);
}

template <int RS>
DEV u32x2 kv4(lptr sm, u32 ka, u32 va, float w0, float w1, float w2, float w3) {
  float kx[8], vx[4], kv[4];
#pragma unroll
  for (int j = 0; j < 4; ++j) { u32x2 r = trread(sm + ka + RS * j); kx[2 * j] = __uint_as_float(r.x); kx[2 * j + 1] = __uint_as_float(r.y); }
#pragma unroll
  for (int j = 0; j < 2; ++j) { u32x2 r = trread(sm + va + RS * j); vx[2 * j] = __uint_as_float(r.x); vx[2 * j + 1] = __uint_as_float(r.y); }
#pragma unroll
  for (int j = 0; j < 4; ++j)
    kv[j] = vmul(vfma(kx[j + 4], w0, vfma(kx[j + 3], w1, vfma(kx[j + 2], w2, vmul(kx[j + 1], w3)))), vx[j]);
  return u32x2{pkbf(kv[0], kv[1]), pkbf(kv[2], kv[3])};
}
DEV void set_word(lptr p, u32 v) {
  asm volatile("" ::: "memory");
  *(volatile LDSP u32*)p = v;
  asm volatile("" ::: "memory");
}

struct Sched {
  int p[136], d[136], ld[136];
  bool first[136];
  int base[17];
  int ldm[40], ldu[40];
  int ku[16];
  int nld;
};
constexpr Sched make_sched() {
  Sched S{};
  int n = 0, nl = 16;
  for (int m = 0; m < 16; ++m) S.ldm[m] = m;
  for (int d = 0; d < 16; ++d) S.ku[d] = -1;
  for (int s = 0; s < 16; ++s) {
    S.base[s] = n;
    S.ldu[s] = n;
    for (int k = 0; k < 8 && s + k < 16; ++k) { S.p[n] = s + k; S.d[n] = k; S.ld[n] = s; S.first[n] = (s == 0) || (k == 7); ++n; }
    int lo = -1, hi = -1, cnt = 0, half = 0;
    if (s == 6) { lo = 8; hi = 8; cnt = 1; half = 2; }
    else if (s >= 7 && s <= 12) { lo = (s & 1) ? s + 2 : s + 1; hi = lo + 1; cnt = hi - 7; half = (s & 1) ? 0 : 1; }
    else if (s == 13 || s == 14) { lo = 15; hi = 15; cnt = 8; half = (s == 13) ? 0 : 1; }
    if (cnt) {
      const int split = (cnt + 1) / 2;
      const int j0 = (half == 1) ? split : 0, j1 = (half == 0) ? split : cnt;
      for (int j = j0; j < j1; ++j) {
        const int mm = cnt - 1 - j;
        S.ldm[nl] = mm; S.ldu[nl] = n;
        for (int t = hi; t >= lo; --t)
          if (t - mm >= 8) { S.p[n] = t; S.d[n] = t - mm; S.ld[n] = nl; S.first[n] = false; ++n; }
        ++nl;
      }
    }
  }
  S.base[16] = n;
  S.nld = nl;
  for (int u = 0; u < n; ++u) if (S.ku[S.d[u]] < 0) S.ku[S.d[u]] = u;
  return S;
}
constexpr Sched SC = make_sched();
static_assert(SC.base[16] == 136, "schedule");
constexpr int NGAP = 272 + 24, CAP = 2;
enum { A_NONE = 0, A_X, A_K, A_Q, A_PXR, A_PXC, A_EP, A_VM };
struct Acts { int n[NGAP]; int a[NGAP][4]; };
constexpr int place_back(Acts& T, int latest, int code, int cap = CAP) {
  int g = latest;
  while (g > 0 && T.n[g] >= cap) --g;
  T.a[g][T.n[g]++] = code;
  return g;
}
constexpr int place_fwd(Acts& T, int earliest, int code, int cap) {
  int g = earliest;
  while (T.n[g] >= (g < 272 ? cap : 4)) ++g;
  T.a[g][T.n[g]++] = code;
  return g;
}
constexpr Acts make_acts() {
  Acts T{};
  for (int l = 15; l >= 1; --l) {
    int g = 2 * SC.ldu[l] - 5;
    for (int i = 3; i >= 0; --i) g = place_back(T, g - 1, A_X | (l << 4) | (i << 12), 1);
    g = place_back(T, g - 1, A_Q | (l << 4) | (1 << 12), 1);
    g = place_back(T, g - 1, A_Q | (l << 4), 1);
    if (!(l & 1)) {
      g = place_back(T, g - 1, A_PXC | (l << 4), 1);
      place_back(T, g - 6, A_PXR, 1);
    }
  }
  for (int l = SC.nld - 1; l >= 16; --l) {
    int g = 2 * SC.ldu[l] - 5;
    for (int i = 3; i >= 0; --i) g = place_back(T, g - 1, A_X | (l << 4) | (i << 12), SC.ldu[l] >= 100 ? 2 : 1);
  }
  for (int d = 15; d >= 8; --d) {
    int g = 2 * SC.ku[d] - 5;
    g = place_back(T, g - 1, A_K | (d << 4) | (1 << 12), 2);
    g = place_back(T, g - 1, A_K | (d << 4), 2);
    if (d == 8) place_back(T, g - 1, A_VM, 2);
  }
  for (int s = 0; s < 16; ++s) {
    int g = 2 * SC.base[s] + 4;
    const int nop = (s & 1) ? 18 : 17;
    for (int e = 0; e < nop; ++e) g = place_fwd(T, g + ((e & 1) && e < 14 ? 0 : 1), A_EP | (s << 4) | (e << 12), s >= 11 ? 3 : CAP);
  }
  return T;
}
constexpr Acts AC = make_acts();

#define MFMA(a, b, c) __builtin_amdgcn_mfma_f32_16x16x32_bf16((a), (b), (c), 0, 0, 0)

__global__ __launch_bounds__(768, 3) void fused_kernel(Params P) {
  extern __shared__ __attribute__((aligned(16))) char smem[];
  const lptr sm = (lptr)smem;
  const u32 smb = (u32)(uintptr_t)sm;
  const int tid = threadIdx.x;
  const int w = __builtin_amdgcn_readfirstlane(tid >> 6);
  const int lane = tid & 63;
  const int wg = blockIdx.x;
  const int xcd = wg & 7, idx = wg >> 3;
  const bool partA = idx < 10;
  const int g = lane >> 4, li = lane & 15, lq = li >> 2, lp = li & 3;
  STAMP(0);
  if (w == 7) {
    unsigned long long pc;
    u32 len;
    asm volatile("s_getpc_b64 %0\n.Lfk_code_begin:\n\ts_mov_b32 %1, .Lfk_end0-.Lfk_code_begin\n\ts_max_i32 %1, %1, .Lfk_end1-.Lfk_code_begin\n\ts_max_i32 %1, %1, .Lfk_end2-.Lfk_code_begin\n\ts_max_i32 %1, %1, .Lfk_end3-.Lfk_code_begin\n\ts_max_i32 %1, %1, .Lfk_end4-.Lfk_code_begin\n\ts_max_i32 %1, %1, .Lfk_end5-.Lfk_code_begin" : "=s"(pc), "=&s"(len) :: "scc");
    const u32 off = min((u32)(lane + 64 * (idx % 3)) * 128u, len & ~3u);
    touch64((const char*)pc + off, smb + PFD);
  }
  gu64* const gflag = (gu64*)(uintptr_t)(P.ex + WS_FLAG);
  gu64* const gepoch = (gu64*)(uintptr_t)(P.ex + WS_EPOCH);
  if (idx >= 30) {
    if (w < 5) { delay(w); touch64(prefetch_addr(P, w, idx, xcd, lane), smb + PFD); }
    if (w == 8 && wg == 240) {
      const unsigned long long tag = __hip_atomic_load(gepoch, RLX_AGENT) + 1;
#pragma unroll 1
      for (int spin = 0; spin < (1 << 16); ++spin) {
        bool ok = true;
#pragma unroll
        for (int j = 0; j < 4; ++j) { const int f = min(lane + 64 * j, 239); ok &= (__hip_atomic_load(gflag + f, RLX_AGENT) == tag); }
        if (__all(ok)) break;
        __builtin_amdgcn_s_sleep(16);
      }
      if (lane == 0) __hip_atomic_store(gepoch, tag, RLX_AGENT);
    }
    vmcnt<0>();
    asm volatile(".Lfk_end1:");
    __builtin_amdgcn_endpgm();
    __builtin_unreachable();
  }
  if (tid < 64) *(LDSP u32*)(sm + FLAGS + 4 * tid) = 0;

  u32 kb, xb, yw;

  if (partA) {
    const int grp = xcd * 10 + idx;
    const int ch0 = grp * 8;
    if (w >= 10) {
      const int h = w - 10;
      const unsigned long long tag = __hip_atomic_load(gepoch, RLX_AGENT) + 1;
      if (h == 0 && lane == 0) __hip_atomic_store(gflag + 160 + grp, tag, RLX_AGENT);
      wgbarrier();
      set_word(sm + FLAGS + 8 + 4 * h, 4);
      __builtin_amdgcn_s_setprio(2);
#pragma unroll 1
      for (int spin = 0; spin < (1 << 16); ++spin) {
        if (__all(__hip_atomic_load(gflag + (grp >> 3) * 16 + (lane & 15), RLX_AGENT) == tag)) break;
        __builtin_amdgcn_s_sleep(8);
      }
      __builtin_amdgcn_fence(__ATOMIC_ACQUIRE, "agent");
      STAMP(12);
#pragma unroll
      for (int hf = 0; hf < 2; ++hf)
#pragma unroll
        for (int c = 0; c < 4; ++c) {
          const int cl = 4 * h + c;
          const u32 go = (u32)(ch0 + cl) * 2048 + 1024 * hf + 16 * lane;
          dma16(P.ex + WS_EXX, go, smb + XIMG + cl * XS + XPADB + 2048 + 1024 * hf);
          dma16(P.ex + WS_EXQ, go, smb + YIMG + cl * YS + 2048 + 1024 * hf);
        }
      vmcnt<8>();
      set_word(sm + FLAGS + 8 + 4 * h, 6);
      vmcnt<0>();
      set_word(sm + FLAGS + 8 + 4 * h, 8);
      STAMP(13);
      const u32 yrd = YIMG + lq * YS + 2 * (128 * h + 16 * g + 4 * lp);
      u16* const outp = P.out + ch0 + (size_t)(128 * h + lane) * (2 * DCH);
#pragma unroll 1
      for (int i = 0; i < 8; ++i) {
        wait_words(sm + FLAGS + 16, lane, 8, (u32)(i + 1));
#pragma unroll
        for (int hf = 0; hf < 2; ++hf) {
          u32x2 a = trread(sm + yrd + 512 * i + 128 * hf);
          u32x2 b = trread(sm + yrd + 512 * i + 128 * hf + 4 * YS);
          *(u32x4*)(outp + (size_t)(i * 256 + 64 * hf) * (2 * DCH)) = u32x4{a.x, a.y, b.x, b.y};
        }
      }
      STAMP_OUT();
      asm volatile(".Lfk_end2:");
      __builtin_amdgcn_endpgm();
      __builtin_unreachable();
    }
    if (w >= 8) {
      const int h = w - 8;
      const u32 rowoff = (u32)(128 * h + lane) * ROWB + (u32)ch0 * 2;
      if (h == 0) {
        if (lane < 4) dma16(P.shift_k, (u32)ch0 * 8 + 16 * lane, smb + SHK);
        if (lane < 4) *(LDSP u32x4*)(sm + RAWK + 16 * lane) = u32x4{0, 0, 0, 0};
        vmcnt<0>();
      }
      const int srow = 8 * g + 4 * (lp >> 1);
      const int shalf = 8 * (lp & 1);
      const u32 ka0 = RAWK + h * 2112 + 16 * (srow + (lq >> 1)) + shalf;
      const u32 va0 = RAWV + h * 2048 + 16 * (srow + (lq >> 1)) + shalf;
      const u32 qa0 = RAWQ + h * 2048 + 16 * (srow + lq) + shalf;
      const u32 xw = XIMG + (lane & 7) * XS + XPADB + 8 * (lane >> 3) + 256 * h;
      const u32 qw = YIMG + (lane & 7) * YS + 8 * (lane >> 3) + 256 * h;
      wgbarrier();
      STAMP(1);
      __builtin_amdgcn_s_setprio(3);
      const u32x2 sw = *(LDSP u32x2*)(sm + SHK + 8 * (lane & 7));
      const float w0 = bflo(sw.x), w1 = bfhi(sw.x), w2 = bflo(sw.y), w3 = bfhi(sw.y);
#pragma unroll 1
      for (int it = -1; it < 4; ++it) {
        int i = it;
        asm volatile("" : "+s"(i));
        const int lb = i + 1;
        if (lb < 4) {
          const u32 ro = rowoff + (u32)lb * (256 * ROWB);
          const bool first = (lb == 0 && h == 0);
          if (lane < 4) dma16(P.k, first ? ro : ro - 4 * ROWB, first ? smb + PFD : smb + RAWK + lb * 4224 + h * 2112);
#pragma unroll
          for (int hf = 0; hf < 2; ++hf) {
            dma16(P.k, ro + hf * 64 * ROWB, smb + RAWK + lb * 4224 + h * 2112 + 64 + hf * 1024);
            dma16(P.v, ro + hf * 64 * ROWB, smb + RAWV + lb * 4096 + h * 2048 + hf * 1024);
            dma16(P.q, ro + hf * 64 * ROWB, smb + RAWQ + lb * 4096 + h * 2048 + hf * 1024);
          }
        }
        if (i >= 0) {
          if (i < 3) vmcnt<7>(); else vmcnt<0>();
#pragma unroll 2
          for (int u = 0; u < 4; ++u) {
            const u32 ka = (lq & 1) ? ka0 + i * 4224 + u * 512 : (u32)ZROW;
            const u32 va = (lq & 1) ? va0 + i * 4096 + u * 512 : (u32)ZROW;
            *(LDSP u32x2*)(sm + qw + 512 * i + 64 * u) = trread(sm + qa0 + i * 4096 + u * 512);
            *(LDSP u32x2*)(sm + xw + 512 * i + 64 * u) = kv4<32>(sm, ka, va, w0, w1, w2, w3);
          }
          set_word(sm + FLAGS + 4 * h, i < 3 ? (u32)(i + 1) : 8u);
          if (i == 0) STAMP(12); if (i == 3) STAMP(13);
        }
      }
      STAMP_OUT();
      asm volatile(".Lfk_end3:");
      __builtin_amdgcn_endpgm();
      __builtin_unreachable();
    }
    const int ch = ch0 + w;
    const u32 kimg = KIMG + w * KS;
#pragma unroll
    for (int j = 0; j < 4; ++j) {
      dma16(P.ssm_k, (u32)ch * (SEQ * 2) + 1024 * j + 16 * lane, smb + kimg + KPADB + 1024 * j);
      if (j == 1) { if (w < 2) { delay(w); touch64(prefetch_addr(P, w, idx, xcd, lane), smb + PFD); } else delay(2); }
    }
    if (lane < 10) *(LDSP u32x4*)(sm + XIMG + w * XS + 16 * lane) = u32x4{0, 0, 0, 0};
    if (lane < 6) *(LDSP u32x4*)(sm + kimg + 16 * lane) = u32x4{0, 0, 0, 0};
    const float dsk = ld_bf_uniform(P.ssm_D, ch);
    if (w == 0) vmcnt<2>();
    wgbarrier();
    STAMP(1);
    if (w == 1) vmcnt<3>(); else vmcnt<2>();
    if (lane == 0) {
      LDSP u16* k0 = (LDSP u16*)(sm + kimg + KPADB);
      *k0 = (u16)pkbf(bf2f(*k0) + dsk, 0.f);
    }
    wavefence();
    kb = kimg + 8 * (4 * lp + 8 * g + lq);
    xb = XIMG + w * XS + 224 + 8 * (lp - 8 * g - lq);
    yw = YIMG + w * YS + 2 * lane;
  } else {
    const int bi = xcd * 20 + (idx - 10);
    const int ch0 = bi * 4;
    if (w >= 8) {
      const int h = w - 8;
      const int pcol = bi >> 4, pchunk = bi & 15;
      unsigned long long tag = 0;
      const char* const pf1 = prefetch_addr(P, h, idx, xcd, lane);
      if (h == 0) { tag = __hip_atomic_load(gepoch, RLX_AGENT) + 1; touch64(pf1, smb + PFD); }
      const int e3 = (idx - 10) * 4 + h;
      const int c3 = e3 < 20 ? 4 : 2 + (e3 - 20) % 3;
      const char* const pf3 = prefetch_addr(P, c3, e3 < 20 ? 10 + e3 : (e3 - 20) / 3, xcd, lane);
      wgbarrier();
      delay(1);
      if (h == 1) touch64(pf1, smb + PFD);
      {
        const u32 po = (u32)(1024 + 64 * pchunk + 16 * h + (lane >> 3)) * ROWB + (u32)pcol * 128 + 16 * (lane & 7);
#pragma unroll
        for (int j = 0; j < 2; ++j) {
          dma16(P.k, po + j * 8 * ROWB, smb + PRAWK + 512 + h * 2048 + j * 1024);
          dma16(P.v, po + j * 8 * ROWB, smb + PRAWV + h * 2048 + j * 1024);
          dma16(P.q, po + j * 8 * ROWB, smb + PRAWQ + h * 2048 + j * 1024);
        }
        if (h == 0 && lane < 32) dma16(P.k, po - 4 * ROWB, smb + PRAWK);
        if (h == 1 && lane < 32) dma16(P.shift_k, (u32)pcol * 512 + 16 * lane, smb + SHK);
      }
      {
        const u32x4 ones = {0x3F803F80u, 0x3F803F80u, 0x3F803F80u, 0x3F803F80u};
#pragma unroll
        for (int i = 0; i < 4; ++i) *(LDSP u32x4*)(sm + B_YIMG + h * YS + 1024 * i + 16 * lane) = ones;
      }
      if (lane < 10) *(LDSP u32x4*)(sm + XIMG + h * XS + 16 * lane) = u32x4{0, 0, 0, 0};
      vmcnt<0>();
      set_word(sm + FLAG2 + 4 * h, 1);
      if (h >= 2) touch64(pf1, smb + PFD);
      if (e3 < 50) touch64(pf3, smb + PFD);
      wait_words(sm + FLAG2, lane, 4, 1);
      STAMP(1);
      {
        const u32x2 sw = *(LDSP u32x2*)(sm + SHK + 8 * (16 * h + li));
        const float w0 = bflo(sw.x), w1 = bfhi(sw.x), w2 = bflo(sw.y), w3 = bfhi(sw.y);
        const u32 co = 32 * h + 8 * lp;
        const u32 stg = PSTG + h * XS;
        const u32 sw_ = stg + li * 128 + 8 * (g & 1);
        const u32 sr_ = stg + (lane >> 3) * 128 + 16 * ((lane ^ (lane >> 3)) & 7);
        const auto rx = __builtin_amdgcn_make_buffer_rsrc(P.ex + WS_EXX, 0, 4u << 20, 0x00020000);
        const u32 eo = (u32)(pcol * 64 + 16 * h + (lane >> 3)) * 2048 + 128 * pchunk + 16 * (lane & 7);
#pragma unroll 1
        for (int rnd = 0; rnd < 2; ++rnd) {
#pragma unroll 2
          for (int u = 0; u < 4; ++u) {
            u32x2 o;
            if (rnd == 0) {
              const u32 ro = 128 * (16 * u + 4 * g + (lq >> 1)) + co;
              float kx[8], vx[4], kv[4];
#pragma unroll
              for (int j = 0; j < 4; ++j) { u32x2 r = trread(sm + ((lq & 1) ? PRAWK + ro + 256 * j : (u32)ZROW)); kx[2 * j] = __uint_as_float(r.x); kx[2 * j + 1] = __uint_as_float(r.y); }
#pragma unroll
              for (int j = 0; j < 2; ++j) { u32x2 r = trread(sm + ((lq & 1) ? PRAWV + ro + 256 * j : (u32)ZROW)); vx[2 * j] = __uint_as_float(r.x); vx[2 * j + 1] = __uint_as_float(r.y); }
#pragma unroll
              for (int j = 0; j < 4; ++j)
                kv[j] = vmul(vfma(kx[j + 4], w0, vfma(kx[j + 3], w1, vfma(kx[j + 2], w2, vmul(kx[j + 1], w3)))), vx[j]);
              o = u32x2{pkbf(kv[0], kv[1]), pkbf(kv[2], kv[3])};
            } else {
              o = trread(sm + PRAWQ + 128 * (16 * u + 4 * g + lq) + co);
            }
            *(LDSP u32x2*)(sm + sw_ + 16 * (((2 * u + (g >> 1)) ^ li) & 7)) = o;
          }
          wavefence();
#pragma unroll
          for (int j = 0; j < 2; ++j) {
            u32x4 d = *(const LDSP u32x4*)(sm + sr_ + 1024 * j);
            __builtin_amdgcn_raw_buffer_store_b128(d, rx, eo + j * 8 * 2048 + rnd * (u32)(WS_EXQ - WS_EXX), 0, 16);
          }
          wavefence();
        }
      }
      vmcnt<0>();
      set_word(sm + FLAG2 + 16 + 4 * h, 1);
      wait_words(sm + FLAG2 + 16, lane, 4, 1);
      if (h == 0 && lane == 0) __hip_atomic_store(gflag + bi, tag, RLX_AGENT);
      STAMP(2);
      const u32 rowoff = (u32)(64 * h + lane) * ROWB + (u32)(ch0 & ~7) * 2;
      const u32 ua = RAWU + 1024 * h + 16 * (16 * g + 4 * lp + lq) + 2 * (ch0 & 4);
      const u32 xw = XIMG + (lane & 3) * XS + XPADB + 8 * (lane >> 2) + 128 * h;
      const u32 yrd = B_YIMG + lq * YS + 2 * (64 * h + 16 * g + 4 * lp);
      u16* const outp = P.out + DCH + ch0 + (size_t)(64 * h + lane) * (2 * DCH);
#pragma unroll 1
      for (int it = -2; it < 8; ++it) {
        int i = it;
        asm volatile("" : "+s"(i));
        if (i + 2 < 8) dma16(P.u, rowoff + (u32)(i + 2) * (256 * ROWB), smb + RAWU + 4096 * (i + 2) + 1024 * h);
        if (i >= 0) {
          if (i < 6) vmcnt<2>(); else if (i == 6) vmcnt<1>(); else vmcnt<0>();
          *(LDSP u32x2*)(sm + xw + 512 * i) = trread(sm + ua + 4096 * i);
          set_word(sm + FLAGS + 4 * h, (u32)(i + 1));
          if (i == 0) STAMP(12); if (i == 3) STAMP(13); if (i == 7) STAMP(14);
        }
      }
#pragma unroll 1
      for (int i = 0; i < 8; ++i) {
        wait_words(sm + FLAGS + 16, lane, 4, (u32)(i + 1));
        *(u32x2*)(outp + (size_t)i * 256 * (2 * DCH)) = trread(sm + yrd + 512 * i);
      }
      STAMP_OUT();
      asm volatile(".Lfk_end4:");
      __builtin_amdgcn_endpgm();
      __builtin_unreachable();
    }
    const int cw = w & 3, ps = w >> 2;
    const int ch = ch0 + cw;
    const size_t o = (size_t)ch * NST + 64 * ps + lane;
    const u16 i_ar = P.A_re[o], i_ai = P.A_im[o], i_br = P.B_re[o], i_bi = P.B_im[o], i_cr = P.C_re[o], i_ci = P.C_im[o];
    const float ldt = ld_bf_uniform(P.log_dt, ch);
    const float dsk = ld_bf_uniform(P.Dskip, ch);
    wgbarrier();
    const float dt = rbf(expf(ldt));
    const float zr = dt * bf2f(i_ar), zi = dt * bf2f(i_ai);
    const cf dB = {dt * bf2f(i_br), dt * bf2f(i_bi)};
    const cf C = {bf2f(i_cr), bf2f(i_ci)};
    const cf W = cmul(C, dB);
    float mx = fmaxf(fabsf(W.r), fabsf(W.i));
    STAMP(1);
    {
      int m = __float_as_int(mx);
      m = max(m, __builtin_amdgcn_update_dpp(0, m, 0x111, 0xf, 0xf, false));
      m = max(m, __builtin_amdgcn_update_dpp(0, m, 0x112, 0xf, 0xf, false));
      m = max(m, __builtin_amdgcn_update_dpp(0, m, 0x114, 0xf, 0xf, false));
      m = max(m, __builtin_amdgcn_update_dpp(0, m, 0x118, 0xf, 0xf, false));
      int a0 = __builtin_amdgcn_readlane(m, 15), a1 = __builtin_amdgcn_readlane(m, 31);
      int a2 = __builtin_amdgcn_readlane(m, 47), a3 = __builtin_amdgcn_readlane(m, 63);
      mx = __int_as_float(max(max(a0, a1), max(a2, a3)));
    }
    int ex = (int)((__float_as_uint(mx) >> 23) & 0xff);
    ex = min(max(ex, 30), 240);
    const float sY = __uint_as_float((u32)(267 - ex) << 23);
    const float so = __uint_as_float((u32)(ex - 27) << 23);
    const float SX = 16384.f;

    f32x4 ak[3];
    const u32 stg = STG + w * STS;
    const u32 rb = stg + li * 272 + g * 16;
    const u32 m0v = smb + w * STS;
#define STGW(row, val_r, val_i) \
    asm volatile("ds_write_addtid_b32 %0 offset:%1" :: "v"(pkh((val_r), (val_i))), "i"(STG + (row) * 272) : "memory")
    {
      const cf E1 = cexp_pow2(zr, zi, 1.f);
      const cf E8c = cexp_pow2(zr, -zi, 8.f);
      const cf E128 = cexp_pow2(zr, zi, 128.f);
      const cf E1024 = cexp_pow2(zr, zi, 1024.f);
      const cf E2 = cmul(E1, E1);
      const cf E4 = cmul(E2, E2);
      u32 keep;
      asm volatile("s_mov_b32 %0, m0\n\ts_mov_b32 m0, %1\n\ts_nop 0" : "=&s"(keep) : "s"(m0v) : "memory");
      cf y = {W.r * sY, -(W.i * sY)};
      cf P1 = {E128.r * SX, E128.i * SX};
      cf v0 = {SX, 0.f}, v1 = P1;
#pragma unroll
      for (int b = 0; b < 8; ++b) {
        STGW(2 * b, y.r, y.i); y = cmul(y, E8c);
        STGW(16 + b, v0.r, v0.i); if (b < 7) v0 = cmul(v0, E1);
        STGW(2 * b + 1, y.r, y.i); if (b < 7) y = cmul(y, E8c);
        STGW(24 + b, v1.r, v1.i); if (b < 7) v1 = cmul(v1, E1);
      }
      f16x8 yf[4];
      const f32x4 zero = {0.f, 0.f, 0.f, 0.f};
#pragma unroll
      for (int s = 0; s < 4; ++s) {
        yf[s] = *(const LDSP f16x8*)(sm + rb + 64 * s);
        f16x8 xf = *(const LDSP f16x8*)(sm + rb + 16 * 272 + 64 * s);
        ak[0] = __builtin_amdgcn_mfma_f32_16x16x32_f16(xf, yf[s], s ? ak[0] : zero, 0, 0, 0);
      }
      asm volatile("" ::: "memory");
      cf Pa = cmul(P1, E128);
      cf Pb = {E1024.r * SX, E1024.i * SX};
#pragma unroll
      for (int m = 0; m < 2; ++m) {
        cf v = Pa;
#pragma unroll
        for (int r = 0; r < 4; ++r) {
          STGW(4 * m + r, v.r, v.i); if (r < 3) v = cmul(v, E2);
          if (r & 1) { STGW(16 + 2 * m + (r >> 1), Pb.r, Pb.i); Pb = cmul(Pb, E128); }
        }
        Pa = cmul(Pa, E128);
      }
#pragma unroll
      for (int m = 0; m < 4; ++m) {
        STGW(8 + 2 * m, Pa.r, Pa.i);
        cf v = cmul(Pa, E4);
        STGW(20 + m, Pb.r, Pb.i); Pb = cmul(Pb, E128);
        STGW(9 + 2 * m, v.r, v.i);
        if (m < 3) Pa = cmul(Pa, E128);
      }
      STGW(24, Pb.r, Pb.i);
      asm volatile("s_mov_b32 m0, %0" :: "s"(keep) : "memory");
#pragma unroll
      for (int s = 0; s < 4; ++s) {
        f16x8 x1 = *(const LDSP f16x8*)(sm + rb + 64 * s);
        f16x8 x2 = *(const LDSP f16x8*)(sm + rb + 16 * 272 + 64 * s);
        ak[1] = __builtin_amdgcn_mfma_f32_16x16x32_f16(x1, yf[s], s ? ak[1] : zero, 0, 0, 0);
        ak[2] = __builtin_amdgcn_mfma_f32_16x16x32_f16(x2, yf[s], s ? ak[2] : zero, 0, 0, 0);
      }
    }
#undef STGW
#pragma unroll
    for (int i = 0; i < 3; ++i) ak[i] *= so;
    STAMP(2);
    asm volatile("" ::: "memory");
    const u32 kimg = B_KIMG + cw * KS;
    if (ps) {
#pragma unroll
      for (int i = 0; i < 3; ++i) *(LDSP f32x4*)(sm + kimg + 1024 * i + 16 * lane) = ak[i];
      asm volatile("s_waitcnt lgkmcnt(0)" ::: "memory");
      *(volatile LDSP u32*)(sm + FLAGS + 48 + 4 * cw) = 1;
      STAMP_OUT();
      asm volatile(".Lfk_end5:");
      __builtin_amdgcn_endpgm();
      __builtin_unreachable();
    }
    wait_words(sm + FLAGS + 48 + 4 * cw, lane, 1, 1);
    {
      const u32 tb = stg;
#pragma unroll
      for (int i = 0; i < 3; ++i) ak[i] += *(const LDSP f32x4*)(sm + kimg + 1024 * i + 16 * lane);
      wavefence();
      const int b = li, p = g;
      if (lane < 6) *(LDSP u32x4*)(sm + kimg + 16 * lane) = u32x4{0, 0, 0, 0};
      {
        float a0 = ak[0][0] + (lane == 0 ? dsk : 0.f), a1 = ak[0][1], a2 = ak[0][2], a3 = ak[0][3];
        *(LDSP u32x2*)(sm + kimg + KPADB + 2 * (128 * (p >> 1) + 8 * b + 4 * (p & 1))) = u32x2{pkbf(a0, a1), pkbf(a2, a3)};
      }
      {
        float a0 = ak[1][0], a1 = ak[1][1], a2 = ak[1][2], a3 = ak[1][3];
        int i01, i23;
        if (p < 2) { i01 = 64 * p + 4 * b; i23 = i01 + 2; }
        else { i01 = 128 + 64 * (p - 2) + 2 * b; i23 = i01 + 32; }
        *(LDSP u32x2*)(sm + tb + 4 * i01) = u32x2{pkbf(a0, a0), pkbf(a1, a1)};
        *(LDSP u32x2*)(sm + tb + 4 * i23) = u32x2{pkbf(a2, a2), pkbf(a3, a3)};
#pragma unroll
        for (int j = 0; j < 4; ++j) {
          float a = ak[2][j];
          int ii = 256 + 16 * (4 * p + j) + b;
          *(LDSP u32*)(sm + tb + 4 * ii) = pkbf(a, a);
        }
      }
      wavefence();
      const LDSP u32* TB = (const LDSP u32*)(sm + tb);
      {
        const int i0 = 4 * (lane & 31);
        u32 g0 = TB[i0], g1 = TB[i0 + 1], g2 = TB[i0 + 2], g3 = TB[i0 + 3], g4 = TB[i0 + 4];
        u32x4 ov = {g0, (g1 & 0xffffu) | (g2 & 0xffff0000u), g2, (g3 & 0xffffu) | (g4 & 0xffff0000u)};
        *(LDSP u32x4*)(sm + kimg + KPADB + 16 * (32 + (lane & 31))) = ov;
      }
      {
        const int i0 = 128 + 2 * lane;
        u32 g0 = TB[i0], g1 = TB[i0 + 1], g2 = TB[i0 + 2];
        u32x4 ov = {g0, (g0 & 0xffffu) | (g1 & 0xffff0000u), g1, g2};
        *(LDSP u32x4*)(sm + kimg + KPADB + 16 * (64 + lane)) = ov;
      }
#pragma unroll
      for (int hh = 0; hh < 2; ++hh) {
        const int i0 = 256 + 64 * hh + lane;
        u32 g0 = TB[i0], g1 = TB[i0 + 1];
        u32 mid = (lane & 1) ? g1 : ((g0 & 0xffffu) | (g1 & 0xffff0000u));
        u32x4 ov = {g0, g0, mid, g1};
        *(LDSP u32x4*)(sm + kimg + KPADB + 16 * (128 + 64 * hh + lane)) = ov;
      }
    }
    wavefence();
    kb = kimg + 8 * (4 * lp + 8 * g + lq);
    xb = XIMG + cw * XS + 224 + 8 * (lp - 8 * g - lq);
    yw = B_YIMG + cw * YS + 2 * lane;
  }

  {
    const bool l63 = (lane == 63);
    const lptr pxp = sm + FLAGS + 4 * (lane & 3);
    const lptr pyp = sm + FLAGS + 16 + 4 * w;
    u32x2 kr[16][2], xr[40][4];
    f32x4 ac[16][2];
    const f32x4 zero = {0.f, 0.f, 0.f, 0.f};
    float ts[16][2], u2[16][2], u1[16][2], yy[16][2];
    u32 pk[16];
    u32 qv[3][2] = {{0, 0}, {0, 0}, {0, 0}}, px = 0;
    const lptr ywp = sm + yw;
    const u32 qa = (u32)(uintptr_t)ywp;
#define RDQ(s_, i_) asm volatile("ds_read_u16_d16_hi %0, %1 offset:%2" : "+v"(qv[(s_) % 3][i_]) : "v"(qa), "i"((s_) * 256 + 128 * (i_)) : "memory")
#define RDXP(l_, i_) xr[l_][i_] = trread(sm + xb + 256 * SC.ldm[l_] + ((i_) >> 1) * 128 + (((i_) & 1) ? 0 : 32))
#pragma unroll
    for (int d = 0; d < 8; ++d) { kr[d][0] = trread(sm + kb + 256 * d); kr[d][1] = trread(sm + kb + 256 * d + 32); }
    STAMP(3);
    wait_words(sm + FLAGS, lane, 4, 1);
    STAMP(4);
    CSTAMP(0);
    RDQ(0, 0); RDQ(0, 1);
    RDXP(0, 0); RDXP(0, 1); RDXP(0, 2); RDXP(0, 3);
    SCHED0();
    static_for<0, NGAP>([&](auto G_) {
      constexpr int G = decltype(G_)::value;
      if constexpr (G < 272) {
        constexpr int u = G >> 1, par = G & 1;
        constexpr int p = SC.p[u], d = SC.d[u], l = SC.ld[u];
        ac[p][par] = MFMA(frag(kr[d][0], kr[d][1]), frag(xr[l][2 * par], xr[l][2 * par + 1]), SC.first[u] ? zero : ac[p][par]);
        SCHED0();
      }
      static_for<0, AC.n[G]>([&](auto I_) {
        constexpr int code = AC.a[G][decltype(I_)::value];
        constexpr int kind = code & 15, a0 = (code >> 4) & 255, a1 = code >> 12;
        if constexpr (kind == A_X) RDXP(a0, a1);
        if constexpr (kind == A_K) kr[a0][a1] = trread(sm + kb + 256 * a0 + 32 * a1);
        if constexpr (kind == A_Q) RDQ(a0, a1);
        if constexpr (kind == A_PXR) px = *(volatile LDSP u32*)pxp;
        if constexpr (kind == A_VM) vmcnt<0>();
        if constexpr (kind == A_PXC) {
          if (__builtin_expect(!__all(px >= (u32)(a0 / 2 + 1)), 0)) wait_words(sm + FLAGS, lane, 4, (u32)(a0 / 2 + 1));
          asm volatile("" ::: "memory");
        }
        if constexpr (kind == A_EP) {
          constexpr int s = a0, e = a1 >> 1, o = a1 & 1;
          if constexpr (a1 < 14) {
            if constexpr (e == 0) {
              float cur = ac[s][o][3], prev = 0.f;
              if constexpr (o) prev = ac[s][0][3]; else if constexpr (s > 0) prev = ac[s - 1][1][3];
              ts[s][o] = l63 ? prev : cur;
            }
            if constexpr (e == 1) { float a = ac[s][o][2]; u2[s][o] = a + ror1(ts[s][o]); }
            if constexpr (e == 2) {
              float prev = 0.f;
              if constexpr (o) prev = u2[s][0]; else if constexpr (s > 0) prev = u2[s - 1][1];
              ts[s][o] = l63 ? prev : u2[s][o];
            }
            if constexpr (e == 3) { float a = ac[s][o][1]; u1[s][o] = a + ror1(ts[s][o]); }
            if constexpr (e == 4) {
              float prev = 0.f;
              if constexpr (o) prev = u1[s][0]; else if constexpr (s > 0) prev = u1[s - 1][1];
              ts[s][o] = l63 ? prev : u1[s][o];
            }
            if constexpr (e == 5) { float a = ac[s][o][0]; yy[s][o] = a + ror1(ts[s][o]); asm volatile("" : "+v"(yy[s][o])); }
            if constexpr (e == 6) {
              asm volatile("" : "+v"(qv[s % 3][o]));
              yy[s][o] *= __uint_as_float(qv[s % 3][o]);
              asm volatile("" : "+v"(yy[s][o]));
            }
          }
          if constexpr (a1 == 14) pk[s] = pkbf(yy[s][0], yy[s][1]);
          if constexpr (a1 == 15) *(LDSP u16*)(ywp + s * 256) = (u16)pk[s];
          if constexpr (a1 == 16) *(LDSP u16*)(ywp + s * 256 + 128) = (u16)(pk[s] >> 16);
          if constexpr (a1 == 17) {
            asm volatile("" ::: "memory");
            *(volatile LDSP u32*)pyp = (u32)(s / 2 + 1);
            asm volatile("" ::: "memory");
          }
        }
        SCHED0();
      });
    });
  }
  STAMP_OUT();
  asm volatile(".Lfk_end0:");
  __builtin_amdgcn_endpgm();
}

extern "C" void kernel_launch(void* const* d_in, const int* in_sizes, int n_in,
                              void* d_out, int out_size, void* d_ws, size_t ws_size,
                              hipStream_t stream) {
  Params P;
  P.q = (const u16*)d_in[0];
  P.k = (const u16*)d_in[1];
  P.v = (const u16*)d_in[2];
  P.shift_k = (const u16*)d_in[3];
  P.ssm_k = (const u16*)d_in[4];
  P.ssm_D = (const u16*)d_in[5];
  P.u = (const u16*)d_in[6];
  P.A_re = (const u16*)d_in[7];
  P.A_im = (const u16*)d_in[8];
  P.B_re = (const u16*)d_in[9];
  P.B_im = (const u16*)d_in[10];
  P.C_re = (const u16*)d_in[11];
  P.C_im = (const u16*)d_in[12];
  P.log_dt = (const u16*)d_in[13];
  P.Dskip = (const u16*)d_in[14];
  P.out = (u16*)d_out;
  P.ws = (unsigned long long*)d_ws;
  P.ex = (char*)d_ws + (16 << 20);
  (void)hipFuncSetAttribute((const void*)fused_kernel, hipFuncAttributeMaxDynamicSharedMemorySize, LDS_BYTES);
  fused_kernel<<<256, 768, LDS_BYTES, stream>>>(P);
}
```

```cpp
#include <hip/hip_runtime.h>
#include <hip/hip_bf16.h>
#include <stdint.h>

typedef __attribute__((ext_vector_type(4))) short s16x4;
typedef __attribute__((ext_vector_type(8))) _Float16 f16x8;
typedef __attribute__((ext_vector_type(8))) __bf16 bf16x8;
typedef __attribute__((ext_vector_type(2))) __bf16 bf16x2;
typedef __attribute__((ext_vector_type(2))) _Float16 f16x2;
typedef __attribute__((ext_vector_type(2))) float f32x2;
typedef __attribute__((ext_vector_type(4))) float f32x4;
typedef __attribute__((ext_vector_type(2))) unsigned int u32x2;
typedef __attribute__((ext_vector_type(4))) unsigned int u32x4;
typedef unsigned int u32;
typedef unsigned short u16;
#define LDSP __attribute__((address_space(3)))
#define DEV __device__ __forceinline__

constexpr int SEQ = 2048;
constexpr int DCH = 640;
constexpr int NST = 128;
constexpr int ROWB = DCH * 2;

constexpr int XS = 4272;
constexpr int XPADB = 160;
constexpr int YS = 4160;
constexpr int KS = 4224;
constexpr int KPADB = 96;
constexpr int FLAGS = 0;
constexpr int ZROW = 64;
constexpr int FLAG2 = 192;
constexpr int PFD = 256;
constexpr int SHK = 512;
constexpr int XIMG = 1024;
constexpr int YIMG = 35200;
constexpr int KIMG = 68480;
constexpr int RAWK = 102272;
constexpr int RAWV = 119168;
constexpr int RAWQ = 135552;
constexpr int STS = 8704;
constexpr int STG = 18176;
constexpr int B_KIMG = 87808;
constexpr int B_YIMG = 104704;
constexpr int RAWU = 121344;
constexpr int PRAWK = RAWU + 16384;
constexpr int PRAWV = PRAWK + 8704;
constexpr int PRAWQ = PRAWV + 8192;
constexpr int PSTG = XIMG + 2208;
constexpr int LDS_BYTES = 154112;

constexpr size_t WS_EPOCH = 0;
constexpr size_t WS_FLAG = 4096;
constexpr size_t WS_EXX = 1 << 20;
constexpr size_t WS_EXQ = 3 << 20;
constexpr size_t WS_STAMP = 8 << 20;

DEV float bf2f(u16 v) { return __uint_as_float(((u32)v) << 16); }
DEV float bflo(u32 v) { return __uint_as_float(v << 16); }
DEV float bfhi(u32 v) { return __uint_as_float(v & 0xffff0000u); }
DEV u32 pkbf(float a, float b) {
  f32x2 t = {a, b};
  bf16x2 r = __builtin_convertvector(t, bf16x2);
  return __builtin_bit_cast(u32, r);
}
DEV u32 pkh(float a, float b) {
  f32x2 t = {a, b};
  f16x2 r = __builtin_convertvector(t, f16x2);
  return __builtin_bit_cast(u32, r);
}
DEV float rbf(float a) { return bflo(pkbf(a, a)); }
DEV float dot2(u32 a, u32 b, float c) {
  return __builtin_amdgcn_fdot2_f32_bf16(__builtin_bit_cast(bf16x2, a), __builtin_bit_cast(bf16x2, b), c, false);
}
DEV float vfma(float a, float b, float c) { float d; asm("v_fma_f32 %0, %1, %2, %3" : "=v"(d) : "v"(a), "v"(b), "v"(c)); return d; }
DEV float vmul(float a, float b) { float d; asm("v_mul_f32 %0, %1, %2" : "=v"(d) : "v"(a), "v"(b)); return d; }
typedef __attribute__((address_space(1))) unsigned long long gu64;
#define RLX_AGENT __ATOMIC_RELAXED, __HIP_MEMORY_SCOPE_AGENT
typedef LDSP char* lptr;
DEV u32x2 trread(lptr p) {
  s16x4 r = __builtin_amdgcn_ds_read_tr16_b64_v4i16((LDSP s16x4*)p);
  return __builtin_bit_cast(u32x2, r);
}
DEV bf16x8 frag(u32x2 a, u32x2 b) {
  u32x4 f = {a.x, a.y, b.x, b.y};
  return __builtin_bit_cast(bf16x8, f);
}
DEV float ror1(float v) {
  int r = __builtin_amdgcn_update_dpp(0, __float_as_int(v), 0x13C, 0xf, 0xf, false);
  return __int_as_float(r);
}
DEV void wavefence() { __builtin_amdgcn_fence(__ATOMIC_ACQ_REL, "wavefront"); __builtin_amdgcn_wave_barrier(); }
DEV void wgbarrier() {
  asm volatile("s_waitcnt lgkmcnt(0)" ::: "memory");
  __builtin_amdgcn_s_barrier();
  asm volatile("" ::: "memory");
}
DEV void dma16(const void* base, u32 voff, u32 ldsaddr) {
  u32 keep;
  ldsaddr = __builtin_amdgcn_readfirstlane(ldsaddr);
  const unsigned long long b64 = (unsigned long long)(uintptr_t)base;
  const unsigned long long sb = ((unsigned long long)(u32)__builtin_amdgcn_readfirstlane((u32)(b64 >> 32)) << 32) |
                                (u32)__builtin_amdgcn_readfirstlane((u32)b64);
  asm volatile("s_mov_b32 %0, m0\n\ts_mov_b32 m0, %3\n\ts_nop 2\n\tglobal_load_lds_dwordx4 %1, %2\n\ts_mov_b32 m0, %0"
               : "=&s"(keep) : "v"(voff), "s"(sb), "s"(ldsaddr) : "memory");
}
DEV void touch64(const void* p, u32 ldsaddr) {
  u32 keep;
  asm volatile("s_mov_b32 %0, m0\n\ts_mov_b32 m0, %2\n\ts_nop 0\n\tglobal_load_lds_dword %1, off\n\ts_mov_b32 m0, %0"
               : "=&s"(keep) : "v"(p), "s"(ldsaddr) : "memory");
}
template <int N> DEV void vmcnt() { asm volatile("s_waitcnt vmcnt(%0)" :: "i"(N) : "memory"); }
#define SCHED0() __builtin_amdgcn_sched_barrier(0)

template <int V> struct IC { static constexpr int value = V; };
template <int I, int N, class F> DEV void static_for(F&& f) {
  if constexpr (I < N) { f(IC<I>{}); static_for<I + 1, N>(f); }
}

struct cf { float r, i; };
DEV cf cmul(cf a, cf b) {
  cf o;
  o.r = fmaf(a.r, b.r, -(a.i * b.i));
  o.i = fmaf(a.r, b.i, a.i * b.r);
  return o;
}
DEV cf cexp_pow2(float zr, float zi, float x) {
  float mag = __builtin_amdgcn_exp2f((zr * x) * 1.44269504088896340736f);
  float th = zi * x;
  const float C_HI = 0.15915494f;
  const float C_LO = 6.4206382e-09f;
  float f = fmaf(th, C_LO, fmaf(th, C_HI, -rintf(th * C_HI)));
  cf o;
  o.r = mag * __builtin_amdgcn_cosf(f);
  o.i = mag * __builtin_amdgcn_sinf(f);
  return o;
}

#define STAMP(n)
#define CSTAMP(n)
#define STAMP_OUT()

struct Params {
  const u16 *q, *k, *v, *shift_k, *ssm_k, *ssm_D;
  const u16 *u, *A_re, *A_im, *B_re, *B_im, *C_re, *C_im, *log_dt, *Dskip;
  u16* out;
  unsigned long long* ws;
  char* ex;
};

DEV float ld_bf_uniform(const u16* p, int idx) {
  u32 d = *(const __attribute__((address_space(4))) u32*)(uintptr_t)(p + (idx & ~1));
  return (idx & 1) ? bfhi(d) : bflo(d);
}

DEV const char* prefetch_addr(const Params& P, int cls, int slice, int xcd, int lane) {
  asm volatile("" : "+v"(lane));
  const unsigned long long TAB = 0xB76A54FE93DC2810ull;
  const u32 nib = (u32)(TAB >> (4 * (2 * cls + (lane >> 5)))) & 15u;
  const bool second = (lane >> 4) & 1;
  const u16* base = (nib < 8) ? (second ? P.v : P.k) : (nib < 12 ? P.q : P.u);
  const u32 blk = (nib < 8) ? nib : 2 * (nib & 3) + (second ? 1 : 0);
  const u32 row = blk * 256u + (u32)slice * 8u + ((lane >> 1) & 7);
  return (const char*)base + (row * ROWB + (u32)(((160 * xcd) >> 7) + (lane & 1)) * 128u);
}
DEV void wait_words(lptr p, int lane, int n, u32 v) {
#pragma unroll 1
  for (int spin = 0; spin < (1 << 16); ++spin) {
    u32 x = *(volatile LDSP u32*)(p + 4 * (lane & (n - 1)));
    if (__all(x >= v)) break;
    __builtin_amdgcn_s_sleep(1);
  }
  asm volatile("" ::: "memory");
}
#define PFS 7
DEV void delay(int n) {
  for (int i = 0; i < n; ++i) __builtin_amdgcn_s_sleep(PFS);
}

template <int RS>
DEV u32x2 kv4(lptr sm, u32 ka, u32 va, float w0, float w1, float w2, float w3) {
  float kx[8], vx[4], kv[4];
#pragma unroll
  for (int j = 0; j < 4; ++j) { u32x2 r = trread(sm + ka + RS * j); kx[2 * j] = __uint_as_float(r.x); kx[2 * j + 1] = __uint_as_float(r.y); }
#pragma unroll
  for (int j = 0; j < 2; ++j) { u32x2 r = trread(sm + va + RS * j); vx[2 * j] = __uint_as_float(r.x); vx[2 * j + 1] = __uint_as_float(r.y); }
#pragma unroll
  for (int j = 0; j < 4; ++j)
    kv[j] = vmul(vfma(kx[j + 4], w0, vfma(kx[j + 3], w1, vfma(kx[j + 2], w2, vmul(kx[j + 1], w3)))), vx[j]);
  return u32x2{pkbf(kv[0], kv[1]), pkbf(kv[2], kv[3])};
}
DEV void set_word(lptr p, u32 v) {
  asm volatile("" ::: "memory");
  *(volatile LDSP u32*)p = v;
  asm volatile("" ::: "memory");
}

struct Sched {
  int p[136], d[136], ld[136];
  bool first[136];
  int base[17];
  int ldm[40], ldu[40];
  int ku[16];
  int nld;
};
constexpr Sched make_sched() {
  Sched S{};
  int n = 0, nl = 16;
  for (int m = 0; m < 16; ++m) S.ldm[m] = m;
  for (int d = 0; d < 16; ++d) S.ku[d] = -1;
  for (int s = 0; s < 16; ++s) {
    S.base[s] = n;
    S.ldu[s] = n;
    for (int k = 0; k < 8 && s + k < 16; ++k) { S.p[n] = s + k; S.d[n] = k; S.ld[n] = s; S.first[n] = (s == 0) || (k == 7); ++n; }
    int lo = -1, hi = -1, cnt = 0, half = 0;
    if (s == 6) { lo = 8; hi = 8; cnt = 1; half = 2; }
    else if (s >= 7 && s <= 12) { lo = (s & 1) ? s + 2 : s + 1; hi = lo + 1; cnt = hi - 7; half = (s & 1) ? 0 : 1; }
    else if (s == 13 || s == 14) { lo = 15; hi = 15; cnt = 8; half = (s == 13) ? 0 : 1; }
    if (cnt) {
      const int split = (cnt + 1) / 2;
      const int j0 = (half == 1) ? split : 0, j1 = (half == 0) ? split : cnt;
      for (int j = j0; j < j1; ++j) {
        const int mm = cnt - 1 - j;
        S.ldm[nl] = mm; S.ldu[nl] = n;
        for (int t = hi; t >= lo; --t)
          if (t - mm >= 8) { S.p[n] = t; S.d[n] = t - mm; S.ld[n] = nl; S.first[n] = false; ++n; }
        ++nl;
      }
    }
  }
  S.base[16] = n;
  S.nld = nl;
  for (int u = 0; u < n; ++u) if (S.ku[S.d[u]] < 0) S.ku[S.d[u]] = u;
  return S;
}
constexpr Sched SC = make_sched();
static_assert(SC.base[16] == 136, "schedule");
constexpr int NGAP = 272 + 24, CAP = 2;
enum { A_NONE = 0, A_X, A_K, A_Q, A_PXR, A_PXC, A_EP, A_VM };
struct Acts { int n[NGAP]; int a[NGAP][4]; };
constexpr int place_back(Acts& T, int latest, int code, int cap = CAP) {
  int g = latest;
  while (g > 0 && T.n[g] >= cap) --g;
  T.a[g][T.n[g]++] = code;
  return g;
}
constexpr int place_fwd(Acts& T, int earliest, int code, int cap) {
  int g = earliest;
  while (T.n[g] >= (g < 272 ? cap : 4)) ++g;
  T.a[g][T.n[g]++] = code;
  return g;
}
constexpr Acts make_acts() {
  Acts T{};
  for (int l = 15; l >= 1; --l) {
    int g = 2 * SC.ldu[l] - 5;
    for (int i = 3; i >= 0; --i) g = place_back(T, g - 1, A_X | (l << 4) | (i << 12), 1);
    g = place_back(T, g - 1, A_Q | (l << 4) | (1 << 12), 1);
    g = place_back(T, g - 1, A_Q | (l << 4), 1);
    if (!(l & 1)) {
      g = place_back(T, g - 1, A_PXC | (l << 4), 1);
      place_back(T, g - 6, A_PXR, 1);
    }
  }
  for (int l = SC.nld - 1; l >= 16; --l) {
    int g = 2 * SC.ldu[l] - 5;
    for (int i = 3; i >= 0; --i) g = place_back(T, g - 1, A_X | (l << 4) | (i << 12), SC.ldu[l] >= 100 ? 2 : 1);
  }
  for (int d = 15; d >= 8; --d) {
    int g = 2 * SC.ku[d] - 5;
    g = place_back(T, g - 1, A_K | (d << 4) | (1 << 12), 2);
    g = place_back(T, g - 1, A_K | (d << 4), 2);
  }
  for (int s = 0; s < 16; ++s) {
    int g = 2 * SC.base[s] + 4;
    const int nop = (s & 1) ? 18 : 17;
    for (int e = 0; e < nop; ++e) g = place_fwd(T, g + ((e & 1) && e < 14 ? 0 : 1), A_EP | (s << 4) | (e << 12), s >= 11 ? 3 : CAP);
  }
  return T;
}
constexpr Acts AC = make_acts();

#define MFMA(a, b, c) __builtin_amdgcn_mfma_f32_16x16x32_bf16((a), (b), (c), 0, 0, 0)

__global__ __launch_bounds__(768, 3) void fused_kernel(Params P) {
  extern __shared__ __attribute__((aligned(16))) char smem[];
  const lptr sm = (lptr)smem;
  const u32 smb = (u32)(uintptr_t)sm;
  const int tid = threadIdx.x;
  const int w = __builtin_amdgcn_readfirstlane(tid >> 6);
  const int lane = tid & 63;
  const int wg = blockIdx.x;
  const int xcd = wg & 7, idx = wg >> 3;
  const bool partA = idx < 10;
  const int g = lane >> 4, li = lane & 15, lq = li >> 2, lp = li & 3;
  STAMP(0);
  if (w == 7) {
    unsigned long long pc;
    u32 len;
    asm volatile("s_getpc_b64 %0\n.Lfk_code_begin:\n\ts_mov_b32 %1, .Lfk_end0-.Lfk_code_begin\n\ts_max_i32 %1, %1, .Lfk_end1-.Lfk_code_begin\n\ts_max_i32 %1, %1, .Lfk_end2-.Lfk_code_begin\n\ts_max_i32 %1, %1, .Lfk_end3-.Lfk_code_begin\n\ts_max_i32 %1, %1, .Lfk_end4-.Lfk_code_begin\n\ts_max_i32 %1, %1, .Lfk_end5-.Lfk_code_begin" : "=s"(pc), "=&s"(len) :: "scc");
    const u32 off = min((u32)(lane + 64 * (idx % 3)) * 128u, len & ~3u);
    touch64((const char*)pc + off, smb + PFD);
  }
  if (idx >= 30) {
    if (w < 8) { delay(w); touch64(prefetch_addr(P, w, idx, xcd, lane), smb + PFD); }
    vmcnt<0>();
    asm volatile(".Lfk_end1:");
    __builtin_amdgcn_endpgm();
    __builtin_unreachable();
  }
  if (tid < 64) *(LDSP u32*)(sm + FLAGS + 4 * tid) = 0;

  u32 kb, xb, yw;

  if (partA) {
    const int grp = xcd * 10 + idx;
    const int ch0 = grp * 8;
    if (w >= 10) {
      const int h = w - 10;
      const int srow = 8 * g + 4 * (lp >> 1);
      const int shalf = 8 * (lp & 1);
      const u32 zsel = (lq & 1) ? 0xffffffffu : 0u;
      const u32 ka0 = (lq & 1) ? RAWK + h * 2112 + 16 * (srow + (lq >> 1)) + shalf : (u32)ZROW;
      const u32 va0 = (lq & 1) ? RAWV + h * 2048 + 16 * (srow + (lq >> 1)) + shalf : (u32)ZROW;
      const u32 qa0 = RAWQ + h * 2048 + 16 * (srow + lq) + shalf;
      const u32 xw = XIMG + (lane & 7) * XS + XPADB + 8 * (lane >> 3) + 256 * h;
      const u32 qw = YIMG + (lane & 7) * YS + 8 * (lane >> 3) + 256 * h;
      wgbarrier();
      __builtin_amdgcn_s_setprio(3);
      const u32x2 sw = *(LDSP u32x2*)(sm + SHK + 8 * (lane & 7));
      const float w0 = bflo(sw.x), w1 = bfhi(sw.x), w2 = bflo(sw.y), w3 = bfhi(sw.y);
#pragma unroll 1
      for (int b = 0; b < 8; ++b) {
        wait_words(sm + FLAG2 + 4 * h, lane, 1, (u32)(b + 1));
        const int sl = b & 3;
        u32x2 o[4], qq[4];
#pragma unroll
        for (int u = 0; u < 4; ++u) qq[u] = trread(sm + qa0 + sl * 4096 + u * 512);
#pragma unroll
        for (int u = 0; u < 4; ++u)
          o[u] = kv4<32>(sm, ka0 + ((sl * 4224 + u * 512) & zsel), va0 + ((sl * 4096 + u * 512) & zsel), w0, w1, w2, w3);
#pragma unroll
        for (int u = 0; u < 4; ++u) {
          *(LDSP u32x2*)(sm + qw + 512 * b + 64 * u) = qq[u];
          *(LDSP u32x2*)(sm + xw + 512 * b + 64 * u) = o[u];
        }
        asm volatile("" ::: "memory");
        *(volatile LDSP u32x2*)(sm + FLAGS + 8 * h) = u32x2{(u32)(b + 1), (u32)(b + 1)};
        asm volatile("" ::: "memory");
        if (b == 0) STAMP(12); if (b == 3) STAMP(13); if (b == 7) STAMP(14);
      }
      STAMP_OUT();
      asm volatile(".Lfk_end2:");
      __builtin_amdgcn_endpgm();
      __builtin_unreachable();
    }
    if (w >= 8) {
      const int h = w - 8;
      const u32 rowoff = (u32)(128 * h + lane) * ROWB + (u32)ch0 * 2;
      if (h == 0) {
        if (lane < 4) dma16(P.shift_k, (u32)ch0 * 8 + 16 * lane, smb + SHK);
        if (lane < 4) *(LDSP u32x4*)(sm + RAWK + 16 * lane) = u32x4{0, 0, 0, 0};
        vmcnt<0>();
      }
      const u32 yrd = YIMG + lq * YS + 2 * (128 * h + 16 * g + 4 * lp);
      u16* const outp = P.out + ch0 + (size_t)(128 * h + lane) * (2 * DCH);
      wgbarrier();
      STAMP(1);
      __builtin_amdgcn_s_setprio(2);
#pragma unroll 1
      for (int it = 0; it < 11; ++it) {
        int b = it;
        asm volatile("" : "+s"(b));
        if (b < 8) {
          if (b >= 4) wait_words(sm + FLAGS + 8 * h, lane, 1, (u32)(b - 3));
          const u32 ro = rowoff + (u32)b * (256 * ROWB);
          const int sl = b & 3;
          const u32 kd = smb + RAWK + sl * 4224 + h * 2112;
          const bool first = (b == 0 && h == 0);
          if (lane < 4) dma16(P.k, first ? ro : ro - 4 * ROWB, first ? smb + PFD : kd);
#pragma unroll
          for (int hf = 0; hf < 2; ++hf) {
            dma16(P.k, ro + hf * 64 * ROWB, kd + 64 + hf * 1024);
            dma16(P.v, ro + hf * 64 * ROWB, smb + RAWV + sl * 4096 + h * 2048 + hf * 1024);
            dma16(P.q, ro + hf * 64 * ROWB, smb + RAWQ + sl * 4096 + h * 2048 + hf * 1024);
          }
        }
        if (b == 0) { vmcnt<0>(); set_word(sm + FLAG2 + 4 * h, 1); }
        if (b == 1) {
#pragma unroll
          for (int c = 0; c < 8; ++c)
            dma16(P.ssm_k, (u32)(ch0 + 4 * h + (c >> 1)) * (SEQ * 2) + 2048 + 1024 * (c & 1) + 16 * lane,
                  smb + KIMG + (4 * h + (c >> 1)) * KS + KPADB + 2048 + 1024 * (c & 1));
        }
        if (b >= 2 && b < 9) {
          if (b == 2) vmcnt<15>(); else if (b == 3) vmcnt<7>(); else if (b < 8) vmcnt<9>(); else vmcnt<2>();
          set_word(sm + FLAG2 + 4 * h, (u32)b);
        }
        if (b >= 3) {
          wait_words(sm + FLAGS + 16, lane, 8, (u32)(b - 2));
#pragma unroll
          for (int hf = 0; hf < 2; ++hf) {
            u32x2 a = trread(sm + yrd + 512 * (b - 3) + 128 * hf);
            u32x2 c = trread(sm + yrd + 512 * (b - 3) + 128 * hf + 4 * YS);
            *(u32x4*)(outp + (size_t)((b - 3) * 256 + 64 * hf) * (2 * DCH)) = u32x4{a.x, a.y, c.x, c.y};
          }
        }
      }
      STAMP_OUT();
      asm volatile(".Lfk_end3:");
      __builtin_amdgcn_endpgm();
      __builtin_unreachable();
    }
    const int ch = ch0 + w;
    const u32 kimg = KIMG + w * KS;
#pragma unroll
    for (int j = 0; j < 2; ++j) dma16(P.ssm_k, (u32)ch * (SEQ * 2) + 1024 * j + 16 * lane, smb + kimg + KPADB + 1024 * j);
    if (w < 2) touch64(prefetch_addr(P, w, idx, xcd, lane), smb + PFD);
    if (lane < 10) *(LDSP u32x4*)(sm + XIMG + w * XS + 16 * lane) = u32x4{0, 0, 0, 0};
    if (lane < 6) *(LDSP u32x4*)(sm + kimg + 16 * lane) = u32x4{0, 0, 0, 0};
    const float dsk = ld_bf_uniform(P.ssm_D, ch);
    if (w == 0) vmcnt<0>();
    wgbarrier();
    STAMP(1);
    if (w == 1) vmcnt<1>(); else vmcnt<0>();
    if (lane == 0) {
      LDSP u16* k0 = (LDSP u16*)(sm + kimg + KPADB);
      *k0 = (u16)pkbf(bf2f(*k0) + dsk, 0.f);
    }
    wavefence();
    kb = kimg + 8 * (4 * lp + 8 * g + lq);
    xb = XIMG + w * XS + 224 + 8 * (lp - 8 * g - lq);
    yw = YIMG + w * YS + 2 * lane;
  } else {
    const int bi = xcd * 20 + (idx - 10);
    const int ch0 = bi * 4;
    if (w >= 8) {
      const int h = w - 8;
      if (h == 0) touch64(prefetch_addr(P, 0, idx, xcd, lane), smb + PFD);
      wgbarrier();
      __builtin_amdgcn_s_setprio(3);
      {
        const u32x4 ones = {0x3F803F80u, 0x3F803F80u, 0x3F803F80u, 0x3F803F80u};
#pragma unroll
        for (int i = 0; i < 4; ++i) *(LDSP u32x4*)(sm + B_YIMG + h * YS + 1024 * i + 16 * lane) = ones;
      }
      if (lane < 10) *(LDSP u32x4*)(sm + XIMG + h * XS + 16 * lane) = u32x4{0, 0, 0, 0};
      {
        const int e3 = (idx - 10) * 4 + h;
#pragma unroll 1
        for (int t = 1; t < 8; ++t) {
          delay(1);
          if ((t & 3) == h) touch64(prefetch_addr(P, t, idx, xcd, lane), smb + PFD);
          if (e3 < 60 && t == 2 + (e3 % 6)) touch64(prefetch_addr(P, t, e3 / 6, xcd, lane), smb + PFD);
        }
      }
      const u32 rowoff = (u32)(64 * h + lane) * ROWB + (u32)(ch0 & ~7) * 2;
      dma16(P.u, rowoff, smb + RAWU + 1024 * h);
      dma16(P.u, rowoff + 256 * ROWB, smb + RAWU + 4096 + 1024 * h);
      STAMP(9);
      const u32 ua = RAWU + 1024 * h + 16 * (16 * g + 4 * lp + lq) + 2 * (ch0 & 4);
      const u32 xw = XIMG + (lane & 3) * XS + XPADB + 8 * (lane >> 2) + 128 * h;
      const u32 yrd = B_YIMG + lq * YS + 2 * (64 * h + 16 * g + 4 * lp);
      u16* const outp = P.out + DCH + ch0 + (size_t)(64 * h + lane) * (2 * DCH);
#pragma unroll 1
      for (int it = 0; it < 8; ++it) {
        int i = it;
        asm volatile("" : "+s"(i));
        if (i + 2 < 8) dma16(P.u, rowoff + (u32)(i + 2) * (256 * ROWB), smb + RAWU + 4096 * (i + 2) + 1024 * h);
        if (i < 6) vmcnt<2>(); else if (i == 6) vmcnt<1>(); else vmcnt<0>();
        *(LDSP u32x2*)(sm + xw + 512 * i) = trread(sm + ua + 4096 * i);
        set_word(sm + FLAGS + 4 * h, (u32)(i + 1));
        if (i == 0) STAMP(13); if (i == 7) STAMP(14);
      }
#pragma unroll 1
      for (int i = 0; i < 8; ++i) {
        wait_words(sm + FLAGS + 16, lane, 4, (u32)(i + 1));
        *(u32x2*)(outp + (size_t)i * 256 * (2 * DCH)) = trread(sm + yrd + 512 * i);
      }
      STAMP_OUT();
      asm volatile(".Lfk_end4:");
      __builtin_amdgcn_endpgm();
      __builtin_unreachable();
    }
    const int cw = w & 3, ps = w >> 2;
    const int ch = ch0 + cw;
    const size_t o = (size_t)ch * NST + 64 * ps + lane;
    const u16 i_ar = P.A_re[o], i_ai = P.A_im[o], i_br = P.B_re[o], i_bi = P.B_im[o], i_cr = P.C_re[o], i_ci = P.C_im[o];
    const float ldt = ld_bf_uniform(P.log_dt, ch);
    const float dsk = ld_bf_uniform(P.Dskip, ch);
    wgbarrier();
    const float dt = rbf(expf(ldt));
    const float zr = dt * bf2f(i_ar), zi = dt * bf2f(i_ai);
    const cf dB = {dt * bf2f(i_br), dt * bf2f(i_bi)};
    const cf C = {bf2f(i_cr), bf2f(i_ci)};
    const cf W = cmul(C, dB);
    float mx = fmaxf(fabsf(W.r), fabsf(W.i));
    STAMP(1);
    {
      int m = __float_as_int(mx);
      m = max(m, __builtin_amdgcn_update_dpp(0, m, 0x111, 0xf, 0xf, false));
      m = max(m, __builtin_amdgcn_update_dpp(0, m, 0x112, 0xf, 0xf, false));
      m = max(m, __builtin_amdgcn_update_dpp(0, m, 0x114, 0xf, 0xf, false));
      m = max(m, __builtin_amdgcn_update_dpp(0, m, 0x118, 0xf, 0xf, false));
      int a0 = __builtin_amdgcn_readlane(m, 15), a1 = __builtin_amdgcn_readlane(m, 31);
      int a2 = __builtin_amdgcn_readlane(m, 47), a3 = __builtin_amdgcn_readlane(m, 63);
      mx = __int_as_float(max(max(a0, a1), max(a2, a3)));
    }
    int ex = (int)((__float_as_uint(mx) >> 23) & 0xff);
    ex = min(max(ex, 30), 240);
    const float sY = __uint_as_float((u32)(267 - ex) << 23);
    const float so = __uint_as_float((u32)(ex - 27) << 23);
    const float SX = 16384.f;

    f32x4 ak[3];
    const u32 stg = STG + w * STS;
    const u32 rb = stg + li * 272 + g * 16;
    const u32 m0v = smb + w * STS;
#define STGW(row, val_r, val_i) \
    asm volatile("ds_write_addtid_b32 %0 offset:%1" :: "v"(pkh((val_r), (val_i))), "i"(STG + (row) * 272) : "memory")
    {
      const cf E1 = cexp_pow2(zr, zi, 1.f);
      const cf E8c = cexp_pow2(zr, -zi, 8.f);
      const cf E128 = cexp_pow2(zr, zi, 128.f);
      const cf E1024 = cexp_pow2(zr, zi, 1024.f);
      const cf E2 = cmul(E1, E1);
      const cf E4 = cmul(E2, E2);
      u32 keep;
      asm volatile("s_mov_b32 %0, m0\n\ts_mov_b32 m0, %1\n\ts_nop 0" : "=&s"(keep) : "s"(m0v) : "memory");
      cf y = {W.r * sY, -(W.i * sY)};
      cf P1 = {E128.r * SX, E128.i * SX};
      cf v0 = {SX, 0.f}, v1 = P1;
#pragma unroll
      for (int b = 0; b < 8; ++b) {
        STGW(2 * b, y.r, y.i); y = cmul(y, E8c);
        STGW(16 + b, v0.r, v0.i); if (b < 7) v0 = cmul(v0, E1);
        STGW(2 * b + 1, y.r, y.i); if (b < 7) y = cmul(y, E8c);
        STGW(24 + b, v1.r, v1.i); if (b < 7) v1 = cmul(v1, E1);
      }
      f16x8 yf[4];
      const f32x4 zero = {0.f, 0.f, 0.f, 0.f};
#pragma unroll
      for (int s = 0; s < 4; ++s) {
        yf[s] = *(const LDSP f16x8*)(sm + rb + 64 * s);
        f16x8 xf = *(const LDSP f16x8*)(sm + rb + 16 * 272 + 64 * s);
        ak[0] = __builtin_amdgcn_mfma_f32_16x16x32_f16(xf, yf[s], s ? ak[0] : zero, 0, 0, 0);
      }
      asm volatile("" ::: "memory");
      cf Pa = cmul(P1, E128);
      cf Pb = {E1024.r * SX, E1024.i * SX};
#pragma unroll
      for (int m = 0; m < 2; ++m) {
        cf v = Pa;
#pragma unroll
        for (int r = 0; r < 4; ++r) {
          STGW(4 * m + r, v.r, v.i); if (r < 3) v = cmul(v, E2);
          if (r & 1) { STGW(16 + 2 * m + (r >> 1), Pb.r, Pb.i); Pb = cmul(Pb, E128); }
        }
        Pa = cmul(Pa, E128);
      }
#pragma unroll
      for (int m = 0; m < 4; ++m) {
        STGW(8 + 2 * m, Pa.r, Pa.i);
        cf v = cmul(Pa, E4);
        STGW(20 + m, Pb.r, Pb.i); Pb = cmul(Pb, E128);
        STGW(9 + 2 * m, v.r, v.i);
        if (m < 3) Pa = cmul(Pa, E128);
      }
      STGW(24, Pb.r, Pb.i);
      asm volatile("s_mov_b32 m0, %0" :: "s"(keep) : "memory");
#pragma unroll
      for (int s = 0; s < 4; ++s) {
        f16x8 x1 = *(const LDSP f16x8*)(sm + rb + 64 * s);
        f16x8 x2 = *(const LDSP f16x8*)(sm + rb + 16 * 272 + 64 * s);
        ak[1] = __builtin_amdgcn_mfma_f32_16x16x32_f16(x1, yf[s], s ? ak[1] : zero, 0, 0, 0);
        ak[2] = __builtin_amdgcn_mfma_f32_16x16x32_f16(x2, yf[s], s ? ak[2] : zero, 0, 0, 0);
      }
    }
#undef STGW
#pragma unroll
    for (int i = 0; i < 3; ++i) ak[i] *= so;
    STAMP(2);
    asm volatile("" ::: "memory");
    const u32 kimg = B_KIMG + cw * KS;
    if (ps) {
#pragma unroll
      for (int i = 0; i < 3; ++i) *(LDSP f32x4*)(sm + kimg + 1024 * i + 16 * lane) = ak[i];
      asm volatile("s_waitcnt lgkmcnt(0)" ::: "memory");
      *(volatile LDSP u32*)(sm + FLAGS + 48 + 4 * cw) = 1;
      STAMP_OUT();
      asm volatile(".Lfk_end5:");
      __builtin_amdgcn_endpgm();
      __builtin_unreachable();
    }
    wait_words(sm + FLAGS + 48 + 4 * cw, lane, 1, 1);
    {
      const u32 tb = stg;
#pragma unroll
      for (int i = 0; i < 3; ++i) ak[i] += *(const LDSP f32x4*)(sm + kimg + 1024 * i + 16 * lane);
      wavefence();
      const int b = li, p = g;
      if (lane < 6) *(LDSP u32x4*)(sm + kimg + 16 * lane) = u32x4{0, 0, 0, 0};
      {
        float a0 = ak[0][0] + (lane == 0 ? dsk : 0.f), a1 = ak[0][1], a2 = ak[0][2], a3 = ak[0][3];
        *(LDSP u32x2*)(sm + kimg + KPADB + 2 * (128 * (p >> 1) + 8 * b + 4 * (p & 1))) = u32x2{pkbf(a0, a1), pkbf(a2, a3)};
      }
      {
        float a0 = ak[1][0], a1 = ak[1][1], a2 = ak[1][2], a3 = ak[1][3];
        int i01, i23;
        if (p < 2) { i01 = 64 * p + 4 * b; i23 = i01 + 2; }
        else { i01 = 128 + 64 * (p - 2) + 2 * b; i23 = i01 + 32; }
        *(LDSP u32x2*)(sm + tb + 4 * i01) = u32x2{pkbf(a0, a0), pkbf(a1, a1)};
        *(LDSP u32x2*)(sm + tb + 4 * i23) = u32x2{pkbf(a2, a2), pkbf(a3, a3)};
#pragma unroll
        for (int j = 0; j < 4; ++j) {
          float a = ak[2][j];
          int ii = 256 + 16 * (4 * p + j) + b;
          *(LDSP u32*)(sm + tb + 4 * ii) = pkbf(a, a);
        }
      }
      wavefence();
      const LDSP u32* TB = (const LDSP u32*)(sm + tb);
      {
        const int i0 = 4 * (lane & 31);
        u32 g0 = TB[i0], g1 = TB[i0 + 1], g2 = TB[i0 + 2], g3 = TB[i0 + 3], g4 = TB[i0 + 4];
        u32x4 ov = {g0, (g1 & 0xffffu) | (g2 & 0xffff0000u), g2, (g3 & 0xffffu) | (g4 & 0xffff0000u)};
        *(LDSP u32x4*)(sm + kimg + KPADB + 16 * (32 + (lane & 31))) = ov;
      }
      {
        const int i0 = 128 + 2 * lane;
        u32 g0 = TB[i0], g1 = TB[i0 + 1], g2 = TB[i0 + 2];
        u32x4 ov = {g0, (g0 & 0xffffu) | (g1 & 0xffff0000u), g1, g2};
        *(LDSP u32x4*)(sm + kimg + KPADB + 16 * (64 + lane)) = ov;
      }
#pragma unroll
      for (int hh = 0; hh < 2; ++hh) {
        const int i0 = 256 + 64 * hh + lane;
        u32 g0 = TB[i0], g1 = TB[i0 + 1];
        u32 mid = (lane & 1) ? g1 : ((g0 & 0xffffu) | (g1 & 0xffff0000u));
        u32x4 ov = {g0, g0, mid, g1};
        *(LDSP u32x4*)(sm + kimg + KPADB + 16 * (128 + 64 * hh + lane)) = ov;
      }
    }
    wavefence();
    kb = kimg + 8 * (4 * lp + 8 * g + lq);
    xb = XIMG + cw * XS + 224 + 8 * (lp - 8 * g - lq);
    yw = B_YIMG + cw * YS + 2 * lane;
  }

  {
    const bool l63 = (lane == 63);
    const lptr pxp = sm + FLAGS + 4 * (lane & 3);
    const lptr pyp = sm + FLAGS + 16 + 4 * w;
    u32x2 kr[16][2], xr[40][4];
    f32x4 ac[16][2];
    const f32x4 zero = {0.f, 0.f, 0.f, 0.f};
    float ts[16][2], u2[16][2], u1[16][2], yy[16][2];
    u32 pk[16];
    u32 qv[3][2] = {{0, 0}, {0, 0}, {0, 0}}, px = 0;
    const lptr ywp = sm + yw;
    const u32 qa = (u32)(uintptr_t)ywp;
#define RDQ(s_, i_) asm volatile("ds_read_u16_d16_hi %0, %1 offset:%2" : "+v"(qv[(s_) % 3][i_]) : "v"(qa), "i"((s_) * 256 + 128 * (i_)) : "memory")
#define RDXP(l_, i_) xr[l_][i_] = trread(sm + xb + 256 * SC.ldm[l_] + ((i_) >> 1) * 128 + (((i_) & 1) ? 0 : 32))
#pragma unroll
    for (int d = 0; d < 8; ++d) { kr[d][0] = trread(sm + kb + 256 * d); kr[d][1] = trread(sm + kb + 256 * d + 32); }
    STAMP(3);
    wait_words(sm + FLAGS, lane, 4, 1);
    STAMP(4);
    CSTAMP(0);
    RDQ(0, 0); RDQ(0, 1);
    RDXP(0, 0); RDXP(0, 1); RDXP(0, 2); RDXP(0, 3);
    SCHED0();
    static_for<0, NGAP>([&](auto G_) {
      constexpr int G = decltype(G_)::value;
      if constexpr (G < 272) {
        constexpr int u = G >> 1, par = G & 1;
        constexpr int p = SC.p[u], d = SC.d[u], l = SC.ld[u];
        ac[p][par] = MFMA(frag(kr[d][0], kr[d][1]), frag(xr[l][2 * par], xr[l][2 * par + 1]), SC.first[u] ? zero : ac[p][par]);
        SCHED0();
      }
      static_for<0, AC.n[G]>([&](auto I_) {
        constexpr int code = AC.a[G][decltype(I_)::value];
        constexpr int kind = code & 15, a0 = (code >> 4) & 255, a1 = code >> 12;
        if constexpr (kind == A_X) RDXP(a0, a1);
        if constexpr (kind == A_K) kr[a0][a1] = trread(sm + kb + 256 * a0 + 32 * a1);
        if constexpr (kind == A_Q) RDQ(a0, a1);
        if constexpr (kind == A_PXR) px = *(volatile LDSP u32*)pxp;
        if constexpr (kind == A_VM) vmcnt<0>();
        if constexpr (kind == A_PXC) {
          if (__builtin_expect(!__all(px >= (u32)(a0 / 2 + 1)), 0)) wait_words(sm + FLAGS, lane, 4, (u32)(a0 / 2 + 1));
          asm volatile("" ::: "memory");
        }
        if constexpr (kind == A_EP) {
          constexpr int s = a0, e = a1 >> 1, o = a1 & 1;
          if constexpr (a1 < 14) {
            if constexpr (e == 0) {
              float cur = ac[s][o][3], prev = 0.f;
              if constexpr (o) prev = ac[s][0][3]; else if constexpr (s > 0) prev = ac[s - 1][1][3];
              ts[s][o] = l63 ? prev : cur;
            }
            if constexpr (e == 1) { float a = ac[s][o][2]; u2[s][o] = a + ror1(ts[s][o]); }
            if constexpr (e == 2) {
              float prev = 0.f;
              if constexpr (o) prev = u2[s][0]; else if constexpr (s > 0) prev = u2[s - 1][1];
              ts[s][o] = l63 ? prev : u2[s][o];
            }
            if constexpr (e == 3) { float a = ac[s][o][1]; u1[s][o] = a + ror1(ts[s][o]); }
            if constexpr (e == 4) {
              float prev = 0.f;
              if constexpr (o) prev = u1[s][0]; else if constexpr (s > 0) prev = u1[s - 1][1];
              ts[s][o] = l63 ? prev : u1[s][o];
            }
            if constexpr (e == 5) { float a = ac[s][o][0]; yy[s][o] = a + ror1(ts[s][o]); asm volatile("" : "+v"(yy[s][o])); }
            if constexpr (e == 6) {
              asm volatile("" : "+v"(qv[s % 3][o]));
              yy[s][o] *= __uint_as_float(qv[s % 3][o]);
              asm volatile("" : "+v"(yy[s][o]));
            }
          }
          if constexpr (a1 == 14) pk[s] = pkbf(yy[s][0], yy[s][1]);
          if constexpr (a1 == 15) *(LDSP u16*)(ywp + s * 256) = (u16)pk[s];
          if constexpr (a1 == 16) *(LDSP u16*)(ywp + s * 256 + 128) = (u16)(pk[s] >> 16);
          if constexpr (a1 == 17) {
            asm volatile("" ::: "memory");
            *(volatile LDSP u32*)pyp = (u32)(s / 2 + 1);
            asm volatile("" ::: "memory");
          }
        }
        SCHED0();
      });
    });
  }
  STAMP_OUT();
  asm volatile(".Lfk_end0:");
  __builtin_amdgcn_endpgm();
}

extern "C" void kernel_launch(void* const* d_in, const int* in_sizes, int n_in,
                              void* d_out, int out_size, void* d_ws, size_t ws_size,
                              hipStream_t stream) {
  Params P;
  P.q = (const u16*)d_in[0];
  P.k = (const u16*)d_in[1];
  P.v = (const u16*)d_in[2];
  P.shift_k = (const u16*)d_in[3];
  P.ssm_k = (const u16*)d_in[4];
  P.ssm_D = (const u16*)d_in[5];
  P.u = (const u16*)d_in[6];
  P.A_re = (const u16*)d_in[7];
  P.A_im = (const u16*)d_in[8];
  P.B_re = (const u16*)d_in[9];
  P.B_im = (const u16*)d_in[10];
  P.C_re = (const u16*)d_in[11];
  P.C_im = (const u16*)d_in[12];
  P.log_dt = (const u16*)d_in[13];
  P.Dskip = (const u16*)d_in[14];
  P.out = (u16*)d_out;
  P.ws = (unsigned long long*)d_ws;
  P.ex = (char*)d_ws + (16 << 20);
  (void)hipFuncSetAttribute((const void*)fused_kernel, hipFuncAttributeMaxDynamicSharedMemorySize, LDS_BYTES);
  fused_kernel<<<256, 768, LDS_BYTES, stream>>>(P);
}
```

```cpp
#include <hip/hip_runtime.h>
#include <hip/hip_bf16.h>
#include <stdint.h>

typedef __attribute__((ext_vector_type(4))) short s16x4;
typedef __attribute__((ext_vector_type(8))) _Float16 f16x8;
typedef __attribute__((ext_vector_type(8))) __bf16 bf16x8;
typedef __attribute__((ext_vector_type(2))) __bf16 bf16x2;
typedef __attribute__((ext_vector_type(2))) _Float16 f16x2;
typedef __attribute__((ext_vector_type(2))) float f32x2;
typedef __attribute__((ext_vector_type(4))) float f32x4;
typedef __attribute__((ext_vector_type(2))) unsigned int u32x2;
typedef __attribute__((ext_vector_type(4))) unsigned int u32x4;
typedef unsigned int u32;
typedef unsigned short u16;
#define LDSP __attribute__((address_space(3)))
#define DEV __device__ __forceinline__

constexpr int SEQ = 2048;
constexpr int DCH = 640;
constexpr int NST = 128;
constexpr int ROWB = DCH * 2;

constexpr int XS = 4272;
constexpr int XPADB = 160;
constexpr int YS = 4160;
constexpr int KS = 4224;
constexpr int KPADB = 96;
constexpr int FLAGS = 0;
constexpr int ZROW = 64;
constexpr int FLAG2 = 192;
constexpr int PFD = 256;
constexpr int SHK = 512;
constexpr int XIMG = 1024;
constexpr int YIMG = 35200;
constexpr int KIMG = 68480;
constexpr int RAWK = 102272;
constexpr int RAWV = 119680;
constexpr int RAWQ = 136064;
constexpr int STS = 8704;
constexpr int STG = 18176;
constexpr int B_KIMG = 87808;
constexpr int B_YIMG = 104704;
constexpr int RAWU = 121344;
constexpr int PRAWK = RAWU + 16384;
constexpr int PRAWV = PRAWK + 8704;
constexpr int PRAWQ = PRAWV + 8192;
constexpr int PSTG = XIMG + 2208;
constexpr int LDS_BYTES = 154112;

constexpr size_t WS_EPOCH = 0;
constexpr size_t WS_FLAG = 4096;
constexpr size_t WS_EXX = 1 << 20;
constexpr size_t WS_EXQ = 3 << 20;
constexpr size_t WS_STAMP = 8 << 20;

DEV float bf2f(u16 v) { return __uint_as_float(((u32)v) << 16); }
DEV float bflo(u32 v) { return __uint_as_float(v << 16); }
DEV float bfhi(u32 v) { return __uint_as_float(v & 0xffff0000u); }
DEV u32 pkbf(float a, float b) {
  f32x2 t = {a, b};
  bf16x2 r = __builtin_convertvector(t, bf16x2);
  return __builtin_bit_cast(u32, r);
}
DEV u32 pkh(float a, float b) {
  f32x2 t = {a, b};
  f16x2 r = __builtin_convertvector(t, f16x2);
  return __builtin_bit_cast(u32, r);
}
DEV float rbf(float a) { return bflo(pkbf(a, a)); }
DEV float dot2(u32 a, u32 b, float c) {
  return __builtin_amdgcn_fdot2_f32_bf16(__builtin_bit_cast(bf16x2, a), __builtin_bit_cast(bf16x2, b), c, false);
}
DEV float vfma(float a, float b, float c) { float d; asm("v_fma_f32 %0, %1, %2, %3" : "=v"(d) : "v"(a), "v"(b), "v"(c)); return d; }
DEV float vmul(float a, float b) { float d; asm("v_mul_f32 %0, %1, %2" : "=v"(d) : "v"(a), "v"(b)); return d; }
typedef __attribute__((address_space(1))) unsigned long long gu64;
#define RLX_AGENT __ATOMIC_RELAXED, __HIP_MEMORY_SCOPE_AGENT
typedef LDSP char* lptr;
DEV u32x2 trread(lptr p) {
  s16x4 r = __builtin_amdgcn_ds_read_tr16_b64_v4i16((LDSP s16x4*)p);
  return __builtin_bit_cast(u32x2, r);
}
DEV bf16x8 frag(u32x2 a, u32x2 b) {
  u32x4 f = {a.x, a.y, b.x, b.y};
  return __builtin_bit_cast(bf16x8, f);
}
DEV float ror1(float v) {
  int r = __builtin_amdgcn_update_dpp(0, __float_as_int(v), 0x13C, 0xf, 0xf, false);
  return __int_as_float(r);
}
DEV void wavefence() { __builtin_amdgcn_fence(__ATOMIC_ACQ_REL, "wavefront"); __builtin_amdgcn_wave_barrier(); }
DEV void wgbarrier() {
  asm volatile("s_waitcnt lgkmcnt(0)" ::: "memory");
  __builtin_amdgcn_s_barrier();
  asm volatile("" ::: "memory");
}
DEV void dma16(const void* base, u32 voff, u32 ldsaddr) {
  u32 keep;
  ldsaddr = __builtin_amdgcn_readfirstlane(ldsaddr);
  const unsigned long long b64 = (unsigned long long)(uintptr_t)base;
  const unsigned long long sb = ((unsigned long long)(u32)__builtin_amdgcn_readfirstlane((u32)(b64 >> 32)) << 32) |
                                (u32)__builtin_amdgcn_readfirstlane((u32)b64);
  asm volatile("s_mov_b32 %0, m0\n\ts_mov_b32 m0, %3\n\ts_nop 2\n\tglobal_load_lds_dwordx4 %1, %2\n\ts_mov_b32 m0, %0"
               : "=&s"(keep) : "v"(voff), "s"(sb), "s"(ldsaddr) : "memory");
}
DEV void touch64(const void* p, u32 ldsaddr) {
  u32 keep;
  asm volatile("s_mov_b32 %0, m0\n\ts_mov_b32 m0, %2\n\ts_nop 0\n\tglobal_load_lds_dword %1, off\n\ts_mov_b32 m0, %0"
               : "=&s"(keep) : "v"(p), "s"(ldsaddr) : "memory");
}
template <int N> DEV void vmcnt() { asm volatile("s_waitcnt vmcnt(%0)" :: "i"(N) : "memory"); }
#define SCHED0() __builtin_amdgcn_sched_barrier(0)

template <int V> struct IC { static constexpr int value = V; };
template <int I, int N, class F> DEV void static_for(F&& f) {
  if constexpr (I < N) { f(IC<I>{}); static_for<I + 1, N>(f); }
}

struct cf { float r, i; };
DEV cf cmul(cf a, cf b) {
  cf o;
  o.r = fmaf(a.r, b.r, -(a.i * b.i));
  o.i = fmaf(a.r, b.i, a.i * b.r);
  return o;
}
DEV cf cexp_pow2(float zr, float zi, float x) {
  float mag = __builtin_amdgcn_exp2f((zr * x) * 1.44269504088896340736f);
  float th = zi * x;
  const float C_HI = 0.15915494f;
  const float C_LO = 6.4206382e-09f;
  float f = fmaf(th, C_LO, fmaf(th, C_HI, -rintf(th * C_HI)));
  cf o;
  o.r = mag * __builtin_amdgcn_cosf(f);
  o.i = mag * __builtin_amdgcn_sinf(f);
  return o;
}

#define STAMP(n)
#define CSTAMP(n)
#define STAMP_OUT()

struct Params {
  const u16 *q, *k, *v, *shift_k, *ssm_k, *ssm_D;
  const u16 *u, *A_re, *A_im, *B_re, *B_im, *C_re, *C_im, *log_dt, *Dskip;
  u16* out;
  unsigned long long* ws;
  char* ex;
};

DEV float ld_bf_uniform(const u16* p, int idx) {
  u32 d = *(const __attribute__((address_space(4))) u32*)(uintptr_t)(p + (idx & ~1));
  return (idx & 1) ? bfhi(d) : bflo(d);
}

DEV const char* prefetch_addr(const Params& P, int cls, int slice, int xcd, int lane) {
  asm volatile("" : "+v"(lane));
  const unsigned long long TAB = 0xB76A54FE93DC2810ull;
  const u32 nib = (u32)(TAB >> (4 * (2 * cls + (lane >> 5)))) & 15u;
  const bool second = (lane >> 4) & 1;
  const u16* base = (nib < 8) ? (second ? P.v : P.k) : (nib < 12 ? P.q : P.u);
  const u32 blk = (nib < 8) ? nib : 2 * (nib & 3) + (second ? 1 : 0);
  const u32 row = blk * 256u + (u32)slice * 8u + ((lane >> 1) & 7);
  return (const char*)base + (row * ROWB + (u32)(((160 * xcd) >> 7) + (lane & 1)) * 128u);
}
DEV void wait_words(lptr p, int lane, int n, u32 v) {
#pragma unroll 1
  for (int spin = 0; spin < (1 << 16); ++spin) {
    u32 x = *(volatile LDSP u32*)(p + 4 * (lane & (n - 1)));
    if (__all(x >= v)) break;
    __builtin_amdgcn_s_sleep(1);
  }
  asm volatile("" ::: "memory");
}
#define PFS 7
DEV void delay(int n) {
  for (int i = 0; i < n; ++i) __builtin_amdgcn_s_sleep(PFS);
}

template <int RS>
DEV u32x2 kv4(lptr sm, u32 ka, u32 va, float w0, float w1, float w2, float w3) {
  float kx[8], vx[4], kv[4];
#pragma unroll
  for (int j = 0; j < 4; ++j) { u32x2 r = trread(sm + ka + RS * j); kx[2 * j] = __uint_as_float(r.x); kx[2 * j + 1] = __uint_as_float(r.y); }
#pragma unroll
  for (int j = 0; j < 2; ++j) { u32x2 r = trread(sm + va + RS * j); vx[2 * j] = __uint_as_float(r.x); vx[2 * j + 1] = __uint_as_float(r.y); }
#pragma unroll
  for (int j = 0; j < 4; ++j)
    kv[j] = vmul(vfma(kx[j + 4], w0, vfma(kx[j + 3], w1, vfma(kx[j + 2], w2, vmul(kx[j + 1], w3)))), vx[j]);
  return u32x2{pkbf(kv[0], kv[1]), pkbf(kv[2], kv[3])};
}
DEV void set_word(lptr p, u32 v) {
  asm volatile("" ::: "memory");
  *(volatile LDSP u32*)p = v;
  asm volatile("" ::: "memory");
}

struct Sched {
  int p[136], d[136], ld[136];
  bool first[136];
  int base[17];
  int ldm[40], ldu[40];
  int ku[16];
  int nld;
};
constexpr Sched make_sched() {
  Sched S{};
  int n = 0, nl = 16;
  for (int m = 0; m < 16; ++m) S.ldm[m] = m;
  for (int d = 0; d < 16; ++d) S.ku[d] = -1;
  for (int s = 0; s < 16; ++s) {
    S.base[s] = n;
    S.ldu[s] = n;
    for (int k = 0; k < 8 && s + k < 16; ++k) { S.p[n] = s + k; S.d[n] = k; S.ld[n] = s; S.first[n] = (s == 0) || (k == 7); ++n; }
    int lo = -1, hi = -1, cnt = 0, half = 0;
    if (s == 6) { lo = 8; hi = 8; cnt = 1; half = 2; }
    else if (s >= 7 && s <= 12) { lo = (s & 1) ? s + 2 : s + 1; hi = lo + 1; cnt = hi - 7; half = (s & 1) ? 0 : 1; }
    else if (s == 13 || s == 14) { lo = 15; hi = 15; cnt = 8; half = (s == 13) ? 0 : 1; }
    if (cnt) {
      const int split = (cnt + 1) / 2;
      const int j0 = (half == 1) ? split : 0, j1 = (half == 0) ? split : cnt;
      for (int j = j0; j < j1; ++j) {
        const int mm = cnt - 1 - j;
        S.ldm[nl] = mm; S.ldu[nl] = n;
        for (int t = hi; t >= lo; --t)
          if (t - mm >= 8) { S.p[n] = t; S.d[n] = t - mm; S.ld[n] = nl; S.first[n] = false; ++n; }
        ++nl;
      }
    }
  }
  S.base[16] = n;
  S.nld = nl;
  for (int u = 0; u < n; ++u) if (S.ku[S.d[u]] < 0) S.ku[S.d[u]] = u;
  return S;
}
constexpr Sched SC = make_sched();
static_assert(SC.base[16] == 136, "schedule");
constexpr int NGAP = 272 + 24, CAP = 2;
enum { A_NONE = 0, A_X, A_K, A_Q, A_PXR, A_PXC, A_EP, A_DMA, A_RR };
struct Acts { int n[NGAP]; int a[NGAP][4]; };
constexpr int place_back(Acts& T, int latest, int code, int cap = CAP) {
  int g = latest;
  while (g > 0 && T.n[g] >= cap) --g;
  T.a[g][T.n[g]++] = code;
  return g;
}
constexpr int place_fwd(Acts& T, int earliest, int code, int cap) {
  int g = earliest;
  while (T.n[g] >= (g < 272 ? cap : 4)) ++g;
  T.a[g][T.n[g]++] = code;
  return g;
}
constexpr Acts make_acts() {
  Acts T{};
  for (int l = 15; l >= 1; --l) {
    int g = 2 * SC.ldu[l] - 5;
    for (int i = 3; i >= 0; --i) g = place_back(T, g - 1, A_X | (l << 4) | (i << 12), 1);
    g = place_back(T, g - 1, A_Q | (l << 4) | (1 << 12), 1);
    g = place_back(T, g - 1, A_Q | (l << 4), 1);
    if (!(l & 1)) {
      g = place_back(T, g - 1, A_PXC | (l << 4), 1);
      place_back(T, g - 6, A_PXR, 1);
    }
  }
  for (int l = SC.nld - 1; l >= 16; --l) {
    int g = 2 * SC.ldu[l] - 5;
    for (int i = 3; i >= 0; --i) g = place_back(T, g - 1, A_X | (l << 4) | (i << 12), SC.ldu[l] >= 100 ? 2 : 1);
  }
  for (int d = 15; d >= 8; --d) {
    int g = 2 * SC.ku[d] - 5;
    g = place_back(T, g - 1, A_K | (d << 4) | (1 << 12), 2);
    g = place_back(T, g - 1, A_K | (d << 4), 2);
  }
  place_fwd(T, 6, A_RR | (1 << 4), 3);
  for (int p = 2; p < 4; ++p) {
    const int g = 2 * SC.base[4 * p - 6];
    place_fwd(T, g + 1, A_DMA | (p << 4), 3);
    place_fwd(T, g + 13, A_DMA | (p << 4) | (1 << 12), 3);
    place_fwd(T, g + 40, A_RR | (p << 4), 3);
  }
  for (int s = 0; s < 16; ++s) {
    int g = 2 * SC.base[s] + 4;
    const int nop = (s & 1) ? 18 : 17;
    for (int e = 0; e < nop; ++e) g = place_fwd(T, g + ((e & 1) && e < 14 ? 0 : 1), A_EP | (s << 4) | (e << 12), s >= 11 ? 3 : CAP);
  }
  return T;
}
constexpr Acts AC = make_acts();

#define MFMA(a, b, c) __builtin_amdgcn_mfma_f32_16x16x32_bf16((a), (b), (c), 0, 0, 0)

__global__ __launch_bounds__(768, 3) void fused_kernel(Params P) {
  extern __shared__ __attribute__((aligned(16))) char smem[];
  const lptr sm = (lptr)smem;
  const u32 smb = (u32)(uintptr_t)sm;
  const int tid = threadIdx.x;
  const int w = __builtin_amdgcn_readfirstlane(tid >> 6);
  const int lane = tid & 63;
  const int wg = blockIdx.x;
  const int xcd = wg & 7, idx = wg >> 3;
  const bool partA = idx < 10;
  const int g = lane >> 4, li = lane & 15, lq = li >> 2, lp = li & 3;
  STAMP(0);
  if (w == 7) {
    unsigned long long pc;
    u32 len;
    asm volatile("s_getpc_b64 %0\n.Lfk_code_begin:\n\ts_mov_b32 %1, .Lfk_end0-.Lfk_code_begin\n\ts_max_i32 %1, %1, .Lfk_end1-.Lfk_code_begin\n\ts_max_i32 %1, %1, .Lfk_end2-.Lfk_code_begin\n\ts_max_i32 %1, %1, .Lfk_end3-.Lfk_code_begin\n\ts_max_i32 %1, %1, .Lfk_end4-.Lfk_code_begin\n\ts_max_i32 %1, %1, .Lfk_end5-.Lfk_code_begin" : "=s"(pc), "=&s"(len) :: "scc");
    const u32 off = min((u32)(lane + 64 * (idx % 3)) * 128u, len & ~3u);
    touch64((const char*)pc + off, smb + PFD);
  }
  if (idx >= 30) {
    if (w < 8) { delay(w); touch64(prefetch_addr(P, w, idx, xcd, lane), smb + PFD); }
    vmcnt<0>();
    asm volatile(".Lfk_end1:");
    __builtin_amdgcn_endpgm();
    __builtin_unreachable();
  }
  if (tid < 64) *(LDSP u32*)(sm + FLAGS + 4 * tid) = 0;

  u32 kb, xb, yw;
  const int ldw = partA ? (w >> 2) : -1;
  auto load_pair = [&](int p) {
    int ln = lane;
    asm volatile("" : "+v"(ln));
    const u32 ro = (u32)(512 * p + 64 * w + ln) * ROWB + (u32)(xcd * 10 + idx) * 16;
    const int ck = (p & 1) * 8 + w;
    dma16(P.k, ro, smb + RAWK + ck * 1088 + 64);
    dma16(P.v, ro, smb + RAWV + ck * 1024);
    const bool first = (p == 0 && w == 0);
    if (ln < 4) dma16(P.k, first ? ro : ro - 4 * ROWB, first ? smb + PFD : smb + RAWK + ck * 1088);
    dma16(P.q, ro, smb + RAWQ + ck * 1024);
  };

  if (partA) {
    const int grp = xcd * 10 + idx;
    const int ch0 = grp * 8;
    if (w >= 8) {
      const int h = w - 8;
      if (h == 0) {
        if (lane < 4) dma16(P.shift_k, (u32)ch0 * 8 + 16 * lane, smb + SHK);
        if (lane < 4) *(LDSP u32x4*)(sm + RAWK + 16 * lane) = u32x4{0, 0, 0, 0};
        vmcnt<0>();
      }
      const int srow = 8 * g + 4 * (lp >> 1);
      const int shalf = 8 * (lp & 1);
      const u32 zsel = (lq & 1) ? 0xffffffffu : 0u;
      const u32 ka0 = (lq & 1) ? RAWK + h * 1088 + 16 * (srow + (lq >> 1)) + shalf : (u32)ZROW;
      const u32 va0 = (lq & 1) ? RAWV + h * 1024 + 16 * (srow + (lq >> 1)) + shalf : (u32)ZROW;
      const u32 qa0 = RAWQ + h * 1024 + 16 * (srow + lq) + shalf;
      const u32 xw = XIMG + (lane & 7) * XS + XPADB + 8 * (lane >> 3) + 128 * h;
      const u32 qw = YIMG + (lane & 7) * YS + 8 * (lane >> 3) + 128 * h;
      const u32 yrd = YIMG + lq * YS + 2 * (64 * h + 16 * g + 4 * lp);
      u16* const outp = P.out + ch0 + (size_t)(64 * h + lane) * (2 * DCH);
      wgbarrier();
      __builtin_amdgcn_s_setprio(3);
      const u32x2 sw = *(LDSP u32x2*)(sm + SHK + 8 * (lane & 7));
      const float w0 = bflo(sw.x), w1 = bfhi(sw.x), w2 = bflo(sw.y), w3 = bfhi(sw.y);
#pragma unroll 1
      for (int it = 0; it < 11; ++it) {
        int b = it;
        asm volatile("" : "+s"(b));
        if (b < 8) {
          wait_words(sm + FLAG2 + 4 * (4 * (b & 1) + h), lane, 1, (u32)((b >> 1) + 1));
          const int sl = b & 3;
          u32x2 o[2], qq[2];
#pragma unroll
          for (int u = 0; u < 2; ++u) qq[u] = trread(sm + qa0 + sl * 4096 + u * 512);
#pragma unroll
          for (int u = 0; u < 2; ++u)
            o[u] = kv4<32>(sm, ka0 + ((sl * 4352 + u * 512) & zsel), va0 + ((sl * 4096 + u * 512) & zsel), w0, w1, w2, w3);
#pragma unroll
          for (int u = 0; u < 2; ++u) {
            *(LDSP u32x2*)(sm + qw + 512 * b + 64 * u) = qq[u];
            *(LDSP u32x2*)(sm + xw + 512 * b + 64 * u) = o[u];
          }
          set_word(sm + FLAGS + 4 * h, (u32)(b + 1));
          if (b == 0) STAMP(12); if (b == 3) STAMP(13); if (b == 7) STAMP(14);
        }
        if (b >= 3) {
          wait_words(sm + FLAGS + 16, lane, 8, (u32)(b - 2));
          u32x2 a = trread(sm + yrd + 512 * (b - 3));
          u32x2 c = trread(sm + yrd + 512 * (b - 3) + 4 * YS);
          *(u32x4*)(outp + (size_t)(b - 3) * 256 * (2 * DCH)) = u32x4{a.x, a.y, c.x, c.y};
        }
      }
      STAMP_OUT();
      asm volatile(".Lfk_end2:\n.Lfk_end3:");
      __builtin_amdgcn_endpgm();
      __builtin_unreachable();
    }
    const int ch = ch0 + w;
    const u32 kimg = KIMG + w * KS;
#pragma unroll
    for (int j = 0; j < 2; ++j) dma16(P.ssm_k, (u32)ch * (SEQ * 2) + 1024 * j + 16 * lane, smb + kimg + KPADB + 1024 * j);
    if (w < 2) touch64(prefetch_addr(P, w, idx, xcd, lane), smb + PFD);
    if (lane < 10) *(LDSP u32x4*)(sm + XIMG + w * XS + 16 * lane) = u32x4{0, 0, 0, 0};
    if (lane < 6) *(LDSP u32x4*)(sm + kimg + 16 * lane) = u32x4{0, 0, 0, 0};
    const float dsk = ld_bf_uniform(P.ssm_D, ch);
    if (w == 0) vmcnt<0>();
    wgbarrier();
    STAMP(1);
    load_pair(0);
    vmcnt<0>();
    set_word(sm + FLAG2 + 4 * w, 1);
    STAMP(2);
    load_pair(1);
#pragma unroll
    for (int j = 2; j < 4; ++j) dma16(P.ssm_k, (u32)ch * (SEQ * 2) + 1024 * j + 16 * lane, smb + kimg + KPADB + 1024 * j);
    if (lane == 0) {
      LDSP u16* k0 = (LDSP u16*)(sm + kimg + KPADB);
      *k0 = (u16)pkbf(bf2f(*k0) + dsk, 0.f);
    }
    wavefence();
    kb = kimg + 8 * (4 * lp + 8 * g + lq);
    xb = XIMG + w * XS + 224 + 8 * (lp - 8 * g - lq);
    yw = YIMG + w * YS + 2 * lane;
  } else {
    const int bi = xcd * 20 + (idx - 10);
    const int ch0 = bi * 4;
    if (w >= 8) {
      const int h = w - 8;
      if (h == 0) touch64(prefetch_addr(P, 0, idx, xcd, lane), smb + PFD);
      wgbarrier();
      __builtin_amdgcn_s_setprio(3);
      {
        const u32x4 ones = {0x3F803F80u, 0x3F803F80u, 0x3F803F80u, 0x3F803F80u};
#pragma unroll
        for (int i = 0; i < 4; ++i) *(LDSP u32x4*)(sm + B_YIMG + h * YS + 1024 * i + 16 * lane) = ones;
      }
      if (lane < 10) *(LDSP u32x4*)(sm + XIMG + h * XS + 16 * lane) = u32x4{0, 0, 0, 0};
      {
        const int e3 = (idx - 10) * 4 + h;
#pragma unroll 1
        for (int t = 1; t < 8; ++t) {
          delay(1);
          if ((t & 3) == h) touch64(prefetch_addr(P, t, idx, xcd, lane), smb + PFD);
          if (e3 < 60 && t == 2 + (e3 % 6)) touch64(prefetch_addr(P, t, e3 / 6, xcd, lane), smb + PFD);
        }
      }
      const u32 rowoff = (u32)(64 * h + lane) * ROWB + (u32)(ch0 & ~7) * 2;
      dma16(P.u, rowoff, smb + RAWU + 1024 * h);
      dma16(P.u, rowoff + 256 * ROWB, smb + RAWU + 4096 + 1024 * h);
      STAMP(9);
      const u32 ua = RAWU + 1024 * h + 16 * (16 * g + 4 * lp + lq) + 2 * (ch0 & 4);
      const u32 xw = XIMG + (lane & 3) * XS + XPADB + 8 * (lane >> 2) + 128 * h;
      const u32 yrd = B_YIMG + lq * YS + 2 * (64 * h + 16 * g + 4 * lp);
      u16* const outp = P.out + DCH + ch0 + (size_t)(64 * h + lane) * (2 * DCH);
#pragma unroll 1
      for (int it = 0; it < 8; ++it) {
        int i = it;
        asm volatile("" : "+s"(i));
        if (i + 2 < 8) dma16(P.u, rowoff + (u32)(i + 2) * (256 * ROWB), smb + RAWU + 4096 * (i + 2) + 1024 * h);
        if (i < 6) vmcnt<2>(); else if (i == 6) vmcnt<1>(); else vmcnt<0>();
        *(LDSP u32x2*)(sm + xw + 512 * i) = trread(sm + ua + 4096 * i);
        set_word(sm + FLAGS + 4 * h, (u32)(i + 1));
        if (i == 0) STAMP(13); if (i == 7) STAMP(14);
      }
#pragma unroll 1
      for (int i = 0; i < 8; ++i) {
        wait_words(sm + FLAGS + 16, lane, 4, (u32)(i + 1));
        *(u32x2*)(outp + (size_t)i * 256 * (2 * DCH)) = trread(sm + yrd + 512 * i);
      }
      STAMP_OUT();
      asm volatile(".Lfk_end4:");
      __builtin_amdgcn_endpgm();
      __builtin_unreachable();
    }
    const int cw = w & 3, ps = w >> 2;
    const int ch = ch0 + cw;
    const size_t o = (size_t)ch * NST + 64 * ps + lane;
    const u16 i_ar = P.A_re[o], i_ai = P.A_im[o], i_br = P.B_re[o], i_bi = P.B_im[o], i_cr = P.C_re[o], i_ci = P.C_im[o];
    const float ldt = ld_bf_uniform(P.log_dt, ch);
    const float dsk = ld_bf_uniform(P.Dskip, ch);
    wgbarrier();
    const float dt = rbf(expf(ldt));
    const float zr = dt * bf2f(i_ar), zi = dt * bf2f(i_ai);
    const cf dB = {dt * bf2f(i_br), dt * bf2f(i_bi)};
    const cf C = {bf2f(i_cr), bf2f(i_ci)};
    const cf W = cmul(C, dB);
    float mx = fmaxf(fabsf(W.r), fabsf(W.i));
    STAMP(1);
    {
      int m = __float_as_int(mx);
      m = max(m, __builtin_amdgcn_update_dpp(0, m, 0x111, 0xf, 0xf, false));
      m = max(m, __builtin_amdgcn_update_dpp(0, m, 0x112, 0xf, 0xf, false));
      m = max(m, __builtin_amdgcn_update_dpp(0, m, 0x114, 0xf, 0xf, false));
      m = max(m, __builtin_amdgcn_update_dpp(0, m, 0x118, 0xf, 0xf, false));
      int a0 = __builtin_amdgcn_readlane(m, 15), a1 = __builtin_amdgcn_readlane(m, 31);
      int a2 = __builtin_amdgcn_readlane(m, 47), a3 = __builtin_amdgcn_readlane(m, 63);
      mx = __int_as_float(max(max(a0, a1), max(a2, a3)));
    }
    int ex = (int)((__float_as_uint(mx) >> 23) & 0xff);
    ex = min(max(ex, 30), 240);
    const float sY = __uint_as_float((u32)(267 - ex) << 23);
    const float so = __uint_as_float((u32)(ex - 27) << 23);
    const float SX = 16384.f;

    f32x4 ak[3];
    const u32 stg = STG + w * STS;
    const u32 rb = stg + li * 272 + g * 16;
    const u32 m0v = smb + w * STS;
#define STGW(row, val_r, val_i) \
    asm volatile("ds_write_addtid_b32 %0 offset:%1" :: "v"(pkh((val_r), (val_i))), "i"(STG + (row) * 272) : "memory")
    {
      const cf E1 = cexp_pow2(zr, zi, 1.f);
      const cf E8c = cexp_pow2(zr, -zi, 8.f);
      const cf E128 = cexp_pow2(zr, zi, 128.f);
      const cf E1024 = cexp_pow2(zr, zi, 1024.f);
      const cf E2 = cmul(E1, E1);
      const cf E4 = cmul(E2, E2);
      u32 keep;
      asm volatile("s_mov_b32 %0, m0\n\ts_mov_b32 m0, %1\n\ts_nop 0" : "=&s"(keep) : "s"(m0v) : "memory");
      cf y = {W.r * sY, -(W.i * sY)};
      cf P1 = {E128.r * SX, E128.i * SX};
      cf v0 = {SX, 0.f}, v1 = P1;
#pragma unroll
      for (int b = 0; b < 8; ++b) {
        STGW(2 * b, y.r, y.i); y = cmul(y, E8c);
        STGW(16 + b, v0.r, v0.i); if (b < 7) v0 = cmul(v0, E1);
        STGW(2 * b + 1, y.r, y.i); if (b < 7) y = cmul(y, E8c);
        STGW(24 + b, v1.r, v1.i); if (b < 7) v1 = cmul(v1, E1);
      }
      f16x8 yf[4];
      const f32x4 zero = {0.f, 0.f, 0.f, 0.f};
#pragma unroll
      for (int s = 0; s < 4; ++s) {
        yf[s] = *(const LDSP f16x8*)(sm + rb + 64 * s);
        f16x8 xf = *(const LDSP f16x8*)(sm + rb + 16 * 272 + 64 * s);
        ak[0] = __builtin_amdgcn_mfma_f32_16x16x32_f16(xf, yf[s], s ? ak[0] : zero, 0, 0, 0);
      }
      asm volatile("" ::: "memory");
      cf Pa = cmul(P1, E128);
      cf Pb = {E1024.r * SX, E1024.i * SX};
#pragma unroll
      for (int m = 0; m < 2; ++m) {
        cf v = Pa;
#pragma unroll
        for (int r = 0; r < 4; ++r) {
          STGW(4 * m + r, v.r, v.i); if (r < 3) v = cmul(v, E2);
          if (r & 1) { STGW(16 + 2 * m + (r >> 1), Pb.r, Pb.i); Pb = cmul(Pb, E128); }
        }
        Pa = cmul(Pa, E128);
      }
#pragma unroll
      for (int m = 0; m < 4; ++m) {
        STGW(8 + 2 * m, Pa.r, Pa.i);
        cf v = cmul(Pa, E4);
        STGW(20 + m, Pb.r, Pb.i); Pb = cmul(Pb, E128);
        STGW(9 + 2 * m, v.r, v.i);
        if (m < 3) Pa = cmul(Pa, E128);
      }
      STGW(24, Pb.r, Pb.i);
      asm volatile("s_mov_b32 m0, %0" :: "s"(keep) : "memory");
#pragma unroll
      for (int s = 0; s < 4; ++s) {
        f16x8 x1 = *(const LDSP f16x8*)(sm + rb + 64 * s);
        f16x8 x2 = *(const LDSP f16x8*)(sm + rb + 16 * 272 + 64 * s);
        ak[1] = __builtin_amdgcn_mfma_f32_16x16x32_f16(x1, yf[s], s ? ak[1] : zero, 0, 0, 0);
        ak[2] = __builtin_amdgcn_mfma_f32_16x16x32_f16(x2, yf[s], s ? ak[2] : zero, 0, 0, 0);
      }
    }
#undef STGW
#pragma unroll
    for (int i = 0; i < 3; ++i) ak[i] *= so;
    STAMP(2);
    asm volatile("" ::: "memory");
    const u32 kimg = B_KIMG + cw * KS;
    if (ps) {
#pragma unroll
      for (int i = 0; i < 3; ++i) *(LDSP f32x4*)(sm + kimg + 1024 * i + 16 * lane) = ak[i];
      asm volatile("s_waitcnt lgkmcnt(0)" ::: "memory");
      *(volatile LDSP u32*)(sm + FLAGS + 48 + 4 * cw) = 1;
      STAMP_OUT();
      asm volatile(".Lfk_end5:");
      __builtin_amdgcn_endpgm();
      __builtin_unreachable();
    }
    wait_words(sm + FLAGS + 48 + 4 * cw, lane, 1, 1);
    {
      const u32 tb = stg;
#pragma unroll
      for (int i = 0; i < 3; ++i) ak[i] += *(const LDSP f32x4*)(sm + kimg + 1024 * i + 16 * lane);
      wavefence();
      const int b = li, p = g;
      if (lane < 6) *(LDSP u32x4*)(sm + kimg + 16 * lane) = u32x4{0, 0, 0, 0};
      {
        float a0 = ak[0][0] + (lane == 0 ? dsk : 0.f), a1 = ak[0][1], a2 = ak[0][2], a3 = ak[0][3];
        *(LDSP u32x2*)(sm + kimg + KPADB + 2 * (128 * (p >> 1) + 8 * b + 4 * (p & 1))) = u32x2{pkbf(a0, a1), pkbf(a2, a3)};
      }
      {
        float a0 = ak[1][0], a1 = ak[1][1], a2 = ak[1][2], a3 = ak[1][3];
        int i01, i23;
        if (p < 2) { i01 = 64 * p + 4 * b; i23 = i01 + 2; }
        else { i01 = 128 + 64 * (p - 2) + 2 * b; i23 = i01 + 32; }
        *(LDSP u32x2*)(sm + tb + 4 * i01) = u32x2{pkbf(a0, a0), pkbf(a1, a1)};
        *(LDSP u32x2*)(sm + tb + 4 * i23) = u32x2{pkbf(a2, a2), pkbf(a3, a3)};
#pragma unroll
        for (int j = 0; j < 4; ++j) {
          float a = ak[2][j];
          int ii = 256 + 16 * (4 * p + j) + b;
          *(LDSP u32*)(sm + tb + 4 * ii) = pkbf(a, a);
        }
      }
      wavefence();
      const LDSP u32* TB = (const LDSP u32*)(sm + tb);
      {
        const int i0 = 4 * (lane & 31);
        u32 g0 = TB[i0], g1 = TB[i0 + 1], g2 = TB[i0 + 2], g3 = TB[i0 + 3], g4 = TB[i0 + 4];
        u32x4 ov = {g0, (g1 & 0xffffu) | (g2 & 0xffff0000u), g2, (g3 & 0xffffu) | (g4 & 0xffff0000u)};
        *(LDSP u32x4*)(sm + kimg + KPADB + 16 * (32 + (lane & 31))) = ov;
      }
      {
        const int i0 = 128 + 2 * lane;
        u32 g0 = TB[i0], g1 = TB[i0 + 1], g2 = TB[i0 + 2];
        u32x4 ov = {g0, (g0 & 0xffffu) | (g1 & 0xffff0000u), g1, g2};
        *(LDSP u32x4*)(sm + kimg + KPADB + 16 * (64 + lane)) = ov;
      }
#pragma unroll
      for (int hh = 0; hh < 2; ++hh) {
        const int i0 = 256 + 64 * hh + lane;
        u32 g0 = TB[i0], g1 = TB[i0 + 1];
        u32 mid = (lane & 1) ? g1 : ((g0 & 0xffffu) | (g1 & 0xffff0000u));
        u32x4 ov = {g0, g0, mid, g1};
        *(LDSP u32x4*)(sm + kimg + KPADB + 16 * (128 + 64 * hh + lane)) = ov;
      }
    }
    wavefence();
    kb = kimg + 8 * (4 * lp + 8 * g + lq);
    xb = XIMG + cw * XS + 224 + 8 * (lp - 8 * g - lq);
    yw = B_YIMG + cw * YS + 2 * lane;
  }

  {
    const bool l63 = (lane == 63);
    const lptr pxp = sm + FLAGS + 4 * (lane & 3);
    const lptr pyp = sm + FLAGS + 16 + 4 * w;
    u32x2 kr[16][2], xr[40][4];
    f32x4 ac[16][2];
    const f32x4 zero = {0.f, 0.f, 0.f, 0.f};
    float ts[16][2], u2[16][2], u1[16][2], yy[16][2];
    u32 pk[16];
    u32 qv[3][2] = {{0, 0}, {0, 0}, {0, 0}}, px = 0;
    const lptr ywp = sm + yw;
    const u32 qa = (u32)(uintptr_t)ywp;
#define RDQ(s_, i_) asm volatile("ds_read_u16_d16_hi %0, %1 offset:%2" : "+v"(qv[(s_) % 3][i_]) : "v"(qa), "i"((s_) * 256 + 128 * (i_)) : "memory")
#define RDXP(l_, i_) xr[l_][i_] = trread(sm + xb + 256 * SC.ldm[l_] + ((i_) >> 1) * 128 + (((i_) & 1) ? 0 : 32))
#pragma unroll
    for (int d = 0; d < 8; ++d) { kr[d][0] = trread(sm + kb + 256 * d); kr[d][1] = trread(sm + kb + 256 * d + 32); }
    STAMP(3);
    wait_words(sm + FLAGS, lane, 4, 1);
    STAMP(4);
    CSTAMP(0);
    RDQ(0, 0); RDQ(0, 1);
    RDXP(0, 0); RDXP(0, 1); RDXP(0, 2); RDXP(0, 3);
    SCHED0();
    static_for<0, NGAP>([&](auto G_) {
      constexpr int G = decltype(G_)::value;
      if constexpr (G < 272) {
        constexpr int u = G >> 1, par = G & 1;
        constexpr int p = SC.p[u], d = SC.d[u], l = SC.ld[u];
        ac[p][par] = MFMA(frag(kr[d][0], kr[d][1]), frag(xr[l][2 * par], xr[l][2 * par + 1]), SC.first[u] ? zero : ac[p][par]);
        SCHED0();
      }
      static_for<0, AC.n[G]>([&](auto I_) {
        constexpr int code = AC.a[G][decltype(I_)::value];
        constexpr int kind = code & 15, a0 = (code >> 4) & 255, a1 = code >> 12;
        if constexpr (kind == A_X) RDXP(a0, a1);
        if constexpr (kind == A_K) kr[a0][a1] = trread(sm + kb + 256 * a0 + 32 * a1);
        if constexpr (kind == A_Q) RDQ(a0, a1);
        if constexpr (kind == A_PXR) px = *(volatile LDSP u32*)pxp;
        if constexpr (kind == A_DMA) { if (ldw == a1) load_pair(a0); }
        if constexpr (kind == A_RR) { if (ldw >= 0) { vmcnt<0>(); set_word(sm + FLAG2 + 4 * w, (u32)(a0 + 1)); } }
        if constexpr (kind == A_PXC) {
          if (__builtin_expect(!__all(px >= (u32)(a0 / 2 + 1)), 0)) wait_words(sm + FLAGS, lane, 4, (u32)(a0 / 2 + 1));
          asm volatile("" ::: "memory");
        }
        if constexpr (kind == A_EP) {
          constexpr int s = a0, e = a1 >> 1, o = a1 & 1;
          if constexpr (a1 < 14) {
            if constexpr (e == 0) {
              float cur = ac[s][o][3], prev = 0.f;
              if constexpr (o) prev = ac[s][0][3]; else if constexpr (s > 0) prev = ac[s - 1][1][3];
              ts[s][o] = l63 ? prev : cur;
            }
            if constexpr (e == 1) { float a = ac[s][o][2]; u2[s][o] = a + ror1(ts[s][o]); }
            if constexpr (e == 2) {
              float prev = 0.f;
              if constexpr (o) prev = u2[s][0]; else if constexpr (s > 0) prev = u2[s - 1][1];
              ts[s][o] = l63 ? prev : u2[s][o];
            }
            if constexpr (e == 3) { float a = ac[s][o][1]; u1[s][o] = a + ror1(ts[s][o]); }
            if constexpr (e == 4) {
              float prev = 0.f;
              if constexpr (o) prev = u1[s][0]; else if constexpr (s > 0) prev = u1[s - 1][1];
              ts[s][o] = l63 ? prev : u1[s][o];
            }
            if constexpr (e == 5) { float a = ac[s][o][0]; yy[s][o] = a + ror1(ts[s][o]); asm volatile("" : "+v"(yy[s][o])); }
            if constexpr (e == 6) {
              asm volatile("" : "+v"(qv[s % 3][o]));
              yy[s][o] *= __uint_as_float(qv[s % 3][o]);
              asm volatile("" : "+v"(yy[s][o]));
            }
          }
          if constexpr (a1 == 14) pk[s] = pkbf(yy[s][0], yy[s][1]);
          if constexpr (a1 == 15) *(LDSP u16*)(ywp + s * 256) = (u16)pk[s];
          if constexpr (a1 == 16) *(LDSP u16*)(ywp + s * 256 + 128) = (u16)(pk[s] >> 16);
          if constexpr (a1 == 17) {
            asm volatile("" ::: "memory");
            *(volatile LDSP u32*)pyp = (u32)(s / 2 + 1);
            asm volatile("" ::: "memory");
          }
        }
        SCHED0();
      });
    });
  }
  STAMP_OUT();
  asm volatile(".Lfk_end0:");
  __builtin_amdgcn_endpgm();
}

extern "C" void kernel_launch(void* const* d_in, const int* in_sizes, int n_in,
                              void* d_out, int out_size, void* d_ws, size_t ws_size,
                              hipStream_t stream) {
  Params P;
  P.q = (const u16*)d_in[0];
  P.k = (const u16*)d_in[1];
  P.v = (const u16*)d_in[2];
  P.shift_k = (const u16*)d_in[3];
  P.ssm_k = (const u16*)d_in[4];
  P.ssm_D = (const u16*)d_in[5];
  P.u = (const u16*)d_in[6];
  P.A_re = (const u16*)d_in[7];
  P.A_im = (const u16*)d_in[8];
  P.B_re = (const u16*)d_in[9];
  P.B_im = (const u16*)d_in[10];
  P.C_re = (const u16*)d_in[11];
  P.C_im = (const u16*)d_in[12];
  P.log_dt = (const u16*)d_in[13];
  P.Dskip = (const u16*)d_in[14];
  P.out = (u16*)d_out;
  P.ws = (unsigned long long*)d_ws;
  P.ex = (char*)d_ws + (16 << 20);
  (void)hipFuncSetAttribute((const void*)fused_kernel, hipFuncAttributeMaxDynamicSharedMemorySize, LDS_BYTES);
  fused_kernel<<<256, 768, LDS_BYTES, stream>>>(P);
}
```

```cpp
#include <hip/hip_runtime.h>
#include <hip/hip_bf16.h>
#include <stdint.h>

typedef __attribute__((ext_vector_type(4))) short s16x4;
typedef __attribute__((ext_vector_type(8))) _Float16 f16x8;
typedef __attribute__((ext_vector_type(8))) __bf16 bf16x8;
typedef __attribute__((ext_vector_type(2))) __bf16 bf16x2;
typedef __attribute__((ext_vector_type(2))) _Float16 f16x2;
typedef __attribute__((ext_vector_type(2))) float f32x2;
typedef __attribute__((ext_vector_type(4))) float f32x4;
typedef __attribute__((ext_vector_type(2))) unsigned int u32x2;
typedef __attribute__((ext_vector_type(4))) unsigned int u32x4;
typedef unsigned int u32;
typedef unsigned short u16;
#define LDSP __attribute__((address_space(3)))
#define DEV __device__ __forceinline__

constexpr int SEQ = 2048;
constexpr int DCH = 640;
constexpr int NST = 128;
constexpr int ROWB = DCH * 2;

constexpr int XS = 4272;
constexpr int XPADB = 160;
constexpr int YS = 4160;
constexpr int KS = 4224;
constexpr int KPADB = 96;
constexpr int FLAGS = 0;
constexpr int ZROW = 64;
constexpr int FLAG2 = 192;
constexpr int PFD = 256;
constexpr int SHK = 512;
constexpr int XIMG = 1024;
constexpr int YIMG = 35200;
constexpr int KIMG = 68480;
constexpr int RAWK = 102272;
constexpr int RAWV = 119680;
constexpr int RAWQ = 136064;
constexpr int STS = 8704;
constexpr int STG = 18176;
constexpr int B_KIMG = 87808;
constexpr int B_YIMG = 104704;
constexpr int RAWU = 121344;
constexpr int PRAWK = RAWU + 16384;
constexpr int PRAWV = PRAWK + 8704;
constexpr int PRAWQ = PRAWV + 8192;
constexpr int PSTG = XIMG + 2208;
constexpr int LDS_BYTES = 162816;

constexpr size_t WS_EPOCH = 0;
constexpr size_t WS_FLAG = 4096;
constexpr size_t WS_EXX = 1 << 20;
constexpr size_t WS_EXQ = 3 << 20;
constexpr size_t WS_STAMP = 8 << 20;

DEV float bf2f(u16 v) { return __uint_as_float(((u32)v) << 16); }
DEV float bflo(u32 v) { return __uint_as_float(v << 16); }
DEV float bfhi(u32 v) { return __uint_as_float(v & 0xffff0000u); }
DEV u32 pkbf(float a, float b) {
  f32x2 t = {a, b};
  bf16x2 r = __builtin_convertvector(t, bf16x2);
  return __builtin_bit_cast(u32, r);
}
DEV u32 pkh(float a, float b) {
  f32x2 t = {a, b};
  f16x2 r = __builtin_convertvector(t, f16x2);
  return __builtin_bit_cast(u32, r);
}
DEV float rbf(float a) { return bflo(pkbf(a, a)); }
DEV float dot2(u32 a, u32 b, float c) {
  return __builtin_amdgcn_fdot2_f32_bf16(__builtin_bit_cast(bf16x2, a), __builtin_bit_cast(bf16x2, b), c, false);
}
DEV float vfma(float a, float b, float c) { float d; asm("v_fma_f32 %0, %1, %2, %3" : "=v"(d) : "v"(a), "v"(b), "v"(c)); return d; }
DEV float vmul(float a, float b) { float d; asm("v_mul_f32 %0, %1, %2" : "=v"(d) : "v"(a), "v"(b)); return d; }
typedef __attribute__((address_space(1))) unsigned long long gu64;
#define RLX_AGENT __ATOMIC_RELAXED, __HIP_MEMORY_SCOPE_AGENT
typedef LDSP char* lptr;
DEV u32x2 trread(lptr p) {
  s16x4 r = __builtin_amdgcn_ds_read_tr16_b64_v4i16((LDSP s16x4*)p);
  return __builtin_bit_cast(u32x2, r);
}
DEV bf16x8 frag(u32x2 a, u32x2 b) {
  u32x4 f = {a.x, a.y, b.x, b.y};
  return __builtin_bit_cast(bf16x8, f);
}
DEV float ror1(float v) {
  int r = __builtin_amdgcn_update_dpp(0, __float_as_int(v), 0x13C, 0xf, 0xf, false);
  return __int_as_float(r);
}
DEV void wavefence() { __builtin_amdgcn_fence(__ATOMIC_ACQ_REL, "wavefront"); __builtin_amdgcn_wave_barrier(); }
DEV void wgbarrier() {
  asm volatile("s_waitcnt lgkmcnt(0)" ::: "memory");
  __builtin_amdgcn_s_barrier();
  asm volatile("" ::: "memory");
}
DEV void dma16(const void* base, u32 voff, u32 ldsaddr) {
  u32 keep;
  ldsaddr = __builtin_amdgcn_readfirstlane(ldsaddr);
  const unsigned long long b64 = (unsigned long long)(uintptr_t)base;
  const unsigned long long sb = ((unsigned long long)(u32)__builtin_amdgcn_readfirstlane((u32)(b64 >> 32)) << 32) |
                                (u32)__builtin_amdgcn_readfirstlane((u32)b64);
  asm volatile("s_mov_b32 %0, m0\n\ts_mov_b32 m0, %3\n\ts_nop 2\n\tglobal_load_lds_dwordx4 %1, %2\n\ts_mov_b32 m0, %0"
               : "=&s"(keep) : "v"(voff), "s"(sb), "s"(ldsaddr) : "memory");
}
DEV void touch64(const void* p, u32 ldsaddr) {
  u32 keep;
  asm volatile("s_mov_b32 %0, m0\n\ts_mov_b32 m0, %2\n\ts_nop 0\n\tglobal_load_lds_dword %1, off\n\ts_mov_b32 m0, %0"
               : "=&s"(keep) : "v"(p), "s"(ldsaddr) : "memory");
}
template <int N> DEV void vmcnt() { asm volatile("s_waitcnt vmcnt(%0)" :: "i"(N) : "memory"); }
#define SCHED0() __builtin_amdgcn_sched_barrier(0)

template <int V> struct IC { static constexpr int value = V; };
template <int I, int N, class F> DEV void static_for(F&& f) {
  if constexpr (I < N) { f(IC<I>{}); static_for<I + 1, N>(f); }
}

struct cf { float r, i; };
DEV cf cmul(cf a, cf b) {
  cf o;
  o.r = fmaf(a.r, b.r, -(a.i * b.i));
  o.i = fmaf(a.r, b.i, a.i * b.r);
  return o;
}
DEV cf cexp_pow2(float zr, float zi, float x) {
  float mag = __builtin_amdgcn_exp2f((zr * x) * 1.44269504088896340736f);
  float th = zi * x;
  const float C_HI = 0.15915494f;
  const float C_LO = 6.4206382e-09f;
  float f = fmaf(th, C_LO, fmaf(th, C_HI, -rintf(th * C_HI)));
  cf o;
  o.r = mag * __builtin_amdgcn_cosf(f);
  o.i = mag * __builtin_amdgcn_sinf(f);
  return o;
}

#define STAMP(n)
#define CSTAMP(n)
#define STAMP_OUT()

struct Params {
  const u16 *q, *k, *v, *shift_k, *ssm_k, *ssm_D;
  const u16 *u, *A_re, *A_im, *B_re, *B_im, *C_re, *C_im, *log_dt, *Dskip;
  u16* out;
  unsigned long long* ws;
  char* ex;
};

DEV float ld_bf_uniform(const u16* p, int idx) {
  u32 d = *(const __attribute__((address_space(4))) u32*)(uintptr_t)(p + (idx & ~1));
  return (idx & 1) ? bfhi(d) : bflo(d);
}

DEV const char* prefetch_addr(const Params& P, int cls, int slice, int xcd, int lane) {
  asm volatile("" : "+v"(lane));
  const unsigned long long TAB = 0xFEDC932810ull;
  const u32 nib = (u32)(TAB >> (4 * (2 * cls + (lane >> 5)))) & 15u;
  const bool second = (lane >> 4) & 1;
  const u16* base = (nib < 8) ? (second ? P.v : P.k) : (nib < 12 ? P.q : P.u);
  const u32 blk = (nib < 8) ? nib : 2 * (nib & 3) + (second ? 1 : 0);
  const u32 row = blk * 256u + (u32)slice * 8u + ((lane >> 1) & 7);
  return (const char*)base + (row * ROWB + (u32)(((160 * xcd) >> 7) + (lane & 1)) * 128u);
}
DEV void wait_words(lptr p, int lane, int n, u32 v) {
#pragma unroll 1
  for (int spin = 0; spin < (1 << 16); ++spin) {
    u32 x = *(volatile LDSP u32*)(p + 4 * (lane & (n - 1)));
    if (__all(x >= v)) break;
    __builtin_amdgcn_s_sleep(1);
  }
  asm volatile("" ::: "memory");
}
#define PFS 7
DEV void delay(int n) {
  for (int i = 0; i < n; ++i) __builtin_amdgcn_s_sleep(PFS);
}

template <int RS>
DEV u32x2 kv4(lptr sm, u32 ka, u32 va, float w0, float w1, float w2, float w3) {
  float kx[8], vx[4], kv[4];
#pragma unroll
  for (int j = 0; j < 4; ++j) { u32x2 r = trread(sm + ka + RS * j); kx[2 * j] = __uint_as_float(r.x); kx[2 * j + 1] = __uint_as_float(r.y); }
#pragma unroll
  for (int j = 0; j < 2; ++j) { u32x2 r = trread(sm + va + RS * j); vx[2 * j] = __uint_as_float(r.x); vx[2 * j + 1] = __uint_as_float(r.y); }
#pragma unroll
  for (int j = 0; j < 4; ++j)
    kv[j] = vmul(vfma(kx[j + 4], w0, vfma(kx[j + 3], w1, vfma(kx[j + 2], w2, vmul(kx[j + 1], w3)))), vx[j]);
  return u32x2{pkbf(kv[0], kv[1]), pkbf(kv[2], kv[3])};
}
DEV void set_word(lptr p, u32 v) {
  asm volatile("" ::: "memory");
  *(volatile LDSP u32*)p = v;
  asm volatile("" ::: "memory");
}

struct Sched {
  int p[136], d[136], ld[136];
  bool first[136];
  int base[17];
  int ldm[40], ldu[40];
  int ku[16];
  int nld;
};
constexpr Sched make_sched() {
  Sched S{};
  int n = 0, nl = 16;
  for (int m = 0; m < 16; ++m) S.ldm[m] = m;
  for (int d = 0; d < 16; ++d) S.ku[d] = -1;
  for (int s = 0; s < 16; ++s) {
    S.base[s] = n;
    S.ldu[s] = n;
    for (int k = 0; k < 8 && s + k < 16; ++k) { S.p[n] = s + k; S.d[n] = k; S.ld[n] = s; S.first[n] = (s == 0) || (k == 7); ++n; }
    int lo = -1, hi = -1, cnt = 0, half = 0;
    if (s == 6) { lo = 8; hi = 8; cnt = 1; half = 2; }
    else if (s >= 7 && s <= 12) { lo = (s & 1) ? s + 2 : s + 1; hi = lo + 1; cnt = hi - 7; half = (s & 1) ? 0 : 1; }
    else if (s == 13 || s == 14) { lo = 15; hi = 15; cnt = 8; half = (s == 13) ? 0 : 1; }
    if (cnt) {
      const int split = (cnt + 1) / 2;
      const int j0 = (half == 1) ? split : 0, j1 = (half == 0) ? split : cnt;
      for (int j = j0; j < j1; ++j) {
        const int mm = cnt - 1 - j;
        S.ldm[nl] = mm; S.ldu[nl] = n;
        for (int t = hi; t >= lo; --t)
          if (t - mm >= 8) { S.p[n] = t; S.d[n] = t - mm; S.ld[n] = nl; S.first[n] = false; ++n; }
        ++nl;
      }
    }
  }
  S.base[16] = n;
  S.nld = nl;
  for (int u = 0; u < n; ++u) if (S.ku[S.d[u]] < 0) S.ku[S.d[u]] = u;
  return S;
}
constexpr Sched SC = make_sched();
static_assert(SC.base[16] == 136, "schedule");
constexpr int NGAP = 272 + 24, CAP = 2;
enum { A_NONE = 0, A_X, A_K, A_Q, A_PXR, A_PXC, A_EP, A_DMA, A_RR };
struct Acts { int n[NGAP]; int a[NGAP][4]; };
constexpr int place_back(Acts& T, int latest, int code, int cap = CAP) {
  int g = latest;
  while (g > 0 && T.n[g] >= cap) --g;
  T.a[g][T.n[g]++] = code;
  return g;
}
constexpr int place_fwd(Acts& T, int earliest, int code, int cap) {
  int g = earliest;
  while (T.n[g] >= (g < 272 ? cap : 4)) ++g;
  T.a[g][T.n[g]++] = code;
  return g;
}
constexpr Acts make_acts() {
  Acts T{};
  for (int l = 15; l >= 1; --l) {
    int g = 2 * SC.ldu[l] - 5;
    for (int i = 3; i >= 0; --i) g = place_back(T, g - 1, A_X | (l << 4) | (i << 12), 1);
    g = place_back(T, g - 1, A_Q | (l << 4) | (1 << 12), 1);
    g = place_back(T, g - 1, A_Q | (l << 4), 1);
    if (!(l & 1)) {
      g = place_back(T, g - 1, A_PXC | (l << 4), 1);
      place_back(T, g - 6, A_PXR, 1);
    }
  }
  for (int l = SC.nld - 1; l >= 16; --l) {
    int g = 2 * SC.ldu[l] - 5;
    for (int i = 3; i >= 0; --i) g = place_back(T, g - 1, A_X | (l << 4) | (i << 12), SC.ldu[l] >= 100 ? 2 : 1);
  }
  for (int d = 15; d >= 8; --d) {
    int g = 2 * SC.ku[d] - 5;
    g = place_back(T, g - 1, A_K | (d << 4) | (1 << 12), 2);
    g = place_back(T, g - 1, A_K | (d << 4), 2);
  }
  place_fwd(T, 6, A_RR | (1 << 4), 3);
  for (int s = 0; s < 16; ++s) {
    int g = 2 * SC.base[s] + 4;
    const int nop = (s & 1) ? 18 : 17;
    for (int e = 0; e < nop; ++e) g = place_fwd(T, g + ((e & 1) && e < 14 ? 0 : 1), A_EP | (s << 4) | (e << 12), s >= 11 ? 3 : CAP);
  }
  return T;
}
constexpr Acts AC = make_acts();

#define MFMA(a, b, c) __builtin_amdgcn_mfma_f32_16x16x32_bf16((a), (b), (c), 0, 0, 0)

__global__ __launch_bounds__(768, 3) void fused_kernel(Params P) {
  extern __shared__ __attribute__((aligned(16))) char smem[];
  const lptr sm = (lptr)smem;
  const u32 smb = (u32)(uintptr_t)sm;
  const int tid = threadIdx.x;
  const int w = __builtin_amdgcn_readfirstlane(tid >> 6);
  const int lane = tid & 63;
  const int wg = blockIdx.x;
  const int xcd = wg & 7, idx = wg >> 3;
  const bool partA = idx < 10;
  const int g = lane >> 4, li = lane & 15, lq = li >> 2, lp = li & 3;
  STAMP(0);
  if (w == 7) {
    unsigned long long pc;
    u32 len;
    asm volatile("s_getpc_b64 %0\n.Lfk_code_begin:\n\ts_mov_b32 %1, .Lfk_end0-.Lfk_code_begin\n\ts_max_i32 %1, %1, .Lfk_end1-.Lfk_code_begin\n\ts_max_i32 %1, %1, .Lfk_end2-.Lfk_code_begin\n\ts_max_i32 %1, %1, .Lfk_end3-.Lfk_code_begin\n\ts_max_i32 %1, %1, .Lfk_end4-.Lfk_code_begin\n\ts_max_i32 %1, %1, .Lfk_end5-.Lfk_code_begin" : "=s"(pc), "=&s"(len) :: "scc");
    const u32 off = min((u32)(lane + 64 * (idx % 3)) * 128u, len & ~3u);
    touch64((const char*)pc + off, smb + PFD);
  }
  gu64* const gflag = (gu64*)(uintptr_t)(P.ex + WS_FLAG);
  gu64* const gepoch = (gu64*)(uintptr_t)(P.ex + WS_EPOCH);
  if (idx >= 30) {
    if (w < 5) { delay(w); touch64(prefetch_addr(P, w, idx, xcd, lane), smb + PFD); }
    if (w == 8 && wg == 240) {
      const unsigned long long tag = __hip_atomic_load(gepoch, RLX_AGENT) + 1;
#pragma unroll 1
      for (int spin = 0; spin < (1 << 16); ++spin) {
        bool ok = true;
#pragma unroll
        for (int j = 0; j < 4; ++j) { const int f = min(lane + 64 * j, 239); ok &= (__hip_atomic_load(gflag + f, RLX_AGENT) == tag); }
        if (__all(ok)) break;
        __builtin_amdgcn_s_sleep(16);
      }
      if (lane == 0) __hip_atomic_store(gepoch, tag, RLX_AGENT);
    }
    vmcnt<0>();
    asm volatile(".Lfk_end1:");
    __builtin_amdgcn_endpgm();
    __builtin_unreachable();
  }
  if (tid < 64) *(LDSP u32*)(sm + FLAGS + 4 * tid) = 0;

  u32 kb, xb, yw;
  const int ldw = partA ? (w >> 2) : -1;
  auto load_pair = [&](int p) {
    int ln = lane;
    asm volatile("" : "+v"(ln));
    const u32 ro = (u32)(512 * p + 64 * w + ln) * ROWB + (u32)(xcd * 10 + idx) * 16;
    const int ck = (p & 1) * 8 + w;
    dma16(P.k, ro, smb + RAWK + ck * 1088 + 64);
    dma16(P.v, ro, smb + RAWV + ck * 1024);
    const bool first = (p == 0 && w == 0);
    if (ln < 4) dma16(P.k, first ? ro : ro - 4 * ROWB, first ? smb + PFD : smb + RAWK + ck * 1088);
    dma16(P.q, ro, smb + RAWQ + ck * 1024);
  };

  if (partA) {
    const int grp = xcd * 10 + idx;
    const int ch0 = grp * 8;
    if (w >= 8) {
      const int h = w - 8;
      const unsigned long long tag = __hip_atomic_load(gepoch, RLX_AGENT) + 1;
      asm volatile("" :: "v"(tag) : "memory");
      if (h == 0) {
        if (lane < 4) dma16(P.shift_k, (u32)ch0 * 8 + 16 * lane, smb + SHK);
        if (lane < 4) *(LDSP u32x4*)(sm + RAWK + 16 * lane) = u32x4{0, 0, 0, 0};
        vmcnt<0>();
      }
      const int srow = 8 * g + 4 * (lp >> 1);
      const int shalf = 8 * (lp & 1);
      const u32 zsel = (lq & 1) ? 0xffffffffu : 0u;
      const u32 ka0 = (lq & 1) ? RAWK + h * 1088 + 16 * (srow + (lq >> 1)) + shalf : (u32)ZROW;
      const u32 va0 = (lq & 1) ? RAWV + h * 1024 + 16 * (srow + (lq >> 1)) + shalf : (u32)ZROW;
      const u32 qa0 = RAWQ + h * 1024 + 16 * (srow + lq) + shalf;
      const u32 xw = XIMG + (lane & 7) * XS + XPADB + 8 * (lane >> 3) + 128 * h;
      const u32 qw = YIMG + (lane & 7) * YS + 8 * (lane >> 3) + 128 * h;
      const u32 yrd = YIMG + lq * YS + 2 * (64 * h + 16 * g + 4 * lp);
      u16* const outp = P.out + ch0 + (size_t)(64 * h + lane) * (2 * DCH);
      wgbarrier();
      if (h == 0 && lane == 0) __hip_atomic_store(gflag + 160 + grp, tag, RLX_AGENT);
      __builtin_amdgcn_s_setprio(3);
      const u32x2 sw = *(LDSP u32x2*)(sm + SHK + 8 * (lane & 7));
      const float w0 = bflo(sw.x), w1 = bfhi(sw.x), w2 = bflo(sw.y), w3 = bfhi(sw.y);
#pragma unroll 1
      for (int b = 0; b < 4; ++b) {
        wait_words(sm + FLAG2 + 4 * (4 * (b & 1) + h), lane, 1, (u32)((b >> 1) + 1));
        u32x2 o[2], qq[2];
#pragma unroll
        for (int u = 0; u < 2; ++u) qq[u] = trread(sm + qa0 + b * 4096 + u * 512);
#pragma unroll
        for (int u = 0; u < 2; ++u)
          o[u] = kv4<32>(sm, ka0 + ((b * 4352 + u * 512) & zsel), va0 + ((b * 4096 + u * 512) & zsel), w0, w1, w2, w3);
#pragma unroll
        for (int u = 0; u < 2; ++u) {
          *(LDSP u32x2*)(sm + qw + 512 * b + 64 * u) = qq[u];
          *(LDSP u32x2*)(sm + xw + 512 * b + 64 * u) = o[u];
        }
        set_word(sm + FLAGS + 4 * h, (u32)(b + 1));
        if (b == 0) STAMP(12); if (b == 3) STAMP(13);
      }
#pragma unroll 1
      for (int spin = 0; spin < (1 << 16); ++spin) {
        if (__all(__hip_atomic_load(gflag + (grp >> 3) * 16 + (lane & 15), RLX_AGENT) == tag)) break;
        __builtin_amdgcn_s_sleep(4);
      }
      __builtin_amdgcn_fence(__ATOMIC_ACQUIRE, "wavefront");
      STAMP(9);
      {
        const auto rx = __builtin_amdgcn_make_buffer_rsrc(P.ex + WS_EXX, 0, 4u << 20, 0x00020000);
        u32x4 d[8];
#pragma unroll
        for (int j = 0; j < 8; ++j)
          d[j] = __builtin_amdgcn_raw_buffer_load_b128(rx, (u32)(ch0 + 2 * h + (j & 1)) * 2048 + 1024 * (j >> 2) + 16 * lane + ((j >> 1) & 1) * (u32)(WS_EXQ - WS_EXX), 0, 16);
#pragma unroll
        for (int j = 0; j < 8; ++j) {
          const int cl = 2 * h + (j & 1);
          const u32 da = ((j >> 1) & 1) ? YIMG + cl * YS : XIMG + cl * XS + XPADB;
          *(LDSP u32x4*)(sm + da + 2048 + 1024 * (j >> 2) + 16 * lane) = d[j];
          if (j == 3) set_word(sm + FLAGS + 4 * h, 6);
        }
        set_word(sm + FLAGS + 4 * h, 8);
      }
      STAMP(14);
#pragma unroll 1
      for (int b = 0; b < 8; ++b) {
        wait_words(sm + FLAGS + 16, lane, 8, (u32)(b + 1));
        u32x2 a = trread(sm + yrd + 512 * b);
        u32x2 c = trread(sm + yrd + 512 * b + 4 * YS);
        *(u32x4*)(outp + (size_t)b * 256 * (2 * DCH)) = u32x4{a.x, a.y, c.x, c.y};
      }
      STAMP_OUT();
      asm volatile(".Lfk_end2:\n.Lfk_end3:");
      __builtin_amdgcn_endpgm();
      __builtin_unreachable();
    }
    const int ch = ch0 + w;
    const u32 kimg = KIMG + w * KS;
#pragma unroll
    for (int j = 0; j < 2; ++j) dma16(P.ssm_k, (u32)ch * (SEQ * 2) + 1024 * j + 16 * lane, smb + kimg + KPADB + 1024 * j);
    if (w < 3) touch64(prefetch_addr(P, w, idx, xcd, lane), smb + PFD);
    if (lane < 10) *(LDSP u32x4*)(sm + XIMG + w * XS + 16 * lane) = u32x4{0, 0, 0, 0};
    if (lane < 6) *(LDSP u32x4*)(sm + kimg + 16 * lane) = u32x4{0, 0, 0, 0};
    const float dsk = ld_bf_uniform(P.ssm_D, ch);
    if (w == 0) vmcnt<0>();
    wgbarrier();
    STAMP(1);
    load_pair(0);
    vmcnt<0>();
    set_word(sm + FLAG2 + 4 * w, 1);
    STAMP(2);
    load_pair(1);
#pragma unroll
    for (int j = 2; j < 4; ++j) dma16(P.ssm_k, (u32)ch * (SEQ * 2) + 1024 * j + 16 * lane, smb + kimg + KPADB + 1024 * j);
    if (lane == 0) {
      LDSP u16* k0 = (LDSP u16*)(sm + kimg + KPADB);
      *k0 = (u16)pkbf(bf2f(*k0) + dsk, 0.f);
    }
    wavefence();
    kb = kimg + 8 * (4 * lp + 8 * g + lq);
    xb = XIMG + w * XS + 224 + 8 * (lp - 8 * g - lq);
    yw = YIMG + w * YS + 2 * lane;
  } else {
    const int bi = xcd * 20 + (idx - 10);
    const int ch0 = bi * 4;
    if (w >= 8) {
      const int h = w - 8;
      const int pcol = bi >> 4, pchunk = bi & 15;
      __builtin_amdgcn_s_setprio(3);
      unsigned long long tag = 0;
      if (h == 0) tag = __hip_atomic_load(gepoch, RLX_AGENT) + 1;
      if (h < 3) touch64(prefetch_addr(P, h, idx, xcd, lane), smb + PFD);
      {
        const u32 po = (u32)(1024 + 64 * pchunk + 16 * h + (lane >> 3)) * ROWB + (u32)pcol * 128 + 16 * (lane & 7);
#pragma unroll
        for (int j = 0; j < 2; ++j) {
          dma16(P.k, po + j * 8 * ROWB, smb + PRAWK + 512 + h * 2048 + j * 1024);
          dma16(P.v, po + j * 8 * ROWB, smb + PRAWV + h * 2048 + j * 1024);
          dma16(P.q, po + j * 8 * ROWB, smb + PRAWQ + h * 2048 + j * 1024);
        }
        if (h == 0 && lane < 32) dma16(P.k, po - 4 * ROWB, smb + PRAWK);
        if (h == 1 && lane < 32) dma16(P.shift_k, (u32)pcol * 512 + 16 * lane, smb + SHK);
      }
      {
        const int cu = (h < 2) ? 3 + h : 3 + ((idx - 10) & 1);
        const int su = (h < 2) ? idx : (idx - 10) >> 1;
        touch64(prefetch_addr(P, cu, su, xcd, lane), smb + PFD);
      }
      wgbarrier();
      {
        const u32x4 ones = {0x3F803F80u, 0x3F803F80u, 0x3F803F80u, 0x3F803F80u};
#pragma unroll
        for (int i = 0; i < 4; ++i) *(LDSP u32x4*)(sm + B_YIMG + h * YS + 1024 * i + 16 * lane) = ones;
      }
      if (lane < 10) *(LDSP u32x4*)(sm + XIMG + h * XS + 16 * lane) = u32x4{0, 0, 0, 0};
      vmcnt<1>();
      set_word(sm + FLAG2 + 4 * h, 1);
      wait_words(sm + FLAG2, lane, 4, 1);
      STAMP(12);
      {
        const u32x2 sw = *(LDSP u32x2*)(sm + SHK + 8 * (16 * h + li));
        const float w0 = bflo(sw.x), w1 = bfhi(sw.x), w2 = bflo(sw.y), w3 = bfhi(sw.y);
        const u32 co = 32 * h + 8 * lp;
        const u32 stg = PSTG + h * XS;
        const u32 sw_ = stg + li * 128 + 8 * (g & 1);
        const u32 sr_ = stg + (lane >> 3) * 128 + 16 * ((lane ^ (lane >> 3)) & 7);
        const auto rx = __builtin_amdgcn_make_buffer_rsrc(P.ex + WS_EXX, 0, 4u << 20, 0x00020000);
        const u32 eo = (u32)(pcol * 64 + 16 * h + (lane >> 3)) * 2048 + 128 * pchunk + 16 * (lane & 7);
        const u32 zst = (lq & 1) ? 256u : 0u;
        const u32 ro = 128 * (4 * g + (lq >> 1)) + co;
        const u32 ka = (lq & 1) ? PRAWK + ro : (u32)ZROW, va = (lq & 1) ? PRAWV + ro : (u32)ZROW;
#pragma unroll
        for (int u = 0; u < 4; ++u)
          *(LDSP u32x2*)(sm + sw_ + 16 * (((2 * u + (g >> 1)) ^ li) & 7)) = trread(sm + PRAWQ + 128 * (16 * u + 4 * g + lq) + co);
        wavefence();
#pragma unroll
        for (int j = 0; j < 2; ++j)
          __builtin_amdgcn_raw_buffer_store_b128(*(const LDSP u32x4*)(sm + sr_ + 1024 * j), rx, eo + j * 8 * 2048 + (u32)(WS_EXQ - WS_EXX), 0, 16);
        wavefence();
#pragma unroll 2
        for (int u = 0; u < 4; ++u) {
          float kx[8], vx[4], kv[4];
#pragma unroll
          for (int j = 0; j < 4; ++j) { u32x2 r = trread(sm + ka + zst * (8 * u + j)); kx[2 * j] = __uint_as_float(r.x); kx[2 * j + 1] = __uint_as_float(r.y); }
#pragma unroll
          for (int j = 0; j < 2; ++j) { u32x2 r = trread(sm + va + zst * (8 * u + j)); vx[2 * j] = __uint_as_float(r.x); vx[2 * j + 1] = __uint_as_float(r.y); }
#pragma unroll
          for (int j = 0; j < 4; ++j)
            kv[j] = vmul(vfma(kx[j + 4], w0, vfma(kx[j + 3], w1, vfma(kx[j + 2], w2, vmul(kx[j + 1], w3)))), vx[j]);
          *(LDSP u32x2*)(sm + sw_ + 16 * (((2 * u + (g >> 1)) ^ li) & 7)) = u32x2{pkbf(kv[0], kv[1]), pkbf(kv[2], kv[3])};
        }
        wavefence();
#pragma unroll
        for (int j = 0; j < 2; ++j)
          __builtin_amdgcn_raw_buffer_store_b128(*(const LDSP u32x4*)(sm + sr_ + 1024 * j), rx, eo + j * 8 * 2048, 0, 16);
      }
      STAMP(1);
      const u32 rowoff = (u32)(64 * h + lane) * ROWB + (u32)(ch0 & ~7) * 2;
      dma16(P.u, rowoff, smb + RAWU + 1024 * h);
      dma16(P.u, rowoff + 256 * ROWB, smb + RAWU + 4096 + 1024 * h);
      vmcnt<2>();
      set_word(sm + FLAG2 + 16 + 4 * h, 1);
      wait_words(sm + FLAG2 + 16, lane, 4, 1);
      if (h == 0 && lane == 0) __hip_atomic_store(gflag + bi, tag, RLX_AGENT);
      STAMP(9);
      const u32 ua = RAWU + 1024 * h + 16 * (16 * g + 4 * lp + lq) + 2 * (ch0 & 4);
      const u32 xw = XIMG + (lane & 3) * XS + XPADB + 8 * (lane >> 2) + 128 * h;
      const u32 yrd = B_YIMG + lq * YS + 2 * (64 * h + 16 * g + 4 * lp);
      u16* const outp = P.out + DCH + ch0 + (size_t)(64 * h + lane) * (2 * DCH);
#pragma unroll 1
      for (int it = 0; it < 8; ++it) {
        int i = it;
        asm volatile("" : "+s"(i));
        if (i + 2 < 8) dma16(P.u, rowoff + (u32)(i + 2) * (256 * ROWB), smb + RAWU + 4096 * (i + 2) + 1024 * h);
        if (i < 6) vmcnt<2>(); else if (i == 6) vmcnt<1>(); else vmcnt<0>();
        *(LDSP u32x2*)(sm + xw + 512 * i) = trread(sm + ua + 4096 * i);
        set_word(sm + FLAGS + 4 * h, (u32)(i + 1));
        if (i == 0) STAMP(13); if (i == 7) STAMP(14);
      }
#pragma unroll 1
      for (int i = 0; i < 8; ++i) {
        wait_words(sm + FLAGS + 16, lane, 4, (u32)(i + 1));
        *(u32x2*)(outp + (size_t)i * 256 * (2 * DCH)) = trread(sm + yrd + 512 * i);
      }
      STAMP_OUT();
      asm volatile(".Lfk_end4:");
      __builtin_amdgcn_endpgm();
      __builtin_unreachable();
    }
    const int cw = w & 3, ps = w >> 2;
    const int ch = ch0 + cw;
    const size_t o = (size_t)ch * NST + 64 * ps + lane;
    const u16 i_ar = P.A_re[o], i_ai = P.A_im[o], i_br = P.B_re[o], i_bi = P.B_im[o], i_cr = P.C_re[o], i_ci = P.C_im[o];
    const float ldt = ld_bf_uniform(P.log_dt, ch);
    const float dsk = ld_bf_uniform(P.Dskip, ch);
    wgbarrier();
    const float dt = rbf(expf(ldt));
    const float zr = dt * bf2f(i_ar), zi = dt * bf2f(i_ai);
    const cf dB = {dt * bf2f(i_br), dt * bf2f(i_bi)};
    const cf C = {bf2f(i_cr), bf2f(i_ci)};
    const cf W = cmul(C, dB);
    float mx = fmaxf(fabsf(W.r), fabsf(W.i));
    STAMP(1);
    {
      int m = __float_as_int(mx);
      m = max(m, __builtin_amdgcn_update_dpp(0, m, 0x111, 0xf, 0xf, false));
      m = max(m, __builtin_amdgcn_update_dpp(0, m, 0x112, 0xf, 0xf, false));
      m = max(m, __builtin_amdgcn_update_dpp(0, m, 0x114, 0xf, 0xf, false));
      m = max(m, __builtin_amdgcn_update_dpp(0, m, 0x118, 0xf, 0xf, false));
      int a0 = __builtin_amdgcn_readlane(m, 15), a1 = __builtin_amdgcn_readlane(m, 31);
      int a2 = __builtin_amdgcn_readlane(m, 47), a3 = __builtin_amdgcn_readlane(m, 63);
      mx = __int_as_float(max(max(a0, a1), max(a2, a3)));
    }
    int ex = (int)((__float_as_uint(mx) >> 23) & 0xff);
    ex = min(max(ex, 30), 240);
    const float sY = __uint_as_float((u32)(267 - ex) << 23);
    const float so = __uint_as_float((u32)(ex - 27) << 23);
    const float SX = 16384.f;

    f32x4 ak[3];
    const u32 stg = STG + w * STS;
    const u32 rb = stg + li * 272 + g * 16;
    const u32 m0v = smb + w * STS;
#define STGW(row, val_r, val_i) \
    asm volatile("ds_write_addtid_b32 %0 offset:%1" :: "v"(pkh((val_r), (val_i))), "i"(STG + (row) * 272) : "memory")
    {
      const cf E1 = cexp_pow2(zr, zi, 1.f);
      const cf E8c = cexp_pow2(zr, -zi, 8.f);
      const cf E128 = cexp_pow2(zr, zi, 128.f);
      const cf E1024 = cexp_pow2(zr, zi, 1024.f);
      const cf E2 = cmul(E1, E1);
      const cf E4 = cmul(E2, E2);
      u32 keep;
      asm volatile("s_mov_b32 %0, m0\n\ts_mov_b32 m0, %1\n\ts_nop 0" : "=&s"(keep) : "s"(m0v) : "memory");
      cf y = {W.r * sY, -(W.i * sY)};
      cf P1 = {E128.r * SX, E128.i * SX};
      cf v0 = {SX, 0.f}, v1 = P1;
#pragma unroll
      for (int b = 0; b < 8; ++b) {
        STGW(2 * b, y.r, y.i); y = cmul(y, E8c);
        STGW(16 + b, v0.r, v0.i); if (b < 7) v0 = cmul(v0, E1);
        STGW(2 * b + 1, y.r, y.i); if (b < 7) y = cmul(y, E8c);
        STGW(24 + b, v1.r, v1.i); if (b < 7) v1 = cmul(v1, E1);
      }
      f16x8 yf[4];
      const f32x4 zero = {0.f, 0.f, 0.f, 0.f};
#pragma unroll
      for (int s = 0; s < 4; ++s) {
        yf[s] = *(const LDSP f16x8*)(sm + rb + 64 * s);
        f16x8 xf = *(const LDSP f16x8*)(sm + rb + 16 * 272 + 64 * s);
        ak[0] = __builtin_amdgcn_mfma_f32_16x16x32_f16(xf, yf[s], s ? ak[0] : zero, 0, 0, 0);
      }
      asm volatile("" ::: "memory");
      cf Pa = cmul(P1, E128);
      cf Pb = {E1024.r * SX, E1024.i * SX};
#pragma unroll
      for (int m = 0; m < 2; ++m) {
        cf v = Pa;
#pragma unroll
        for (int r = 0; r < 4; ++r) {
          STGW(4 * m + r, v.r, v.i); if (r < 3) v = cmul(v, E2);
          if (r & 1) { STGW(16 + 2 * m + (r >> 1), Pb.r, Pb.i); Pb = cmul(Pb, E128); }
        }
        Pa = cmul(Pa, E128);
      }
#pragma unroll
      for (int m = 0; m < 4; ++m) {
        STGW(8 + 2 * m, Pa.r, Pa.i);
        cf v = cmul(Pa, E4);
        STGW(20 + m, Pb.r, Pb.i); Pb = cmul(Pb, E128);
        STGW(9 + 2 * m, v.r, v.i);
        if (m < 3) Pa = cmul(Pa, E128);
      }
      STGW(24, Pb.r, Pb.i);
      asm volatile("s_mov_b32 m0, %0" :: "s"(keep) : "memory");
#pragma unroll
      for (int s = 0; s < 4; ++s) {
        f16x8 x1 = *(const LDSP f16x8*)(sm + rb + 64 * s);
        f16x8 x2 = *(const LDSP f16x8*)(sm + rb + 16 * 272 + 64 * s);
        ak[1] = __builtin_amdgcn_mfma_f32_16x16x32_f16(x1, yf[s], s ? ak[1] : zero, 0, 0, 0);
        ak[2] = __builtin_amdgcn_mfma_f32_16x16x32_f16(x2, yf[s], s ? ak[2] : zero, 0, 0, 0);
      }
    }
#undef STGW
#pragma unroll
    for (int i = 0; i < 3; ++i) ak[i] *= so;
    STAMP(2);
    asm volatile("" ::: "memory");
    const u32 kimg = B_KIMG + cw * KS;
    if (ps) {
#pragma unroll
      for (int i = 0; i < 3; ++i) *(LDSP f32x4*)(sm + kimg + 1024 * i + 16 * lane) = ak[i];
      asm volatile("s_waitcnt lgkmcnt(0)" ::: "memory");
      *(volatile LDSP u32*)(sm + FLAGS + 48 + 4 * cw) = 1;
      STAMP_OUT();
      asm volatile(".Lfk_end5:");
      __builtin_amdgcn_endpgm();
      __builtin_unreachable();
    }
    wait_words(sm + FLAGS + 48 + 4 * cw, lane, 1, 1);
    {
      const u32 tb = stg;
#pragma unroll
      for (int i = 0; i < 3; ++i) ak[i] += *(const LDSP f32x4*)(sm + kimg + 1024 * i + 16 * lane);
      wavefence();
      const int b = li, p = g;
      if (lane < 6) *(LDSP u32x4*)(sm + kimg + 16 * lane) = u32x4{0, 0, 0, 0};
      {
        float a0 = ak[0][0] + (lane == 0 ? dsk : 0.f), a1 = ak[0][1], a2 = ak[0][2], a3 = ak[0][3];
        *(LDSP u32x2*)(sm + kimg + KPADB + 2 * (128 * (p >> 1) + 8 * b + 4 * (p & 1))) = u32x2{pkbf(a0, a1), pkbf(a2, a3)};
      }
      {
        float a0 = ak[1][0], a1 = ak[1][1], a2 = ak[1][2], a3 = ak[1][3];
        int i01, i23;
        if (p < 2) { i01 = 64 * p + 4 * b; i23 = i01 + 2; }
        else { i01 = 128 + 64 * (p - 2) + 2 * b; i23 = i01 + 32; }
        *(LDSP u32x2*)(sm + tb + 4 * i01) = u32x2{pkbf(a0, a0), pkbf(a1, a1)};
        *(LDSP u32x2*)(sm + tb + 4 * i23) = u32x2{pkbf(a2, a2), pkbf(a3, a3)};
#pragma unroll
        for (int j = 0; j < 4; ++j) {
          float a = ak[2][j];
          int ii = 256 + 16 * (4 * p + j) + b;
          *(LDSP u32*)(sm + tb + 4 * ii) = pkbf(a, a);
        }
      }
      wavefence();
      const LDSP u32* TB = (const LDSP u32*)(sm + tb);
      {
        const int i0 = 4 * (lane & 31);
        u32 g0 = TB[i0], g1 = TB[i0 + 1], g2 = TB[i0 + 2], g3 = TB[i0 + 3], g4 = TB[i0 + 4];
        u32x4 ov = {g0, (g1 & 0xffffu) | (g2 & 0xffff0000u), g2, (g3 & 0xffffu) | (g4 & 0xffff0000u)};
        *(LDSP u32x4*)(sm + kimg + KPADB + 16 * (32 + (lane & 31))) = ov;
      }
      {
        const int i0 = 128 + 2 * lane;
        u32 g0 = TB[i0], g1 = TB[i0 + 1], g2 = TB[i0 + 2];
        u32x4 ov = {g0, (g0 & 0xffffu) | (g1 & 0xffff0000u), g1, g2};
        *(LDSP u32x4*)(sm + kimg + KPADB + 16 * (64 + lane)) = ov;
      }
#pragma unroll
      for (int hh = 0; hh < 2; ++hh) {
        const int i0 = 256 + 64 * hh + lane;
        u32 g0 = TB[i0], g1 = TB[i0 + 1];
        u32 mid = (lane & 1) ? g1 : ((g0 & 0xffffu) | (g1 & 0xffff0000u));
        u32x4 ov = {g0, g0, mid, g1};
        *(LDSP u32x4*)(sm + kimg + KPADB + 16 * (128 + 64 * hh + lane)) = ov;
      }
    }
    wavefence();
    kb = kimg + 8 * (4 * lp + 8 * g + lq);
    xb = XIMG + cw * XS + 224 + 8 * (lp - 8 * g - lq);
    yw = B_YIMG + cw * YS + 2 * lane;
  }

  {
    const bool l63 = (lane == 63);
    const lptr pxp = sm + FLAGS + 4 * (lane & 3);
    const lptr pyp = sm + FLAGS + 16 + 4 * w;
    u32x2 kr[16][2], xr[40][4];
    f32x4 ac[16][2];
    const f32x4 zero = {0.f, 0.f, 0.f, 0.f};
    float ts[16][2], u2[16][2], u1[16][2], yy[16][2];
    u32 pk[16];
    u32 qv[3][2] = {{0, 0}, {0, 0}, {0, 0}}, px = 0;
    const lptr ywp = sm + yw;
    const u32 qa = (u32)(uintptr_t)ywp;
#define RDQ(s_, i_) asm volatile("ds_read_u16_d16_hi %0, %1 offset:%2" : "+v"(qv[(s_) % 3][i_]) : "v"(qa), "i"((s_) * 256 + 128 * (i_)) : "memory")
#define RDXP(l_, i_) xr[l_][i_] = trread(sm + xb + 256 * SC.ldm[l_] + ((i_) >> 1) * 128 + (((i_) & 1) ? 0 : 32))
#pragma unroll
    for (int d = 0; d < 8; ++d) { kr[d][0] = trread(sm + kb + 256 * d); kr[d][1] = trread(sm + kb + 256 * d + 32); }
    STAMP(3);
    wait_words(sm + FLAGS, lane, 4, 1);
    STAMP(4);
    CSTAMP(0);
    RDQ(0, 0); RDQ(0, 1);
    RDXP(0, 0); RDXP(0, 1); RDXP(0, 2); RDXP(0, 3);
    SCHED0();
    static_for<0, NGAP>([&](auto G_) {
      constexpr int G = decltype(G_)::value;
      if constexpr (G < 272) {
        constexpr int u = G >> 1, par = G & 1;
        constexpr int p = SC.p[u], d = SC.d[u], l = SC.ld[u];
        ac[p][par] = MFMA(frag(kr[d][0], kr[d][1]), frag(xr[l][2 * par], xr[l][2 * par + 1]), SC.first[u] ? zero : ac[p][par]);
        SCHED0();
      }
      static_for<0, AC.n[G]>([&](auto I_) {
        constexpr int code = AC.a[G][decltype(I_)::value];
        constexpr int kind = code & 15, a0 = (code >> 4) & 255, a1 = code >> 12;
        if constexpr (kind == A_X) RDXP(a0, a1);
        if constexpr (kind == A_K) kr[a0][a1] = trread(sm + kb + 256 * a0 + 32 * a1);
        if constexpr (kind == A_Q) RDQ(a0, a1);
        if constexpr (kind == A_PXR) px = *(volatile LDSP u32*)pxp;
        if constexpr (kind == A_DMA) { if (ldw == a1) load_pair(a0); }
        if constexpr (kind == A_RR) { if (ldw >= 0) { vmcnt<0>(); set_word(sm + FLAG2 + 4 * w, (u32)(a0 + 1)); } }
        if constexpr (kind == A_PXC) {
          if (__builtin_expect(!__all(px >= (u32)(a0 / 2 + 1)), 0)) wait_words(sm + FLAGS, lane, 4, (u32)(a0 / 2 + 1));
          asm volatile("" ::: "memory");
        }
        if constexpr (kind == A_EP) {
          constexpr int s = a0, e = a1 >> 1, o = a1 & 1;
          if constexpr (a1 < 14) {
            if constexpr (e == 0) {
              float cur = ac[s][o][3], prev = 0.f;
              if constexpr (o) prev = ac[s][0][3]; else if constexpr (s > 0) prev = ac[s - 1][1][3];
              ts[s][o] = l63 ? prev : cur;
            }
            if constexpr (e == 1) { float a = ac[s][o][2]; u2[s][o] = a + ror1(ts[s][o]); }
            if constexpr (e == 2) {
              float prev = 0.f;
              if constexpr (o) prev = u2[s][0]; else if constexpr (s > 0) prev = u2[s - 1][1];
              ts[s][o] = l63 ? prev : u2[s][o];
            }
            if constexpr (e == 3) { float a = ac[s][o][1]; u1[s][o] = a + ror1(ts[s][o]); }
            if constexpr (e == 4) {
              float prev = 0.f;
              if constexpr (o) prev = u1[s][0]; else if constexpr (s > 0) prev = u1[s - 1][1];
              ts[s][o] = l63 ? prev : u1[s][o];
            }
            if constexpr (e == 5) { float a = ac[s][o][0]; yy[s][o] = a + ror1(ts[s][o]); asm volatile("" : "+v"(yy[s][o])); }
            if constexpr (e == 6) {
              asm volatile("" : "+v"(qv[s % 3][o]));
              yy[s][o] *= __uint_as_float(qv[s % 3][o]);
              asm volatile("" : "+v"(yy[s][o]));
            }
          }
          if constexpr (a1 == 14) pk[s] = pkbf(yy[s][0], yy[s][1]);
          if constexpr (a1 == 15) *(LDSP u16*)(ywp + s * 256) = (u16)pk[s];
          if constexpr (a1 == 16) *(LDSP u16*)(ywp + s * 256 + 128) = (u16)(pk[s] >> 16);
          if constexpr (a1 == 17) {
            asm volatile("" ::: "memory");
            *(volatile LDSP u32*)pyp = (u32)(s / 2 + 1);
            asm volatile("" ::: "memory");
          }
        }
        SCHED0();
      });
    });
  }
  STAMP_OUT();
  asm volatile(".Lfk_end0:");
  __builtin_amdgcn_endpgm();
}

extern "C" void kernel_launch(void* const* d_in, const int* in_sizes, int n_in,
                              void* d_out, int out_size, void* d_ws, size_t ws_size,
                              hipStream_t stream) {
  Params P;
  P.q = (const u16*)d_in[0];
  P.k = (const u16*)d_in[1];
  P.v = (const u16*)d_in[2];
  P.shift_k = (const u16*)d_in[3];
  P.ssm_k = (const u16*)d_in[4];
  P.ssm_D = (const u16*)d_in[5];
  P.u = (const u16*)d_in[6];
  P.A_re = (const u16*)d_in[7];
  P.A_im = (const u16*)d_in[8];
  P.B_re = (const u16*)d_in[9];
  P.B_im = (const u16*)d_in[10];
  P.C_re = (const u16*)d_in[11];
  P.C_im = (const u16*)d_in[12];
  P.log_dt = (const u16*)d_in[13];
  P.Dskip = (const u16*)d_in[14];
  P.out = (u16*)d_out;
  P.ws = (unsigned long long*)d_ws;
  P.ex = (char*)d_ws + (16 << 20);
  (void)hipFuncSetAttribute((const void*)fused_kernel, hipFuncAttributeMaxDynamicSharedMemorySize, LDS_BYTES);
  fused_kernel<<<256, 768, LDS_BYTES, stream>>>(P);
}
```

```cpp
#include <hip/hip_runtime.h>
#include <hip/hip_bf16.h>
#include <stdint.h>

typedef __attribute__((ext_vector_type(4))) short s16x4;
typedef __attribute__((ext_vector_type(8))) _Float16 f16x8;
typedef __attribute__((ext_vector_type(8))) __bf16 bf16x8;
typedef __attribute__((ext_vector_type(2))) __bf16 bf16x2;
typedef __attribute__((ext_vector_type(2))) _Float16 f16x2;
typedef __attribute__((ext_vector_type(2))) float f32x2;
typedef __attribute__((ext_vector_type(4))) float f32x4;
typedef __attribute__((ext_vector_type(2))) unsigned int u32x2;
typedef __attribute__((ext_vector_type(4))) unsigned int u32x4;
typedef unsigned int u32;
typedef unsigned short u16;
#define LDSP __attribute__((address_space(3)))
#define DEV __device__ __forceinline__

constexpr int SEQ = 2048;
constexpr int DCH = 640;
constexpr int NST = 128;
constexpr int ROWB = DCH * 2;

constexpr int XS = 4272;
constexpr int XPADB = 160;
constexpr int YS = 4160;
constexpr int KS = 4224;
constexpr int KPADB = 96;
constexpr int FLAGS = 0;
constexpr int ZROW = 64;
constexpr int FLAG2 = 192;
constexpr int PFD = 256;
constexpr int SHK = 512;
constexpr int XIMG = 1024;
constexpr int YIMG = 35200;
constexpr int KIMG = 68480;
constexpr int RAWK = 102272;
constexpr int RAWV = 119680;
constexpr int RAWQ = 136064;
constexpr int STS = 8704;
constexpr int STG = 18176;
constexpr int B_KIMG = 87808;
constexpr int B_YIMG = 104704;
constexpr int RAWU = 121344;
constexpr int PRAWK = RAWU;
constexpr int PRAWV = PRAWK + 12800;
constexpr int PRAWQ = PRAWV + 12288;
constexpr int PSTG = XIMG + XPADB;
constexpr int LDS_BYTES = 158720;

constexpr size_t WS_EPOCH = 0;
constexpr size_t WS_FLAG = 4096;
constexpr size_t WS_EXX = 1 << 20;
constexpr size_t WS_EXQ = 3 << 20;
constexpr size_t WS_STAMP = 8 << 20;

DEV float bf2f(u16 v) { return __uint_as_float(((u32)v) << 16); }
DEV float bflo(u32 v) { return __uint_as_float(v << 16); }
DEV float bfhi(u32 v) { return __uint_as_float(v & 0xffff0000u); }
DEV u32 pkbf(float a, float b) {
  f32x2 t = {a, b};
  bf16x2 r = __builtin_convertvector(t, bf16x2);
  return __builtin_bit_cast(u32, r);
}
DEV u32 pkh(float a, float b) {
  f32x2 t = {a, b};
  f16x2 r = __builtin_convertvector(t, f16x2);
  return __builtin_bit_cast(u32, r);
}
DEV float rbf(float a) { return bflo(pkbf(a, a)); }
DEV float dot2(u32 a, u32 b, float c) {
  return __builtin_amdgcn_fdot2_f32_bf16(__builtin_bit_cast(bf16x2, a), __builtin_bit_cast(bf16x2, b), c, false);
}
DEV float vfma(float a, float b, float c) { float d; asm("v_fma_f32 %0, %1, %2, %3" : "=v"(d) : "v"(a), "v"(b), "v"(c)); return d; }
DEV float vmul(float a, float b) { float d; asm("v_mul_f32 %0, %1, %2" : "=v"(d) : "v"(a), "v"(b)); return d; }
typedef __attribute__((address_space(1))) unsigned long long gu64;
#define RLX_AGENT __ATOMIC_RELAXED, __HIP_MEMORY_SCOPE_AGENT
typedef LDSP char* lptr;
DEV u32x2 trread(lptr p) {
  s16x4 r = __builtin_amdgcn_ds_read_tr16_b64_v4i16((LDSP s16x4*)p);
  return __builtin_bit_cast(u32x2, r);
}
DEV bf16x8 frag(u32x2 a, u32x2 b) {
  u32x4 f = {a.x, a.y, b.x, b.y};
  return __builtin_bit_cast(bf16x8, f);
}
DEV float ror1(float v) {
  int r = __builtin_amdgcn_update_dpp(0, __float_as_int(v), 0x13C, 0xf, 0xf, false);
  return __int_as_float(r);
}
DEV void wavefence() { __builtin_amdgcn_fence(__ATOMIC_ACQ_REL, "wavefront"); __builtin_amdgcn_wave_barrier(); }
DEV void wgbarrier() {
  asm volatile("s_waitcnt lgkmcnt(0)" ::: "memory");
  __builtin_amdgcn_s_barrier();
  asm volatile("" ::: "memory");
}
DEV void dma16(const void* base, u32 voff, u32 ldsaddr) {
  u32 keep;
  ldsaddr = __builtin_amdgcn_readfirstlane(ldsaddr);
  const unsigned long long b64 = (unsigned long long)(uintptr_t)base;
  const unsigned long long sb = ((unsigned long long)(u32)__builtin_amdgcn_readfirstlane((u32)(b64 >> 32)) << 32) |
                                (u32)__builtin_amdgcn_readfirstlane((u32)b64);
  asm volatile("s_mov_b32 %0, m0\n\ts_mov_b32 m0, %3\n\ts_nop 2\n\tglobal_load_lds_dwordx4 %1, %2\n\ts_mov_b32 m0, %0"
               : "=&s"(keep) : "v"(voff), "s"(sb), "s"(ldsaddr) : "memory");
}
DEV void touch64(const void* p, u32 ldsaddr) {
  u32 keep;
  asm volatile("s_mov_b32 %0, m0\n\ts_mov_b32 m0, %2\n\ts_nop 0\n\tglobal_load_lds_dword %1, off\n\ts_mov_b32 m0, %0"
               : "=&s"(keep) : "v"(p), "s"(ldsaddr) : "memory");
}
template <int N> DEV void vmcnt() { asm volatile("s_waitcnt vmcnt(%0)" :: "i"(N) : "memory"); }
#define SCHED0() __builtin_amdgcn_sched_barrier(0)

template <int V> struct IC { static constexpr int value = V; };
template <int I, int N, class F> DEV void static_for(F&& f) {
  if constexpr (I < N) { f(IC<I>{}); static_for<I + 1, N>(f); }
}

struct cf { float r, i; };
DEV cf cmul(cf a, cf b) {
  cf o;
  o.r = fmaf(a.r, b.r, -(a.i * b.i));
  o.i = fmaf(a.r, b.i, a.i * b.r);
  return o;
}
DEV cf cexp_pow2(float zr, float zi, float x) {
  float mag = __builtin_amdgcn_exp2f((zr * x) * 1.44269504088896340736f);
  float th = zi * x;
  const float C_HI = 0.15915494f;
  const float C_LO = 6.4206382e-09f;
  float f = fmaf(th, C_LO, fmaf(th, C_HI, -rintf(th * C_HI)));
  cf o;
  o.r = mag * __builtin_amdgcn_cosf(f);
  o.i = mag * __builtin_amdgcn_sinf(f);
  return o;
}

#define STAMP(n)
#define CSTAMP(n)
#define STAMP_OUT()

struct Params {
  const u16 *q, *k, *v, *shift_k, *ssm_k, *ssm_D;
  const u16 *u, *A_re, *A_im, *B_re, *B_im, *C_re, *C_im, *log_dt, *Dskip;
  u16* out;
  unsigned long long* ws;
  char* ex;
};

DEV float ld_bf_uniform(const u16* p, int idx) {
  u32 d = *(const __attribute__((address_space(4))) u32*)(uintptr_t)(p + (idx & ~1));
  return (idx & 1) ? bfhi(d) : bflo(d);
}

DEV const char* prefetch_addr(const Params& P, int cls, int slice, int xcd, int lane) {
  asm volatile("" : "+v"(lane));
  const unsigned long long TAB = 0xFFEDC810ull;
  const u32 nib = (u32)(TAB >> (4 * (2 * cls + (lane >> 5)))) & 15u;
  const bool second = (lane >> 4) & 1;
  const u16* base = (nib < 8) ? (second ? P.v : P.k) : (nib < 12 ? P.q : P.u);
  const u32 blk = (nib < 8) ? nib : 2 * (nib & 3) + (second ? 1 : 0);
  const u32 row = blk * 256u + (u32)slice * 8u + ((lane >> 1) & 7);
  return (const char*)base + (row * ROWB + (u32)(((160 * xcd) >> 7) + (lane & 1)) * 128u);
}
DEV void wait_words(lptr p, int lane, int n, u32 v) {
#pragma unroll 1
  for (int spin = 0; spin < (1 << 16); ++spin) {
    u32 x = *(volatile LDSP u32*)(p + 4 * (lane & (n - 1)));
    if (__all(x >= v)) break;
    __builtin_amdgcn_s_sleep(1);
  }
  asm volatile("" ::: "memory");
}
#define PFS 3
#define PFP 4
#define PFU 12
DEV void delay(int n) {
  for (int i = 0; i < n; ++i) __builtin_amdgcn_s_sleep(PFS);
}

template <int RS>
DEV u32x2 kv4(lptr sm, u32 ka, u32 va, float w0, float w1, float w2, float w3) {
  float kx[8], vx[4], kv[4];
#pragma unroll
  for (int j = 0; j < 4; ++j) { u32x2 r = trread(sm + ka + RS * j); kx[2 * j] = __uint_as_float(r.x); kx[2 * j + 1] = __uint_as_float(r.y); }
#pragma unroll
  for (int j = 0; j < 2; ++j) { u32x2 r = trread(sm + va + RS * j); vx[2 * j] = __uint_as_float(r.x); vx[2 * j + 1] = __uint_as_float(r.y); }
#pragma unroll
  for (int j = 0; j < 4; ++j)
    kv[j] = vmul(vfma(kx[j + 4], w0, vfma(kx[j + 3], w1, vfma(kx[j + 2], w2, vmul(kx[j + 1], w3)))), vx[j]);
  return u32x2{pkbf(kv[0], kv[1]), pkbf(kv[2], kv[3])};
}
DEV void set_word(lptr p, u32 v) {
  asm volatile("" ::: "memory");
  *(volatile LDSP u32*)p = v;
  asm volatile("" ::: "memory");
}

struct Sched {
  int p[136], d[136], ld[136];
  bool first[136];
  int base[17];
  int ldm[40], ldu[40];
  int ku[16];
  int nld;
};
constexpr Sched make_sched() {
  Sched S{};
  int n = 0, nl = 16;
  for (int m = 0; m < 16; ++m) S.ldm[m] = m;
  for (int d = 0; d < 16; ++d) S.ku[d] = -1;
  for (int s = 0; s < 16; ++s) {
    S.base[s] = n;
    S.ldu[s] = n;
    for (int k = 0; k < 8 && s + k < 16; ++k) { S.p[n] = s + k; S.d[n] = k; S.ld[n] = s; S.first[n] = (s == 0) || (k == 7); ++n; }
    int lo = -1, hi = -1, cnt = 0, half = 0;
    if (s == 6) { lo = 8; hi = 8; cnt = 1; half = 2; }
    else if (s >= 7 && s <= 12) { lo = (s & 1) ? s + 2 : s + 1; hi = lo + 1; cnt = hi - 7; half = (s & 1) ? 0 : 1; }
    else if (s == 13 || s == 14) { lo = 15; hi = 15; cnt = 8; half = (s == 13) ? 0 : 1; }
    if (cnt) {
      const int split = (cnt + 1) / 2;
      const int j0 = (half == 1) ? split : 0, j1 = (half == 0) ? split : cnt;
      for (int j = j0; j < j1; ++j) {
        const int mm = cnt - 1 - j;
        S.ldm[nl] = mm; S.ldu[nl] = n;
        for (int t = hi; t >= lo; --t)
          if (t - mm >= 8) { S.p[n] = t; S.d[n] = t - mm; S.ld[n] = nl; S.first[n] = false; ++n; }
        ++nl;
      }
    }
  }
  S.base[16] = n;
  S.nld = nl;
  for (int u = 0; u < n; ++u) if (S.ku[S.d[u]] < 0) S.ku[S.d[u]] = u;
  return S;
}
constexpr Sched SC = make_sched();
static_assert(SC.base[16] == 136, "schedule");
constexpr int NGAP = 272 + 24, CAP = 2;
enum { A_NONE = 0, A_X, A_K, A_Q, A_PXR, A_PXC, A_EP, A_DMA, A_RR };
struct Acts { int n[NGAP]; int a[NGAP][4]; };
constexpr int place_back(Acts& T, int latest, int code, int cap = CAP) {
  int g = latest;
  while (g > 0 && T.n[g] >= cap) --g;
  T.a[g][T.n[g]++] = code;
  return g;
}
constexpr int place_fwd(Acts& T, int earliest, int code, int cap) {
  int g = earliest;
  while (T.n[g] >= (g < 272 ? cap : 4)) ++g;
  T.a[g][T.n[g]++] = code;
  return g;
}
constexpr Acts make_acts() {
  Acts T{};
  for (int l = 15; l >= 1; --l) {
    int g = 2 * SC.ldu[l] - 5;
    for (int i = 3; i >= 0; --i) g = place_back(T, g - 1, A_X | (l << 4) | (i << 12), 1);
    g = place_back(T, g - 1, A_Q | (l << 4) | (1 << 12), 1);
    g = place_back(T, g - 1, A_Q | (l << 4), 1);
    if (!(l & 1)) {
      g = place_back(T, g - 1, A_PXC | (l << 4), 1);
      place_back(T, g - 6, A_PXR, 1);
    }
  }
  for (int l = SC.nld - 1; l >= 16; --l) {
    int g = 2 * SC.ldu[l] - 5;
    for (int i = 3; i >= 0; --i) g = place_back(T, g - 1, A_X | (l << 4) | (i << 12), SC.ldu[l] >= 100 ? 2 : 1);
  }
  for (int d = 15; d >= 8; --d) {
    int g = 2 * SC.ku[d] - 5;
    g = place_back(T, g - 1, A_K | (d << 4) | (1 << 12), 2);
    g = place_back(T, g - 1, A_K | (d << 4), 2);
  }
  place_fwd(T, 6, A_RR | (1 << 4), 3);
  for (int s = 0; s < 16; ++s) {
    int g = 2 * SC.base[s] + 4;
    const int nop = (s & 1) ? 18 : 17;
    for (int e = 0; e < nop; ++e) g = place_fwd(T, g + ((e & 1) && e < 14 ? 0 : 1), A_EP | (s << 4) | (e << 12), s >= 11 ? 3 : CAP);
  }
  return T;
}
constexpr Acts AC = make_acts();

#define MFMA(a, b, c) __builtin_amdgcn_mfma_f32_16x16x32_bf16((a), (b), (c), 0, 0, 0)

__global__ __launch_bounds__(768, 3) void fused_kernel(Params P) {
  extern __shared__ __attribute__((aligned(16))) char smem[];
  const lptr sm = (lptr)smem;
  const u32 smb = (u32)(uintptr_t)sm;
  const int tid = threadIdx.x;
  const int w = __builtin_amdgcn_readfirstlane(tid >> 6);
  const int lane = tid & 63;
  const int wg = blockIdx.x;
  const int xcd = wg & 7, idx = wg >> 3;
  const bool partA = idx < 10;
  const int g = lane >> 4, li = lane & 15, lq = li >> 2, lp = li & 3;
  STAMP(0);
  if (w == 7) {
    unsigned long long pc;
    u32 len;
    asm volatile("s_getpc_b64 %0\n.Lfk_code_begin:\n\ts_mov_b32 %1, .Lfk_end0-.Lfk_code_begin\n\ts_max_i32 %1, %1, .Lfk_end1-.Lfk_code_begin\n\ts_max_i32 %1, %1, .Lfk_end2-.Lfk_code_begin\n\ts_max_i32 %1, %1, .Lfk_end3-.Lfk_code_begin\n\ts_max_i32 %1, %1, .Lfk_end4-.Lfk_code_begin\n\ts_max_i32 %1, %1, .Lfk_end5-.Lfk_code_begin" : "=s"(pc), "=&s"(len) :: "scc");
    const u32 off = min((u32)(lane + 64 * (idx % 3)) * 128u, len & ~3u);
    touch64((const char*)pc + off, smb + PFD);
  }
  gu64* const gflag = (gu64*)(uintptr_t)(P.ex + WS_FLAG);
  gu64* const gepoch = (gu64*)(uintptr_t)(P.ex + WS_EPOCH);
  if (idx >= 30) {
    if (w < 4) { if (w >= 2) delay(PFU); touch64(prefetch_addr(P, w, idx, xcd, lane), smb + PFD); }
    if (w == 8 && wg == 240) {
      const unsigned long long tag = __hip_atomic_load(gepoch, RLX_AGENT) + 1;
#pragma unroll 1
      for (int spin = 0; spin < (1 << 16); ++spin) {
        bool ok = true;
#pragma unroll
        for (int j = 0; j < 4; ++j) { const int f = min(lane + 64 * j, 239); ok &= (__hip_atomic_load(gflag + f, RLX_AGENT) == tag); }
        if (__all(ok)) break;
        __builtin_amdgcn_s_sleep(16);
      }
      if (lane == 0) __hip_atomic_store(gepoch, tag, RLX_AGENT);
    }
    vmcnt<0>();
    asm volatile(".Lfk_end1:");
    __builtin_amdgcn_endpgm();
    __builtin_unreachable();
  }
  if (tid < 64) *(LDSP u32*)(sm + FLAGS + 4 * tid) = 0;

  u32 kb, xb, yw;
  auto load_pair = [&](int p) {
    int ln = lane;
    asm volatile("" : "+v"(ln));
    const u32 ro = (u32)(512 * p + 64 * w + ln) * ROWB + (u32)(xcd * 10 + idx) * 16;
    const int ck = (p & 1) * 8 + w;
    dma16(P.k, ro, smb + RAWK + ck * 1088 + 64);
    dma16(P.v, ro, smb + RAWV + ck * 1024);
    const bool first = (p == 0 && w == 0);
    if (ln < 4) dma16(P.k, first ? ro : ro - 4 * ROWB, first ? smb + PFD : smb + RAWK + ck * 1088);
    dma16(P.q, ro, smb + RAWQ + ck * 1024);
  };

  if (partA) {
    const int grp = xcd * 10 + idx;
    const int ch0 = grp * 8;
    if (w >= 8) {
      const int h = w - 8;
      const unsigned long long tag = __hip_atomic_load(gepoch, RLX_AGENT) + 1;
      asm volatile("" :: "v"(tag) : "memory");
      if (h == 0) {
        if (lane < 4) dma16(P.shift_k, (u32)ch0 * 8 + 16 * lane, smb + SHK);
        if (lane < 4) *(LDSP u32x4*)(sm + RAWK + 16 * lane) = u32x4{0, 0, 0, 0};
        vmcnt<0>();
      }
      const int srow = 8 * g + 4 * (lp >> 1);
      const int shalf = 8 * (lp & 1);
      const u32 zsel = (lq & 1) ? 0xffffffffu : 0u;
      const u32 ka0 = (lq & 1) ? RAWK + h * 1088 + 16 * (srow + (lq >> 1)) + shalf : (u32)ZROW;
      const u32 va0 = (lq & 1) ? RAWV + h * 1024 + 16 * (srow + (lq >> 1)) + shalf : (u32)ZROW;
      const u32 qa0 = RAWQ + h * 1024 + 16 * (srow + lq) + shalf;
      const u32 xw = XIMG + (lane & 7) * XS + XPADB + 8 * (lane >> 3) + 128 * h;
      const u32 qw = YIMG + (lane & 7) * YS + 8 * (lane >> 3) + 128 * h;
      const u32 yrd = YIMG + lq * YS + 2 * (64 * h + 16 * g + 4 * lp);
      u16* const outp = P.out + ch0 + (size_t)(64 * h + lane) * (2 * DCH);
      wgbarrier();
      if (h == 0 && lane == 0) __hip_atomic_store(gflag + 160 + grp, tag, RLX_AGENT);
      __builtin_amdgcn_s_setprio(3);
      const u32x2 sw = *(LDSP u32x2*)(sm + SHK + 8 * (lane & 7));
      const float w0 = bflo(sw.x), w1 = bfhi(sw.x), w2 = bflo(sw.y), w3 = bfhi(sw.y);
#pragma unroll 1
      for (int b = 0; b < 2; ++b) {
        wait_words(sm + FLAG2 + 4 * (4 * b + h), lane, 1, 1);
        u32x2 o[2], qq[2];
#pragma unroll
        for (int u = 0; u < 2; ++u) qq[u] = trread(sm + qa0 + b * 4096 + u * 512);
#pragma unroll
        for (int u = 0; u < 2; ++u)
          o[u] = kv4<32>(sm, ka0 + ((b * 4352 + u * 512) & zsel), va0 + ((b * 4096 + u * 512) & zsel), w0, w1, w2, w3);
#pragma unroll
        for (int u = 0; u < 2; ++u) {
          *(LDSP u32x2*)(sm + qw + 512 * b + 64 * u) = qq[u];
          *(LDSP u32x2*)(sm + xw + 512 * b + 64 * u) = o[u];
        }
        set_word(sm + FLAGS + 4 * h, (u32)(b + 1));
        if (b == 0) STAMP(12); if (b == 1) STAMP(13);
      }
#pragma unroll 1
      for (int spin = 0; spin < (1 << 16); ++spin) {
        if (__all(__hip_atomic_load(gflag + (grp >> 3) * 16 + (lane & 15), RLX_AGENT) == tag)) break;
        __builtin_amdgcn_s_sleep(4);
      }
      __builtin_amdgcn_fence(__ATOMIC_ACQUIRE, "wavefront");
      STAMP(9);
      {
        const auto rx = __builtin_amdgcn_make_buffer_rsrc(P.ex + WS_EXX, 0, 4u << 20, 0x00020000);
        u32x4 d[12];
#pragma unroll
        for (int j = 0; j < 12; ++j)
          d[j] = __builtin_amdgcn_raw_buffer_load_b128(rx, (u32)(ch0 + 2 * h + (j & 1)) * 3072 + 1024 * (j >> 2) + 16 * lane + ((j >> 1) & 1) * (u32)(WS_EXQ - WS_EXX), 0, 16);
#pragma unroll
        for (int j = 0; j < 12; ++j) {
          const int cl = 2 * h + (j & 1);
          const u32 da = ((j >> 1) & 1) ? YIMG + cl * YS : XIMG + cl * XS + XPADB;
          *(LDSP u32x4*)(sm + da + 1024 + 1024 * (j >> 2) + 16 * lane) = d[j];
          if ((j & 3) == 3) set_word(sm + FLAGS + 4 * h, (u32)(4 + 2 * (j >> 2)));
        }
      }
      STAMP(14);
#pragma unroll 1
      for (int b = 0; b < 8; ++b) {
        wait_words(sm + FLAGS + 16, lane, 8, (u32)(b + 1));
        u32x2 a = trread(sm + yrd + 512 * b);
        u32x2 c = trread(sm + yrd + 512 * b + 4 * YS);
        *(u32x4*)(outp + (size_t)b * 256 * (2 * DCH)) = u32x4{a.x, a.y, c.x, c.y};
      }
      STAMP_OUT();
      asm volatile(".Lfk_end2:\n.Lfk_end3:");
      __builtin_amdgcn_endpgm();
      __builtin_unreachable();
    }
    const int ch = ch0 + w;
    const u32 kimg = KIMG + w * KS;
#pragma unroll
    for (int j = 0; j < 2; ++j) dma16(P.ssm_k, (u32)ch * (SEQ * 2) + 1024 * j + 16 * lane, smb + kimg + KPADB + 1024 * j);
    if (w < 2) touch64(prefetch_addr(P, w, idx, xcd, lane), smb + PFD);
    if (lane < 10) *(LDSP u32x4*)(sm + XIMG + w * XS + 16 * lane) = u32x4{0, 0, 0, 0};
    if (lane < 6) *(LDSP u32x4*)(sm + kimg + 16 * lane) = u32x4{0, 0, 0, 0};
    const float dsk = ld_bf_uniform(P.ssm_D, ch);
    if (w < 2) vmcnt<0>();
    wgbarrier();
    STAMP(1);
    load_pair(0);
    vmcnt<0>();
    set_word(sm + FLAG2 + 4 * w, 1);
    STAMP(2);
#pragma unroll
    for (int j = 2; j < 4; ++j) dma16(P.ssm_k, (u32)ch * (SEQ * 2) + 1024 * j + 16 * lane, smb + kimg + KPADB + 1024 * j);
    if (lane == 0) {
      LDSP u16* k0 = (LDSP u16*)(sm + kimg + KPADB);
      *k0 = (u16)pkbf(bf2f(*k0) + dsk, 0.f);
    }
    wavefence();
    kb = kimg + 8 * (4 * lp + 8 * g + lq);
    xb = XIMG + w * XS + 224 + 8 * (lp - 8 * g - lq);
    yw = YIMG + w * YS + 2 * lane;
  } else {
    const int bi = xcd * 20 + (idx - 10);
    const int ch0 = bi * 4;
    if (w >= 8) {
      const int h = w - 8;
      const int pcol = bi >> 4, pchunk = bi & 15;
      __builtin_amdgcn_s_setprio(3);
      unsigned long long tag = 0;
      if (h == 0) tag = __hip_atomic_load(gepoch, RLX_AGENT) + 1;
      if (h < 2) touch64(prefetch_addr(P, h, idx, xcd, lane), smb + PFD);
      wgbarrier();
      delay(PFP);
      {
        const u32 po = (u32)(512 + 96 * pchunk + 24 * h + (lane >> 3)) * ROWB + (u32)pcol * 128 + 16 * (lane & 7);
#pragma unroll
        for (int j = 0; j < 3; ++j) {
          dma16(P.k, po + j * 8 * ROWB, smb + PRAWK + 512 + h * 3072 + j * 1024);
          dma16(P.v, po + j * 8 * ROWB, smb + PRAWV + h * 3072 + j * 1024);
          dma16(P.q, po + j * 8 * ROWB, smb + PRAWQ + h * 3072 + j * 1024);
        }
        if (h == 0 && lane < 32) dma16(P.k, po - 4 * ROWB, smb + PRAWK);
        if (h == 1 && lane < 32) dma16(P.shift_k, (u32)pcol * 512 + 16 * lane, smb + SHK);
      }
      {
        const u32x4 ones = {0x3F803F80u, 0x3F803F80u, 0x3F803F80u, 0x3F803F80u};
#pragma unroll
        for (int i = 0; i < 4; ++i) *(LDSP u32x4*)(sm + B_YIMG + h * YS + 1024 * i + 16 * lane) = ones;
      }
      if (lane < 10) *(LDSP u32x4*)(sm + XIMG + h * XS + 16 * lane) = u32x4{0, 0, 0, 0};
      delay(PFU - PFP);
      {
        const int cu = (h < 2) ? 2 + h : 2 + ((idx - 10) & 1);
        const int su = (h < 2) ? idx : (idx - 10) >> 1;
        touch64(prefetch_addr(P, cu, su, xcd, lane), smb + PFD);
      }
      vmcnt<1>();
      set_word(sm + FLAG2 + 4 * h, 1);
      wait_words(sm + FLAG2, lane, 4, 1);
      STAMP(12);
      {
        const u32x2 sw = *(LDSP u32x2*)(sm + SHK + 8 * (16 * h + li));
        const float w0 = bflo(sw.x), w1 = bfhi(sw.x), w2 = bflo(sw.y), w3 = bfhi(sw.y);
        const u32 co = 32 * h + 8 * lp;
        const u32 stg = PSTG + h * XS;
        const u32 sw_ = stg + li * 256 + 8 * (g & 1);
        const auto rx = __builtin_amdgcn_make_buffer_rsrc(P.ex + WS_EXX, 0, 4u << 20, 0x00020000);
        u32 sr_[3], eo[3];
#pragma unroll
        for (int j = 0; j < 3; ++j) {
          const int e = lane + 64 * j, c = e / 12, pc = e % 12;
          sr_[j] = stg + c * 256 + 16 * ((pc ^ c) & 15);
          eo[j] = (u32)(pcol * 64 + 16 * h + c) * 3072 + 192 * pchunk + 16 * pc;
        }
        const u32 zst = (lq & 1) ? 256u : 0u;
        const u32 ro = 128 * (4 * g + (lq >> 1)) + co;
        const u32 ka = (lq & 1) ? PRAWK + ro : (u32)ZROW, va = (lq & 1) ? PRAWV + ro : (u32)ZROW;
#pragma unroll
        for (int u = 0; u < 6; ++u)
          *(LDSP u32x2*)(sm + sw_ + 16 * (((2 * u + (g >> 1)) ^ li) & 15)) = trread(sm + PRAWQ + 128 * (16 * u + 4 * g + lq) + co);
        wavefence();
#pragma unroll
        for (int j = 0; j < 3; ++j)
          __builtin_amdgcn_raw_buffer_store_b128(*(const LDSP u32x4*)(sm + sr_[j]), rx, eo[j] + (u32)(WS_EXQ - WS_EXX), 0, 16);
        wavefence();
#pragma unroll 2
        for (int u = 0; u < 6; ++u) {
          float kx[8], vx[4], kv[4];
#pragma unroll
          for (int j = 0; j < 4; ++j) { u32x2 r = trread(sm + ka + zst * (8 * u + j)); kx[2 * j] = __uint_as_float(r.x); kx[2 * j + 1] = __uint_as_float(r.y); }
#pragma unroll
          for (int j = 0; j < 2; ++j) { u32x2 r = trread(sm + va + zst * (8 * u + j)); vx[2 * j] = __uint_as_float(r.x); vx[2 * j + 1] = __uint_as_float(r.y); }
#pragma unroll
          for (int j = 0; j < 4; ++j)
            kv[j] = vmul(vfma(kx[j + 4], w0, vfma(kx[j + 3], w1, vfma(kx[j + 2], w2, vmul(kx[j + 1], w3)))), vx[j]);
          *(LDSP u32x2*)(sm + sw_ + 16 * (((2 * u + (g >> 1)) ^ li) & 15)) = u32x2{pkbf(kv[0], kv[1]), pkbf(kv[2], kv[3])};
        }
        set_word(sm + FLAG2 + 4 * h, 2);
        wavefence();
#pragma unroll
        for (int j = 0; j < 3; ++j)
          __builtin_amdgcn_raw_buffer_store_b128(*(const LDSP u32x4*)(sm + sr_[j]), rx, eo[j], 0, 16);
      }
      STAMP(1);
      const u32 rowoff = (u32)(64 * h + lane) * ROWB + (u32)(ch0 & ~7) * 2;
      wait_words(sm + FLAG2, lane, 4, 2);
      dma16(P.u, rowoff, smb + RAWU + 1024 * h);
      dma16(P.u, rowoff + 256 * ROWB, smb + RAWU + 4096 + 1024 * h);
      vmcnt<2>();
      set_word(sm + FLAG2 + 16 + 4 * h, 1);
      wait_words(sm + FLAG2 + 16, lane, 4, 1);
      if (h == 0 && lane == 0) __hip_atomic_store(gflag + bi, tag, RLX_AGENT);
      STAMP(9);
      const u32 ua = RAWU + 1024 * h + 16 * (16 * g + 4 * lp + lq) + 2 * (ch0 & 4);
      const u32 xw = XIMG + (lane & 3) * XS + XPADB + 8 * (lane >> 2) + 128 * h;
      const u32 yrd = B_YIMG + lq * YS + 2 * (64 * h + 16 * g + 4 * lp);
      u16* const outp = P.out + DCH + ch0 + (size_t)(64 * h + lane) * (2 * DCH);
#pragma unroll 1
      for (int it = 0; it < 8; ++it) {
        int i = it;
        asm volatile("" : "+s"(i));
        if (i + 2 < 8) dma16(P.u, rowoff + (u32)(i + 2) * (256 * ROWB), smb + RAWU + 4096 * (i + 2) + 1024 * h);
        if (i < 6) vmcnt<2>(); else if (i == 6) vmcnt<1>(); else vmcnt<0>();
        *(LDSP u32x2*)(sm + xw + 512 * i) = trread(sm + ua + 4096 * i);
        set_word(sm + FLAGS + 4 * h, (u32)(i + 1));
        if (i == 0) STAMP(13); if (i == 7) STAMP(14);
      }
#pragma unroll 1
      for (int i = 0; i < 8; ++i) {
        wait_words(sm + FLAGS + 16, lane, 4, (u32)(i + 1));
        *(u32x2*)(outp + (size_t)i * 256 * (2 * DCH)) = trread(sm + yrd + 512 * i);
      }
      STAMP_OUT();
      asm volatile(".Lfk_end4:");
      __builtin_amdgcn_endpgm();
      __builtin_unreachable();
    }
    const int cw = w & 3, ps = w >> 2;
    const int ch = ch0 + cw;
    const size_t o = (size_t)ch * NST + 64 * ps + lane;
    const u16 i_ar = P.A_re[o], i_ai = P.A_im[o], i_br = P.B_re[o], i_bi = P.B_im[o], i_cr = P.C_re[o], i_ci = P.C_im[o];
    const float ldt = ld_bf_uniform(P.log_dt, ch);
    const float dsk = ld_bf_uniform(P.Dskip, ch);
    wgbarrier();
    const float dt = rbf(expf(ldt));
    const float zr = dt * bf2f(i_ar), zi = dt * bf2f(i_ai);
    const cf dB = {dt * bf2f(i_br), dt * bf2f(i_bi)};
    const cf C = {bf2f(i_cr), bf2f(i_ci)};
    const cf W = cmul(C, dB);
    float mx = fmaxf(fabsf(W.r), fabsf(W.i));
    STAMP(1);
    {
      int m = __float_as_int(mx);
      m = max(m, __builtin_amdgcn_update_dpp(0, m, 0x111, 0xf, 0xf, false));
      m = max(m, __builtin_amdgcn_update_dpp(0, m, 0x112, 0xf, 0xf, false));
      m = max(m, __builtin_amdgcn_update_dpp(0, m, 0x114, 0xf, 0xf, false));
      m = max(m, __builtin_amdgcn_update_dpp(0, m, 0x118, 0xf, 0xf, false));
      int a0 = __builtin_amdgcn_readlane(m, 15), a1 = __builtin_amdgcn_readlane(m, 31);
      int a2 = __builtin_amdgcn_readlane(m, 47), a3 = __builtin_amdgcn_readlane(m, 63);
      mx = __int_as_float(max(max(a0, a1), max(a2, a3)));
    }
    int ex = (int)((__float_as_uint(mx) >> 23) & 0xff);
    ex = min(max(ex, 30), 240);
    const float sY = __uint_as_float((u32)(267 - ex) << 23);
    const float so = __uint_as_float((u32)(ex - 27) << 23);
    const float SX = 16384.f;

    f32x4 ak[3];
    const u32 stg = STG + w * STS;
    const u32 rb = stg + li * 272 + g * 16;
    const u32 m0v = smb + w * STS;
#define STGW(row, val_r, val_i) \
    asm volatile("ds_write_addtid_b32 %0 offset:%1" :: "v"(pkh((val_r), (val_i))), "i"(STG + (row) * 272) : "memory")
    {
      const cf E1 = cexp_pow2(zr, zi, 1.f);
      const cf E8c = cexp_pow2(zr, -zi, 8.f);
      const cf E128 = cexp_pow2(zr, zi, 128.f);
      const cf E1024 = cexp_pow2(zr, zi, 1024.f);
      const cf E2 = cmul(E1, E1);
      const cf E4 = cmul(E2, E2);
      u32 keep;
      asm volatile("s_mov_b32 %0, m0\n\ts_mov_b32 m0, %1\n\ts_nop 0" : "=&s"(keep) : "s"(m0v) : "memory");
      cf y = {W.r * sY, -(W.i * sY)};
      cf P1 = {E128.r * SX, E128.i * SX};
      cf v0 = {SX, 0.f}, v1 = P1;
#pragma unroll
      for (int b = 0; b < 8; ++b) {
        STGW(2 * b, y.r, y.i); y = cmul(y, E8c);
        STGW(16 + b, v0.r, v0.i); if (b < 7) v0 = cmul(v0, E1);
        STGW(2 * b + 1, y.r, y.i); if (b < 7) y = cmul(y, E8c);
        STGW(24 + b, v1.r, v1.i); if (b < 7) v1 = cmul(v1, E1);
      }
      f16x8 yf[4];
      const f32x4 zero = {0.f, 0.f, 0.f, 0.f};
#pragma unroll
      for (int s = 0; s < 4; ++s) {
        yf[s] = *(const LDSP f16x8*)(sm + rb + 64 * s);
        f16x8 xf = *(const LDSP f16x8*)(sm + rb + 16 * 272 + 64 * s);
        ak[0] = __builtin_amdgcn_mfma_f32_16x16x32_f16(xf, yf[s], s ? ak[0] : zero, 0, 0, 0);
      }
      asm volatile("" ::: "memory");
      cf Pa = cmul(P1, E128);
      cf Pb = {E1024.r * SX, E1024.i * SX};
#pragma unroll
      for (int m = 0; m < 2; ++m) {
        cf v = Pa;
#pragma unroll
        for (int r = 0; r < 4; ++r) {
          STGW(4 * m + r, v.r, v.i); if (r < 3) v = cmul(v, E2);
          if (r & 1) { STGW(16 + 2 * m + (r >> 1), Pb.r, Pb.i); Pb = cmul(Pb, E128); }
        }
        Pa = cmul(Pa, E128);
      }
#pragma unroll
      for (int m = 0; m < 4; ++m) {
        STGW(8 + 2 * m, Pa.r, Pa.i);
        cf v = cmul(Pa, E4);
        STGW(20 + m, Pb.r, Pb.i); Pb = cmul(Pb, E128);
        STGW(9 + 2 * m, v.r, v.i);
        if (m < 3) Pa = cmul(Pa, E128);
      }
      STGW(24, Pb.r, Pb.i);
      asm volatile("s_mov_b32 m0, %0" :: "s"(keep) : "memory");
#pragma unroll
      for (int s = 0; s < 4; ++s) {
        f16x8 x1 = *(const LDSP f16x8*)(sm + rb + 64 * s);
        f16x8 x2 = *(const LDSP f16x8*)(sm + rb + 16 * 272 + 64 * s);
        ak[1] = __builtin_amdgcn_mfma_f32_16x16x32_f16(x1, yf[s], s ? ak[1] : zero, 0, 0, 0);
        ak[2] = __builtin_amdgcn_mfma_f32_16x16x32_f16(x2, yf[s], s ? ak[2] : zero, 0, 0, 0);
      }
    }
#undef STGW
#pragma unroll
    for (int i = 0; i < 3; ++i) ak[i] *= so;
    STAMP(2);
    asm volatile("" ::: "memory");
    const u32 kimg = B_KIMG + cw * KS;
    if (ps) {
#pragma unroll
      for (int i = 0; i < 3; ++i) *(LDSP f32x4*)(sm + kimg + 1024 * i + 16 * lane) = ak[i];
      asm volatile("s_waitcnt lgkmcnt(0)" ::: "memory");
      *(volatile LDSP u32*)(sm + FLAGS + 48 + 4 * cw) = 1;
      STAMP_OUT();
      asm volatile(".Lfk_end5:");
      __builtin_amdgcn_endpgm();
      __builtin_unreachable();
    }
    wait_words(sm + FLAGS + 48 + 4 * cw, lane, 1, 1);
    {
      const u32 tb = stg;
#pragma unroll
      for (int i = 0; i < 3; ++i) ak[i] += *(const LDSP f32x4*)(sm + kimg + 1024 * i + 16 * lane);
      wavefence();
      const int b = li, p = g;
      if (lane < 6) *(LDSP u32x4*)(sm + kimg + 16 * lane) = u32x4{0, 0, 0, 0};
      {
        float a0 = ak[0][0] + (lane == 0 ? dsk : 0.f), a1 = ak[0][1], a2 = ak[0][2], a3 = ak[0][3];
        *(LDSP u32x2*)(sm + kimg + KPADB + 2 * (128 * (p >> 1) + 8 * b + 4 * (p & 1))) = u32x2{pkbf(a0, a1), pkbf(a2, a3)};
      }
      {
        float a0 = ak[1][0], a1 = ak[1][1], a2 = ak[1][2], a3 = ak[1][3];
        int i01, i23;
        if (p < 2) { i01 = 64 * p + 4 * b; i23 = i01 + 2; }
        else { i01 = 128 + 64 * (p - 2) + 2 * b; i23 = i01 + 32; }
        *(LDSP u32x2*)(sm + tb + 4 * i01) = u32x2{pkbf(a0, a0), pkbf(a1, a1)};
        *(LDSP u32x2*)(sm + tb + 4 * i23) = u32x2{pkbf(a2, a2), pkbf(a3, a3)};
#pragma unroll
        for (int j = 0; j < 4; ++j) {
          float a = ak[2][j];
          int ii = 256 + 16 * (4 * p + j) + b;
          *(LDSP u32*)(sm + tb + 4 * ii) = pkbf(a, a);
        }
      }
      wavefence();
      const LDSP u32* TB = (const LDSP u32*)(sm + tb);
      {
        const int i0 = 4 * (lane & 31);
        u32 g0 = TB[i0], g1 = TB[i0 + 1], g2 = TB[i0 + 2], g3 = TB[i0 + 3], g4 = TB[i0 + 4];
        u32x4 ov = {g0, (g1 & 0xffffu) | (g2 & 0xffff0000u), g2, (g3 & 0xffffu) | (g4 & 0xffff0000u)};
        *(LDSP u32x4*)(sm + kimg + KPADB + 16 * (32 + (lane & 31))) = ov;
      }
      {
        const int i0 = 128 + 2 * lane;
        u32 g0 = TB[i0], g1 = TB[i0 + 1], g2 = TB[i0 + 2];
        u32x4 ov = {g0, (g0 & 0xffffu) | (g1 & 0xffff0000u), g1, g2};
        *(LDSP u32x4*)(sm + kimg + KPADB + 16 * (64 + lane)) = ov;
      }
#pragma unroll
      for (int hh = 0; hh < 2; ++hh) {
        const int i0 = 256 + 64 * hh + lane;
        u32 g0 = TB[i0], g1 = TB[i0 + 1];
        u32 mid = (lane & 1) ? g1 : ((g0 & 0xffffu) | (g1 & 0xffff0000u));
        u32x4 ov = {g0, g0, mid, g1};
        *(LDSP u32x4*)(sm + kimg + KPADB + 16 * (128 + 64 * hh + lane)) = ov;
      }
    }
    wavefence();
    kb = kimg + 8 * (4 * lp + 8 * g + lq);
    xb = XIMG + cw * XS + 224 + 8 * (lp - 8 * g - lq);
    yw = B_YIMG + cw * YS + 2 * lane;
  }

  {
    const bool l63 = (lane == 63);
    const lptr pxp = sm + FLAGS + 4 * (lane & 3);
    const lptr pyp = sm + FLAGS + 16 + 4 * w;
    u32x2 kr[16][2], xr[40][4];
    f32x4 ac[16][2];
    const f32x4 zero = {0.f, 0.f, 0.f, 0.f};
    float ts[16][2], u2[16][2], u1[16][2], yy[16][2];
    u32 pk[16];
    u32 qv[3][2] = {{0, 0}, {0, 0}, {0, 0}}, px = 0;
    const lptr ywp = sm + yw;
    const u32 qa = (u32)(uintptr_t)ywp;
#define RDQ(s_, i_) asm volatile("ds_read_u16_d16_hi %0, %1 offset:%2" : "+v"(qv[(s_) % 3][i_]) : "v"(qa), "i"((s_) * 256 + 128 * (i_)) : "memory")
#define RDXP(l_, i_) xr[l_][i_] = trread(sm + xb + 256 * SC.ldm[l_] + ((i_) >> 1) * 128 + (((i_) & 1) ? 0 : 32))
#pragma unroll
    for (int d = 0; d < 8; ++d) { kr[d][0] = trread(sm + kb + 256 * d); kr[d][1] = trread(sm + kb + 256 * d + 32); }
    STAMP(3);
    wait_words(sm + FLAGS, lane, 4, 1);
    STAMP(4);
    CSTAMP(0);
    RDQ(0, 0); RDQ(0, 1);
    RDXP(0, 0); RDXP(0, 1); RDXP(0, 2); RDXP(0, 3);
    SCHED0();
    static_for<0, NGAP>([&](auto G_) {
      constexpr int G = decltype(G_)::value;
      if constexpr (G < 272) {
        constexpr int u = G >> 1, par = G & 1;
        constexpr int p = SC.p[u], d = SC.d[u], l = SC.ld[u];
        ac[p][par] = MFMA(frag(kr[d][0], kr[d][1]), frag(xr[l][2 * par], xr[l][2 * par + 1]), SC.first[u] ? zero : ac[p][par]);
        SCHED0();
      }
      static_for<0, AC.n[G]>([&](auto I_) {
        constexpr int code = AC.a[G][decltype(I_)::value];
        constexpr int kind = code & 15, a0 = (code >> 4) & 255, a1 = code >> 12;
        if constexpr (kind == A_X) RDXP(a0, a1);
        if constexpr (kind == A_K) kr[a0][a1] = trread(sm + kb + 256 * a0 + 32 * a1);
        if constexpr (kind == A_Q) RDQ(a0, a1);
        if constexpr (kind == A_PXR) px = *(volatile LDSP u32*)pxp;
        if constexpr (kind == A_RR) vmcnt<0>();
        if constexpr (kind == A_PXC) {
          if (__builtin_expect(!__all(px >= (u32)(a0 / 2 + 1)), 0)) wait_words(sm + FLAGS, lane, 4, (u32)(a0 / 2 + 1));
          asm volatile("" ::: "memory");
        }
        if constexpr (kind == A_EP) {
          constexpr int s = a0, e = a1 >> 1, o = a1 & 1;
          if constexpr (a1 < 14) {
            if constexpr (e == 0) {
              float cur = ac[s][o][3], prev = 0.f;
              if constexpr (o) prev = ac[s][0][3]; else if constexpr (s > 0) prev = ac[s - 1][1][3];
              ts[s][o] = l63 ? prev : cur;
            }
            if constexpr (e == 1) { float a = ac[s][o][2]; u2[s][o] = a + ror1(ts[s][o]); }
            if constexpr (e == 2) {
              float prev = 0.f;
              if constexpr (o) prev = u2[s][0]; else if constexpr (s > 0) prev = u2[s - 1][1];
              ts[s][o] = l63 ? prev : u2[s][o];
            }
            if constexpr (e == 3) { float a = ac[s][o][1]; u1[s][o] = a + ror1(ts[s][o]); }
            if constexpr (e == 4) {
              float prev = 0.f;
              if constexpr (o) prev = u1[s][0]; else if constexpr (s > 0) prev = u1[s - 1][1];
              ts[s][o] = l63 ? prev : u1[s][o];
            }
            if constexpr (e == 5) { float a = ac[s][o][0]; yy[s][o] = a + ror1(ts[s][o]); asm volatile("" : "+v"(yy[s][o])); }
            if constexpr (e == 6) {
              asm volatile("" : "+v"(qv[s % 3][o]));
              yy[s][o] *= __uint_as_float(qv[s % 3][o]);
              asm volatile("" : "+v"(yy[s][o]));
            }
          }
          if constexpr (a1 == 14) pk[s] = pkbf(yy[s][0], yy[s][1]);
          if constexpr (a1 == 15) *(LDSP u16*)(ywp + s * 256) = (u16)pk[s];
          if constexpr (a1 == 16) *(LDSP u16*)(ywp + s * 256 + 128) = (u16)(pk[s] >> 16);
          if constexpr (a1 == 17) {
            asm volatile("" ::: "memory");
            *(volatile LDSP u32*)pyp = (u32)(s / 2 + 1);
            asm volatile("" ::: "memory");
          }
        }
        SCHED0();
      });
    });
  }
  STAMP_OUT();
  asm volatile(".Lfk_end0:");
  __builtin_amdgcn_endpgm();
}

extern "C" void kernel_launch(void* const* d_in, const int* in_sizes, int n_in,
                              void* d_out, int out_size, void* d_ws, size_t ws_size,
                              hipStream_t stream) {
  Params P;
  P.q = (const u16*)d_in[0];
  P.k = (const u16*)d_in[1];
  P.v = (const u16*)d_in[2];
  P.shift_k = (const u16*)d_in[3];
  P.ssm_k = (const u16*)d_in[4];
  P.ssm_D = (const u16*)d_in[5];
  P.u = (const u16*)d_in[6];
  P.A_re = (const u16*)d_in[7];
  P.A_im = (const u16*)d_in[8];
  P.B_re = (const u16*)d_in[9];
  P.B_im = (const u16*)d_in[10];
  P.C_re = (const u16*)d_in[11];
  P.C_im = (const u16*)d_in[12];
  P.log_dt = (const u16*)d_in[13];
  P.Dskip = (const u16*)d_in[14];
  P.out = (u16*)d_out;
  P.ws = (unsigned long long*)d_ws;
  P.ex = (char*)d_ws + (16 << 20);
  (void)hipFuncSetAttribute((const void*)fused_kernel, hipFuncAttributeMaxDynamicSharedMemorySize, LDS_BYTES);
  fused_kernel<<<256, 768, LDS_BYTES, stream>>>(P);
}
```

```cpp
#include <hip/hip_runtime.h>
#include <hip/hip_bf16.h>
#include <stdint.h>

typedef __attribute__((ext_vector_type(4))) short s16x4;
typedef __attribute__((ext_vector_type(8))) _Float16 f16x8;
typedef __attribute__((ext_vector_type(8))) __bf16 bf16x8;
typedef __attribute__((ext_vector_type(2))) __bf16 bf16x2;
typedef __attribute__((ext_vector_type(2))) _Float16 f16x2;
typedef __attribute__((ext_vector_type(2))) float f32x2;
typedef __attribute__((ext_vector_type(4))) float f32x4;
typedef __attribute__((ext_vector_type(2))) unsigned int u32x2;
typedef __attribute__((ext_vector_type(4))) unsigned int u32x4;
typedef unsigned int u32;
typedef unsigned short u16;
#define LDSP __attribute__((address_space(3)))
#define DEV __device__ __forceinline__

constexpr int SEQ = 2048;
constexpr int DCH = 640;
constexpr int NST = 128;
constexpr int ROWB = DCH * 2;

constexpr int XS = 4272;
constexpr int XPADB = 160;
constexpr int YS = 4160;
constexpr int KS = 4224;
constexpr int KPADB = 96;
constexpr int FLAGS = 0;
constexpr int ZROW = 64;
constexpr int FLAG2 = 192;
constexpr int PFD = 256;
constexpr int SHK = 512;
constexpr int XIMG = 1024;
constexpr int YIMG = 35200;
constexpr int KIMG = 68480;
constexpr int RAWK = 102272;
constexpr int RAWV = 119680;
constexpr int RAWQ = 136064;
constexpr int STS = 8704;
constexpr int STG = 18176;
constexpr int B_KIMG = 87808;
constexpr int B_YIMG = 104704;
constexpr int RAWU = 121344;
constexpr int PRAWK = RAWU + 16384;
constexpr int PRAWV = PRAWK + 8704;
constexpr int PRAWQ = PRAWV + 8192;
constexpr int PSTG = XIMG + 2208;
constexpr int LDS_BYTES = 162816;

constexpr size_t WS_EPOCH = 0;
constexpr size_t WS_FLAG = 4096;
constexpr size_t WS_EXX = 1 << 20;
constexpr size_t WS_EXQ = 3 << 20;
constexpr size_t WS_STAMP = 8 << 20;

DEV float bf2f(u16 v) { return __uint_as_float(((u32)v) << 16); }
DEV float bflo(u32 v) { return __uint_as_float(v << 16); }
DEV float bfhi(u32 v) { return __uint_as_float(v & 0xffff0000u); }
DEV u32 pkbf(float a, float b) {
  f32x2 t = {a, b};
  bf16x2 r = __builtin_convertvector(t, bf16x2);
  return __builtin_bit_cast(u32, r);
}
DEV u32 pkh(float a, float b) {
  f32x2 t = {a, b};
  f16x2 r = __builtin_convertvector(t, f16x2);
  return __builtin_bit_cast(u32, r);
}
DEV float rbf(float a) { return bflo(pkbf(a, a)); }
DEV float dot2(u32 a, u32 b, float c) {
  return __builtin_amdgcn_fdot2_f32_bf16(__builtin_bit_cast(bf16x2, a), __builtin_bit_cast(bf16x2, b), c, false);
}
DEV float vfma(float a, float b, float c) { float d; asm("v_fma_f32 %0, %1, %2, %3" : "=v"(d) : "v"(a), "v"(b), "v"(c)); return d; }
DEV float vmul(float a, float b) { float d; asm("v_mul_f32 %0, %1, %2" : "=v"(d) : "v"(a), "v"(b)); return d; }
typedef __attribute__((address_space(1))) unsigned long long gu64;
#define RLX_AGENT __ATOMIC_RELAXED, __HIP_MEMORY_SCOPE_AGENT
typedef LDSP char* lptr;
DEV u32x2 trread(lptr p) {
  s16x4 r = __builtin_amdgcn_ds_read_tr16_b64_v4i16((LDSP s16x4*)p);
  return __builtin_bit_cast(u32x2, r);
}
DEV bf16x8 frag(u32x2 a, u32x2 b) {
  u32x4 f = {a.x, a.y, b.x, b.y};
  return __builtin_bit_cast(bf16x8, f);
}
DEV float ror1(float v) {
  int r = __builtin_amdgcn_update_dpp(0, __float_as_int(v), 0x13C, 0xf, 0xf, false);
  return __int_as_float(r);
}
DEV void wavefence() { __builtin_amdgcn_fence(__ATOMIC_ACQ_REL, "wavefront"); __builtin_amdgcn_wave_barrier(); }
DEV void wgbarrier() {
  asm volatile("s_waitcnt lgkmcnt(0)" ::: "memory");
  __builtin_amdgcn_s_barrier();
  asm volatile("" ::: "memory");
}
DEV void dma16(const void* base, u32 voff, u32 ldsaddr) {
  u32 keep;
  ldsaddr = __builtin_amdgcn_readfirstlane(ldsaddr);
  const unsigned long long b64 = (unsigned long long)(uintptr_t)base;
  const unsigned long long sb = ((unsigned long long)(u32)__builtin_amdgcn_readfirstlane((u32)(b64 >> 32)) << 32) |
                                (u32)__builtin_amdgcn_readfirstlane((u32)b64);
  asm volatile("s_mov_b32 %0, m0\n\ts_mov_b32 m0, %3\n\ts_nop 2\n\tglobal_load_lds_dwordx4 %1, %2\n\ts_mov_b32 m0, %0"
               : "=&s"(keep) : "v"(voff), "s"(sb), "s"(ldsaddr) : "memory");
}
DEV void touch64(const void* p, u32 ldsaddr) {
  u32 keep;
  asm volatile("s_mov_b32 %0, m0\n\ts_mov_b32 m0, %2\n\ts_nop 0\n\tglobal_load_lds_dword %1, off\n\ts_mov_b32 m0, %0"
               : "=&s"(keep) : "v"(p), "s"(ldsaddr) : "memory");
}
template <int N> DEV void vmcnt() { asm volatile("s_waitcnt vmcnt(%0)" :: "i"(N) : "memory"); }
#define SCHED0() __builtin_amdgcn_sched_barrier(0)

template <int V> struct IC { static constexpr int value = V; };
template <int I, int N, class F> DEV void static_for(F&& f) {
  if constexpr (I < N) { f(IC<I>{}); static_for<I + 1, N>(f); }
}

struct cf { float r, i; };
DEV cf cmul(cf a, cf b) {
  cf o;
  o.r = fmaf(a.r, b.r, -(a.i * b.i));
  o.i = fmaf(a.r, b.i, a.i * b.r);
  return o;
}
DEV cf cexp_pow2(float zr, float zi, float x) {
  float mag = __builtin_amdgcn_exp2f((zr * x) * 1.44269504088896340736f);
  float th = zi * x;
  const float C_HI = 0.15915494f;
  const float C_LO = 6.4206382e-09f;
  float f = fmaf(th, C_LO, fmaf(th, C_HI, -rintf(th * C_HI)));
  cf o;
  o.r = mag * __builtin_amdgcn_cosf(f);
  o.i = mag * __builtin_amdgcn_sinf(f);
  return o;
}

#define STAMP(n)
#define CSTAMP(n)
#define STAMP_OUT()

struct Params {
  const u16 *q, *k, *v, *shift_k, *ssm_k, *ssm_D;
  const u16 *u, *A_re, *A_im, *B_re, *B_im, *C_re, *C_im, *log_dt, *Dskip;
  u16* out;
  unsigned long long* ws;
  char* ex;
};

DEV float ld_bf_uniform(const u16* p, int idx) {
  u32 d = *(const __attribute__((address_space(4))) u32*)(uintptr_t)(p + (idx & ~1));
  return (idx & 1) ? bfhi(d) : bflo(d);
}

DEV const char* prefetch_addr(const Params& P, int cls, int slice, int xcd, int lane) {
  asm volatile("" : "+v"(lane));
  const unsigned long long TAB = 0xFEDC932810ull;
  const u32 nib = (u32)(TAB >> (4 * (2 * cls + (lane >> 5)))) & 15u;
  const bool second = (lane >> 4) & 1;
  const u16* base = (nib < 8) ? (second ? P.v : P.k) : (nib < 12 ? P.q : P.u);
  const u32 blk = (nib < 8) ? nib : 2 * (nib & 3) + (second ? 1 : 0);
  const u32 row = blk * 256u + (u32)slice * 8u + ((lane >> 1) & 7);
  return (const char*)base + (row * ROWB + (u32)(((160 * xcd) >> 7) + (lane & 1)) * 128u);
}
DEV void wait_words(lptr p, int lane, int n, u32 v) {
#pragma unroll 1
  for (int spin = 0; spin < (1 << 16); ++spin) {
    u32 x = *(volatile LDSP u32*)(p + 4 * (lane & (n - 1)));
    if (__all(x >= v)) break;
    __builtin_amdgcn_s_sleep(1);
  }
  asm volatile("" ::: "memory");
}
#define PFS 3
#define PFP 3
#define PFU 8
#define PFH 4
DEV void delay(int n) {
  for (int i = 0; i < n; ++i) __builtin_amdgcn_s_sleep(PFS);
}

template <int RS>
DEV u32x2 kv4(lptr sm, u32 ka, u32 va, float w0, float w1, float w2, float w3) {
  float kx[8], vx[4], kv[4];
#pragma unroll
  for (int j = 0; j < 4; ++j) { u32x2 r = trread(sm + ka + RS * j); kx[2 * j] = __uint_as_float(r.x); kx[2 * j + 1] = __uint_as_float(r.y); }
#pragma unroll
  for (int j = 0; j < 2; ++j) { u32x2 r = trread(sm + va + RS * j); vx[2 * j] = __uint_as_float(r.x); vx[2 * j + 1] = __uint_as_float(r.y); }
#pragma unroll
  for (int j = 0; j < 4; ++j)
    kv[j] = vmul(vfma(kx[j + 4], w0, vfma(kx[j + 3], w1, vfma(kx[j + 2], w2, vmul(kx[j + 1], w3)))), vx[j]);
  return u32x2{pkbf(kv[0], kv[1]), pkbf(kv[2], kv[3])};
}
DEV void set_word(lptr p, u32 v) {
  asm volatile("" ::: "memory");
  *(volatile LDSP u32*)p = v;
  asm volatile("" ::: "memory");
}

struct Sched {
  int p[136], d[136], ld[136];
  bool first[136];
  int base[17];
  int ldm[40], ldu[40];
  int ku[16];
  int nld;
};
constexpr Sched make_sched() {
  Sched S{};
  int n = 0, nl = 16;
  for (int m = 0; m < 16; ++m) S.ldm[m] = m;
  for (int d = 0; d < 16; ++d) S.ku[d] = -1;
  for (int s = 0; s < 16; ++s) {
    S.base[s] = n;
    S.ldu[s] = n;
    for (int k = 0; k < 8 && s + k < 16; ++k) { S.p[n] = s + k; S.d[n] = k; S.ld[n] = s; S.first[n] = (s == 0) || (k == 7); ++n; }
    int lo = -1, hi = -1, cnt = 0, half = 0;
    if (s == 6) { lo = 8; hi = 8; cnt = 1; half = 2; }
    else if (s >= 7 && s <= 12) { lo = (s & 1) ? s + 2 : s + 1; hi = lo + 1; cnt = hi - 7; half = (s & 1) ? 0 : 1; }
    else if (s == 13 || s == 14) { lo = 15; hi = 15; cnt = 8; half = (s == 13) ? 0 : 1; }
    if (cnt) {
      const int split = (cnt + 1) / 2;
      const int j0 = (half == 1) ? split : 0, j1 = (half == 0) ? split : cnt;
      for (int j = j0; j < j1; ++j) {
        const int mm = cnt - 1 - j;
        S.ldm[nl] = mm; S.ldu[nl] = n;
        for (int t = hi; t >= lo; --t)
          if (t - mm >= 8) { S.p[n] = t; S.d[n] = t - mm; S.ld[n] = nl; S.first[n] = false; ++n; }
        ++nl;
      }
    }
  }
  S.base[16] = n;
  S.nld = nl;
  for (int u = 0; u < n; ++u) if (S.ku[S.d[u]] < 0) S.ku[S.d[u]] = u;
  return S;
}
constexpr Sched SC = make_sched();
static_assert(SC.base[16] == 136, "schedule");
constexpr int NGAP = 272 + 24, CAP = 2;
enum { A_NONE = 0, A_X, A_K, A_Q, A_PXR, A_PXC, A_EP, A_DMA, A_RR };
struct Acts { int n[NGAP]; int a[NGAP][4]; };
constexpr int place_back(Acts& T, int latest, int code, int cap = CAP) {
  int g = latest;
  while (g > 0 && T.n[g] >= cap) --g;
  T.a[g][T.n[g]++] = code;
  return g;
}
constexpr int place_fwd(Acts& T, int earliest, int code, int cap) {
  int g = earliest;
  while (T.n[g] >= (g < 272 ? cap : 4)) ++g;
  T.a[g][T.n[g]++] = code;
  return g;
}
constexpr Acts make_acts() {
  Acts T{};
  for (int l = 15; l >= 1; --l) {
    int g = 2 * SC.ldu[l] - 5;
    for (int i = 3; i >= 0; --i) g = place_back(T, g - 1, A_X | (l << 4) | (i << 12), 1);
    g = place_back(T, g - 1, A_Q | (l << 4) | (1 << 12), 1);
    g = place_back(T, g - 1, A_Q | (l << 4), 1);
    if (!(l & 1)) {
      g = place_back(T, g - 1, A_PXC | (l << 4), 1);
      place_back(T, g - 6, A_PXR, 1);
    }
  }
  for (int l = SC.nld - 1; l >= 16; --l) {
    int g = 2 * SC.ldu[l] - 5;
    for (int i = 3; i >= 0; --i) g = place_back(T, g - 1, A_X | (l << 4) | (i << 12), SC.ldu[l] >= 100 ? 2 : 1);
  }
  for (int d = 15; d >= 8; --d) {
    int g = 2 * SC.ku[d] - 5;
    g = place_back(T, g - 1, A_K | (d << 4) | (1 << 12), 2);
    g = place_back(T, g - 1, A_K | (d << 4), 2);
  }
  place_fwd(T, 6, A_RR | (1 << 4), 3);
  for (int s = 0; s < 16; ++s) {
    int g = 2 * SC.base[s] + 4;
    const int nop = (s & 1) ? 18 : 17;
    for (int e = 0; e < nop; ++e) g = place_fwd(T, g + ((e & 1) && e < 14 ? 0 : 1), A_EP | (s << 4) | (e << 12), s >= 11 ? 3 : CAP);
  }
  return T;
}
constexpr Acts AC = make_acts();

#define MFMA(a, b, c) __builtin_amdgcn_mfma_f32_16x16x32_bf16((a), (b), (c), 0, 0, 0)

__global__ __launch_bounds__(768, 3) void fused_kernel(Params P) {
  extern __shared__ __attribute__((aligned(16))) char smem[];
  const lptr sm = (lptr)smem;
  const u32 smb = (u32)(uintptr_t)sm;
  const int tid = threadIdx.x;
  const int w = __builtin_amdgcn_readfirstlane(tid >> 6);
  const int lane = tid & 63;
  const int wg = blockIdx.x;
  const int xcd = wg & 7, idx = wg >> 3;
  const bool partA = idx < 10;
  const int g = lane >> 4, li = lane & 15, lq = li >> 2, lp = li & 3;
  STAMP(0);
  if (w == 7) {
    unsigned long long pc;
    u32 len;
    asm volatile("s_getpc_b64 %0\n.Lfk_code_begin:\n\ts_mov_b32 %1, .Lfk_end0-.Lfk_code_begin\n\ts_max_i32 %1, %1, .Lfk_end1-.Lfk_code_begin\n\ts_max_i32 %1, %1, .Lfk_end2-.Lfk_code_begin\n\ts_max_i32 %1, %1, .Lfk_end3-.Lfk_code_begin\n\ts_max_i32 %1, %1, .Lfk_end4-.Lfk_code_begin\n\ts_max_i32 %1, %1, .Lfk_end5-.Lfk_code_begin" : "=s"(pc), "=&s"(len) :: "scc");
    const u32 off = min((u32)(lane + 64 * (idx % 3)) * 128u, len & ~3u);
    touch64((const char*)pc + off, smb + PFD);
  }
  gu64* const gflag = (gu64*)(uintptr_t)(P.ex + WS_FLAG);
  gu64* const gepoch = (gu64*)(uintptr_t)(P.ex + WS_EPOCH);
  if (idx >= 30) {
    if (w < 5) { if (w >= 3) delay(PFU); touch64(prefetch_addr(P, w, idx, xcd, lane), smb + PFD); }
    if (w == 8 && wg == 240) {
      const unsigned long long tag = __hip_atomic_load(gepoch, RLX_AGENT) + 1;
#pragma unroll 1
      for (int spin = 0; spin < (1 << 16); ++spin) {
        bool ok = true;
#pragma unroll
        for (int j = 0; j < 4; ++j) { const int f = min(lane + 64 * j, 239); ok &= (__hip_atomic_load(gflag + 16 * f, RLX_AGENT) == tag); }
        if (__all(ok)) break;
        __builtin_amdgcn_s_sleep(64);
      }
      if (lane == 0) __hip_atomic_store(gepoch, tag, RLX_AGENT);
    }
    vmcnt<0>();
    asm volatile(".Lfk_end1:");
    __builtin_amdgcn_endpgm();
    __builtin_unreachable();
  }
  if (tid < 64) *(LDSP u32*)(sm + FLAGS + 4 * tid) = 0;

  u32 kb, xb, yw;
  auto load_chunk = [&](int ck) {
    int ln = lane;
    asm volatile("" : "+v"(ln));
    const u32 ro = (u32)(64 * ck + ln) * ROWB + (u32)(xcd * 10 + idx) * 16;
    dma16(P.k, ro, smb + RAWK + ck * 1088 + 64);
    dma16(P.v, ro, smb + RAWV + ck * 1024);
    if (ln < 4) dma16(P.k, ck == 0 ? ro : ro - 4 * ROWB, ck == 0 ? smb + PFD : smb + RAWK + ck * 1088);
    dma16(P.q, ro, smb + RAWQ + ck * 1024);
  };

  if (partA) {
    const int grp = xcd * 10 + idx;
    const int ch0 = grp * 8;
    if (w >= 8) {
      const int h = w - 8;
      unsigned long long tag = 0;
      if (h == 0) {
        tag = __hip_atomic_load(gepoch, RLX_AGENT) + 1;
        if (lane < 4) dma16(P.shift_k, (u32)ch0 * 8 + 16 * lane, smb + SHK);
        if (lane < 4) *(LDSP u32x4*)(sm + RAWK + 16 * lane) = u32x4{0, 0, 0, 0};
        vmcnt<0>();
      }
      const int srow = 8 * g + 4 * (lp >> 1);
      const int shalf = 8 * (lp & 1);
      const u32 zsel = (lq & 1) ? 0xffffffffu : 0u;
      const u32 ka0 = (lq & 1) ? RAWK + h * 1088 + 16 * (srow + (lq >> 1)) + shalf : (u32)ZROW;
      const u32 va0 = (lq & 1) ? RAWV + h * 1024 + 16 * (srow + (lq >> 1)) + shalf : (u32)ZROW;
      const u32 qa0 = RAWQ + h * 1024 + 16 * (srow + lq) + shalf;
      const u32 xw = XIMG + (lane & 7) * XS + XPADB + 8 * (lane >> 3) + 128 * h;
      const u32 qw = YIMG + (lane & 7) * YS + 8 * (lane >> 3) + 128 * h;
      const u32 yrd = YIMG + lq * YS + 2 * (64 * h + 16 * g + 4 * lp);
      u16* const outp = P.out + ch0 + (size_t)(64 * h + lane) * (2 * DCH);
      wgbarrier();
      if (h == 0 && lane == 0) __hip_atomic_store(gflag + 16 * (160 + grp), tag, RLX_AGENT);
      __builtin_amdgcn_s_setprio(3);
      const u32x2 sw = *(LDSP u32x2*)(sm + SHK + 8 * (lane & 7));
      const float w0 = bflo(sw.x), w1 = bfhi(sw.x), w2 = bflo(sw.y), w3 = bfhi(sw.y);
      delay(PFH);
      load_chunk(8 + h);
#pragma unroll 1
      for (int it = 0; it < 4; ++it) {
        int b = it;
        asm volatile("" : "+s"(b));
        if (b < 2) wait_words(sm + FLAG2 + 4 * (4 * b + h), lane, 1, 1);
        else if (b == 2) vmcnt<4>(); else vmcnt<0>();
        u32x2 o[2], qq[2];
#pragma unroll
        for (int u = 0; u < 2; ++u) qq[u] = trread(sm + qa0 + b * 4096 + u * 512);
#pragma unroll
        for (int u = 0; u < 2; ++u)
          o[u] = kv4<32>(sm, ka0 + ((b * 4352 + u * 512) & zsel), va0 + ((b * 4096 + u * 512) & zsel), w0, w1, w2, w3);
#pragma unroll
        for (int u = 0; u < 2; ++u) {
          *(LDSP u32x2*)(sm + qw + 512 * b + 64 * u) = qq[u];
          *(LDSP u32x2*)(sm + xw + 512 * b + 64 * u) = o[u];
        }
        set_word(sm + FLAGS + 4 * h, (u32)(b + 1));
        if (b == 0) load_chunk(12 + h);
        if (b == 0) STAMP(12); if (b == 3) STAMP(13);
      }
      if (h == 0) {
#pragma unroll 1
        for (int spin = 0; spin < (1 << 16); ++spin) {
          if (__all(__hip_atomic_load(gflag + 16 * ((grp >> 3) * 16 + (lane & 15)), RLX_AGENT) == tag)) break;
          __builtin_amdgcn_s_sleep(8);
        }
        set_word(sm + FLAGS + 48, 1);
      } else {
        wait_words(sm + FLAGS + 48, lane, 1, 1);
      }
      __builtin_amdgcn_fence(__ATOMIC_ACQUIRE, "wavefront");
      STAMP(9);
      {
        const auto rx = __builtin_amdgcn_make_buffer_rsrc(P.ex + WS_EXX, 0, 4u << 20, 0x00020000);
        u32x4 d[8];
#pragma unroll
        for (int j = 0; j < 8; ++j)
          d[j] = __builtin_amdgcn_raw_buffer_load_b128(rx, (u32)(ch0 + 2 * h + (j & 1)) * 2048 + 1024 * (j >> 2) + 16 * lane + ((j >> 1) & 1) * (u32)(WS_EXQ - WS_EXX), 0, 16);
#pragma unroll
        for (int j = 0; j < 8; ++j) {
          const int cl = 2 * h + (j & 1);
          const u32 da = ((j >> 1) & 1) ? YIMG + cl * YS : XIMG + cl * XS + XPADB;
          *(LDSP u32x4*)(sm + da + 2048 + 1024 * (j >> 2) + 16 * lane) = d[j];
          if (j == 3) set_word(sm + FLAGS + 4 * h, 6);
        }
        set_word(sm + FLAGS + 4 * h, 8);
      }
      STAMP(14);
#pragma unroll 1
      for (int b = 0; b < 8; ++b) {
        wait_words(sm + FLAGS + 16, lane, 8, (u32)(b + 1));
        u32x2 a = trread(sm + yrd + 512 * b);
        u32x2 c = trread(sm + yrd + 512 * b + 4 * YS);
        *(u32x4*)(outp + (size_t)b * 256 * (2 * DCH)) = u32x4{a.x, a.y, c.x, c.y};
      }
      STAMP_OUT();
      asm volatile(".Lfk_end2:\n.Lfk_end3:");
      __builtin_amdgcn_endpgm();
      __builtin_unreachable();
    }
    const int ch = ch0 + w;
    const u32 kimg = KIMG + w * KS;
#pragma unroll
    for (int j = 0; j < 2; ++j) dma16(P.ssm_k, (u32)ch * (SEQ * 2) + 1024 * j + 16 * lane, smb + kimg + KPADB + 1024 * j);
    if (w < 3) touch64(prefetch_addr(P, w, idx, xcd, lane), smb + PFD);
    if (lane < 10) *(LDSP u32x4*)(sm + XIMG + w * XS + 16 * lane) = u32x4{0, 0, 0, 0};
    if (lane < 6) *(LDSP u32x4*)(sm + kimg + 16 * lane) = u32x4{0, 0, 0, 0};
    const float dsk = ld_bf_uniform(P.ssm_D, ch);
    if (w < 2) vmcnt<0>();
    wgbarrier();
    STAMP(1);
    load_chunk(w);
    vmcnt<0>();
    set_word(sm + FLAG2 + 4 * w, 1);
    STAMP(2);
#pragma unroll
    for (int j = 2; j < 4; ++j) dma16(P.ssm_k, (u32)ch * (SEQ * 2) + 1024 * j + 16 * lane, smb + kimg + KPADB + 1024 * j);
    if (lane == 0) {
      LDSP u16* k0 = (LDSP u16*)(sm + kimg + KPADB);
      *k0 = (u16)pkbf(bf2f(*k0) + dsk, 0.f);
    }
    wavefence();
    kb = kimg + 8 * (4 * lp + 8 * g + lq);
    xb = XIMG + w * XS + 224 + 8 * (lp - 8 * g - lq);
    yw = YIMG + w * YS + 2 * lane;
  } else {
    const int bi = xcd * 20 + (idx - 10);
    const int ch0 = bi * 4;
    if (w >= 8) {
      const int h = w - 8;
      const int pcol = bi >> 4, pchunk = bi & 15;
      __builtin_amdgcn_s_setprio(3);
      unsigned long long tag = 0;
      if (h == 0) tag = __hip_atomic_load(gepoch, RLX_AGENT) + 1;
      if (h < 3) touch64(prefetch_addr(P, h, idx, xcd, lane), smb + PFD);
      wgbarrier();
      delay(PFP);
      {
        const u32 po = (u32)(1024 + 64 * pchunk + 16 * h + (lane >> 3)) * ROWB + (u32)pcol * 128 + 16 * (lane & 7);
#pragma unroll
        for (int j = 0; j < 2; ++j) {
          dma16(P.k, po + j * 8 * ROWB, smb + PRAWK + 512 + h * 2048 + j * 1024);
          dma16(P.v, po + j * 8 * ROWB, smb + PRAWV + h * 2048 + j * 1024);
          dma16(P.q, po + j * 8 * ROWB, smb + PRAWQ + h * 2048 + j * 1024);
        }
        if (h == 0 && lane < 32) dma16(P.k, po - 4 * ROWB, smb + PRAWK);
        if (h == 1 && lane < 32) dma16(P.shift_k, (u32)pcol * 512 + 16 * lane, smb + SHK);
      }
      {
        const u32x4 ones = {0x3F803F80u, 0x3F803F80u, 0x3F803F80u, 0x3F803F80u};
#pragma unroll
        for (int i = 0; i < 4; ++i) *(LDSP u32x4*)(sm + B_YIMG + h * YS + 1024 * i + 16 * lane) = ones;
      }
      if (lane < 10) *(LDSP u32x4*)(sm + XIMG + h * XS + 16 * lane) = u32x4{0, 0, 0, 0};
      delay(PFU - PFP);
      {
        const int cu = (h < 2) ? 3 + h : 3 + ((idx - 10) & 1);
        const int su = (h < 2) ? idx : (idx - 10) >> 1;
        touch64(prefetch_addr(P, cu, su, xcd, lane), smb + PFD);
      }
      vmcnt<1>();
      set_word(sm + FLAG2 + 4 * h, 1);
      wait_words(sm + FLAG2, lane, 4, 1);
      STAMP(12);
      {
        const u32x2 sw = *(LDSP u32x2*)(sm + SHK + 8 * (16 * h + li));
        const float w0 = bflo(sw.x), w1 = bfhi(sw.x), w2 = bflo(sw.y), w3 = bfhi(sw.y);
        const u32 co = 32 * h + 8 * lp;
        const u32 stg = PSTG + h * XS;
        const u32 sw_ = stg + li * 128 + 8 * (g & 1);
        const u32 sr_ = stg + (lane >> 3) * 128 + 16 * ((lane ^ (lane >> 3)) & 7);
        const auto rx = __builtin_amdgcn_make_buffer_rsrc(P.ex + WS_EXX, 0, 4u << 20, 0x00020000);
        const u32 eo = (u32)(pcol * 64 + 16 * h + (lane >> 3)) * 2048 + 128 * pchunk + 16 * (lane & 7);
        const u32 zst = (lq & 1) ? 256u : 0u;
        const u32 ro = 128 * (4 * g + (lq >> 1)) + co;
        const u32 ka = (lq & 1) ? PRAWK + ro : (u32)ZROW, va = (lq & 1) ? PRAWV + ro : (u32)ZROW;
#pragma unroll
        for (int u = 0; u < 4; ++u)
          *(LDSP u32x2*)(sm + sw_ + 16 * (((2 * u + (g >> 1)) ^ li) & 7)) = trread(sm + PRAWQ + 128 * (16 * u + 4 * g + lq) + co);
        wavefence();
#pragma unroll
        for (int j = 0; j < 2; ++j)
          __builtin_amdgcn_raw_buffer_store_b128(*(const LDSP u32x4*)(sm + sr_ + 1024 * j), rx, eo + j * 8 * 2048 + (u32)(WS_EXQ - WS_EXX), 0, 16);
        wavefence();
#pragma unroll 2
        for (int u = 0; u < 4; ++u) {
          float kx[8], vx[4], kv[4];
#pragma unroll
          for (int j = 0; j < 4; ++j) { u32x2 r = trread(sm + ka + zst * (8 * u + j)); kx[2 * j] = __uint_as_float(r.x); kx[2 * j + 1] = __uint_as_float(r.y); }
#pragma unroll
          for (int j = 0; j < 2; ++j) { u32x2 r = trread(sm + va + zst * (8 * u + j)); vx[2 * j] = __uint_as_float(r.x); vx[2 * j + 1] = __uint_as_float(r.y); }
#pragma unroll
          for (int j = 0; j < 4; ++j)
            kv[j] = vmul(vfma(kx[j + 4], w0, vfma(kx[j + 3], w1, vfma(kx[j + 2], w2, vmul(kx[j + 1], w3)))), vx[j]);
          *(LDSP u32x2*)(sm + sw_ + 16 * (((2 * u + (g >> 1)) ^ li) & 7)) = u32x2{pkbf(kv[0], kv[1]), pkbf(kv[2], kv[3])};
        }
        wavefence();
#pragma unroll
        for (int j = 0; j < 2; ++j)
          __builtin_amdgcn_raw_buffer_store_b128(*(const LDSP u32x4*)(sm + sr_ + 1024 * j), rx, eo + j * 8 * 2048, 0, 16);
      }
      STAMP(1);
      const u32 rowoff = (u32)(64 * h + lane) * ROWB + (u32)(ch0 & ~7) * 2;
      dma16(P.u, rowoff, smb + RAWU + 1024 * h);
      dma16(P.u, rowoff + 256 * ROWB, smb + RAWU + 4096 + 1024 * h);
      vmcnt<2>();
      set_word(sm + FLAG2 + 16 + 4 * h, 1);
      wait_words(sm + FLAG2 + 16, lane, 4, 1);
      if (h == 0 && lane == 0) __hip_atomic_store(gflag + 16 * bi, tag, RLX_AGENT);
      STAMP(9);
      const u32 ua = RAWU + 1024 * h + 16 * (16 * g + 4 * lp + lq) + 2 * (ch0 & 4);
      const u32 xw = XIMG + (lane & 3) * XS + XPADB + 8 * (lane >> 2) + 128 * h;
      const u32 yrd = B_YIMG + lq * YS + 2 * (64 * h + 16 * g + 4 * lp);
      u16* const outp = P.out + DCH + ch0 + (size_t)(64 * h + lane) * (2 * DCH);
#pragma unroll 1
      for (int it = 0; it < 8; ++it) {
        int i = it;
        asm volatile("" : "+s"(i));
        if (i + 2 < 8) dma16(P.u, rowoff + (u32)(i + 2) * (256 * ROWB), smb + RAWU + 4096 * (i + 2) + 1024 * h);
        if (i < 6) vmcnt<2>(); else if (i == 6) vmcnt<1>(); else vmcnt<0>();
        *(LDSP u32x2*)(sm + xw + 512 * i) = trread(sm + ua + 4096 * i);
        set_word(sm + FLAGS + 4 * h, (u32)(i + 1));
        if (i == 0) STAMP(13); if (i == 7) STAMP(14);
      }
#pragma unroll 1
      for (int i = 0; i < 8; ++i) {
        wait_words(sm + FLAGS + 16, lane, 4, (u32)(i + 1));
        *(u32x2*)(outp + (size_t)i * 256 * (2 * DCH)) = trread(sm + yrd + 512 * i);
      }
      STAMP_OUT();
      asm volatile(".Lfk_end4:");
      __builtin_amdgcn_endpgm();
      __builtin_unreachable();
    }
    const int cw = w & 3, ps = w >> 2;
    const int ch = ch0 + cw;
    const size_t o = (size_t)ch * NST + 64 * ps + lane;
    const u16 i_ar = P.A_re[o], i_ai = P.A_im[o], i_br = P.B_re[o], i_bi = P.B_im[o], i_cr = P.C_re[o], i_ci = P.C_im[o];
    const float ldt = ld_bf_uniform(P.log_dt, ch);
    const float dsk = ld_bf_uniform(P.Dskip, ch);
    wgbarrier();
    const float dt = rbf(expf(ldt));
    const float zr = dt * bf2f(i_ar), zi = dt * bf2f(i_ai);
    const cf dB = {dt * bf2f(i_br), dt * bf2f(i_bi)};
    const cf C = {bf2f(i_cr), bf2f(i_ci)};
    const cf W = cmul(C, dB);
    float mx = fmaxf(fabsf(W.r), fabsf(W.i));
    STAMP(1);
    {
      int m = __float_as_int(mx);
      m = max(m, __builtin_amdgcn_update_dpp(0, m, 0x111, 0xf, 0xf, false));
      m = max(m, __builtin_amdgcn_update_dpp(0, m, 0x112, 0xf, 0xf, false));
      m = max(m, __builtin_amdgcn_update_dpp(0, m, 0x114, 0xf, 0xf, false));
      m = max(m, __builtin_amdgcn_update_dpp(0, m, 0x118, 0xf, 0xf, false));
      int a0 = __builtin_amdgcn_readlane(m, 15), a1 = __builtin_amdgcn_readlane(m, 31);
      int a2 = __builtin_amdgcn_readlane(m, 47), a3 = __builtin_amdgcn_readlane(m, 63);
      mx = __int_as_float(max(max(a0, a1), max(a2, a3)));
    }
    int ex = (int)((__float_as_uint(mx) >> 23) & 0xff);
    ex = min(max(ex, 30), 240);
    const float sY = __uint_as_float((u32)(267 - ex) << 23);
    const float so = __uint_as_float((u32)(ex - 27) << 23);
    const float SX = 16384.f;

    f32x4 ak[3];
    const u32 stg = STG + w * STS;
    const u32 rb = stg + li * 272 + g * 16;
    const u32 m0v = smb + w * STS;
#define STGW(row, val_r, val_i) \
    asm volatile("ds_write_addtid_b32 %0 offset:%1" :: "v"(pkh((val_r), (val_i))), "i"(STG + (row) * 272) : "memory")
    {
      const cf E1 = cexp_pow2(zr, zi, 1.f);
      const cf E8c = cexp_pow2(zr, -zi, 8.f);
      const cf E128 = cexp_pow2(zr, zi, 128.f);
      const cf E1024 = cexp_pow2(zr, zi, 1024.f);
      const cf E2 = cmul(E1, E1);
      const cf E4 = cmul(E2, E2);
      u32 keep;
      asm volatile("s_mov_b32 %0, m0\n\ts_mov_b32 m0, %1\n\ts_nop 0" : "=&s"(keep) : "s"(m0v) : "memory");
      cf y = {W.r * sY, -(W.i * sY)};
      cf P1 = {E128.r * SX, E128.i * SX};
      cf v0 = {SX, 0.f}, v1 = P1;
#pragma unroll
      for (int b = 0; b < 8; ++b) {
        STGW(2 * b, y.r, y.i); y = cmul(y, E8c);
        STGW(16 + b, v0.r, v0.i); if (b < 7) v0 = cmul(v0, E1);
        STGW(2 * b + 1, y.r, y.i); if (b < 7) y = cmul(y, E8c);
        STGW(24 + b, v1.r, v1.i); if (b < 7) v1 = cmul(v1, E1);
      }
      f16x8 yf[4];
      const f32x4 zero = {0.f, 0.f, 0.f, 0.f};
#pragma unroll
      for (int s = 0; s < 4; ++s) {
        yf[s] = *(const LDSP f16x8*)(sm + rb + 64 * s);
        f16x8 xf = *(const LDSP f16x8*)(sm + rb + 16 * 272 + 64 * s);
        ak[0] = __builtin_amdgcn_mfma_f32_16x16x32_f16(xf, yf[s], s ? ak[0] : zero, 0, 0, 0);
      }
      asm volatile("" ::: "memory");
      cf Pa = cmul(P1, E128);
      cf Pb = {E1024.r * SX, E1024.i * SX};
#pragma unroll
      for (int m = 0; m < 2; ++m) {
        cf v = Pa;
#pragma unroll
        for (int r = 0; r < 4; ++r) {
          STGW(4 * m + r, v.r, v.i); if (r < 3) v = cmul(v, E2);
          if (r & 1) { STGW(16 + 2 * m + (r >> 1), Pb.r, Pb.i); Pb = cmul(Pb, E128); }
        }
        Pa = cmul(Pa, E128);
      }
#pragma unroll
      for (int m = 0; m < 4; ++m) {
        STGW(8 + 2 * m, Pa.r, Pa.i);
        cf v = cmul(Pa, E4);
        STGW(20 + m, Pb.r, Pb.i); Pb = cmul(Pb, E128);
        STGW(9 + 2 * m, v.r, v.i);
        if (m < 3) Pa = cmul(Pa, E128);
      }
      STGW(24, Pb.r, Pb.i);
      asm volatile("s_mov_b32 m0, %0" :: "s"(keep) : "memory");
#pragma unroll
      for (int s = 0; s < 4; ++s) {
        f16x8 x1 = *(const LDSP f16x8*)(sm + rb + 64 * s);
        f16x8 x2 = *(const LDSP f16x8*)(sm + rb + 16 * 272 + 64 * s);
        ak[1] = __builtin_amdgcn_mfma_f32_16x16x32_f16(x1, yf[s], s ? ak[1] : zero, 0, 0, 0);
        ak[2] = __builtin_amdgcn_mfma_f32_16x16x32_f16(x2, yf[s], s ? ak[2] : zero, 0, 0, 0);
      }
    }
#undef STGW
#pragma unroll
    for (int i = 0; i < 3; ++i) ak[i] *= so;
    STAMP(2);
    asm volatile("" ::: "memory");
    const u32 kimg = B_KIMG + cw * KS;
    if (ps) {
#pragma unroll
      for (int i = 0; i < 3; ++i) *(LDSP f32x4*)(sm + kimg + 1024 * i + 16 * lane) = ak[i];
      asm volatile("s_waitcnt lgkmcnt(0)" ::: "memory");
      *(volatile LDSP u32*)(sm + FLAGS + 48 + 4 * cw) = 1;
      STAMP_OUT();
      asm volatile(".Lfk_end5:");
      __builtin_amdgcn_endpgm();
      __builtin_unreachable();
    }
    wait_words(sm + FLAGS + 48 + 4 * cw, lane, 1, 1);
    {
      const u32 tb = stg;
#pragma unroll
      for (int i = 0; i < 3; ++i) ak[i] += *(const LDSP f32x4*)(sm + kimg + 1024 * i + 16 * lane);
      wavefence();
      const int b = li, p = g;
      if (lane < 6) *(LDSP u32x4*)(sm + kimg + 16 * lane) = u32x4{0, 0, 0, 0};
      {
        float a0 = ak[0][0] + (lane == 0 ? dsk : 0.f), a1 = ak[0][1], a2 = ak[0][2], a3 = ak[0][3];
        *(LDSP u32x2*)(sm + kimg + KPADB + 2 * (128 * (p >> 1) + 8 * b + 4 * (p & 1))) = u32x2{pkbf(a0, a1), pkbf(a2, a3)};
      }
      {
        float a0 = ak[1][0], a1 = ak[1][1], a2 = ak[1][2], a3 = ak[1][3];
        int i01, i23;
        if (p < 2) { i01 = 64 * p + 4 * b; i23 = i01 + 2; }
        else { i01 = 128 + 64 * (p - 2) + 2 * b; i23 = i01 + 32; }
        *(LDSP u32x2*)(sm + tb + 4 * i01) = u32x2{pkbf(a0, a0), pkbf(a1, a1)};
        *(LDSP u32x2*)(sm + tb + 4 * i23) = u32x2{pkbf(a2, a2), pkbf(a3, a3)};
#pragma unroll
        for (int j = 0; j < 4; ++j) {
          float a = ak[2][j];
          int ii = 256 + 16 * (4 * p + j) + b;
          *(LDSP u32*)(sm + tb + 4 * ii) = pkbf(a, a);
        }
      }
      wavefence();
      const LDSP u32* TB = (const LDSP u32*)(sm + tb);
      {
        const int i0 = 4 * (lane & 31);
        u32 g0 = TB[i0], g1 = TB[i0 + 1], g2 = TB[i0 + 2], g3 = TB[i0 + 3], g4 = TB[i0 + 4];
        u32x4 ov = {g0, (g1 & 0xffffu) | (g2 & 0xffff0000u), g2, (g3 & 0xffffu) | (g4 & 0xffff0000u)};
        *(LDSP u32x4*)(sm + kimg + KPADB + 16 * (32 + (lane & 31))) = ov;
      }
      {
        const int i0 = 128 + 2 * lane;
        u32 g0 = TB[i0], g1 = TB[i0 + 1], g2 = TB[i0 + 2];
        u32x4 ov = {g0, (g0 & 0xffffu) | (g1 & 0xffff0000u), g1, g2};
        *(LDSP u32x4*)(sm + kimg + KPADB + 16 * (64 + lane)) = ov;
      }
#pragma unroll
      for (int hh = 0; hh < 2; ++hh) {
        const int i0 = 256 + 64 * hh + lane;
        u32 g0 = TB[i0], g1 = TB[i0 + 1];
        u32 mid = (lane & 1) ? g1 : ((g0 & 0xffffu) | (g1 & 0xffff0000u));
        u32x4 ov = {g0, g0, mid, g1};
        *(LDSP u32x4*)(sm + kimg + KPADB + 16 * (128 + 64 * hh + lane)) = ov;
      }
    }
    wavefence();
    kb = kimg + 8 * (4 * lp + 8 * g + lq);
    xb = XIMG + cw * XS + 224 + 8 * (lp - 8 * g - lq);
    yw = B_YIMG + cw * YS + 2 * lane;
  }

  {
    const bool l63 = (lane == 63);
    const lptr pxp = sm + FLAGS + 4 * (lane & 3);
    const lptr pyp = sm + FLAGS + 16 + 4 * w;
    u32x2 kr[16][2], xr[40][4];
    f32x4 ac[16][2];
    const f32x4 zero = {0.f, 0.f, 0.f, 0.f};
    float ts[16][2], u2[16][2], u1[16][2], yy[16][2];
    u32 pk[16];
    u32 qv[3][2] = {{0, 0}, {0, 0}, {0, 0}}, px = 0;
    const lptr ywp = sm + yw;
    const u32 qa = (u32)(uintptr_t)ywp;
#define RDQ(s_, i_) asm volatile("ds_read_u16_d16_hi %0, %1 offset:%2" : "+v"(qv[(s_) % 3][i_]) : "v"(qa), "i"((s_) * 256 + 128 * (i_)) : "memory")
#define RDXP(l_, i_) xr[l_][i_] = trread(sm + xb + 256 * SC.ldm[l_] + ((i_) >> 1) * 128 + (((i_) & 1) ? 0 : 32))
#pragma unroll
    for (int d = 0; d < 8; ++d) { kr[d][0] = trread(sm + kb + 256 * d); kr[d][1] = trread(sm + kb + 256 * d + 32); }
    STAMP(3);
    wait_words(sm + FLAGS, lane, 4, 1);
    STAMP(4);
    CSTAMP(0);
    RDQ(0, 0); RDQ(0, 1);
    RDXP(0, 0); RDXP(0, 1); RDXP(0, 2); RDXP(0, 3);
    SCHED0();
    static_for<0, NGAP>([&](auto G_) {
      constexpr int G = decltype(G_)::value;
      if constexpr (G < 272) {
        constexpr int u = G >> 1, par = G & 1;
        constexpr int p = SC.p[u], d = SC.d[u], l = SC.ld[u];
        ac[p][par] = MFMA(frag(kr[d][0], kr[d][1]), frag(xr[l][2 * par], xr[l][2 * par + 1]), SC.first[u] ? zero : ac[p][par]);
        SCHED0();
      }
      static_for<0, AC.n[G]>([&](auto I_) {
        constexpr int code = AC.a[G][decltype(I_)::value];
        constexpr int kind = code & 15, a0 = (code >> 4) & 255, a1 = code >> 12;
        if constexpr (kind == A_X) RDXP(a0, a1);
        if constexpr (kind == A_K) kr[a0][a1] = trread(sm + kb + 256 * a0 + 32 * a1);
        if constexpr (kind == A_Q) RDQ(a0, a1);
        if constexpr (kind == A_PXR) px = *(volatile LDSP u32*)pxp;
        if constexpr (kind == A_RR) vmcnt<0>();
        if constexpr (kind == A_PXC) {
          if (__builtin_expect(!__all(px >= (u32)(a0 / 2 + 1)), 0)) wait_words(sm + FLAGS, lane, 4, (u32)(a0 / 2 + 1));
          asm volatile("" ::: "memory");
        }
        if constexpr (kind == A_EP) {
          constexpr int s = a0, e = a1 >> 1, o = a1 & 1;
          if constexpr (a1 < 14) {
            if constexpr (e == 0) {
              float cur = ac[s][o][3], prev = 0.f;
              if constexpr (o) prev = ac[s][0][3]; else if constexpr (s > 0) prev = ac[s - 1][1][3];
              ts[s][o] = l63 ? prev : cur;
            }
            if constexpr (e == 1) { float a = ac[s][o][2]; u2[s][o] = a + ror1(ts[s][o]); }
            if constexpr (e == 2) {
              float prev = 0.f;
              if constexpr (o) prev = u2[s][0]; else if constexpr (s > 0) prev = u2[s - 1][1];
              ts[s][o] = l63 ? prev : u2[s][o];
            }
            if constexpr (e == 3) { float a = ac[s][o][1]; u1[s][o] = a + ror1(ts[s][o]); }
            if constexpr (e == 4) {
              float prev = 0.f;
              if constexpr (o) prev = u1[s][0]; else if constexpr (s > 0) prev = u1[s - 1][1];
              ts[s][o] = l63 ? prev : u1[s][o];
            }
            if constexpr (e == 5) { float a = ac[s][o][0]; yy[s][o] = a + ror1(ts[s][o]); asm volatile("" : "+v"(yy[s][o])); }
            if constexpr (e == 6) {
              asm volatile("" : "+v"(qv[s % 3][o]));
              yy[s][o] *= __uint_as_float(qv[s % 3][o]);
              asm volatile("" : "+v"(yy[s][o]));
            }
          }
          if constexpr (a1 == 14) pk[s] = pkbf(yy[s][0], yy[s][1]);
          if constexpr (a1 == 15) *(LDSP u16*)(ywp + s * 256) = (u16)pk[s];
          if constexpr (a1 == 16) *(LDSP u16*)(ywp + s * 256 + 128) = (u16)(pk[s] >> 16);
          if constexpr (a1 == 17) {
            asm volatile("" ::: "memory");
            *(volatile LDSP u32*)pyp = (u32)(s / 2 + 1);
            asm volatile("" ::: "memory");
          }
        }
        SCHED0();
      });
    });
  }
  STAMP_OUT();
  asm volatile(".Lfk_end0:");
  __builtin_amdgcn_endpgm();
}

extern "C" void kernel_launch(void* const* d_in, const int* in_sizes, int n_in,
                              void* d_out, int out_size, void* d_ws, size_t ws_size,
                              hipStream_t stream) {
  Params P;
  P.q = (const u16*)d_in[0];
  P.k = (const u16*)d_in[1];
  P.v = (const u16*)d_in[2];
  P.shift_k = (const u16*)d_in[3];
  P.ssm_k = (const u16*)d_in[4];
  P.ssm_D = (const u16*)d_in[5];
  P.u = (const u16*)d_in[6];
  P.A_re = (const u16*)d_in[7];
  P.A_im = (const u16*)d_in[8];
  P.B_re = (const u16*)d_in[9];
  P.B_im = (const u16*)d_in[10];
  P.C_re = (const u16*)d_in[11];
  P.C_im = (const u16*)d_in[12];
  P.log_dt = (const u16*)d_in[13];
  P.Dskip = (const u16*)d_in[14];
  P.out = (u16*)d_out;
  P.ws = (unsigned long long*)d_ws;
  P.ex = (char*)d_ws + (16 << 20);
  (void)hipFuncSetAttribute((const void*)fused_kernel, hipFuncAttributeMaxDynamicSharedMemorySize, LDS_BYTES);
  fused_kernel<<<256, 768, LDS_BYTES, stream>>>(P);
}
```

```cpp
#define STAMPS
#include <hip/hip_runtime.h>
#include <hip/hip_bf16.h>
#include <stdint.h>

typedef __attribute__((ext_vector_type(4))) short s16x4;
typedef __attribute__((ext_vector_type(8))) _Float16 f16x8;
typedef __attribute__((ext_vector_type(8))) __bf16 bf16x8;
typedef __attribute__((ext_vector_type(2))) __bf16 bf16x2;
typedef __attribute__((ext_vector_type(2))) _Float16 f16x2;
typedef __attribute__((ext_vector_type(2))) float f32x2;
typedef __attribute__((ext_vector_type(4))) float f32x4;
typedef __attribute__((ext_vector_type(2))) unsigned int u32x2;
typedef __attribute__((ext_vector_type(4))) unsigned int u32x4;
typedef unsigned int u32;
typedef unsigned short u16;
#define LDSP __attribute__((address_space(3)))
#define DEV __device__ __forceinline__

constexpr int SEQ = 2048;
constexpr int DCH = 640;
constexpr int NST = 128;
constexpr int ROWB = DCH * 2;

constexpr int XS = 4272;
constexpr int XPADB = 160;
constexpr int YS = 4160;
constexpr int KS = 4224;
constexpr int KPADB = 96;
constexpr int FLAGS = 0;
constexpr int ZROW = 64;
constexpr int FLAG2 = 192;
constexpr int PFD = 256;
constexpr int SHK = 512;
constexpr int XIMG = 1024;
constexpr int YIMG = 35200;
constexpr int KIMG = 68480;
constexpr int RAWK = 102272;
constexpr int RAWV = 119680;
constexpr int RAWQ = 136064;
constexpr int STS = 8704;
constexpr int STG = 18176;
constexpr int B_KIMG = 87808;
constexpr int B_YIMG = 104704;
constexpr int RAWU = 121344;
constexpr int PRAWK = RAWU + 16384;
constexpr int PRAWV = PRAWK + 8704;
constexpr int PRAWQ = PRAWV + 8192;
constexpr int PSTG = XIMG + 2208;
constexpr int LDS_BYTES = 162816;

constexpr size_t WS_EPOCH = 0;
constexpr size_t WS_FLAG = 4096;
constexpr size_t WS_EXX = 1 << 20;
constexpr size_t WS_EXQ = 3 << 20;
constexpr size_t WS_STAMP = 8 << 20;

DEV float bf2f(u16 v) { return __uint_as_float(((u32)v) << 16); }
DEV float bflo(u32 v) { return __uint_as_float(v << 16); }
DEV float bfhi(u32 v) { return __uint_as_float(v & 0xffff0000u); }
DEV u32 pkbf(float a, float b) {
  f32x2 t = {a, b};
  bf16x2 r = __builtin_convertvector(t, bf16x2);
  return __builtin_bit_cast(u32, r);
}
DEV u32 pkh(float a, float b) {
  f32x2 t = {a, b};
  f16x2 r = __builtin_convertvector(t, f16x2);
  return __builtin_bit_cast(u32, r);
}
DEV float rbf(float a) { return bflo(pkbf(a, a)); }
DEV float dot2(u32 a, u32 b, float c) {
  return __builtin_amdgcn_fdot2_f32_bf16(__builtin_bit_cast(bf16x2, a), __builtin_bit_cast(bf16x2, b), c, false);
}
DEV float vfma(float a, float b, float c) { float d; asm("v_fma_f32 %0, %1, %2, %3" : "=v"(d) : "v"(a), "v"(b), "v"(c)); return d; }
DEV float vmul(float a, float b) { float d; asm("v_mul_f32 %0, %1, %2" : "=v"(d) : "v"(a), "v"(b)); return d; }
typedef __attribute__((address_space(1))) unsigned long long gu64;
#define RLX_AGENT __ATOMIC_RELAXED, __HIP_MEMORY_SCOPE_AGENT
typedef LDSP char* lptr;
DEV u32x2 trread(lptr p) {
  s16x4 r = __builtin_amdgcn_ds_read_tr16_b64_v4i16((LDSP s16x4*)p);
  return __builtin_bit_cast(u32x2, r);
}
DEV bf16x8 frag(u32x2 a, u32x2 b) {
  u32x4 f = {a.x, a.y, b.x, b.y};
  return __builtin_bit_cast(bf16x8, f);
}
DEV float ror1(float v) {
  int r = __builtin_amdgcn_update_dpp(0, __float_as_int(v), 0x13C, 0xf, 0xf, false);
  return __int_as_float(r);
}
DEV void wavefence() { __builtin_amdgcn_fence(__ATOMIC_ACQ_REL, "wavefront"); __builtin_amdgcn_wave_barrier(); }
DEV void wgbarrier() {
  asm volatile("s_waitcnt lgkmcnt(0)" ::: "memory");
  __builtin_amdgcn_s_barrier();
  asm volatile("" ::: "memory");
}
DEV void dma16(const void* base, u32 voff, u32 ldsaddr) {
  u32 keep;
  ldsaddr = __builtin_amdgcn_readfirstlane(ldsaddr);
  const unsigned long long b64 = (unsigned long long)(uintptr_t)base;
  const unsigned long long sb = ((unsigned long long)(u32)__builtin_amdgcn_readfirstlane((u32)(b64 >> 32)) << 32) |
                                (u32)__builtin_amdgcn_readfirstlane((u32)b64);
  asm volatile("s_mov_b32 %0, m0\n\ts_mov_b32 m0, %3\n\ts_nop 2\n\tglobal_load_lds_dwordx4 %1, %2\n\ts_mov_b32 m0, %0"
               : "=&s"(keep) : "v"(voff), "s"(sb), "s"(ldsaddr) : "memory");
}
DEV void touch64(const void* p, u32 ldsaddr) {
  u32 keep;
  asm volatile("s_mov_b32 %0, m0\n\ts_mov_b32 m0, %2\n\ts_nop 0\n\tglobal_load_lds_dword %1, off\n\ts_mov_b32 m0, %0"
               : "=&s"(keep) : "v"(p), "s"(ldsaddr) : "memory");
}
template <int N> DEV void vmcnt() { asm volatile("s_waitcnt vmcnt(%0)" :: "i"(N) : "memory"); }
#define SCHED0() __builtin_amdgcn_sched_barrier(0)

template <int V> struct IC { static constexpr int value = V; };
template <int I, int N, class F> DEV void static_for(F&& f) {
  if constexpr (I < N) { f(IC<I>{}); static_for<I + 1, N>(f); }
}

struct cf { float r, i; };
DEV cf cmul(cf a, cf b) {
  cf o;
  o.r = fmaf(a.r, b.r, -(a.i * b.i));
  o.i = fmaf(a.r, b.i, a.i * b.r);
  return o;
}
DEV cf cexp_pow2(float zr, float zi, float x) {
  float mag = __builtin_amdgcn_exp2f((zr * x) * 1.44269504088896340736f);
  float th = zi * x;
  const float C_HI = 0.15915494f;
  const float C_LO = 6.4206382e-09f;
  float f = fmaf(th, C_LO, fmaf(th, C_HI, -rintf(th * C_HI)));
  cf o;
  o.r = mag * __builtin_amdgcn_cosf(f);
  o.i = mag * __builtin_amdgcn_sinf(f);
  return o;
}

#define STAMP_TYPE(wg) (((wg) >> 3) < 10 ? 0 : 1)
#define STAMP(n) do { if ((n) != 0 && (n) != 11) break; __builtin_amdgcn_sched_barrier(0); unsigned long long _t = __builtin_amdgcn_s_memrealtime(); __builtin_amdgcn_s_waitcnt(0xC07F); stv[n] = _t; __builtin_amdgcn_sched_barrier(0); } while (0)
#define CSTAMP(n) do { break; __builtin_amdgcn_sched_barrier(0); unsigned long long _t = __builtin_amdgcn_s_memtime(); __builtin_amdgcn_s_waitcnt(0xC07F); cyv[n] = _t; __builtin_amdgcn_sched_barrier(0); } while (0)
#define STAMP_OUT() do { STAMP(10); if (cyv[1]) stv[15] = stv[0] + wacc * 10 + 1; vmcnt<0>(); STAMP(11); if (lane == 0) { _Pragma("unroll") for (int i = 0; i < 16; ++i) P.ws[(blockIdx.x * 16 + w) * 16 + i] = stv[i]; } } while (0)

struct Params {
  const u16 *q, *k, *v, *shift_k, *ssm_k, *ssm_D;
  const u16 *u, *A_re, *A_im, *B_re, *B_im, *C_re, *C_im, *log_dt, *Dskip;
  u16* out;
  unsigned long long* ws;
  char* ex;
};

DEV float ld_bf_uniform(const u16* p, int idx) {
  u32 d = *(const __attribute__((address_space(4))) u32*)(uintptr_t)(p + (idx & ~1));
  return (idx & 1) ? bfhi(d) : bflo(d);
}

DEV const char* prefetch_addr(const Params& P, int cls, int slice, int xcd, int lane) {
  asm volatile("" : "+v"(lane));
  const unsigned long long TAB = 0xFEDC932810ull;
  const u32 nib = (u32)(TAB >> (4 * (2 * cls + (lane >> 5)))) & 15u;
  const bool second = (lane >> 4) & 1;
  const u16* base = (nib < 8) ? (second ? P.v : P.k) : (nib < 12 ? P.q : P.u);
  const u32 blk = (nib < 8) ? nib : 2 * (nib & 3) + (second ? 1 : 0);
  const u32 row = blk * 256u + (u32)slice * 8u + ((lane >> 1) & 7);
  return (const char*)base + (row * ROWB + (u32)(((160 * xcd) >> 7) + (lane & 1)) * 128u);
}
DEV void wait_words(lptr p, int lane, int n, u32 v) {
#pragma unroll 1
  for (int spin = 0; spin < (1 << 16); ++spin) {
    u32 x = *(volatile LDSP u32*)(p + 4 * (lane & (n - 1)));
    if (__all(x >= v)) break;
    __builtin_amdgcn_s_sleep(1);
  }
  asm volatile("" ::: "memory");
}
#define PFS 3
#define PFP 3
#define PFU 8
#define PFH 4
DEV void delay(int n) {
  for (int i = 0; i < n; ++i) __builtin_amdgcn_s_sleep(PFS);
}

template <int RS>
DEV u32x2 kv4(lptr sm, u32 ka, u32 va, float w0, float w1, float w2, float w3) {
  float kx[8], vx[4], kv[4];
#pragma unroll
  for (int j = 0; j < 4; ++j) { u32x2 r = trread(sm + ka + RS * j); kx[2 * j] = __uint_as_float(r.x); kx[2 * j + 1] = __uint_as_float(r.y); }
#pragma unroll
  for (int j = 0; j < 2; ++j) { u32x2 r = trread(sm + va + RS * j); vx[2 * j] = __uint_as_float(r.x); vx[2 * j + 1] = __uint_as_float(r.y); }
#pragma unroll
  for (int j = 0; j < 4; ++j)
    kv[j] = vmul(vfma(kx[j + 4], w0, vfma(kx[j + 3], w1, vfma(kx[j + 2], w2, vmul(kx[j + 1], w3)))), vx[j]);
  return u32x2{pkbf(kv[0], kv[1]), pkbf(kv[2], kv[3])};
}
DEV void set_word(lptr p, u32 v) {
  asm volatile("" ::: "memory");
  *(volatile LDSP u32*)p = v;
  asm volatile("" ::: "memory");
}

struct Sched {
  int p[136], d[136], ld[136];
  bool first[136];
  int base[17];
  int ldm[40], ldu[40];
  int ku[16];
  int nld;
};
constexpr Sched make_sched() {
  Sched S{};
  int n = 0, nl = 16;
  for (int m = 0; m < 16; ++m) S.ldm[m] = m;
  for (int d = 0; d < 16; ++d) S.ku[d] = -1;
  for (int s = 0; s < 16; ++s) {
    S.base[s] = n;
    S.ldu[s] = n;
    for (int k = 0; k < 8 && s + k < 16; ++k) { S.p[n] = s + k; S.d[n] = k; S.ld[n] = s; S.first[n] = (s == 0) || (k == 7); ++n; }
    int lo = -1, hi = -1, cnt = 0, half = 0;
    if (s == 6) { lo = 8; hi = 8; cnt = 1; half = 2; }
    else if (s >= 7 && s <= 12) { lo = (s & 1) ? s + 2 : s + 1; hi = lo + 1; cnt = hi - 7; half = (s & 1) ? 0 : 1; }
    else if (s == 13 || s == 14) { lo = 15; hi = 15; cnt = 8; half = (s == 13) ? 0 : 1; }
    if (cnt) {
      const int split = (cnt + 1) / 2;
      const int j0 = (half == 1) ? split : 0, j1 = (half == 0) ? split : cnt;
      for (int j = j0; j < j1; ++j) {
        const int mm = cnt - 1 - j;
        S.ldm[nl] = mm; S.ldu[nl] = n;
        for (int t = hi; t >= lo; --t)
          if (t - mm >= 8) { S.p[n] = t; S.d[n] = t - mm; S.ld[n] = nl; S.first[n] = false; ++n; }
        ++nl;
      }
    }
  }
  S.base[16] = n;
  S.nld = nl;
  for (int u = 0; u < n; ++u) if (S.ku[S.d[u]] < 0) S.ku[S.d[u]] = u;
  return S;
}
constexpr Sched SC = make_sched();
static_assert(SC.base[16] == 136, "schedule");
constexpr int NGAP = 272 + 24, CAP = 2;
enum { A_NONE = 0, A_X, A_K, A_Q, A_PXR, A_PXC, A_EP, A_DMA, A_RR };
struct Acts { int n[NGAP]; int a[NGAP][4]; };
constexpr int place_back(Acts& T, int latest, int code, int cap = CAP) {
  int g = latest;
  while (g > 0 && T.n[g] >= cap) --g;
  T.a[g][T.n[g]++] = code;
  return g;
}
constexpr int place_fwd(Acts& T, int earliest, int code, int cap) {
  int g = earliest;
  while (T.n[g] >= (g < 272 ? cap : 4)) ++g;
  T.a[g][T.n[g]++] = code;
  return g;
}
constexpr Acts make_acts() {
  Acts T{};
  for (int l = 15; l >= 1; --l) {
    int g = 2 * SC.ldu[l] - 5;
    for (int i = 3; i >= 0; --i) g = place_back(T, g - 1, A_X | (l << 4) | (i << 12), 1);
    g = place_back(T, g - 1, A_Q | (l << 4) | (1 << 12), 1);
    g = place_back(T, g - 1, A_Q | (l << 4), 1);
    if (!(l & 1)) {
      g = place_back(T, g - 1, A_PXC | (l << 4), 1);
      place_back(T, g - 6, A_PXR, 1);
    }
  }
  for (int l = SC.nld - 1; l >= 16; --l) {
    int g = 2 * SC.ldu[l] - 5;
    for (int i = 3; i >= 0; --i) g = place_back(T, g - 1, A_X | (l << 4) | (i << 12), SC.ldu[l] >= 100 ? 2 : 1);
  }
  for (int d = 15; d >= 8; --d) {
    int g = 2 * SC.ku[d] - 5;
    g = place_back(T, g - 1, A_K | (d << 4) | (1 << 12), 2);
    g = place_back(T, g - 1, A_K | (d << 4), 2);
  }
  place_fwd(T, 6, A_RR | (1 << 4), 3);
  for (int s = 0; s < 16; ++s) {
    int g = 2 * SC.base[s] + 4;
    const int nop = (s & 1) ? 18 : 17;
    for (int e = 0; e < nop; ++e) g = place_fwd(T, g + ((e & 1) && e < 14 ? 0 : 1), A_EP | (s << 4) | (e << 12), s >= 11 ? 3 : CAP);
  }
  return T;
}
constexpr Acts AC = make_acts();

#define MFMA(a, b, c) __builtin_amdgcn_mfma_f32_16x16x32_bf16((a), (b), (c), 0, 0, 0)

__global__ __launch_bounds__(768, 3) void fused_kernel(Params P) {
  extern __shared__ __attribute__((aligned(16))) char smem[];
  const lptr sm = (lptr)smem;
  const u32 smb = (u32)(uintptr_t)sm;
  const int tid = threadIdx.x;
  const int w = __builtin_amdgcn_readfirstlane(tid >> 6);
  const int lane = tid & 63;
  const int wg = blockIdx.x;
  const int xcd = wg & 7, idx = wg >> 3;
  const bool partA = idx < 10;
  const int g = lane >> 4, li = lane & 15, lq = li >> 2, lp = li & 3;
  unsigned long long stv[16]; unsigned long long cyv[2] = {0, 0}; unsigned long long wacc = 0;
#pragma unroll
  for (int i = 0; i < 16; ++i) stv[i] = 0;
  STAMP(0);
  if (w == 7) {
    unsigned long long pc;
    u32 len;
    asm volatile("s_getpc_b64 %0\n.Lfk_code_begin:\n\ts_mov_b32 %1, .Lfk_end0-.Lfk_code_begin\n\ts_max_i32 %1, %1, .Lfk_end1-.Lfk_code_begin\n\ts_max_i32 %1, %1, .Lfk_end2-.Lfk_code_begin\n\ts_max_i32 %1, %1, .Lfk_end3-.Lfk_code_begin\n\ts_max_i32 %1, %1, .Lfk_end4-.Lfk_code_begin\n\ts_max_i32 %1, %1, .Lfk_end5-.Lfk_code_begin" : "=s"(pc), "=&s"(len) :: "scc");
    const u32 off = min((u32)(lane + 64 * (idx % 3)) * 128u, len & ~3u);
    touch64((const char*)pc + off, smb + PFD);
  }
  gu64* const gflag = (gu64*)(uintptr_t)(P.ex + WS_FLAG);
  gu64* const gepoch = (gu64*)(uintptr_t)(P.ex + WS_EPOCH);
  if (idx >= 30) {
    if (w < 5) { if (w >= 3) delay(PFU); touch64(prefetch_addr(P, w, idx, xcd, lane), smb + PFD); }
    if (w == 8 && wg == 240) {
      const unsigned long long tag = __hip_atomic_load(gepoch, RLX_AGENT) + 1;
#pragma unroll 1
      for (int spin = 0; spin < (1 << 16); ++spin) {
        bool ok = true;
#pragma unroll
        for (int j = 0; j < 4; ++j) { const int f = min(lane + 64 * j, 239); ok &= (__hip_atomic_load(gflag + 16 * f, RLX_AGENT) == tag); }
        if (__all(ok)) break;
        __builtin_amdgcn_s_sleep(64);
      }
      if (lane == 0) __hip_atomic_store(gepoch, tag, RLX_AGENT);
    }
    vmcnt<0>();
    asm volatile(".Lfk_end1:");
    __builtin_amdgcn_endpgm();
    __builtin_unreachable();
  }
  if (tid < 64) *(LDSP u32*)(sm + FLAGS + 4 * tid) = 0;

  u32 kb, xb, yw;
  auto load_chunk = [&](int ck) {
    int ln = lane;
    asm volatile("" : "+v"(ln));
    const u32 ro = (u32)(64 * ck + ln) * ROWB + (u32)(xcd * 10 + idx) * 16;
    dma16(P.k, ro, smb + RAWK + ck * 1088 + 64);
    dma16(P.v, ro, smb + RAWV + ck * 1024);
    if (ln < 4) dma16(P.k, ck == 0 ? ro : ro - 4 * ROWB, ck == 0 ? smb + PFD : smb + RAWK + ck * 1088);
    dma16(P.q, ro, smb + RAWQ + ck * 1024);
  };

  if (partA) {
    const int grp = xcd * 10 + idx;
    const int ch0 = grp * 8;
    if (w >= 8) {
      const int h = w - 8;
      unsigned long long tag = 0;
      if (h == 0) {
        tag = __hip_atomic_load(gepoch, RLX_AGENT) + 1;
        if (lane < 4) dma16(P.shift_k, (u32)ch0 * 8 + 16 * lane, smb + SHK);
        if (lane < 4) *(LDSP u32x4*)(sm + RAWK + 16 * lane) = u32x4{0, 0, 0, 0};
        vmcnt<0>();
      }
      const int srow = 8 * g + 4 * (lp >> 1);
      const int shalf = 8 * (lp & 1);
      const u32 zsel = (lq & 1) ? 0xffffffffu : 0u;
      const u32 ka0 = (lq & 1) ? RAWK + h * 1088 + 16 * (srow + (lq >> 1)) + shalf : (u32)ZROW;
      const u32 va0 = (lq & 1) ? RAWV + h * 1024 + 16 * (srow + (lq >> 1)) + shalf : (u32)ZROW;
      const u32 qa0 = RAWQ + h * 1024 + 16 * (srow + lq) + shalf;
      const u32 xw = XIMG + (lane & 7) * XS + XPADB + 8 * (lane >> 3) + 128 * h;
      const u32 qw = YIMG + (lane & 7) * YS + 8 * (lane >> 3) + 128 * h;
      const u32 yrd = YIMG + lq * YS + 2 * (64 * h + 16 * g + 4 * lp);
      u16* const outp = P.out + ch0 + (size_t)(64 * h + lane) * (2 * DCH);
      wgbarrier();
      if (h == 0 && lane == 0) __hip_atomic_store(gflag + 16 * (160 + grp), tag, RLX_AGENT);
      __builtin_amdgcn_s_setprio(3);
      const u32x2 sw = *(LDSP u32x2*)(sm + SHK + 8 * (lane & 7));
      const float w0 = bflo(sw.x), w1 = bfhi(sw.x), w2 = bflo(sw.y), w3 = bfhi(sw.y);
      delay(PFH);
      load_chunk(8 + h);
#pragma unroll 1
      for (int it = 0; it < 4; ++it) {
        int b = it;
        asm volatile("" : "+s"(b));
        if (b < 2) wait_words(sm + FLAG2 + 4 * (4 * b + h), lane, 1, 1);
        else if (b == 2) vmcnt<4>(); else vmcnt<0>();
        u32x2 o[2], qq[2];
#pragma unroll
        for (int u = 0; u < 2; ++u) qq[u] = trread(sm + qa0 + b * 4096 + u * 512);
#pragma unroll
        for (int u = 0; u < 2; ++u)
          o[u] = kv4<32>(sm, ka0 + ((b * 4352 + u * 512) & zsel), va0 + ((b * 4096 + u * 512) & zsel), w0, w1, w2, w3);
#pragma unroll
        for (int u = 0; u < 2; ++u) {
          *(LDSP u32x2*)(sm + qw + 512 * b + 64 * u) = qq[u];
          *(LDSP u32x2*)(sm + xw + 512 * b + 64 * u) = o[u];
        }
        set_word(sm + FLAGS + 4 * h, (u32)(b + 1));
        if (b == 0) load_chunk(12 + h);
        if (b == 0) STAMP(12); if (b == 3) STAMP(13);
      }
      if (h == 0) {
#pragma unroll 1
        for (int spin = 0; spin < (1 << 16); ++spin) {
          if (__all(__hip_atomic_load(gflag + 16 * ((grp >> 3) * 16 + (lane & 15)), RLX_AGENT) == tag)) break;
          __builtin_amdgcn_s_sleep(8);
        }
        set_word(sm + FLAGS + 48, 1);
      } else {
        wait_words(sm + FLAGS + 48, lane, 1, 1);
      }
      __builtin_amdgcn_fence(__ATOMIC_ACQUIRE, "wavefront");
      STAMP(9);
      {
        const auto rx = __builtin_amdgcn_make_buffer_rsrc(P.ex + WS_EXX, 0, 4u << 20, 0x00020000);
        u32x4 d[8];
#pragma unroll
        for (int j = 0; j < 8; ++j)
          d[j] = __builtin_amdgcn_raw_buffer_load_b128(rx, (u32)(ch0 + 2 * h + (j & 1)) * 2048 + 1024 * (j >> 2) + 16 * lane + ((j >> 1) & 1) * (u32)(WS_EXQ - WS_EXX), 0, 16);
#pragma unroll
        for (int j = 0; j < 8; ++j) {
          const int cl = 2 * h + (j & 1);
          const u32 da = ((j >> 1) & 1) ? YIMG + cl * YS : XIMG + cl * XS + XPADB;
          *(LDSP u32x4*)(sm + da + 2048 + 1024 * (j >> 2) + 16 * lane) = d[j];
          if (j == 3) set_word(sm + FLAGS + 4 * h, 6);
        }
        set_word(sm + FLAGS + 4 * h, 8);
      }
      STAMP(14);
#pragma unroll 1
      for (int b = 0; b < 8; ++b) {
        wait_words(sm + FLAGS + 16, lane, 8, (u32)(b + 1));
        u32x2 a = trread(sm + yrd + 512 * b);
        u32x2 c = trread(sm + yrd + 512 * b + 4 * YS);
        *(u32x4*)(outp + (size_t)b * 256 * (2 * DCH)) = u32x4{a.x, a.y, c.x, c.y};
      }
      STAMP_OUT();
      asm volatile(".Lfk_end2:\n.Lfk_end3:");
      __builtin_amdgcn_endpgm();
      __builtin_unreachable();
    }
    const int ch = ch0 + w;
    const u32 kimg = KIMG + w * KS;
#pragma unroll
    for (int j = 0; j < 2; ++j) dma16(P.ssm_k, (u32)ch * (SEQ * 2) + 1024 * j + 16 * lane, smb + kimg + KPADB + 1024 * j);
    if (w < 3) touch64(prefetch_addr(P, w, idx, xcd, lane), smb + PFD);
    if (lane < 10) *(LDSP u32x4*)(sm + XIMG + w * XS + 16 * lane) = u32x4{0, 0, 0, 0};
    if (lane < 6) *(LDSP u32x4*)(sm + kimg + 16 * lane) = u32x4{0, 0, 0, 0};
    const float dsk = ld_bf_uniform(P.ssm_D, ch);
    if (w < 2) vmcnt<0>();
    wgbarrier();
    STAMP(1);
    load_chunk(w);
    vmcnt<0>();
    set_word(sm + FLAG2 + 4 * w, 1);
    STAMP(2);
#pragma unroll
    for (int j = 2; j < 4; ++j) dma16(P.ssm_k, (u32)ch * (SEQ * 2) + 1024 * j + 16 * lane, smb + kimg + KPADB + 1024 * j);
    if (lane == 0) {
      LDSP u16* k0 = (LDSP u16*)(sm + kimg + KPADB);
      *k0 = (u16)pkbf(bf2f(*k0) + dsk, 0.f);
    }
    wavefence();
    kb = kimg + 8 * (4 * lp + 8 * g + lq);
    xb = XIMG + w * XS + 224 + 8 * (lp - 8 * g - lq);
    yw = YIMG + w * YS + 2 * lane;
  } else {
    const int bi = xcd * 20 + (idx - 10);
    const int ch0 = bi * 4;
    if (w >= 8) {
      const int h = w - 8;
      const int pcol = bi >> 4, pchunk = bi & 15;
      __builtin_amdgcn_s_setprio(3);
      unsigned long long tag = 0;
      if (h == 0) tag = __hip_atomic_load(gepoch, RLX_AGENT) + 1;
      if (h < 3) touch64(prefetch_addr(P, h, idx, xcd, lane), smb + PFD);
      wgbarrier();
      delay(PFP);
      {
        const u32 po = (u32)(1024 + 64 * pchunk + 16 * h + (lane >> 3)) * ROWB + (u32)pcol * 128 + 16 * (lane & 7);
#pragma unroll
        for (int j = 0; j < 2; ++j) {
          dma16(P.k, po + j * 8 * ROWB, smb + PRAWK + 512 + h * 2048 + j * 1024);
          dma16(P.v, po + j * 8 * ROWB, smb + PRAWV + h * 2048 + j * 1024);
          dma16(P.q, po + j * 8 * ROWB, smb + PRAWQ + h * 2048 + j * 1024);
        }
        if (h == 0 && lane < 32) dma16(P.k, po - 4 * ROWB, smb + PRAWK);
        if (h == 1 && lane < 32) dma16(P.shift_k, (u32)pcol * 512 + 16 * lane, smb + SHK);
      }
      {
        const u32x4 ones = {0x3F803F80u, 0x3F803F80u, 0x3F803F80u, 0x3F803F80u};
#pragma unroll
        for (int i = 0; i < 4; ++i) *(LDSP u32x4*)(sm + B_YIMG + h * YS + 1024 * i + 16 * lane) = ones;
      }
      if (lane < 10) *(LDSP u32x4*)(sm + XIMG + h * XS + 16 * lane) = u32x4{0, 0, 0, 0};
      delay(PFU - PFP);
      {
        const int cu = (h < 2) ? 3 + h : 3 + ((idx - 10) & 1);
        const int su = (h < 2) ? idx : (idx - 10) >> 1;
        touch64(prefetch_addr(P, cu, su, xcd, lane), smb + PFD);
      }
      vmcnt<1>();
      set_word(sm + FLAG2 + 4 * h, 1);
      wait_words(sm + FLAG2, lane, 4, 1);
      STAMP(12);
      {
        const u32x2 sw = *(LDSP u32x2*)(sm + SHK + 8 * (16 * h + li));
        const float w0 = bflo(sw.x), w1 = bfhi(sw.x), w2 = bflo(sw.y), w3 = bfhi(sw.y);
        const u32 co = 32 * h + 8 * lp;
        const u32 stg = PSTG + h * XS;
        const u32 sw_ = stg + li * 128 + 8 * (g & 1);
        const u32 sr_ = stg + (lane >> 3) * 128 + 16 * ((lane ^ (lane >> 3)) & 7);
        const auto rx = __builtin_amdgcn_make_buffer_rsrc(P.ex + WS_EXX, 0, 4u << 20, 0x00020000);
        const u32 eo = (u32)(pcol * 64 + 16 * h + (lane >> 3)) * 2048 + 128 * pchunk + 16 * (lane & 7);
        const u32 zst = (lq & 1) ? 256u : 0u;
        const u32 ro = 128 * (4 * g + (lq >> 1)) + co;
        const u32 ka = (lq & 1) ? PRAWK + ro : (u32)ZROW, va = (lq & 1) ? PRAWV + ro : (u32)ZROW;
#pragma unroll
        for (int u = 0; u < 4; ++u)
          *(LDSP u32x2*)(sm + sw_ + 16 * (((2 * u + (g >> 1)) ^ li) & 7)) = trread(sm + PRAWQ + 128 * (16 * u + 4 * g + lq) + co);
        wavefence();
#pragma unroll
        for (int j = 0; j < 2; ++j)
          __builtin_amdgcn_raw_buffer_store_b128(*(const LDSP u32x4*)(sm + sr_ + 1024 * j), rx, eo + j * 8 * 2048 + (u32)(WS_EXQ - WS_EXX), 0, 16);
        wavefence();
#pragma unroll 2
        for (int u = 0; u < 4; ++u) {
          float kx[8], vx[4], kv[4];
#pragma unroll
          for (int j = 0; j < 4; ++j) { u32x2 r = trread(sm + ka + zst * (8 * u + j)); kx[2 * j] = __uint_as_float(r.x); kx[2 * j + 1] = __uint_as_float(r.y); }
#pragma unroll
          for (int j = 0; j < 2; ++j) { u32x2 r = trread(sm + va + zst * (8 * u + j)); vx[2 * j] = __uint_as_float(r.x); vx[2 * j + 1] = __uint_as_float(r.y); }
#pragma unroll
          for (int j = 0; j < 4; ++j)
            kv[j] = vmul(vfma(kx[j + 4], w0, vfma(kx[j + 3], w1, vfma(kx[j + 2], w2, vmul(kx[j + 1], w3)))), vx[j]);
          *(LDSP u32x2*)(sm + sw_ + 16 * (((2 * u + (g >> 1)) ^ li) & 7)) = u32x2{pkbf(kv[0], kv[1]), pkbf(kv[2], kv[3])};
        }
        wavefence();
#pragma unroll
        for (int j = 0; j < 2; ++j)
          __builtin_amdgcn_raw_buffer_store_b128(*(const LDSP u32x4*)(sm + sr_ + 1024 * j), rx, eo + j * 8 * 2048, 0, 16);
      }
      STAMP(1);
      const u32 rowoff = (u32)(64 * h + lane) * ROWB + (u32)(ch0 & ~7) * 2;
      dma16(P.u, rowoff, smb + RAWU + 1024 * h);
      dma16(P.u, rowoff + 256 * ROWB, smb + RAWU + 4096 + 1024 * h);
      vmcnt<2>();
      set_word(sm + FLAG2 + 16 + 4 * h, 1);
      wait_words(sm + FLAG2 + 16, lane, 4, 1);
      if (h == 0 && lane == 0) __hip_atomic_store(gflag + 16 * bi, tag, RLX_AGENT);
      STAMP(9);
      const u32 ua = RAWU + 1024 * h + 16 * (16 * g + 4 * lp + lq) + 2 * (ch0 & 4);
      const u32 xw = XIMG + (lane & 3) * XS + XPADB + 8 * (lane >> 2) + 128 * h;
      const u32 yrd = B_YIMG + lq * YS + 2 * (64 * h + 16 * g + 4 * lp);
      u16* const outp = P.out + DCH + ch0 + (size_t)(64 * h + lane) * (2 * DCH);
#pragma unroll 1
      for (int it = 0; it < 8; ++it) {
        int i = it;
        asm volatile("" : "+s"(i));
        if (i + 2 < 8) dma16(P.u, rowoff + (u32)(i + 2) * (256 * ROWB), smb + RAWU + 4096 * (i + 2) + 1024 * h);
        if (i < 6) vmcnt<2>(); else if (i == 6) vmcnt<1>(); else vmcnt<0>();
        *(LDSP u32x2*)(sm + xw + 512 * i) = trread(sm + ua + 4096 * i);
        set_word(sm + FLAGS + 4 * h, (u32)(i + 1));
        if (i == 0) STAMP(13); if (i == 7) STAMP(14);
      }
#pragma unroll 1
      for (int i = 0; i < 8; ++i) {
        wait_words(sm + FLAGS + 16, lane, 4, (u32)(i + 1));
        *(u32x2*)(outp + (size_t)i * 256 * (2 * DCH)) = trread(sm + yrd + 512 * i);
      }
      STAMP_OUT();
      asm volatile(".Lfk_end4:");
      __builtin_amdgcn_endpgm();
      __builtin_unreachable();
    }
    const int cw = w & 3, ps = w >> 2;
    const int ch = ch0 + cw;
    const size_t o = (size_t)ch * NST + 64 * ps + lane;
    const u16 i_ar = P.A_re[o], i_ai = P.A_im[o], i_br = P.B_re[o], i_bi = P.B_im[o], i_cr = P.C_re[o], i_ci = P.C_im[o];
    const float ldt = ld_bf_uniform(P.log_dt, ch);
    const float dsk = ld_bf_uniform(P.Dskip, ch);
    wgbarrier();
    const float dt = rbf(expf(ldt));
    const float zr = dt * bf2f(i_ar), zi = dt * bf2f(i_ai);
    const cf dB = {dt * bf2f(i_br), dt * bf2f(i_bi)};
    const cf C = {bf2f(i_cr), bf2f(i_ci)};
    const cf W = cmul(C, dB);
    float mx = fmaxf(fabsf(W.r), fabsf(W.i));
    STAMP(1);
    {
      int m = __float_as_int(mx);
      m = max(m, __builtin_amdgcn_update_dpp(0, m, 0x111, 0xf, 0xf, false));
      m = max(m, __builtin_amdgcn_update_dpp(0, m, 0x112, 0xf, 0xf, false));
      m = max(m, __builtin_amdgcn_update_dpp(0, m, 0x114, 0xf, 0xf, false));
      m = max(m, __builtin_amdgcn_update_dpp(0, m, 0x118, 0xf, 0xf, false));
      int a0 = __builtin_amdgcn_readlane(m, 15), a1 = __builtin_amdgcn_readlane(m, 31);
      int a2 = __builtin_amdgcn_readlane(m, 47), a3 = __builtin_amdgcn_readlane(m, 63);
      mx = __int_as_float(max(max(a0, a1), max(a2, a3)));
    }
    int ex = (int)((__float_as_uint(mx) >> 23) & 0xff);
    ex = min(max(ex, 30), 240);
    const float sY = __uint_as_float((u32)(267 - ex) << 23);
    const float so = __uint_as_float((u32)(ex - 27) << 23);
    const float SX = 16384.f;

    f32x4 ak[3];
    const u32 stg = STG + w * STS;
    const u32 rb = stg + li * 272 + g * 16;
    const u32 m0v = smb + w * STS;
#define STGW(row, val_r, val_i) \
    asm volatile("ds_write_addtid_b32 %0 offset:%1" :: "v"(pkh((val_r), (val_i))), "i"(STG + (row) * 272) : "memory")
    {
      const cf E1 = cexp_pow2(zr, zi, 1.f);
      const cf E8c = cexp_pow2(zr, -zi, 8.f);
      const cf E128 = cexp_pow2(zr, zi, 128.f);
      const cf E1024 = cexp_pow2(zr, zi, 1024.f);
      const cf E2 = cmul(E1, E1);
      const cf E4 = cmul(E2, E2);
      u32 keep;
      asm volatile("s_mov_b32 %0, m0\n\ts_mov_b32 m0, %1\n\ts_nop 0" : "=&s"(keep) : "s"(m0v) : "memory");
      cf y = {W.r * sY, -(W.i * sY)};
      cf P1 = {E128.r * SX, E128.i * SX};
      cf v0 = {SX, 0.f}, v1 = P1;
#pragma unroll
      for (int b = 0; b < 8; ++b) {
        STGW(2 * b, y.r, y.i); y = cmul(y, E8c);
        STGW(16 + b, v0.r, v0.i); if (b < 7) v0 = cmul(v0, E1);
        STGW(2 * b + 1, y.r, y.i); if (b < 7) y = cmul(y, E8c);
        STGW(24 + b, v1.r, v1.i); if (b < 7) v1 = cmul(v1, E1);
      }
      f16x8 yf[4];
      const f32x4 zero = {0.f, 0.f, 0.f, 0.f};
#pragma unroll
      for (int s = 0; s < 4; ++s) {
        yf[s] = *(const LDSP f16x8*)(sm + rb + 64 * s);
        f16x8 xf = *(const LDSP f16x8*)(sm + rb + 16 * 272 + 64 * s);
        ak[0] = __builtin_amdgcn_mfma_f32_16x16x32_f16(xf, yf[s], s ? ak[0] : zero, 0, 0, 0);
      }
      asm volatile("" ::: "memory");
      cf Pa = cmul(P1, E128);
      cf Pb = {E1024.r * SX, E1024.i * SX};
#pragma unroll
      for (int m = 0; m < 2; ++m) {
        cf v = Pa;
#pragma unroll
        for (int r = 0; r < 4; ++r) {
          STGW(4 * m + r, v.r, v.i); if (r < 3) v = cmul(v, E2);
          if (r & 1) { STGW(16 + 2 * m + (r >> 1), Pb.r, Pb.i); Pb = cmul(Pb, E128); }
        }
        Pa = cmul(Pa, E128);
      }
#pragma unroll
      for (int m = 0; m < 4; ++m) {
        STGW(8 + 2 * m, Pa.r, Pa.i);
        cf v = cmul(Pa, E4);
        STGW(20 + m, Pb.r, Pb.i); Pb = cmul(Pb, E128);
        STGW(9 + 2 * m, v.r, v.i);
        if (m < 3) Pa = cmul(Pa, E128);
      }
      STGW(24, Pb.r, Pb.i);
      asm volatile("s_mov_b32 m0, %0" :: "s"(keep) : "memory");
#pragma unroll
      for (int s = 0; s < 4; ++s) {
        f16x8 x1 = *(const LDSP f16x8*)(sm + rb + 64 * s);
        f16x8 x2 = *(const LDSP f16x8*)(sm + rb + 16 * 272 + 64 * s);
        ak[1] = __builtin_amdgcn_mfma_f32_16x16x32_f16(x1, yf[s], s ? ak[1] : zero, 0, 0, 0);
        ak[2] = __builtin_amdgcn_mfma_f32_16x16x32_f16(x2, yf[s], s ? ak[2] : zero, 0, 0, 0);
      }
    }
#undef STGW
#pragma unroll
    for (int i = 0; i < 3; ++i) ak[i] *= so;
    STAMP(2);
    asm volatile("" ::: "memory");
    const u32 kimg = B_KIMG + cw * KS;
    if (ps) {
#pragma unroll
      for (int i = 0; i < 3; ++i) *(LDSP f32x4*)(sm + kimg + 1024 * i + 16 * lane) = ak[i];
      asm volatile("s_waitcnt lgkmcnt(0)" ::: "memory");
      *(volatile LDSP u32*)(sm + FLAGS + 48 + 4 * cw) = 1;
      STAMP_OUT();
      asm volatile(".Lfk_end5:");
      __builtin_amdgcn_endpgm();
      __builtin_unreachable();
    }
    wait_words(sm + FLAGS + 48 + 4 * cw, lane, 1, 1);
    {
      const u32 tb = stg;
#pragma unroll
      for (int i = 0; i < 3; ++i) ak[i] += *(const LDSP f32x4*)(sm + kimg + 1024 * i + 16 * lane);
      wavefence();
      const int b = li, p = g;
      if (lane < 6) *(LDSP u32x4*)(sm + kimg + 16 * lane) = u32x4{0, 0, 0, 0};
      {
        float a0 = ak[0][0] + (lane == 0 ? dsk : 0.f), a1 = ak[0][1], a2 = ak[0][2], a3 = ak[0][3];
        *(LDSP u32x2*)(sm + kimg + KPADB + 2 * (128 * (p >> 1) + 8 * b + 4 * (p & 1))) = u32x2{pkbf(a0, a1), pkbf(a2, a3)};
      }
      {
        float a0 = ak[1][0], a1 = ak[1][1], a2 = ak[1][2], a3 = ak[1][3];
        int i01, i23;
        if (p < 2) { i01 = 64 * p + 4 * b; i23 = i01 + 2; }
        else { i01 = 128 + 64 * (p - 2) + 2 * b; i23 = i01 + 32; }
        *(LDSP u32x2*)(sm + tb + 4 * i01) = u32x2{pkbf(a0, a0), pkbf(a1, a1)};
        *(LDSP u32x2*)(sm + tb + 4 * i23) = u32x2{pkbf(a2, a2), pkbf(a3, a3)};
#pragma unroll
        for (int j = 0; j < 4; ++j) {
          float a = ak[2][j];
          int ii = 256 + 16 * (4 * p + j) + b;
          *(LDSP u32*)(sm + tb + 4 * ii) = pkbf(a, a);
        }
      }
      wavefence();
      const LDSP u32* TB = (const LDSP u32*)(sm + tb);
      {
        const int i0 = 4 * (lane & 31);
        u32 g0 = TB[i0], g1 = TB[i0 + 1], g2 = TB[i0 + 2], g3 = TB[i0 + 3], g4 = TB[i0 + 4];
        u32x4 ov = {g0, (g1 & 0xffffu) | (g2 & 0xffff0000u), g2, (g3 & 0xffffu) | (g4 & 0xffff0000u)};
        *(LDSP u32x4*)(sm + kimg + KPADB + 16 * (32 + (lane & 31))) = ov;
      }
      {
        const int i0 = 128 + 2 * lane;
        u32 g0 = TB[i0], g1 = TB[i0 + 1], g2 = TB[i0 + 2];
        u32x4 ov = {g0, (g0 & 0xffffu) | (g1 & 0xffff0000u), g1, g2};
        *(LDSP u32x4*)(sm + kimg + KPADB + 16 * (64 + lane)) = ov;
      }
#pragma unroll
      for (int hh = 0; hh < 2; ++hh) {
        const int i0 = 256 + 64 * hh + lane;
        u32 g0 = TB[i0], g1 = TB[i0 + 1];
        u32 mid = (lane & 1) ? g1 : ((g0 & 0xffffu) | (g1 & 0xffff0000u));
        u32x4 ov = {g0, g0, mid, g1};
        *(LDSP u32x4*)(sm + kimg + KPADB + 16 * (128 + 64 * hh + lane)) = ov;
      }
    }
    wavefence();
    kb = kimg + 8 * (4 * lp + 8 * g + lq);
    xb = XIMG + cw * XS + 224 + 8 * (lp - 8 * g - lq);
    yw = B_YIMG + cw * YS + 2 * lane;
  }

  {
    const bool l63 = (lane == 63);
    const lptr pxp = sm + FLAGS + 4 * (lane & 3);
    const lptr pyp = sm + FLAGS + 16 + 4 * w;
    u32x2 kr[16][2], xr[40][4];
    f32x4 ac[16][2];
    const f32x4 zero = {0.f, 0.f, 0.f, 0.f};
    float ts[16][2], u2[16][2], u1[16][2], yy[16][2];
    u32 pk[16];
    u32 qv[3][2] = {{0, 0}, {0, 0}, {0, 0}}, px = 0;
    const lptr ywp = sm + yw;
    const u32 qa = (u32)(uintptr_t)ywp;
#define RDQ(s_, i_) asm volatile("ds_read_u16_d16_hi %0, %1 offset:%2" : "+v"(qv[(s_) % 3][i_]) : "v"(qa), "i"((s_) * 256 + 128 * (i_)) : "memory")
#define RDXP(l_, i_) xr[l_][i_] = trread(sm + xb + 256 * SC.ldm[l_] + ((i_) >> 1) * 128 + (((i_) & 1) ? 0 : 32))
#pragma unroll
    for (int d = 0; d < 8; ++d) { kr[d][0] = trread(sm + kb + 256 * d); kr[d][1] = trread(sm + kb + 256 * d + 32); }
    STAMP(3);
    wait_words(sm + FLAGS, lane, 4, 1);
    STAMP(4);
    CSTAMP(0);
    RDQ(0, 0); RDQ(0, 1);
    RDXP(0, 0); RDXP(0, 1); RDXP(0, 2); RDXP(0, 3);
    SCHED0();
    static_for<0, NGAP>([&](auto G_) {
      constexpr int G = decltype(G_)::value;
      if constexpr (G < 272) {
        constexpr int u = G >> 1, par = G & 1;
        constexpr int p = SC.p[u], d = SC.d[u], l = SC.ld[u];
        ac[p][par] = MFMA(frag(kr[d][0], kr[d][1]), frag(xr[l][2 * par], xr[l][2 * par + 1]), SC.first[u] ? zero : ac[p][par]);
        SCHED0();
      }
      static_for<0, AC.n[G]>([&](auto I_) {
        constexpr int code = AC.a[G][decltype(I_)::value];
        constexpr int kind = code & 15, a0 = (code >> 4) & 255, a1 = code >> 12;
        if constexpr (kind == A_X) RDXP(a0, a1);
        if constexpr (kind == A_K) kr[a0][a1] = trread(sm + kb + 256 * a0 + 32 * a1);
        if constexpr (kind == A_Q) RDQ(a0, a1);
        if constexpr (kind == A_PXR) px = *(volatile LDSP u32*)pxp;
        if constexpr (kind == A_RR) vmcnt<0>();
        if constexpr (kind == A_PXC) {
          if (__builtin_expect(!__all(px >= (u32)(a0 / 2 + 1)), 0)) { unsigned long long w0 = __builtin_amdgcn_s_memrealtime(); wait_words(sm + FLAGS, lane, 4, (u32)(a0 / 2 + 1)); wacc += __builtin_amdgcn_s_memrealtime() - w0; }
          asm volatile("" ::: "memory");
        }
        if constexpr (kind == A_EP) {
          constexpr int s = a0, e = a1 >> 1, o = a1 & 1;
          if constexpr (a1 < 14) {
            if constexpr (e == 0) {
              float cur = ac[s][o][3], prev = 0.f;
              if constexpr (o) prev = ac[s][0][3]; else if constexpr (s > 0) prev = ac[s - 1][1][3];
              ts[s][o] = l63 ? prev : cur;
            }
            if constexpr (e == 1) { float a = ac[s][o][2]; u2[s][o] = a + ror1(ts[s][o]); }
            if constexpr (e == 2) {
              float prev = 0.f;
              if constexpr (o) prev = u2[s][0]; else if constexpr (s > 0) prev = u2[s - 1][1];
              ts[s][o] = l63 ? prev : u2[s][o];
            }
            if constexpr (e == 3) { float a = ac[s][o][1]; u1[s][o] = a + ror1(ts[s][o]); }
            if constexpr (e == 4) {
              float prev = 0.f;
              if constexpr (o) prev = u1[s][0]; else if constexpr (s > 0) prev = u1[s - 1][1];
              ts[s][o] = l63 ? prev : u1[s][o];
            }
            if constexpr (e == 5) { float a = ac[s][o][0]; yy[s][o] = a + ror1(ts[s][o]); asm volatile("" : "+v"(yy[s][o])); }
            if constexpr (e == 6) {
              asm volatile("" : "+v"(qv[s % 3][o]));
              yy[s][o] *= __uint_as_float(qv[s % 3][o]);
              asm volatile("" : "+v"(yy[s][o]));
            }
          }
          if constexpr (a1 == 14) pk[s] = pkbf(yy[s][0], yy[s][1]);
          if constexpr (a1 == 15) *(LDSP u16*)(ywp + s * 256) = (u16)pk[s];
          if constexpr (a1 == 16) *(LDSP u16*)(ywp + s * 256 + 128) = (u16)(pk[s] >> 16);
          if constexpr (a1 == 17) {
            asm volatile("" ::: "memory");
            *(volatile LDSP u32*)pyp = (u32)(s / 2 + 1);
            asm volatile("" ::: "memory");
            if (s == 3) STAMP(5); if (s == 7) STAMP(6); if (s == 11) STAMP(7); if (s == 15) { STAMP(8); CSTAMP(1); }
          }
        }
        SCHED0();
      });
    });
  }
  STAMP_OUT();
  asm volatile(".Lfk_end0:");
  __builtin_amdgcn_endpgm();
}

__global__ void report_kernel(unsigned long long* ws) {
  unsigned long long t0 = __builtin_amdgcn_s_memrealtime();
  unsigned long long mn = ~0ull, mx = 0;
  for (int i = threadIdx.x; i < 240 * 12; i += 64) { int b = i / 12, w = i % 12; unsigned long long v = ws[(b * 16 + w) * 16]; if (v < mn) mn = v;
     unsigned long long e = ws[(b * 16 + w) * 16 + 11]; if (e > mx) mx = e; }
  for (int s = 1; s < 64; s <<= 1) { unsigned long long a = __shfl_xor(mn, s), c = __shfl_xor(mx, s); if (a < mn) mn = a; if (c > mx) mx = c; }
  unsigned long long q = 0;
  if (q > 20000) q = 20000;
  unsigned long long tend = t0 + 1000 + 50 * q;
  while (__builtin_amdgcn_s_memrealtime() < tend) __builtin_amdgcn_s_sleep(1);
}
extern "C" void kernel_launch(void* const* d_in, const int* in_sizes, int n_in,
                              void* d_out, int out_size, void* d_ws, size_t ws_size,
                              hipStream_t stream) {
  Params P;
  P.q = (const u16*)d_in[0];
  P.k = (const u16*)d_in[1];
  P.v = (const u16*)d_in[2];
  P.shift_k = (const u16*)d_in[3];
  P.ssm_k = (const u16*)d_in[4];
  P.ssm_D = (const u16*)d_in[5];
  P.u = (const u16*)d_in[6];
  P.A_re = (const u16*)d_in[7];
  P.A_im = (const u16*)d_in[8];
  P.B_re = (const u16*)d_in[9];
  P.B_im = (const u16*)d_in[10];
  P.C_re = (const u16*)d_in[11];
  P.C_im = (const u16*)d_in[12];
  P.log_dt = (const u16*)d_in[13];
  P.Dskip = (const u16*)d_in[14];
  P.out = (u16*)d_out;
  P.ws = (unsigned long long*)d_ws;
  P.ex = (char*)d_ws + (16 << 20);
  (void)hipFuncSetAttribute((const void*)fused_kernel, hipFuncAttributeMaxDynamicSharedMemorySize, LDS_BYTES);
  fused_kernel<<<256, 768, LDS_BYTES, stream>>>(P);
  report_kernel<<<1, 64, 0, stream>>>((unsigned long long*)d_ws);
}
```

```cpp
#include <hip/hip_runtime.h>
#include <hip/hip_bf16.h>
#include <stdint.h>

typedef __attribute__((ext_vector_type(4))) short s16x4;
typedef __attribute__((ext_vector_type(8))) _Float16 f16x8;
typedef __attribute__((ext_vector_type(8))) __bf16 bf16x8;
typedef __attribute__((ext_vector_type(2))) __bf16 bf16x2;
typedef __attribute__((ext_vector_type(2))) _Float16 f16x2;
typedef __attribute__((ext_vector_type(2))) float f32x2;
typedef __attribute__((ext_vector_type(4))) float f32x4;
typedef __attribute__((ext_vector_type(2))) unsigned int u32x2;
typedef __attribute__((ext_vector_type(4))) unsigned int u32x4;
typedef unsigned int u32;
typedef unsigned short u16;
#define LDSP __attribute__((address_space(3)))
#define DEV __device__ __forceinline__

constexpr int SEQ = 2048;
constexpr int DCH = 640;
constexpr int NST = 128;
constexpr int ROWB = DCH * 2;

constexpr int XS = 4272;
constexpr int XPADB = 160;
constexpr int YS = 4160;
constexpr int KS = 4224;
constexpr int KPADB = 96;
constexpr int FLAGS = 0;
constexpr int ZROW = 64;
constexpr int FLAG2 = 192;
constexpr int FLAG3 = 256;
constexpr int PFD = 320;
constexpr int SHK = 1024;
constexpr int XIMG = 2048;
constexpr int PUNIT = 8704 + 8192 + 8192;
constexpr int YIMG = 36224;
constexpr int KIMG = 69504;
constexpr int A_PRAW = 103296;
constexpr int A_PSTG = YIMG;
constexpr int STS = 8704;
constexpr int STG = 19200;
constexpr int H_XIMG = STG + 4 * STS;
constexpr int H_KIMG = H_XIMG + 4 * XS;
constexpr int B_KIMG = 88832;
constexpr int B_YIMG = 105728;
constexpr int HYS = 1344;
constexpr int H_YIMG = 122368;
constexpr int RAWU = 127744;
constexpr int B_PRAW = RAWU + 8192;
constexpr int B_PSTG = XIMG + 2208;
constexpr int LDS_BYTES = 161024;

constexpr size_t WS_EPOCH = 0;
constexpr size_t WS_FLAG = 4096;
constexpr size_t WS_EXX = 1 << 20;
constexpr size_t WS_EXQ = 4 << 20;

DEV float bf2f(u16 v) { return __uint_as_float(((u32)v) << 16); }
DEV float bflo(u32 v) { return __uint_as_float(v << 16); }
DEV float bfhi(u32 v) { return __uint_as_float(v & 0xffff0000u); }
DEV u32 pkbf(float a, float b) {
  f32x2 t = {a, b};
  bf16x2 r = __builtin_convertvector(t, bf16x2);
  return __builtin_bit_cast(u32, r);
}
DEV u32 pkh(float a, float b) {
  f32x2 t = {a, b};
  f16x2 r = __builtin_convertvector(t, f16x2);
  return __builtin_bit_cast(u32, r);
}
DEV float rbf(float a) { return bflo(pkbf(a, a)); }
DEV float dot2(u32 a, u32 b, float c) {
  return __builtin_amdgcn_fdot2_f32_bf16(__builtin_bit_cast(bf16x2, a), __builtin_bit_cast(bf16x2, b), c, false);
}
DEV float vfma(float a, float b, float c) { float d; asm("v_fma_f32 %0, %1, %2, %3" : "=v"(d) : "v"(a), "v"(b), "v"(c)); return d; }
DEV float vmul(float a, float b) { float d; asm("v_mul_f32 %0, %1, %2" : "=v"(d) : "v"(a), "v"(b)); return d; }
typedef __attribute__((address_space(1))) unsigned long long gu64;
#define RLX_AGENT __ATOMIC_RELAXED, __HIP_MEMORY_SCOPE_AGENT
typedef LDSP char* lptr;
DEV u32x2 trread(lptr p) {
  s16x4 r = __builtin_amdgcn_ds_read_tr16_b64_v4i16((LDSP s16x4*)p);
  return __builtin_bit_cast(u32x2, r);
}
DEV bf16x8 frag(u32x2 a, u32x2 b) {
  u32x4 f = {a.x, a.y, b.x, b.y};
  return __builtin_bit_cast(bf16x8, f);
}
DEV float ror1(float v) {
  int r = __builtin_amdgcn_update_dpp(0, __float_as_int(v), 0x13C, 0xf, 0xf, false);
  return __int_as_float(r);
}
DEV void wavefence() { __builtin_amdgcn_fence(__ATOMIC_ACQ_REL, "wavefront"); __builtin_amdgcn_wave_barrier(); }
DEV void wgbarrier() {
  asm volatile("s_waitcnt lgkmcnt(0)" ::: "memory");
  __builtin_amdgcn_s_barrier();
  asm volatile("" ::: "memory");
}
DEV void dma16(const void* base, u32 voff, u32 ldsaddr) {
  u32 keep;
  ldsaddr = __builtin_amdgcn_readfirstlane(ldsaddr);
  const unsigned long long b64 = (unsigned long long)(uintptr_t)base;
  const unsigned long long sb = ((unsigned long long)(u32)__builtin_amdgcn_readfirstlane((u32)(b64 >> 32)) << 32) |
                                (u32)__builtin_amdgcn_readfirstlane((u32)b64);
  asm volatile("s_mov_b32 %0, m0\n\ts_mov_b32 m0, %3\n\ts_nop 2\n\tglobal_load_lds_dwordx4 %1, %2\n\ts_mov_b32 m0, %0"
               : "=&s"(keep) : "v"(voff), "s"(sb), "s"(ldsaddr) : "memory");
}
DEV void touch64(const void* p, u32 ldsaddr) {
  u32 keep;
  asm volatile("s_mov_b32 %0, m0\n\ts_mov_b32 m0, %2\n\ts_nop 0\n\tglobal_load_lds_dword %1, off\n\ts_mov_b32 m0, %0"
               : "=&s"(keep) : "v"(p), "s"(ldsaddr) : "memory");
}
template <int N> DEV void vmcnt() { asm volatile("s_waitcnt vmcnt(%0)" :: "i"(N) : "memory"); }
#define SCHED0() __builtin_amdgcn_sched_barrier(0)

template <int V> struct IC { static constexpr int value = V; };
template <int I, int N, class F> DEV void static_for(F&& f) {
  if constexpr (I < N) { f(IC<I>{}); static_for<I + 1, N>(f); }
}

struct cf { float r, i; };
DEV cf cmul(cf a, cf b) {
  cf o;
  o.r = fmaf(a.r, b.r, -(a.i * b.i));
  o.i = fmaf(a.r, b.i, a.i * b.r);
  return o;
}
DEV cf cexp_pow2(float zr, float zi, float x) {
  float mag = __builtin_amdgcn_exp2f((zr * x) * 1.44269504088896340736f);
  float th = zi * x;
  const float C_HI = 0.15915494f;
  const float C_LO = 6.4206382e-09f;
  float f = fmaf(th, C_LO, fmaf(th, C_HI, -rintf(th * C_HI)));
  cf o;
  o.r = mag * __builtin_amdgcn_cosf(f);
  o.i = mag * __builtin_amdgcn_sinf(f);
  return o;
}

#define STAMP(n)
#define CSTAMP(n)
#define STAMP_OUT()

struct Params {
  const u16 *q, *k, *v, *shift_k, *ssm_k, *ssm_D;
  const u16 *u, *A_re, *A_im, *B_re, *B_im, *C_re, *C_im, *log_dt, *Dskip;
  u16* out;
  unsigned long long* ws;
  char* ex;
};

DEV float ld_bf_uniform(const u16* p, int idx) {
  u32 d = *(const __attribute__((address_space(4))) u32*)(uintptr_t)(p + (idx & ~1));
  return (idx & 1) ? bfhi(d) : bflo(d);
}

DEV const char* prefetch_addr(const Params& P, int cls, int slice, int xcd, int lane) {
  asm volatile("" : "+v"(lane));
  const unsigned long long TAB = 0xFEDCull;
  const u32 nib = (u32)(TAB >> (4 * (2 * cls + (lane >> 5)))) & 15u;
  const bool second = (lane >> 4) & 1;
  const u16* base = (nib < 8) ? (second ? P.v : P.k) : (nib < 12 ? P.q : P.u);
  const u32 blk = (nib < 8) ? nib : 2 * (nib & 3) + (second ? 1 : 0);
  const u32 row = blk * 256u + (u32)slice * 8u + ((lane >> 1) & 7);
  return (const char*)base + (row * ROWB + (u32)(((160 * xcd) >> 7) + (lane & 1)) * 128u);
}
DEV void wait_words(lptr p, int lane, int n, u32 v) {
#pragma unroll 1
  for (int spin = 0; spin < (1 << 16); ++spin) {
    u32 x = *(volatile LDSP u32*)(p + 4 * (lane & (n - 1)));
    if (__all(x >= v)) break;
    __builtin_amdgcn_s_sleep(1);
  }
  asm volatile("" ::: "memory");
}
#define PFS 3
#define PFP 3
#define PFU 8
#define PFH 4
DEV void delay(int n) {
  for (int i = 0; i < n; ++i) __builtin_amdgcn_s_sleep(PFS);
}

DEV void set_word(lptr p, u32 v) {
  asm volatile("" ::: "memory");
  *(volatile LDSP u32*)p = v;
  asm volatile("" ::: "memory");
}

struct Sched {
  int n;
  int p[272], par[272], d[272], ld[272];
  bool first[272];
  int base[16];
  int cm[16], om[16];
  int ldm[40], ldu[40], ldp[40];
  int ku[16];
  int nld, nst;
};
constexpr void add_unit(Sched& S, int p, int d, int ld, int pm, bool first) {
  for (int par = 0; par < 2; ++par)
    if ((pm >> par) & 1) {
      S.p[S.n] = p; S.par[S.n] = par; S.d[S.n] = d; S.ld[S.n] = ld; S.first[S.n] = first;
      if (S.ldu[ld] < 0) S.ldu[ld] = S.n;
      S.ldp[ld] |= 1 << par;
      if (S.ku[d] < 0) S.ku[d] = S.n;
      ++S.n;
    }
}
constexpr Sched make_sched(int mode) {
  Sched S{};
  const int last = (mode == 1) ? 11 : 15;
  S.nst = last + 1;
  int nl = 16;
  for (int l = 0; l < 40; ++l) { S.ldu[l] = -1; S.ldm[l] = l; }
  for (int d = 0; d < 16; ++d) S.ku[d] = -1;
  for (int s = 0; s <= last; ++s) {
    S.base[s] = S.n;
    S.cm[s] = (mode == 2 && s < 11) ? 0 : ((mode == 1 && s == 11) ? 1 : 3);
    S.om[s] = (mode == 2 && s == 11) ? 2 : S.cm[s];
    if (mode == 2) {
      for (int p = (s > 11 ? s : 11); p <= 15; ++p) add_unit(S, p, p - s, s, 3, s == 0);
      continue;
    }
    for (int k = 0; k < 8 && s + k <= last; ++k)
      add_unit(S, s + k, k, s, (mode == 1 && s + k == 11) ? 1 : 3, (s == 0) || (k == 7));
    int lo = -1, hi = -1, cnt = 0, half = 0;
    if (s == 6) { lo = 8; hi = 8; cnt = 1; half = 2; }
    else if (s >= 7 && s <= last - 3) { lo = (s & 1) ? s + 2 : s + 1; hi = lo + 1; cnt = hi - 7; half = (s & 1) ? 0 : 1; }
    else if (s == last - 2 || s == last - 1) { lo = last; hi = last; cnt = last - 7; half = (s == last - 2) ? 0 : 1; }
    if (cnt) {
      const int split = (cnt + 1) / 2;
      const int j0 = (half == 1) ? split : 0, j1 = (half == 0) ? split : cnt;
      for (int j = j0; j < j1; ++j) {
        const int mm = cnt - 1 - j;
        S.ldm[nl] = mm;
        for (int t = hi; t >= lo; --t)
          if (t - mm >= 8) add_unit(S, t, t - mm, nl, (mode == 1 && t == 11) ? 1 : 3, false);
        ++nl;
      }
    }
  }
  S.nld = nl;
  return S;
}
constexpr int NGAP = 272 + 40, CAP = 2, KPRE = 16;
enum { A_NONE = 0, A_X, A_K, A_Q, A_PXR, A_PXC, A_EP };
struct Acts { int n[NGAP]; int a[NGAP][4]; int end; };
constexpr int place_back(Acts& T, int latest, int code, int cap) {
  int g = latest;
  while (g > 0 && T.n[g] >= cap) --g;
  T.a[g][T.n[g]++] = code;
  return g;
}
constexpr int place_fwd(Acts& T, int earliest, int code, int cap, int nm) {
  int g = earliest;
  while (T.n[g] >= (g < nm ? cap : 4)) ++g;
  T.a[g][T.n[g]++] = code;
  if (g + 1 > T.end) T.end = g + 1;
  return g;
}
constexpr Acts make_acts(const Sched& S, int mode) {
  Acts T{};
  T.end = S.n;
  const int dense = (mode == 0) ? 200 : (mode == 1 ? 90 : 100);
  for (int l = S.nst - 1; l >= 1; --l) {
    int g = S.ldu[l] - 5;
    const int cap = S.ldu[l] >= dense && mode ? 2 : 1;
    for (int i = 3; i >= 0; --i) if ((S.ldp[l] >> (i >> 1)) & 1) g = place_back(T, g - 1, A_X | (l << 4) | (i << 12), cap);
    for (int o = 1; o >= 0; --o) if ((S.om[l] >> o) & 1) g = place_back(T, g - 1, A_Q | (l << 4) | (o << 12), cap);
    if (mode == 0 && !(l & 1)) {
      g = place_back(T, g - 1, A_PXC | (l << 4), 1);
      place_back(T, g - 6, A_PXR, 1);
    }
  }
  for (int l = S.nld - 1; l >= 16; --l) {
    int g = S.ldu[l] - 5;
    for (int i = 3; i >= 0; --i) if ((S.ldp[l] >> (i >> 1)) & 1) g = place_back(T, g - 1, A_X | (l << 4) | (i << 12), S.ldu[l] >= dense ? 2 : 1);
  }
  for (int d = 0; d < 16; ++d) {
    if (S.ku[d] < KPRE) continue;
    int g = S.ku[d] - 5;
    g = place_back(T, g - 1, A_K | (d << 4) | (1 << 12), 2);
    place_back(T, g - 1, A_K | (d << 4), 2);
  }
  int prev[18] = {};
  for (int s = 0; s < S.nst; ++s) {
    if (!S.cm[s]) continue;
    int g = S.base[s] + 4;
    const int p16 = prev[16];
    for (int e = 0; e < ((s & 1) ? 18 : 17); ++e) {
      if (e < 14 && !(((e >= 8 ? S.om[s] : S.cm[s]) >> (e & 1)) & 1)) continue;
      if ((e == 15 && !(S.om[s] & 1)) || (e == 16 && !(S.om[s] & 2))) continue;
      int lo = g + (((e & 1) && e < 14) ? 0 : 1);
      if (lo <= prev[e | 1]) lo = prev[e | 1] + 1;
      if (e == 17 && lo <= p16) lo = p16 + 1;
      g = place_fwd(T, lo, A_EP | (s << 4) | (e << 12), S.base[s] >= dense ? 3 : CAP, S.n);
      prev[e] = g;
    }
  }
  return T;
}
template <int MODE> struct Tab {
  static constexpr Sched S = make_sched(MODE);
  static constexpr Acts A = make_acts(S, MODE);
};
static_assert(Tab<0>::S.n == 272 && Tab<1>::S.n == 144 && Tab<2>::S.n == 140, "schedule");

#define MFMA(a, b, c) __builtin_amdgcn_mfma_f32_16x16x32_bf16((a), (b), (c), 0, 0, 0)
#define STV_ARG
#define STV_PASS

template <int MODE>
DEV void conv_stream(lptr sm, u32 kimg, u32 ximg, lptr ywp, lptr pyp, int lane STV_ARG) {
  using T = Tab<MODE>;
  const int g = lane >> 4, lq = (lane & 15) >> 2, lp = lane & 3;
  const lptr kb = sm + kimg + 8 * (4 * lp + 8 * g + lq);
  const lptr xb = sm + ximg + 224 + 8 * (lp - 8 * g - lq);
  const bool l63 = (lane == 63);
  const lptr pxp = sm + FLAGS + 4 * (lane & 3);
  const u32 qa = (u32)(uintptr_t)ywp;
  u32x2 kr[16][2], xr[40][4];
  f32x4 ac[16][2];
  const f32x4 zero = {0.f, 0.f, 0.f, 0.f};
  float ts[16][2], u2[16][2], u1[16][2], yy[16][2];
  u32 pk[16];
  constexpr int NQ = MODE == 0 ? 3 : 5;
  u32 qv[NQ][2], px = 0;
#pragma unroll
  for (int i = 0; i < NQ; ++i) { qv[i][0] = 0; qv[i][1] = 0; }
#define RDQ(s_, i_) asm volatile("ds_read_u16_d16_hi %0, %1 offset:%2" : "+v"(qv[(s_) % NQ][i_]) : "v"(qa), "i"((s_) * 256 + 128 * (i_)) : "memory")
#define RDXP(l_, i_) xr[l_][i_] = trread(xb + 256 * T::S.ldm[l_] + ((i_) >> 1) * 128 + (((i_) & 1) ? 0 : 32))
  static_for<0, 16>([&](auto D_) {
    constexpr int d = decltype(D_)::value;
    if constexpr (T::S.ku[d] >= 0 && T::S.ku[d] < KPRE) { kr[d][0] = trread(kb + 256 * d); kr[d][1] = trread(kb + 256 * d + 32); }
  });
  if constexpr (MODE == 0) wait_words(sm + FLAGS, lane, 4, 1);
  STAMP(4);
  if constexpr (MODE != 2) { RDQ(0, 0); RDQ(0, 1); }
  RDXP(0, 0); RDXP(0, 1); RDXP(0, 2); RDXP(0, 3);
  SCHED0();
  static_for<0, T::A.end>([&](auto G_) {
    constexpr int G = decltype(G_)::value;
    if constexpr (G < T::S.n) {
      constexpr int p = T::S.p[G], par = T::S.par[G], d = T::S.d[G], l = T::S.ld[G];
      ac[p][par] = MFMA(frag(kr[d][0], kr[d][1]), frag(xr[l][2 * par], xr[l][2 * par + 1]), T::S.first[G] ? zero : ac[p][par]);
      SCHED0();
    }
    static_for<0, T::A.n[G]>([&](auto I_) {
      constexpr int code = T::A.a[G][decltype(I_)::value];
      constexpr int kind = code & 15, a0 = (code >> 4) & 255, a1 = code >> 12;
      if constexpr (kind == A_X) RDXP(a0, a1);
      if constexpr (kind == A_K) kr[a0][a1] = trread(kb + 256 * a0 + 32 * a1);
      if constexpr (kind == A_Q) RDQ(a0, a1);
      if constexpr (kind == A_PXR) px = *(volatile LDSP u32*)pxp;
      if constexpr (kind == A_PXC) {
        if (__builtin_expect(!__all(px >= (u32)(a0 / 2 + 1)), 0)) wait_words(sm + FLAGS, lane, 4, (u32)(a0 / 2 + 1));
        asm volatile("" ::: "memory");
      }
      if constexpr (kind == A_EP) {
        constexpr int s = a0, e = a1 >> 1, o = a1 & 1;
        constexpr bool hp = s > 0 && T::S.cm[s > 0 ? s - 1 : 0] != 0;
        if constexpr (a1 < 14) {
          if constexpr (e == 0) {
            float cur = ac[s][o][3], prev = 0.f;
            if constexpr (o) prev = ac[s][0][3]; else if constexpr (hp) prev = ac[s - 1][1][3];
            ts[s][o] = l63 ? prev : cur;
          }
          if constexpr (e == 1) { float a = ac[s][o][2]; u2[s][o] = a + ror1(ts[s][o]); }
          if constexpr (e == 2) {
            float prev = 0.f;
            if constexpr (o) prev = u2[s][0]; else if constexpr (hp) prev = u2[s - 1][1];
            ts[s][o] = l63 ? prev : u2[s][o];
          }
          if constexpr (e == 3) { float a = ac[s][o][1]; u1[s][o] = a + ror1(ts[s][o]); }
          if constexpr (e == 4) {
            float prev = 0.f;
            if constexpr (o) prev = u1[s][0]; else if constexpr (hp) prev = u1[s - 1][1];
            ts[s][o] = l63 ? prev : u1[s][o];
          }
          if constexpr (e == 5) { float a = ac[s][o][0]; yy[s][o] = a + ror1(ts[s][o]); asm volatile("" : "+v"(yy[s][o])); }
          if constexpr (e == 6) {
            asm volatile("" : "+v"(qv[s % NQ][o]));
            yy[s][o] *= __uint_as_float(qv[s % NQ][o]);
            asm volatile("" : "+v"(yy[s][o]));
          }
        }
        if constexpr (a1 == 14) pk[s] = pkbf(yy[s][(T::S.om[s] & 1) ? 0 : 1], yy[s][(T::S.om[s] & 2) ? 1 : 0]);
        if constexpr (a1 == 15) *(LDSP u16*)(ywp + s * 256) = (u16)pk[s];
        if constexpr (a1 == 16) *(LDSP u16*)(ywp + s * 256 + 128) = (u16)(pk[s] >> 16);
        if constexpr (a1 == 17) set_word(pyp, (u32)(s / 2 + 1));
      }
      SCHED0();
    });
  });
  STAMP(8);
#undef RDQ
#undef RDXP
}

#define ROLE_END(n) do { STAMP_OUT(); asm volatile(".Lfk_end" #n ":"); return; } while (0)

__global__ __launch_bounds__(768, 3) void fused_kernel(Params P) {
  extern __shared__ __attribute__((aligned(16))) char smem[];
  const lptr sm = (lptr)smem;
  const u32 smb = (u32)(uintptr_t)sm;
  const int tid = threadIdx.x;
  const int w = __builtin_amdgcn_readfirstlane(tid >> 6);
  const int lane = tid & 63;
  const int wg = blockIdx.x;
  const int xcd = wg & 7, idx = wg >> 3;
  const bool partA = idx < 10;
  const int g = lane >> 4, li = lane & 15, lq = li >> 2, lp = li & 3;
  STAMP(0);
  if (w == 11) {
    unsigned long long pc;
    u32 len;
    asm volatile("s_getpc_b64 %0\n.Lfk_code_begin:\n\ts_mov_b32 %1, .Lfk_end0-.Lfk_code_begin"
                 "\n\ts_max_i32 %1, %1, .Lfk_end1-.Lfk_code_begin\n\ts_max_i32 %1, %1, .Lfk_end2-.Lfk_code_begin"
                 "\n\ts_max_i32 %1, %1, .Lfk_end3-.Lfk_code_begin\n\ts_max_i32 %1, %1, .Lfk_end4-.Lfk_code_begin"
                 "\n\ts_max_i32 %1, %1, .Lfk_end5-.Lfk_code_begin" : "=s"(pc), "=&s"(len) :: "scc");
    const u32 off = min((u32)(lane + 64 * (idx & 3)) * 128u, len & ~3u);
    touch64((const char*)pc + off, smb + PFD);
  }
  gu64* const gflag = (gu64*)(uintptr_t)(P.ex + WS_FLAG);
  gu64* const gepoch = (gu64*)(uintptr_t)(P.ex + WS_EPOCH);
  if (idx >= 30) {
    if (w < 2) { delay(PFU); touch64(prefetch_addr(P, w, idx, xcd, lane), smb + PFD); }
    if (w == 8 && wg == 240) {
      const unsigned long long tag = __hip_atomic_load(gepoch, RLX_AGENT) + 1;
      delay(40);
#pragma unroll 1
      for (int spin = 0; spin < (1 << 16); ++spin) {
        bool ok = true;
#pragma unroll
        for (int j = 0; j < 5; ++j) ok &= (__hip_atomic_load(gflag + 16 * (lane + 64 * j), RLX_AGENT) == tag);
        if (__all(ok)) break;
        __builtin_amdgcn_s_sleep(64);
      }
      if (lane == 0) __hip_atomic_store(gepoch, tag, RLX_AGENT);
    }
    vmcnt<0>();
    asm volatile(".Lfk_end1:");
    return;
  }
  if (tid < 80) *(LDSP u32*)(sm + FLAGS + 4 * tid) = 0;

  const int gi = partA ? xcd * 10 + idx : xcd * 20 + (idx - 10);
  const auto rx = __builtin_amdgcn_make_buffer_rsrc(P.ex + WS_EXX, 0, 6u << 20, 0x00020000);

  if (partA && w < 8) {
#pragma unroll
    for (int j = 0; j < 3; ++j)
      dma16(P.ssm_k, (u32)(gi * 8 + w) * (SEQ * 2) + 1024 * j + 16 * lane, smb + KIMG + w * KS + KPADB + 1024 * j);
  }

  if (w >= (partA ? 4 : 8)) {
    const int h = w & 3, pg = (w < 8) ? 1 : 0;
    const int un = partA ? gi * 2 + pg : gi;
    const int pcol = un >> 4, pchunk = (un & 15) + (partA ? 16 : 0);
    const u32 praw = partA ? A_PRAW + pg * PUNIT : B_PRAW;
    const u32 shk = SHK + pg * 512;
    const lptr fw = sm + FLAG2 + pg * 32;
    __builtin_amdgcn_s_setprio(3);
    unsigned long long tag = 0;
    if (w == 8) tag = __hip_atomic_load(gepoch, RLX_AGENT) + 1;
    {
      const u32 po = (u32)(64 * pchunk + 16 * h + (lane >> 3)) * ROWB + (u32)pcol * 128 + 16 * (lane & 7);
#pragma unroll
      for (int j = 0; j < 2; ++j) {
        dma16(P.k, po + j * 8 * ROWB, smb + praw + 512 + h * 2048 + j * 1024);
        dma16(P.v, po + j * 8 * ROWB, smb + praw + 8704 + h * 2048 + j * 1024);
        dma16(P.q, po + j * 8 * ROWB, smb + praw + 16896 + h * 2048 + j * 1024);
      }
      if (h == 0 && lane < 32) {
        if (pchunk) dma16(P.k, po - 4 * ROWB, smb + praw);
        else *(LDSP u32x4*)(sm + praw + 16 * lane) = u32x4{0, 0, 0, 0};
      }
      if (h == 1 && lane < 32) dma16(P.shift_k, (u32)pcol * 512 + 16 * lane, smb + shk);
    }
    wgbarrier();
    if (!partA) {
      const u32x4 ones = {0x3F803F80u, 0x3F803F80u, 0x3F803F80u, 0x3F803F80u};
#pragma unroll
      for (int i = 0; i < 4; ++i) *(LDSP u32x4*)(sm + B_YIMG + h * YS + 1024 * i + 16 * lane) = ones;
      if (lane < 10) *(LDSP u32x4*)(sm + XIMG + h * XS + 16 * lane) = u32x4{0, 0, 0, 0};
    }
    if (w >= 10) {
      delay(PFP);
      touch64(prefetch_addr(P, w - 10, idx, xcd, lane), smb + PFD);
      vmcnt<1>();
    } else {
      vmcnt<0>();
    }
    if (w == 8) {
      *(LDSP unsigned long long*)(sm + FLAG3 + 8) = tag;
      set_word(sm + FLAG3, 1);
    }
    set_word(fw + 4 * h, 1);
    wait_words(fw, lane, 4, 1);
    STAMP(12);
    {
      const u32x2 sw = *(LDSP u32x2*)(sm + shk + 8 * (16 * h + li));
      const float w0 = bflo(sw.x), w1 = bfhi(sw.x), w2 = bflo(sw.y), w3 = bfhi(sw.y);
      const u32 co = praw + 32 * h + 8 * lp;
      const u32 stg = partA ? A_PSTG + (w - 4) * YS : B_PSTG + h * XS;
      const u32 sw_ = stg + li * 128 + 8 * (g & 1);
      const u32 sr_ = stg + (lane >> 3) * 128 + 16 * ((lane ^ (lane >> 3)) & 7);
      const u32 eo = (u32)(pcol * 64 + 16 * h + (lane >> 3)) * 4096 + 128 * pchunk + 16 * (lane & 7);
      const u32 zst = (lq & 1) ? 256u : 0u;
      const u32 ka = (lq & 1) ? co + 128 * (4 * g + (lq >> 1)) : (u32)ZROW;
      const u32 va = (lq & 1) ? co + 8704 + 128 * (4 * g + (lq >> 1)) : (u32)ZROW;
#pragma unroll
      for (int u = 0; u < 4; ++u)
        *(LDSP u32x2*)(sm + sw_ + 16 * (((2 * u + (g >> 1)) ^ li) & 7)) = trread(sm + co + 16896 + 128 * (16 * u + 4 * g + lq));
      wavefence();
#pragma unroll
      for (int j = 0; j < 2; ++j)
        __builtin_amdgcn_raw_buffer_store_b128(*(const LDSP u32x4*)(sm + sr_ + 1024 * j), rx, eo + j * 8 * 4096 + (u32)(WS_EXQ - WS_EXX), 0, 16);
      wavefence();
#pragma unroll 2
      for (int u = 0; u < 4; ++u) {
        float kx[8], vx[4], kv[4];
#pragma unroll
        for (int j = 0; j < 4; ++j) { u32x2 r = trread(sm + ka + zst * (8 * u + j)); kx[2 * j] = __uint_as_float(r.x); kx[2 * j + 1] = __uint_as_float(r.y); }
#pragma unroll
        for (int j = 0; j < 2; ++j) { u32x2 r = trread(sm + va + zst * (8 * u + j)); vx[2 * j] = __uint_as_float(r.x); vx[2 * j + 1] = __uint_as_float(r.y); }
#pragma unroll
        for (int j = 0; j < 4; ++j)
          kv[j] = vmul(vfma(kx[j + 4], w0, vfma(kx[j + 3], w1, vfma(kx[j + 2], w2, vmul(kx[j + 1], w3)))), vx[j]);
        *(LDSP u32x2*)(sm + sw_ + 16 * (((2 * u + (g >> 1)) ^ li) & 7)) = u32x2{pkbf(kv[0], kv[1]), pkbf(kv[2], kv[3])};
      }
      wavefence();
#pragma unroll
      for (int j = 0; j < 2; ++j)
        __builtin_amdgcn_raw_buffer_store_b128(*(const LDSP u32x4*)(sm + sr_ + 1024 * j), rx, eo + j * 8 * 4096, 0, 16);
    }
    STAMP(13);
    const u32 urow = (u32)(64 * h + lane) * ROWB + (u32)((gi * 4) & ~7) * 2;
    if (!partA) {
      dma16(P.u, urow, smb + RAWU + 1024 * h);
      dma16(P.u, urow + 256 * ROWB, smb + RAWU + 4096 + 1024 * h);
      vmcnt<2>();
    } else {
      vmcnt<0>();
    }
    set_word(fw + 16 + 4 * h, 1);
    wait_words(fw + 16, lane, 4, 1);
    if (h == 0) {
      wait_words(sm + FLAG3, lane, 1, 1);
      tag = *(volatile LDSP unsigned long long*)(sm + FLAG3 + 8);
      if (lane == 0) __hip_atomic_store(gflag + 16 * (pcol * 32 + pchunk), tag, RLX_AGENT);
    }
    STAMP(9);
    if (w >= 8) {
      if (partA) {
        const u32 yrd = YIMG + lq * YS + 2 * (64 * h + 16 * g + 4 * lp);
        u16* const outp = P.out + gi * 8 + (size_t)(64 * h + lane) * (2 * DCH);
#pragma unroll 1
        for (int b = 0; b < (h < 3 ? 6 : 5); ++b) {
          wait_words(sm + FLAGS + 16, lane, 8, (u32)(b + 1));
          u32x2 a = trread(sm + yrd + 512 * b);
          u32x2 c = trread(sm + yrd + 512 * b + 4 * YS);
          *(u32x4*)(outp + (size_t)b * 256 * (2 * DCH)) = u32x4{a.x, a.y, c.x, c.y};
        }
        ROLE_END(2);
      }
      const u32 ua = RAWU + 1024 * h + 16 * (16 * g + 4 * lp + lq) + 2 * ((gi * 4) & 4);
      const u32 xw = XIMG + (lane & 3) * XS + XPADB + 8 * (lane >> 2) + 128 * h;
      const u32 yrd = B_YIMG + lq * YS + 2 * (64 * h + 16 * g + 4 * lp);
      const u32 hrd = H_YIMG + lq * HYS + 2 * (64 * h + 16 * g + 4 * lp - 1408);
      u16* const outp = P.out + gi * 4 + (size_t)(64 * h + lane) * (2 * DCH);
#pragma unroll 1
      for (int it = 0; it < 8; ++it) {
        int i = it;
        asm volatile("" : "+s"(i));
        if (i + 2 < 8) dma16(P.u, urow + (u32)(i + 2) * (256 * ROWB), smb + RAWU + 4096 * (i + 2) + 1024 * h);
        if (i < 6) vmcnt<2>(); else if (i == 6) vmcnt<1>(); else vmcnt<0>();
        *(LDSP u32x2*)(sm + xw + 512 * i) = trread(sm + ua + 4096 * i);
        set_word(sm + FLAGS + 4 * h, (u32)(i + 1));
      }
      STAMP(14);
#pragma unroll 1
      for (int i = 0; i < 8; ++i) {
        wait_words(sm + FLAGS + 16, lane, 4, (u32)(i + 1));
        *(u32x2*)(outp + DCH + (size_t)i * 256 * (2 * DCH)) = trread(sm + yrd + 512 * i);
      }
#pragma unroll 1
      for (int i = (h == 3 ? 5 : 6); i < 8; ++i) {
        wait_words(sm + FLAGS + 32, lane, 4, (u32)(i + 1));
        *(u32x2*)(outp + (size_t)i * 256 * (2 * DCH)) = trread(sm + hrd + 512 * i);
      }
      ROLE_END(3);
    }
    __builtin_amdgcn_s_setprio(0);
  } else if (partA) {
    wgbarrier();
  }

  auto await_exchange = [&](bool poller, int col) {
    if (poller) {
      wait_words(sm + FLAG3, lane, 1, 1);
      const unsigned long long tag = *(volatile LDSP unsigned long long*)(sm + FLAG3 + 8);
#pragma unroll 1
      for (int spin = 0; spin < (1 << 16); ++spin) {
        if (__all(__hip_atomic_load(gflag + 16 * (col * 32 + (lane & 31)), RLX_AGENT) == tag)) break;
        __builtin_amdgcn_s_sleep(6);
      }
      set_word(sm + FLAG3 + 4, 1);
    } else {
      wait_words(sm + FLAG3 + 4, lane, 1, 1);
    }
    __builtin_amdgcn_fence(__ATOMIC_ACQUIRE, "wavefront");
  };

  if (partA) {
    const int ch = gi * 8 + w;
    const u32 kimg = KIMG + w * KS, ximg = XIMG + w * XS, yimg = YIMG + w * YS;
    if (lane < 10) *(LDSP u32x4*)(sm + ximg + 16 * lane) = u32x4{0, 0, 0, 0};
    if (lane < 6) *(LDSP u32x4*)(sm + kimg + 16 * lane) = u32x4{0, 0, 0, 0};
    const float dsk = ld_bf_uniform(P.ssm_D, ch);
    vmcnt<0>();
    if (lane == 0) {
      LDSP u16* k0 = (LDSP u16*)(sm + kimg + KPADB);
      *k0 = (u16)pkbf(bf2f(*k0) + dsk, 0.f);
    }
    wait_words(sm + FLAG2 + 16, lane, 4, 1);
    wait_words(sm + FLAG2 + 48, lane, 4, 1);
    await_exchange(w == 0, gi >> 3);
    STAMP(2);
    {
      u32x4 d[6];
#pragma unroll
      for (int j = 0; j < 6; ++j)
        d[j] = __builtin_amdgcn_raw_buffer_load_b128(rx, (u32)ch * 4096 + 1024 * (j >> 1) + 16 * lane + (j & 1) * (u32)(WS_EXQ - WS_EXX), 0, 16);
#pragma unroll
      for (int j = 0; j < 6; ++j) *(LDSP u32x4*)(sm + ((j & 1) ? yimg : ximg + XPADB) + 1024 * (j >> 1) + 16 * lane) = d[j];
    }
    wavefence();
    conv_stream<1>(sm, kimg, ximg, sm + yimg + 2 * lane, sm + FLAGS + 16 + 4 * w, lane STV_PASS);
    ROLE_END(4);
  }

  {
    const int ch0 = gi * 4;
    const int cw = w & 3, ps = w >> 2;
    const int ch = ch0 + cw;
    const size_t o = (size_t)ch * NST + 64 * ps + lane;
    const u16 i_ar = P.A_re[o], i_ai = P.A_im[o], i_br = P.B_re[o], i_bi = P.B_im[o], i_cr = P.C_re[o], i_ci = P.C_im[o];
    const float ldt = ld_bf_uniform(P.log_dt, ch);
    const float dsk = ld_bf_uniform(P.Dskip, ch);
    wgbarrier();
    const float dt = rbf(expf(ldt));
    const float zr = dt * bf2f(i_ar), zi = dt * bf2f(i_ai);
    const cf dB = {dt * bf2f(i_br), dt * bf2f(i_bi)};
    const cf C = {bf2f(i_cr), bf2f(i_ci)};
    const cf W = cmul(C, dB);
    float mx = fmaxf(fabsf(W.r), fabsf(W.i));
    STAMP(1);
    {
      int m = __float_as_int(mx);
      m = max(m, __builtin_amdgcn_update_dpp(0, m, 0x111, 0xf, 0xf, false));
      m = max(m, __builtin_amdgcn_update_dpp(0, m, 0x112, 0xf, 0xf, false));
      m = max(m, __builtin_amdgcn_update_dpp(0, m, 0x114, 0xf, 0xf, false));
      m = max(m, __builtin_amdgcn_update_dpp(0, m, 0x118, 0xf, 0xf, false));
      int a0 = __builtin_amdgcn_readlane(m, 15), a1 = __builtin_amdgcn_readlane(m, 31);
      int a2 = __builtin_amdgcn_readlane(m, 47), a3 = __builtin_amdgcn_readlane(m, 63);
      mx = __int_as_float(max(max(a0, a1), max(a2, a3)));
    }
    int ex = (int)((__float_as_uint(mx) >> 23) & 0xff);
    ex = min(max(ex, 30), 240);
    const float sY = __uint_as_float((u32)(267 - ex) << 23);
    const float so = __uint_as_float((u32)(ex - 27) << 23);
    const float SX = 16384.f;

    f32x4 ak[3];
    const u32 stg = STG + w * STS;
    const u32 rb = stg + li * 272 + g * 16;
    const u32 m0v = smb + w * STS;
#define STGW(row, val_r, val_i) \
    asm volatile("ds_write_addtid_b32 %0 offset:%1" :: "v"(pkh((val_r), (val_i))), "i"(STG + (row) * 272) : "memory")
    {
      const cf E1 = cexp_pow2(zr, zi, 1.f);
      const cf E8c = cexp_pow2(zr, -zi, 8.f);
      const cf E128 = cexp_pow2(zr, zi, 128.f);
      const cf E1024 = cexp_pow2(zr, zi, 1024.f);
      const cf E2 = cmul(E1, E1);
      const cf E4 = cmul(E2, E2);
      u32 keep;
      asm volatile("s_mov_b32 %0, m0\n\ts_mov_b32 m0, %1\n\ts_nop 0" : "=&s"(keep) : "s"(m0v) : "memory");
      cf y = {W.r * sY, -(W.i * sY)};
      cf P1 = {E128.r * SX, E128.i * SX};
      cf v0 = {SX, 0.f}, v1 = P1;
#pragma unroll
      for (int b = 0; b < 8; ++b) {
        STGW(2 * b, y.r, y.i); y = cmul(y, E8c);
        STGW(16 + b, v0.r, v0.i); if (b < 7) v0 = cmul(v0, E1);
        STGW(2 * b + 1, y.r, y.i); if (b < 7) y = cmul(y, E8c);
        STGW(24 + b, v1.r, v1.i); if (b < 7) v1 = cmul(v1, E1);
      }
      f16x8 yf[4];
      const f32x4 zero = {0.f, 0.f, 0.f, 0.f};
#pragma unroll
      for (int s = 0; s < 4; ++s) {
        yf[s] = *(const LDSP f16x8*)(sm + rb + 64 * s);
        f16x8 xf = *(const LDSP f16x8*)(sm + rb + 16 * 272 + 64 * s);
        ak[0] = __builtin_amdgcn_mfma_f32_16x16x32_f16(xf, yf[s], s ? ak[0] : zero, 0, 0, 0);
      }
      asm volatile("" ::: "memory");
      cf Pa = cmul(P1, E128);
      cf Pb = {E1024.r * SX, E1024.i * SX};
#pragma unroll
      for (int m = 0; m < 2; ++m) {
        cf v = Pa;
#pragma unroll
        for (int r = 0; r < 4; ++r) {
          STGW(4 * m + r, v.r, v.i); if (r < 3) v = cmul(v, E2);
          if (r & 1) { STGW(16 + 2 * m + (r >> 1), Pb.r, Pb.i); Pb = cmul(Pb, E128); }
        }
        Pa = cmul(Pa, E128);
      }
#pragma unroll
      for (int m = 0; m < 4; ++m) {
        STGW(8 + 2 * m, Pa.r, Pa.i);
        cf v = cmul(Pa, E4);
        STGW(20 + m, Pb.r, Pb.i); Pb = cmul(Pb, E128);
        STGW(9 + 2 * m, v.r, v.i);
        if (m < 3) Pa = cmul(Pa, E128);
      }
      STGW(24, Pb.r, Pb.i);
      asm volatile("s_mov_b32 m0, %0" :: "s"(keep) : "memory");
#pragma unroll
      for (int s = 0; s < 4; ++s) {
        f16x8 x1 = *(const LDSP f16x8*)(sm + rb + 64 * s);
        f16x8 x2 = *(const LDSP f16x8*)(sm + rb + 16 * 272 + 64 * s);
        ak[1] = __builtin_amdgcn_mfma_f32_16x16x32_f16(x1, yf[s], s ? ak[1] : zero, 0, 0, 0);
        ak[2] = __builtin_amdgcn_mfma_f32_16x16x32_f16(x2, yf[s], s ? ak[2] : zero, 0, 0, 0);
      }
    }
#undef STGW
#pragma unroll
    for (int i = 0; i < 3; ++i) ak[i] *= so;
    STAMP(2);
    asm volatile("" ::: "memory");
    const u32 kimg = B_KIMG + cw * KS;
    if (ps) {
#pragma unroll
      for (int i = 0; i < 3; ++i) *(LDSP f32x4*)(sm + kimg + 1024 * i + 16 * lane) = ak[i];
      set_word(sm + FLAGS + 48 + 4 * cw, 1);
      const u32 hx = stg, hk = stg + XS, hy = H_YIMG + cw * HYS;
      asm volatile("s_waitcnt lgkmcnt(0)" ::: "memory");
#pragma unroll
      for (int j = 0; j < 4; ++j) dma16(P.ssm_k, (u32)ch * (SEQ * 2) + 1024 * j + 16 * lane, smb + hk + KPADB + 1024 * j);
      if (lane < 10) *(LDSP u32x4*)(sm + hx + 16 * lane) = u32x4{0, 0, 0, 0};
      if (lane < 6) *(LDSP u32x4*)(sm + hk + 16 * lane) = u32x4{0, 0, 0, 0};
      const float dh = ld_bf_uniform(P.ssm_D, ch);
      await_exchange(w == 4, gi >> 4);
      STAMP(5);
      {
        u32x4 d[6];
#pragma unroll
        for (int j = 0; j < 4; ++j) d[j] = __builtin_amdgcn_raw_buffer_load_b128(rx, (u32)ch * 4096 + 1024 * j + 16 * lane, 0, 16);
#pragma unroll
        for (int j = 0; j < 2; ++j)
          d[4 + j] = __builtin_amdgcn_raw_buffer_load_b128(rx, (u32)ch * 4096 + 2816 + 256 * j + 16 * lane + (u32)(WS_EXQ - WS_EXX), 0, 16);
        vmcnt<0>();
        if (lane == 0) {
          LDSP u16* k0 = (LDSP u16*)(sm + hk + KPADB);
          *k0 = (u16)pkbf(bf2f(*k0) + dh, 0.f);
        }
#pragma unroll
        for (int j = 0; j < 4; ++j) *(LDSP u32x4*)(sm + hx + XPADB + 1024 * j + 16 * lane) = d[j];
#pragma unroll
        for (int j = 0; j < 2; ++j) *(LDSP u32x4*)(sm + hy + 256 * j + 16 * lane) = d[4 + j];
      }
      wavefence();
      conv_stream<2>(sm, hk, hx, sm + hy + 2 * lane - 11 * 256, sm + FLAGS + 16 + 4 * w, lane STV_PASS);
      ROLE_END(5);
    }
    wait_words(sm + FLAGS + 48 + 4 * cw, lane, 1, 1);
    {
      const u32 tb = stg;
#pragma unroll
      for (int i = 0; i < 3; ++i) ak[i] += *(const LDSP f32x4*)(sm + kimg + 1024 * i + 16 * lane);
      wavefence();
      const int b = li, p = g;
      if (lane < 6) *(LDSP u32x4*)(sm + kimg + 16 * lane) = u32x4{0, 0, 0, 0};
      {
        float a0 = ak[0][0] + (lane == 0 ? dsk : 0.f), a1 = ak[0][1], a2 = ak[0][2], a3 = ak[0][3];
        *(LDSP u32x2*)(sm + kimg + KPADB + 2 * (128 * (p >> 1) + 8 * b + 4 * (p & 1))) = u32x2{pkbf(a0, a1), pkbf(a2, a3)};
      }
      {
        float a0 = ak[1][0], a1 = ak[1][1], a2 = ak[1][2], a3 = ak[1][3];
        int i01, i23;
        if (p < 2) { i01 = 64 * p + 4 * b; i23 = i01 + 2; }
        else { i01 = 128 + 64 * (p - 2) + 2 * b; i23 = i01 + 32; }
        *(LDSP u32x2*)(sm + tb + 4 * i01) = u32x2{pkbf(a0, a0), pkbf(a1, a1)};
        *(LDSP u32x2*)(sm + tb + 4 * i23) = u32x2{pkbf(a2, a2), pkbf(a3, a3)};
#pragma unroll
        for (int j = 0; j < 4; ++j) {
          float a = ak[2][j];
          int ii = 256 + 16 * (4 * p + j) + b;
          *(LDSP u32*)(sm + tb + 4 * ii) = pkbf(a, a);
        }
      }
      wavefence();
      const LDSP u32* TB = (const LDSP u32*)(sm + tb);
      {
        const int i0 = 4 * (lane & 31);
        u32 g0 = TB[i0], g1 = TB[i0 + 1], g2 = TB[i0 + 2], g3 = TB[i0 + 3], g4 = TB[i0 + 4];
        u32x4 ov = {g0, (g1 & 0xffffu) | (g2 & 0xffff0000u), g2, (g3 & 0xffffu) | (g4 & 0xffff0000u)};
        *(LDSP u32x4*)(sm + kimg + KPADB + 16 * (32 + (lane & 31))) = ov;
      }
      {
        const int i0 = 128 + 2 * lane;
        u32 g0 = TB[i0], g1 = TB[i0 + 1], g2 = TB[i0 + 2];
        u32x4 ov = {g0, (g0 & 0xffffu) | (g1 & 0xffff0000u), g1, g2};
        *(LDSP u32x4*)(sm + kimg + KPADB + 16 * (64 + lane)) = ov;
      }
#pragma unroll
      for (int hh = 0; hh < 2; ++hh) {
        const int i0 = 256 + 64 * hh + lane;
        u32 g0 = TB[i0], g1 = TB[i0 + 1];
        u32 mid = (lane & 1) ? g1 : ((g0 & 0xffffu) | (g1 & 0xffff0000u));
        u32x4 ov = {g0, g0, mid, g1};
        *(LDSP u32x4*)(sm + kimg + KPADB + 16 * (128 + 64 * hh + lane)) = ov;
      }
    }
    wavefence();
    STAMP(3);
    conv_stream<0>(sm, kimg, XIMG + cw * XS, sm + B_YIMG + cw * YS + 2 * lane, sm + FLAGS + 16 + 4 * w, lane STV_PASS);
    ROLE_END(0);
  }
}

extern "C" void kernel_launch(void* const* d_in, const int* in_sizes, int n_in,
                              void* d_out, int out_size, void* d_ws, size_t ws_size,
                              hipStream_t stream) {
  Params P;
  P.q = (const u16*)d_in[0];
  P.k = (const u16*)d_in[1];
  P.v = (const u16*)d_in[2];
  P.shift_k = (const u16*)d_in[3];
  P.ssm_k = (const u16*)d_in[4];
  P.ssm_D = (const u16*)d_in[5];
  P.u = (const u16*)d_in[6];
  P.A_re = (const u16*)d_in[7];
  P.A_im = (const u16*)d_in[8];
  P.B_re = (const u16*)d_in[9];
  P.B_im = (const u16*)d_in[10];
  P.C_re = (const u16*)d_in[11];
  P.C_im = (const u16*)d_in[12];
  P.log_dt = (const u16*)d_in[13];
  P.Dskip = (const u16*)d_in[14];
  P.out = (u16*)d_out;
  P.ws = (unsigned long long*)d_ws;
  P.ex = (char*)d_ws + (16 << 20);
  (void)hipFuncSetAttribute((const void*)fused_kernel, hipFuncAttributeMaxDynamicSharedMemorySize, LDS_BYTES);
  fused_kernel<<<256, 768, LDS_BYTES, stream>>>(P);
}
```

```cpp
#include <hip/hip_runtime.h>
#include <hip/hip_bf16.h>
#include <stdint.h>

typedef __attribute__((ext_vector_type(4))) short s16x4;
typedef __attribute__((ext_vector_type(8))) _Float16 f16x8;
typedef __attribute__((ext_vector_type(8))) __bf16 bf16x8;
typedef __attribute__((ext_vector_type(2))) __bf16 bf16x2;
typedef __attribute__((ext_vector_type(2))) _Float16 f16x2;
typedef __attribute__((ext_vector_type(2))) float f32x2;
typedef __attribute__((ext_vector_type(4))) float f32x4;
typedef __attribute__((ext_vector_type(2))) unsigned int u32x2;
typedef __attribute__((ext_vector_type(4))) unsigned int u32x4;
typedef unsigned int u32;
typedef unsigned short u16;
#define LDSP __attribute__((address_space(3)))
#define DEV __device__ __forceinline__

constexpr int SEQ = 2048;
constexpr int DCH = 640;
constexpr int NST = 128;
constexpr int ROWB = DCH * 2;

constexpr int XS = 4272;
constexpr int XPADB = 160;
constexpr int YS = 4160;
constexpr int KS = 4224;
constexpr int KPADB = 96;
constexpr int FLAGS = 0;
constexpr int ZROW = 64;
constexpr int FLAG2 = 192;
constexpr int FLAG3 = 256;
constexpr int PFD = 320;
constexpr int SHK = 1024;
constexpr int XIMG = 2048;
constexpr int PUNIT = 8704 + 8192 + 8192;
constexpr int YIMG = 36224;
constexpr int KIMG = 69504;
constexpr int A_PRAW = 103296;
constexpr int A_PSTG = YIMG;
constexpr int STS = 8704;
constexpr int STG = 19200;
constexpr int H_XIMG = STG + 4 * STS;
constexpr int H_KIMG = H_XIMG + 4 * XS;
constexpr int B_KIMG = 88832;
constexpr int B_YIMG = 105728;
constexpr int HYS = 1344;
constexpr int H_YIMG = 122368;
constexpr int RAWU = 127744;
constexpr int B_PRAW = RAWU + 8192;
constexpr int B_PSTG = XIMG + XPADB;
constexpr int LDS_BYTES = 161024;

constexpr size_t WS_EPOCH = 0;
constexpr size_t WS_FLAG = 4096;
constexpr size_t WS_EXX = 1 << 20;
constexpr size_t WS_EXQ = 4 << 20;

DEV float bf2f(u16 v) { return __uint_as_float(((u32)v) << 16); }
DEV float bflo(u32 v) { return __uint_as_float(v << 16); }
DEV float bfhi(u32 v) { return __uint_as_float(v & 0xffff0000u); }
DEV u32 pkbf(float a, float b) {
  f32x2 t = {a, b};
  bf16x2 r = __builtin_convertvector(t, bf16x2);
  return __builtin_bit_cast(u32, r);
}
DEV u32 pkh(float a, float b) {
  f32x2 t = {a, b};
  f16x2 r = __builtin_convertvector(t, f16x2);
  return __builtin_bit_cast(u32, r);
}
DEV float rbf(float a) { return bflo(pkbf(a, a)); }
DEV float dot2(u32 a, u32 b, float c) {
  return __builtin_amdgcn_fdot2_f32_bf16(__builtin_bit_cast(bf16x2, a), __builtin_bit_cast(bf16x2, b), c, false);
}
DEV float vfma(float a, float b, float c) { float d; asm("v_fma_f32 %0, %1, %2, %3" : "=v"(d) : "v"(a), "v"(b), "v"(c)); return d; }
DEV float vmul(float a, float b) { float d; asm("v_mul_f32 %0, %1, %2" : "=v"(d) : "v"(a), "v"(b)); return d; }
typedef __attribute__((address_space(1))) unsigned long long gu64;
#define RLX_AGENT __ATOMIC_RELAXED, __HIP_MEMORY_SCOPE_AGENT
typedef LDSP char* lptr;
DEV u32x2 trread(lptr p) {
  s16x4 r = __builtin_amdgcn_ds_read_tr16_b64_v4i16((LDSP s16x4*)p);
  return __builtin_bit_cast(u32x2, r);
}
DEV bf16x8 frag(u32x2 a, u32x2 b) {
  u32x4 f = {a.x, a.y, b.x, b.y};
  return __builtin_bit_cast(bf16x8, f);
}
DEV float ror1(float v) {
  int r = __builtin_amdgcn_update_dpp(0, __float_as_int(v), 0x13C, 0xf, 0xf, false);
  return __int_as_float(r);
}
DEV void wavefence() { __builtin_amdgcn_fence(__ATOMIC_ACQ_REL, "wavefront"); __builtin_amdgcn_wave_barrier(); }
DEV void wgbarrier() {
  asm volatile("s_waitcnt lgkmcnt(0)" ::: "memory");
  __builtin_amdgcn_s_barrier();
  asm volatile("" ::: "memory");
}
DEV void dma16(const void* base, u32 voff, u32 ldsaddr) {
  u32 keep;
  ldsaddr = __builtin_amdgcn_readfirstlane(ldsaddr);
  const unsigned long long b64 = (unsigned long long)(uintptr_t)base;
  const unsigned long long sb = ((unsigned long long)(u32)__builtin_amdgcn_readfirstlane((u32)(b64 >> 32)) << 32) |
                                (u32)__builtin_amdgcn_readfirstlane((u32)b64);
  asm volatile("s_mov_b32 %0, m0\n\ts_mov_b32 m0, %3\n\ts_nop 2\n\tglobal_load_lds_dwordx4 %1, %2\n\ts_mov_b32 m0, %0"
               : "=&s"(keep) : "v"(voff), "s"(sb), "s"(ldsaddr) : "memory");
}
DEV void touch64(const void* p, u32 ldsaddr) {
  u32 keep;
  asm volatile("s_mov_b32 %0, m0\n\ts_mov_b32 m0, %2\n\ts_nop 0\n\tglobal_load_lds_dword %1, off\n\ts_mov_b32 m0, %0"
               : "=&s"(keep) : "v"(p), "s"(ldsaddr) : "memory");
}
template <int N> DEV void vmcnt() { asm volatile("s_waitcnt vmcnt(%0)" :: "i"(N) : "memory"); }
#define SCHED0() __builtin_amdgcn_sched_barrier(0)

template <int V> struct IC { static constexpr int value = V; };
template <int I, int N, class F> DEV void static_for(F&& f) {
  if constexpr (I < N) { f(IC<I>{}); static_for<I + 1, N>(f); }
}

struct cf { float r, i; };
DEV cf cmul(cf a, cf b) {
  cf o;
  o.r = fmaf(a.r, b.r, -(a.i * b.i));
  o.i = fmaf(a.r, b.i, a.i * b.r);
  return o;
}
DEV cf cexp_pow2(float zr, float zi, float x) {
  float mag = __builtin_amdgcn_exp2f((zr * x) * 1.44269504088896340736f);
  float th = zi * x;
  const float C_HI = 0.15915494f;
  const float C_LO = 6.4206382e-09f;
  float f = fmaf(th, C_LO, fmaf(th, C_HI, -rintf(th * C_HI)));
  cf o;
  o.r = mag * __builtin_amdgcn_cosf(f);
  o.i = mag * __builtin_amdgcn_sinf(f);
  return o;
}

#define STAMP(n)
#define CSTAMP(n)
#define STAMP_OUT()

struct Params {
  const u16 *q, *k, *v, *shift_k, *ssm_k, *ssm_D;
  const u16 *u, *A_re, *A_im, *B_re, *B_im, *C_re, *C_im, *log_dt, *Dskip;
  u16* out;
  unsigned long long* ws;
  char* ex;
};

DEV float ld_bf_uniform(const u16* p, int idx) {
  u32 d = *(const __attribute__((address_space(4))) u32*)(uintptr_t)(p + (idx & ~1));
  return (idx & 1) ? bfhi(d) : bflo(d);
}

DEV const char* prefetch_addr(const Params& P, int cls, int slice, int xcd, int lane) {
  asm volatile("" : "+v"(lane));
  const unsigned long long TAB = 0xFEDCull;
  const u32 nib = (u32)(TAB >> (4 * (2 * cls + (lane >> 5)))) & 15u;
  const bool second = (lane >> 4) & 1;
  const u16* base = (nib < 8) ? (second ? P.v : P.k) : (nib < 12 ? P.q : P.u);
  const u32 blk = (nib < 8) ? nib : 2 * (nib & 3) + (second ? 1 : 0);
  const u32 row = blk * 256u + (u32)slice * 8u + ((lane >> 1) & 7);
  return (const char*)base + (row * ROWB + (u32)(((160 * xcd) >> 7) + (lane & 1)) * 128u);
}
DEV void wait_words(lptr p, int lane, int n, u32 v) {
#pragma unroll 1
  for (int spin = 0; spin < (1 << 16); ++spin) {
    u32 x = *(volatile LDSP u32*)(p + 4 * (lane & (n - 1)));
    if (__all(x >= v)) break;
    __builtin_amdgcn_s_sleep(1);
  }
  asm volatile("" ::: "memory");
}
#define PFS 3
#define PFP 3
#define PFU 8
#define PFH 4
#define FSP 16
#define PSL 6
DEV void delay(int n) {
  for (int i = 0; i < n; ++i) __builtin_amdgcn_s_sleep(PFS);
}

DEV void set_word(lptr p, u32 v) {
  asm volatile("" ::: "memory");
  *(volatile LDSP u32*)p = v;
  asm volatile("" ::: "memory");
}

struct Sched {
  int n;
  int p[272], par[272], d[272], ld[272];
  bool first[272];
  int base[16];
  int cm[16], om[16];
  int ldm[40], ldu[40], ldp[40];
  int ku[16];
  int nld, nst;
};
constexpr void add_unit(Sched& S, int p, int d, int ld, int pm, bool first) {
  for (int par = 0; par < 2; ++par)
    if ((pm >> par) & 1) {
      S.p[S.n] = p; S.par[S.n] = par; S.d[S.n] = d; S.ld[S.n] = ld; S.first[S.n] = first;
      if (S.ldu[ld] < 0) S.ldu[ld] = S.n;
      S.ldp[ld] |= 1 << par;
      if (S.ku[d] < 0) S.ku[d] = S.n;
      ++S.n;
    }
}
constexpr Sched make_sched(int mode) {
  Sched S{};
  const int last = (mode == 1) ? 11 : 15;
  S.nst = last + 1;
  int nl = 16;
  for (int l = 0; l < 40; ++l) { S.ldu[l] = -1; S.ldm[l] = l; }
  for (int d = 0; d < 16; ++d) S.ku[d] = -1;
  for (int s = 0; s <= last; ++s) {
    S.base[s] = S.n;
    S.cm[s] = (mode == 2 && s < 11) ? 0 : ((mode == 1 && s == 11) ? 1 : 3);
    S.om[s] = (mode == 2 && s == 11) ? 2 : S.cm[s];
    if (mode == 2) {
      for (int p = (s > 11 ? s : 11); p <= 15; ++p) add_unit(S, p, p - s, s, 3, s == 0);
      continue;
    }
    for (int k = 0; k < 8 && s + k <= last; ++k)
      add_unit(S, s + k, k, s, (mode == 1 && s + k == 11) ? 1 : 3, (s == 0) || (k == 7));
    int lo = -1, hi = -1, cnt = 0, half = 0;
    if (s == 6) { lo = 8; hi = 8; cnt = 1; half = 2; }
    else if (s >= 7 && s <= last - 3) { lo = (s & 1) ? s + 2 : s + 1; hi = lo + 1; cnt = hi - 7; half = (s & 1) ? 0 : 1; }
    else if (s == last - 2 || s == last - 1) { lo = last; hi = last; cnt = last - 7; half = (s == last - 2) ? 0 : 1; }
    if (cnt) {
      const int split = (cnt + 1) / 2;
      const int j0 = (half == 1) ? split : 0, j1 = (half == 0) ? split : cnt;
      for (int j = j0; j < j1; ++j) {
        const int mm = cnt - 1 - j;
        S.ldm[nl] = mm;
        for (int t = hi; t >= lo; --t)
          if (t - mm >= 8) add_unit(S, t, t - mm, nl, (mode == 1 && t == 11) ? 1 : 3, false);
        ++nl;
      }
    }
  }
  S.nld = nl;
  return S;
}
constexpr int NGAP = 272 + 40, CAP = 2, KPRE = 16;
enum { A_NONE = 0, A_X, A_K, A_Q, A_PXR, A_PXC, A_EP };
struct Acts { int n[NGAP]; int a[NGAP][4]; int end; };
constexpr int place_back(Acts& T, int latest, int code, int cap) {
  int g = latest;
  while (g > 0 && T.n[g] >= cap) --g;
  T.a[g][T.n[g]++] = code;
  return g;
}
constexpr int place_fwd(Acts& T, int earliest, int code, int cap, int nm) {
  int g = earliest;
  while (T.n[g] >= (g < nm ? cap : 4)) ++g;
  T.a[g][T.n[g]++] = code;
  if (g + 1 > T.end) T.end = g + 1;
  return g;
}
constexpr Acts make_acts(const Sched& S, int mode) {
  Acts T{};
  T.end = S.n;
  const int dense = (mode == 0) ? 200 : (mode == 1 ? 90 : 100);
  for (int l = S.nst - 1; l >= 1; --l) {
    int g = S.ldu[l] - 5;
    const int cap = S.ldu[l] >= dense && mode ? 2 : 1;
    for (int i = 3; i >= 0; --i) if ((S.ldp[l] >> (i >> 1)) & 1) g = place_back(T, g - 1, A_X | (l << 4) | (i << 12), cap);
    for (int o = 1; o >= 0; --o) if ((S.om[l] >> o) & 1) g = place_back(T, g - 1, A_Q | (l << 4) | (o << 12), cap);
    if ((mode == 0 && !(l & 1)) || (mode == 1 && !(l & 3))) {
      g = place_back(T, g - 1, A_PXC | (l << 4), 1);
      place_back(T, g - 6, A_PXR, 1);
    }
  }
  for (int l = S.nld - 1; l >= 16; --l) {
    int g = S.ldu[l] - 5;
    for (int i = 3; i >= 0; --i) if ((S.ldp[l] >> (i >> 1)) & 1) g = place_back(T, g - 1, A_X | (l << 4) | (i << 12), S.ldu[l] >= dense ? 2 : 1);
  }
  for (int d = 0; d < 16; ++d) {
    if (S.ku[d] < KPRE) continue;
    int g = S.ku[d] - 5;
    g = place_back(T, g - 1, A_K | (d << 4) | (1 << 12), 2);
    place_back(T, g - 1, A_K | (d << 4), 2);
  }
  int prev[18] = {};
  for (int s = 0; s < S.nst; ++s) {
    if (!S.cm[s]) continue;
    int g = S.base[s] + 4;
    const int p16 = prev[16];
    for (int e = 0; e < ((s & 1) ? 18 : 17); ++e) {
      if (e < 14 && !(((e >= 8 ? S.om[s] : S.cm[s]) >> (e & 1)) & 1)) continue;
      if ((e == 15 && !(S.om[s] & 1)) || (e == 16 && !(S.om[s] & 2))) continue;
      int lo = g + (((e & 1) && e < 14) ? 0 : 1);
      if (lo <= prev[e | 1]) lo = prev[e | 1] + 1;
      if (e == 17 && lo <= p16) lo = p16 + 1;
      g = place_fwd(T, lo, A_EP | (s << 4) | (e << 12), S.base[s] >= dense ? 3 : CAP, S.n);
      prev[e] = g;
    }
  }
  return T;
}
template <int MODE> struct Tab {
  static constexpr Sched S = make_sched(MODE);
  static constexpr Acts A = make_acts(S, MODE);
};
static_assert(Tab<0>::S.n == 272 && Tab<1>::S.n == 144 && Tab<2>::S.n == 140, "schedule");

#define MFMA(a, b, c) __builtin_amdgcn_mfma_f32_16x16x32_bf16((a), (b), (c), 0, 0, 0)
#define STV_ARG
#define STV_PASS

template <int MODE>
DEV void conv_stream(lptr sm, u32 kimg, u32 ximg, lptr ywp, lptr pyp, int lane STV_ARG) {
  using T = Tab<MODE>;
  const int g = lane >> 4, lq = (lane & 15) >> 2, lp = lane & 3;
  const lptr kb = sm + kimg + 8 * (4 * lp + 8 * g + lq);
  const lptr xb = sm + ximg + 224 + 8 * (lp - 8 * g - lq);
  const bool l63 = (lane == 63);
  const lptr pxp = sm + FLAGS + 4 * (lane & 3);
  const u32 qa = (u32)(uintptr_t)ywp;
  u32x2 kr[16][2], xr[40][4];
  f32x4 ac[16][2];
  const f32x4 zero = {0.f, 0.f, 0.f, 0.f};
  float ts[16][2], u2[16][2], u1[16][2], yy[16][2];
  u32 pk[16];
  constexpr int NQ = MODE == 0 ? 3 : 5;
  u32 qv[NQ][2], px = 0;
#pragma unroll
  for (int i = 0; i < NQ; ++i) { qv[i][0] = 0; qv[i][1] = 0; }
#define RDQ(s_, i_) asm volatile("ds_read_u16_d16_hi %0, %1 offset:%2" : "+v"(qv[(s_) % NQ][i_]) : "v"(qa), "i"((s_) * 256 + 128 * (i_)) : "memory")
#define RDXP(l_, i_) xr[l_][i_] = trread(xb + 256 * T::S.ldm[l_] + ((i_) >> 1) * 128 + (((i_) & 1) ? 0 : 32))
  static_for<0, 16>([&](auto D_) {
    constexpr int d = decltype(D_)::value;
    if constexpr (T::S.ku[d] >= 0 && T::S.ku[d] < KPRE) { kr[d][0] = trread(kb + 256 * d); kr[d][1] = trread(kb + 256 * d + 32); }
  });
  if constexpr (MODE != 2) wait_words(sm + FLAGS, lane, 4, 1);
  STAMP(4);
  if constexpr (MODE != 2) { RDQ(0, 0); RDQ(0, 1); }
  RDXP(0, 0); RDXP(0, 1); RDXP(0, 2); RDXP(0, 3);
  SCHED0();
  static_for<0, T::A.end>([&](auto G_) {
    constexpr int G = decltype(G_)::value;
    if constexpr (G < T::S.n) {
      constexpr int p = T::S.p[G], par = T::S.par[G], d = T::S.d[G], l = T::S.ld[G];
      ac[p][par] = MFMA(frag(kr[d][0], kr[d][1]), frag(xr[l][2 * par], xr[l][2 * par + 1]), T::S.first[G] ? zero : ac[p][par]);
      SCHED0();
    }
    static_for<0, T::A.n[G]>([&](auto I_) {
      constexpr int code = T::A.a[G][decltype(I_)::value];
      constexpr int kind = code & 15, a0 = (code >> 4) & 255, a1 = code >> 12;
      if constexpr (kind == A_X) RDXP(a0, a1);
      if constexpr (kind == A_K) kr[a0][a1] = trread(kb + 256 * a0 + 32 * a1);
      if constexpr (kind == A_Q) RDQ(a0, a1);
      if constexpr (kind == A_PXR) px = *(volatile LDSP u32*)pxp;
      if constexpr (kind == A_PXC) {
        if (__builtin_expect(!__all(px >= (u32)(a0 / 2 + 1)), 0)) wait_words(sm + FLAGS, lane, 4, (u32)(a0 / 2 + 1));
        asm volatile("" ::: "memory");
      }
      if constexpr (kind == A_EP) {
        constexpr int s = a0, e = a1 >> 1, o = a1 & 1;
        constexpr bool hp = s > 0 && T::S.cm[s > 0 ? s - 1 : 0] != 0;
        if constexpr (a1 < 14) {
          if constexpr (e == 0) {
            float cur = ac[s][o][3], prev = 0.f;
            if constexpr (o) prev = ac[s][0][3]; else if constexpr (hp) prev = ac[s - 1][1][3];
            ts[s][o] = l63 ? prev : cur;
          }
          if constexpr (e == 1) { float a = ac[s][o][2]; u2[s][o] = a + ror1(ts[s][o]); }
          if constexpr (e == 2) {
            float prev = 0.f;
            if constexpr (o) prev = u2[s][0]; else if constexpr (hp) prev = u2[s - 1][1];
            ts[s][o] = l63 ? prev : u2[s][o];
          }
          if constexpr (e == 3) { float a = ac[s][o][1]; u1[s][o] = a + ror1(ts[s][o]); }
          if constexpr (e == 4) {
            float prev = 0.f;
            if constexpr (o) prev = u1[s][0]; else if constexpr (hp) prev = u1[s - 1][1];
            ts[s][o] = l63 ? prev : u1[s][o];
          }
          if constexpr (e == 5) { float a = ac[s][o][0]; yy[s][o] = a + ror1(ts[s][o]); asm volatile("" : "+v"(yy[s][o])); }
          if constexpr (e == 6) {
            asm volatile("" : "+v"(qv[s % NQ][o]));
            yy[s][o] *= __uint_as_float(qv[s % NQ][o]);
            asm volatile("" : "+v"(yy[s][o]));
          }
        }
        if constexpr (a1 == 14) pk[s] = pkbf(yy[s][(T::S.om[s] & 1) ? 0 : 1], yy[s][(T::S.om[s] & 2) ? 1 : 0]);
        if constexpr (a1 == 15) *(LDSP u16*)(ywp + s * 256) = (u16)pk[s];
        if constexpr (a1 == 16) *(LDSP u16*)(ywp + s * 256 + 128) = (u16)(pk[s] >> 16);
        if constexpr (a1 == 17) set_word(pyp, (u32)(s / 2 + 1));
      }
      SCHED0();
    });
  });
  STAMP(8);
#undef RDQ
#undef RDXP
}

#define ROLE_END(n) do { STAMP_OUT(); asm volatile(".Lfk_end" #n ":"); return; } while (0)

__global__ __launch_bounds__(768, 3) void fused_kernel(Params P) {
  extern __shared__ __attribute__((aligned(16))) char smem[];
  const lptr sm = (lptr)smem;
  const u32 smb = (u32)(uintptr_t)sm;
  const int tid = threadIdx.x;
  const int w = __builtin_amdgcn_readfirstlane(tid >> 6);
  const int lane = tid & 63;
  const int wg = blockIdx.x;
  const int xcd = wg & 7, idx = wg >> 3;
  const bool partA = idx < 10;
  const int g = lane >> 4, li = lane & 15, lq = li >> 2, lp = li & 3;
  STAMP(0);
  if (w == 11) {
    unsigned long long pc;
    u32 len;
    asm volatile("s_getpc_b64 %0\n.Lfk_code_begin:\n\ts_mov_b32 %1, .Lfk_end0-.Lfk_code_begin"
                 "\n\ts_max_i32 %1, %1, .Lfk_end1-.Lfk_code_begin\n\ts_max_i32 %1, %1, .Lfk_end2-.Lfk_code_begin"
                 "\n\ts_max_i32 %1, %1, .Lfk_end3-.Lfk_code_begin\n\ts_max_i32 %1, %1, .Lfk_end4-.Lfk_code_begin"
                 "\n\ts_max_i32 %1, %1, .Lfk_end5-.Lfk_code_begin" : "=s"(pc), "=&s"(len) :: "scc");
    const u32 off = min((u32)(lane + 64 * (idx & 3)) * 128u, len & ~3u);
    touch64((const char*)pc + off, smb + PFD);
  }
  gu64* const gflag = (gu64*)(uintptr_t)(P.ex + WS_FLAG);
  gu64* const gepoch = (gu64*)(uintptr_t)(P.ex + WS_EPOCH);
  if (idx >= 30) {
    if (w < 2) { delay(PFU); touch64(prefetch_addr(P, w, idx, xcd, lane), smb + PFD); }
    if (w == 8 && wg == 240) {
      const unsigned long long tag = __hip_atomic_load(gepoch, RLX_AGENT) + 1;
      delay(40);
#pragma unroll 1
      for (int spin = 0; spin < (1 << 16); ++spin) {
        bool ok = true;
#pragma unroll
        for (int j = 0; j < 5; ++j) { const int f = lane + 64 * j; ok &= (__hip_atomic_load(gflag + FSP * ((f >> 5) * 128 + (f & 31)), RLX_AGENT) == tag); }
        if (__all(ok)) break;
        __builtin_amdgcn_s_sleep(64);
      }
      if (lane == 0) __hip_atomic_store(gepoch, tag, RLX_AGENT);
    }
    vmcnt<0>();
    asm volatile(".Lfk_end1:");
    return;
  }
  if (tid < 80) *(LDSP u32*)(sm + FLAGS + 4 * tid) = 0;

  const int gi = partA ? xcd * 10 + idx : xcd * 20 + (idx - 10);
  const auto rx = __builtin_amdgcn_make_buffer_rsrc(P.ex + WS_EXX, 0, 6u << 20, 0x00020000);

  auto poll_exchange = [&](int col, int first, int nst) {
    wait_words(sm + FLAG3, lane, 1, 1);
    const unsigned long long tag = *(volatile LDSP unsigned long long*)(sm + FLAG3 + 8);
    bool ok = lane >= 8 * nst || lane < 8 * first;
#pragma unroll 1
    for (int spin = 0; spin < (1 << 16); ++spin) {
      if (!ok) ok = (__hip_atomic_load(gflag + FSP * (col * 32 + lane), RLX_AGENT) == tag);
      const unsigned long long miss = __ballot(!ok);
      const int done = miss ? (__builtin_ctzll(miss) >> 3) : nst;
      if (done > first) set_word(sm + FLAG3 + 4, (u32)done);
      if (!miss) break;
      __builtin_amdgcn_s_sleep(PSL);
    }
  };

  if (w >= (partA ? 4 : 8)) {
    const int h = w & 3, pg = (w < 8) ? 1 : 0;
    const int un = partA ? wg * 2 + pg : wg - 80;
    const int pcol = un % 10, pchunk = un / 10 + (partA ? 16 : 0);
    const u32 praw = partA ? A_PRAW + pg * PUNIT : B_PRAW;
    const u32 shk = SHK + pg * 512;
    const lptr fw = sm + FLAG2 + pg * 32;
    __builtin_amdgcn_s_setprio(3);
    unsigned long long tag = 0;
    if (w == 8) tag = __hip_atomic_load(gepoch, RLX_AGENT) + 1;
    {
      const u32 po = (u32)(64 * pchunk + 16 * h + (lane >> 3)) * ROWB + (u32)pcol * 128 + 16 * (lane & 7);
#pragma unroll
      for (int j = 0; j < 2; ++j) {
        dma16(P.k, po + j * 8 * ROWB, smb + praw + 512 + h * 2048 + j * 1024);
        dma16(P.v, po + j * 8 * ROWB, smb + praw + 8704 + h * 2048 + j * 1024);
        dma16(P.q, po + j * 8 * ROWB, smb + praw + 16896 + h * 2048 + j * 1024);
      }
      if (h == 0 && lane < 32) {
        if (pchunk) dma16(P.k, po - 4 * ROWB, smb + praw);
        else *(LDSP u32x4*)(sm + praw + 16 * lane) = u32x4{0, 0, 0, 0};
      }
      if (h == 1 && lane < 32) dma16(P.shift_k, (u32)pcol * 512 + 16 * lane, smb + shk);
    }
    STAMP(7);
    wgbarrier();
    if (!partA) {
      const u32x4 ones = {0x3F803F80u, 0x3F803F80u, 0x3F803F80u, 0x3F803F80u};
#pragma unroll
      for (int i = 0; i < 4; ++i) *(LDSP u32x4*)(sm + B_YIMG + h * YS + 1024 * i + 16 * lane) = ones;
      if (lane < 10) *(LDSP u32x4*)(sm + XIMG + h * XS + 16 * lane) = u32x4{0, 0, 0, 0};
    }
    vmcnt<0>();
    if (w == 8) {
      *(LDSP unsigned long long*)(sm + FLAG3 + 8) = tag;
      set_word(sm + FLAG3, 1);
    }
    set_word(fw + 4 * h, 1);
    if (w >= 10) touch64(prefetch_addr(P, w - 10, idx, xcd, lane), smb + PFD);
    wait_words(fw, lane, 4, 1);
    STAMP(12);
    {
      const u32x2 sw = *(LDSP u32x2*)(sm + shk + 8 * (16 * h + li));
      const float w0 = bflo(sw.x), w1 = bfhi(sw.x), w2 = bflo(sw.y), w3 = bfhi(sw.y);
      const u32 co = praw + 32 * h + 8 * lp;
      const u32 stg = partA ? A_PSTG + (w - 4) * YS : B_PSTG + h * XS;
      const u32 sw_ = stg + li * 128 + 8 * (g & 1);
      const u32 sr_ = stg + (lane >> 3) * 128 + 16 * ((lane ^ (lane >> 3)) & 7);
      const u32 eo = (u32)(pcol * 64 + 16 * h + (lane >> 3)) * 4096 + 128 * pchunk + 16 * (lane & 7);
      const u32 zst = (lq & 1) ? 256u : 0u;
      const u32 ka = (lq & 1) ? co + 128 * (4 * g + (lq >> 1)) : (u32)ZROW;
      const u32 va = (lq & 1) ? co + 8704 + 128 * (4 * g + (lq >> 1)) : (u32)ZROW;
      u32x2 qq[4], kr_[4][4], vr_[4][2];
#pragma unroll
      for (int u = 0; u < 4; ++u) {
#pragma unroll
        for (int j = 0; j < 4; ++j) kr_[u][j] = trread(sm + ka + zst * (8 * u + j));
#pragma unroll
        for (int j = 0; j < 2; ++j) vr_[u][j] = trread(sm + va + zst * (8 * u + j));
        qq[u] = trread(sm + co + 16896 + 128 * (16 * u + 4 * g + lq));
      }
#pragma unroll
      for (int u = 0; u < 4; ++u) {
        float kx[8], vx[4], kv[4];
#pragma unroll
        for (int j = 0; j < 4; ++j) { kx[2 * j] = __uint_as_float(kr_[u][j].x); kx[2 * j + 1] = __uint_as_float(kr_[u][j].y); }
#pragma unroll
        for (int j = 0; j < 2; ++j) { vx[2 * j] = __uint_as_float(vr_[u][j].x); vx[2 * j + 1] = __uint_as_float(vr_[u][j].y); }
#pragma unroll
        for (int j = 0; j < 4; ++j)
          kv[j] = vmul(vfma(kx[j + 4], w0, vfma(kx[j + 3], w1, vfma(kx[j + 2], w2, vmul(kx[j + 1], w3)))), vx[j]);
        const u32 so = sw_ + 16 * (((2 * u + (g >> 1)) ^ li) & 7);
        *(LDSP u32x2*)(sm + so) = u32x2{pkbf(kv[0], kv[1]), pkbf(kv[2], kv[3])};
        *(LDSP u32x2*)(sm + so + 2048) = qq[u];
      }
      wavefence();
      STAMP(6);
#pragma unroll
      for (int j = 0; j < 4; ++j)
        __builtin_amdgcn_raw_buffer_store_b128(*(const LDSP u32x4*)(sm + sr_ + 1024 * j), rx, eo + (j & 1) * 8 * 4096 + (j >> 1) * (u32)(WS_EXQ - WS_EXX), 0, 16);
    }
    STAMP(13);
    const u32 urow = (u32)(64 * h + lane) * ROWB + (u32)((gi * 4) & ~7) * 2;
    set_word(fw + 16 + 4 * h, 1);
    wait_words(sm + FLAG3, lane, 1, 1);
    tag = *(volatile LDSP unsigned long long*)(sm + FLAG3 + 8);
    vmcnt<0>();
    if (lane == 0) __hip_atomic_store(gflag + FSP * ((pcol * 4 + h) * 32 + pchunk), tag, RLX_AGENT);
    if (!partA) {
      dma16(P.u, urow, smb + RAWU + 1024 * h);
      dma16(P.u, urow + 256 * ROWB, smb + RAWU + 4096 + 1024 * h);
      wait_words(fw + 16, lane, 4, 1);
    }
    STAMP(9);
    if (w >= 8) {
      if (partA) {
        wait_words(sm + FLAG2 + 16, lane, 4, 1);
        wait_words(sm + FLAG2 + 48, lane, 4, 1);
#pragma unroll 1
        for (int st = 0; st < 3; ++st) {
          if (h == 0) poll_exchange(gi >> 1, st, st + 1); else wait_words(sm + FLAG3 + 4, lane, 1, (u32)(st + 1));
          __builtin_amdgcn_fence(__ATOMIC_ACQUIRE, "wavefront");
          u32x4 d[4];
#pragma unroll
          for (int j = 0; j < 4; ++j)
            d[j] = __builtin_amdgcn_raw_buffer_load_b128(rx, (u32)(gi * 8 + 2 * h + (j & 1)) * 4096 + 1024 * st + 16 * lane + (j >> 1) * (u32)(WS_EXQ - WS_EXX), 0, 16);
#pragma unroll
          for (int j = 0; j < 4; ++j)
            *(LDSP u32x4*)(sm + ((j >> 1) ? YIMG + (2 * h + (j & 1)) * YS : XIMG + (2 * h + (j & 1)) * XS + XPADB) + 1024 * st + 16 * lane) = d[j];
          set_word(sm + FLAGS + 4 * h, (u32)(2 * st + 2));
          if (st == 0) STAMP(5); if (st == 2) STAMP(14);
        }
        const u32 yrd = YIMG + lq * YS + 2 * (64 * h + 16 * g + 4 * lp);
        u16* const outp = P.out + gi * 8 + (size_t)(64 * h + lane) * (2 * DCH);
#pragma unroll 1
        for (int b = 0; b < (h < 3 ? 6 : 5); ++b) {
          wait_words(sm + FLAGS + 16, lane, 8, (u32)(b + 1));
          u32x2 a = trread(sm + yrd + 512 * b);
          u32x2 c = trread(sm + yrd + 512 * b + 4 * YS);
          *(u32x4*)(outp + (size_t)b * 256 * (2 * DCH)) = u32x4{a.x, a.y, c.x, c.y};
        }
        ROLE_END(2);
      }
      const u32 ua = RAWU + 1024 * h + 16 * (16 * g + 4 * lp + lq) + 2 * ((gi * 4) & 4);
      const u32 xw = XIMG + (lane & 3) * XS + XPADB + 8 * (lane >> 2) + 128 * h;
      const u32 yrd = B_YIMG + lq * YS + 2 * (64 * h + 16 * g + 4 * lp);
      const u32 hrd = H_YIMG + lq * HYS + 2 * (64 * h + 16 * g + 4 * lp - 1408);
      u16* const outp = P.out + gi * 4 + (size_t)(64 * h + lane) * (2 * DCH);
#pragma unroll 1
      for (int it = 0; it < 8; ++it) {
        int i = it;
        asm volatile("" : "+s"(i));
        if (i + 2 < 8) dma16(P.u, urow + (u32)(i + 2) * (256 * ROWB), smb + RAWU + 4096 * (i + 2) + 1024 * h);
        if (i < 6) vmcnt<2>(); else if (i == 6) vmcnt<1>(); else vmcnt<0>();
        *(LDSP u32x2*)(sm + xw + 512 * i) = trread(sm + ua + 4096 * i);
        set_word(sm + FLAGS + 4 * h, (u32)(i + 1));
      }
      STAMP(14);
#pragma unroll 1
      for (int i = 0; i < 8; ++i) {
        wait_words(sm + FLAGS + 16, lane, 4, (u32)(i + 1));
        *(u32x2*)(outp + DCH + (size_t)i * 256 * (2 * DCH)) = trread(sm + yrd + 512 * i);
      }
#pragma unroll 1
      for (int i = (h == 3 ? 5 : 6); i < 8; ++i) {
        wait_words(sm + FLAGS + 32, lane, 4, (u32)(i + 1));
        *(u32x2*)(outp + (size_t)i * 256 * (2 * DCH)) = trread(sm + hrd + 512 * i);
      }
      ROLE_END(3);
    }
    __builtin_amdgcn_s_setprio(0);
  } else if (partA) {
    wgbarrier();
  }

  if (partA) {
    const int ch = gi * 8 + w;
    const u32 kimg = KIMG + w * KS, ximg = XIMG + w * XS, yimg = YIMG + w * YS;
    if (lane < 10) *(LDSP u32x4*)(sm + ximg + 16 * lane) = u32x4{0, 0, 0, 0};
    if (lane < 6) *(LDSP u32x4*)(sm + kimg + 16 * lane) = u32x4{0, 0, 0, 0};
    const float dsk = ld_bf_uniform(P.ssm_D, ch);
    wait_words(sm + FLAG2, lane, 4, 1);
#pragma unroll
    for (int j = 0; j < 3; ++j) dma16(P.ssm_k, (u32)ch * (SEQ * 2) + 1024 * j + 16 * lane, smb + kimg + KPADB + 1024 * j);
    vmcnt<0>();
    if (lane == 0) {
      LDSP u16* k0 = (LDSP u16*)(sm + kimg + KPADB);
      *k0 = (u16)pkbf(bf2f(*k0) + dsk, 0.f);
    }
    STAMP(2);
    wavefence();
    conv_stream<1>(sm, kimg, ximg, sm + yimg + 2 * lane, sm + FLAGS + 16 + 4 * w, lane STV_PASS);
    ROLE_END(4);
  }

  {
    const int ch0 = gi * 4;
    const int cw = w & 3, ps = w >> 2;
    const int ch = ch0 + cw;
    const size_t o = (size_t)ch * NST + 64 * ps + lane;
    const u16 i_ar = P.A_re[o], i_ai = P.A_im[o], i_br = P.B_re[o], i_bi = P.B_im[o], i_cr = P.C_re[o], i_ci = P.C_im[o];
    const float ldt = ld_bf_uniform(P.log_dt, ch);
    const float dsk = ld_bf_uniform(P.Dskip, ch);
    wgbarrier();
    const float dt = rbf(expf(ldt));
    const float zr = dt * bf2f(i_ar), zi = dt * bf2f(i_ai);
    const cf dB = {dt * bf2f(i_br), dt * bf2f(i_bi)};
    const cf C = {bf2f(i_cr), bf2f(i_ci)};
    const cf W = cmul(C, dB);
    float mx = fmaxf(fabsf(W.r), fabsf(W.i));
    STAMP(1);
    {
      int m = __float_as_int(mx);
      m = max(m, __builtin_amdgcn_update_dpp(0, m, 0x111, 0xf, 0xf, false));
      m = max(m, __builtin_amdgcn_update_dpp(0, m, 0x112, 0xf, 0xf, false));
      m = max(m, __builtin_amdgcn_update_dpp(0, m, 0x114, 0xf, 0xf, false));
      m = max(m, __builtin_amdgcn_update_dpp(0, m, 0x118, 0xf, 0xf, false));
      int a0 = __builtin_amdgcn_readlane(m, 15), a1 = __builtin_amdgcn_readlane(m, 31);
      int a2 = __builtin_amdgcn_readlane(m, 47), a3 = __builtin_amdgcn_readlane(m, 63);
      mx = __int_as_float(max(max(a0, a1), max(a2, a3)));
    }
    int ex = (int)((__float_as_uint(mx) >> 23) & 0xff);
    ex = min(max(ex, 30), 240);
    const float sY = __uint_as_float((u32)(267 - ex) << 23);
    const float so = __uint_as_float((u32)(ex - 27) << 23);
    const float SX = 16384.f;

    f32x4 ak[3];
    const u32 stg = STG + w * STS;
    const u32 rb = stg + li * 272 + g * 16;
    const u32 m0v = smb + w * STS;
#define STGW(row, val_r, val_i) \
    asm volatile("ds_write_addtid_b32 %0 offset:%1" :: "v"(pkh((val_r), (val_i))), "i"(STG + (row) * 272) : "memory")
    {
      const cf E1 = cexp_pow2(zr, zi, 1.f);
      const cf E8c = cexp_pow2(zr, -zi, 8.f);
      const cf E128 = cexp_pow2(zr, zi, 128.f);
      const cf E1024 = cexp_pow2(zr, zi, 1024.f);
      const cf E2 = cmul(E1, E1);
      const cf E4 = cmul(E2, E2);
      u32 keep;
      asm volatile("s_mov_b32 %0, m0\n\ts_mov_b32 m0, %1\n\ts_nop 0" : "=&s"(keep) : "s"(m0v) : "memory");
      cf y = {W.r * sY, -(W.i * sY)};
      cf P1 = {E128.r * SX, E128.i * SX};
      cf v0 = {SX, 0.f}, v1 = P1;
#pragma unroll
      for (int b = 0; b < 8; ++b) {
        STGW(2 * b, y.r, y.i); y = cmul(y, E8c);
        STGW(16 + b, v0.r, v0.i); if (b < 7) v0 = cmul(v0, E1);
        STGW(2 * b + 1, y.r, y.i); if (b < 7) y = cmul(y, E8c);
        STGW(24 + b, v1.r, v1.i); if (b < 7) v1 = cmul(v1, E1);
      }
      f16x8 yf[4];
      const f32x4 zero = {0.f, 0.f, 0.f, 0.f};
#pragma unroll
      for (int s = 0; s < 4; ++s) {
        yf[s] = *(const LDSP f16x8*)(sm + rb + 64 * s);
        f16x8 xf = *(const LDSP f16x8*)(sm + rb + 16 * 272 + 64 * s);
        ak[0] = __builtin_amdgcn_mfma_f32_16x16x32_f16(xf, yf[s], s ? ak[0] : zero, 0, 0, 0);
      }
      asm volatile("" ::: "memory");
      cf Pa = cmul(P1, E128);
      cf Pb = {E1024.r * SX, E1024.i * SX};
#pragma unroll
      for (int m = 0; m < 2; ++m) {
        cf v = Pa;
#pragma unroll
        for (int r = 0; r < 4; ++r) {
          STGW(4 * m + r, v.r, v.i); if (r < 3) v = cmul(v, E2);
          if (r & 1) { STGW(16 + 2 * m + (r >> 1), Pb.r, Pb.i); Pb = cmul(Pb, E128); }
        }
        Pa = cmul(Pa, E128);
      }
#pragma unroll
      for (int m = 0; m < 4; ++m) {
        STGW(8 + 2 * m, Pa.r, Pa.i);
        cf v = cmul(Pa, E4);
        STGW(20 + m, Pb.r, Pb.i); Pb = cmul(Pb, E128);
        STGW(9 + 2 * m, v.r, v.i);
        if (m < 3) Pa = cmul(Pa, E128);
      }
      STGW(24, Pb.r, Pb.i);
      asm volatile("s_mov_b32 m0, %0" :: "s"(keep) : "memory");
#pragma unroll
      for (int s = 0; s < 4; ++s) {
        f16x8 x1 = *(const LDSP f16x8*)(sm + rb + 64 * s);
        f16x8 x2 = *(const LDSP f16x8*)(sm + rb + 16 * 272 + 64 * s);
        ak[1] = __builtin_amdgcn_mfma_f32_16x16x32_f16(x1, yf[s], s ? ak[1] : zero, 0, 0, 0);
        ak[2] = __builtin_amdgcn_mfma_f32_16x16x32_f16(x2, yf[s], s ? ak[2] : zero, 0, 0, 0);
      }
    }
#undef STGW
#pragma unroll
    for (int i = 0; i < 3; ++i) ak[i] *= so;
    STAMP(2);
    asm volatile("" ::: "memory");
    const u32 kimg = B_KIMG + cw * KS;
    if (ps) {
#pragma unroll
      for (int i = 0; i < 3; ++i) *(LDSP f32x4*)(sm + kimg + 1024 * i + 16 * lane) = ak[i];
      set_word(sm + FLAGS + 48 + 4 * cw, 1);
      const u32 hx = stg, hk = stg + XS, hy = H_YIMG + cw * HYS;
      asm volatile("s_waitcnt lgkmcnt(0)" ::: "memory");
#pragma unroll
      for (int j = 0; j < 4; ++j) dma16(P.ssm_k, (u32)ch * (SEQ * 2) + 1024 * j + 16 * lane, smb + hk + KPADB + 1024 * j);
      if (lane < 10) *(LDSP u32x4*)(sm + hx + 16 * lane) = u32x4{0, 0, 0, 0};
      if (lane < 6) *(LDSP u32x4*)(sm + hk + 16 * lane) = u32x4{0, 0, 0, 0};
      const float dh = ld_bf_uniform(P.ssm_D, ch);
      if (w == 4) poll_exchange(gi >> 2, 0, 4); else wait_words(sm + FLAG3 + 4, lane, 1, 4);
      __builtin_amdgcn_fence(__ATOMIC_ACQUIRE, "wavefront");
      STAMP(5);
      {
        u32x4 d[6];
#pragma unroll
        for (int j = 0; j < 4; ++j) d[j] = __builtin_amdgcn_raw_buffer_load_b128(rx, (u32)ch * 4096 + 1024 * j + 16 * lane, 0, 16);
#pragma unroll
        for (int j = 0; j < 2; ++j)
          d[4 + j] = __builtin_amdgcn_raw_buffer_load_b128(rx, (u32)ch * 4096 + 2816 + 256 * j + 16 * lane + (u32)(WS_EXQ - WS_EXX), 0, 16);
        vmcnt<0>();
        if (lane == 0) {
          LDSP u16* k0 = (LDSP u16*)(sm + hk + KPADB);
          *k0 = (u16)pkbf(bf2f(*k0) + dh, 0.f);
        }
#pragma unroll
        for (int j = 0; j < 4; ++j) *(LDSP u32x4*)(sm + hx + XPADB + 1024 * j + 16 * lane) = d[j];
#pragma unroll
        for (int j = 0; j < 2; ++j) *(LDSP u32x4*)(sm + hy + 256 * j + 16 * lane) = d[4 + j];
      }
      wavefence();
      conv_stream<2>(sm, hk, hx, sm + hy + 2 * lane - 11 * 256, sm + FLAGS + 16 + 4 * w, lane STV_PASS);
      ROLE_END(5);
    }
    wait_words(sm + FLAGS + 48 + 4 * cw, lane, 1, 1);
    {
      const u32 tb = stg;
#pragma unroll
      for (int i = 0; i < 3; ++i) ak[i] += *(const LDSP f32x4*)(sm + kimg + 1024 * i + 16 * lane);
      wavefence();
      const int b = li, p = g;
      if (lane < 6) *(LDSP u32x4*)(sm + kimg + 16 * lane) = u32x4{0, 0, 0, 0};
      {
        float a0 = ak[0][0] + (lane == 0 ? dsk : 0.f), a1 = ak[0][1], a2 = ak[0][2], a3 = ak[0][3];
        *(LDSP u32x2*)(sm + kimg + KPADB + 2 * (128 * (p >> 1) + 8 * b + 4 * (p & 1))) = u32x2{pkbf(a0, a1), pkbf(a2, a3)};
      }
      {
        float a0 = ak[1][0], a1 = ak[1][1], a2 = ak[1][2], a3 = ak[1][3];
        int i01, i23;
        if (p < 2) { i01 = 64 * p + 4 * b; i23 = i01 + 2; }
        else { i01 = 128 + 64 * (p - 2) + 2 * b; i23 = i01 + 32; }
        *(LDSP u32x2*)(sm + tb + 4 * i01) = u32x2{pkbf(a0, a0), pkbf(a1, a1)};
        *(LDSP u32x2*)(sm + tb + 4 * i23) = u32x2{pkbf(a2, a2), pkbf(a3, a3)};
#pragma unroll
        for (int j = 0; j < 4; ++j) {
          float a = ak[2][j];
          int ii = 256 + 16 * (4 * p + j) + b;
          *(LDSP u32*)(sm + tb + 4 * ii) = pkbf(a, a);
        }
      }
      wavefence();
      const LDSP u32* TB = (const LDSP u32*)(sm + tb);
      {
        const int i0 = 4 * (lane & 31);
        u32 g0 = TB[i0], g1 = TB[i0 + 1], g2 = TB[i0 + 2], g3 = TB[i0 + 3], g4 = TB[i0 + 4];
        u32x4 ov = {g0, (g1 & 0xffffu) | (g2 & 0xffff0000u), g2, (g3 & 0xffffu) | (g4 & 0xffff0000u)};
        *(LDSP u32x4*)(sm + kimg + KPADB + 16 * (32 + (lane & 31))) = ov;
      }
      {
        const int i0 = 128 + 2 * lane;
        u32 g0 = TB[i0], g1 = TB[i0 + 1], g2 = TB[i0 + 2];
        u32x4 ov = {g0, (g0 & 0xffffu) | (g1 & 0xffff0000u), g1, g2};
        *(LDSP u32x4*)(sm + kimg + KPADB + 16 * (64 + lane)) = ov;
      }
#pragma unroll
      for (int hh = 0; hh < 2; ++hh) {
        const int i0 = 256 + 64 * hh + lane;
        u32 g0 = TB[i0], g1 = TB[i0 + 1];
        u32 mid = (lane & 1) ? g1 : ((g0 & 0xffffu) | (g1 & 0xffff0000u));
        u32x4 ov = {g0, g0, mid, g1};
        *(LDSP u32x4*)(sm + kimg + KPADB + 16 * (128 + 64 * hh + lane)) = ov;
      }
    }
    wavefence();
    STAMP(3);
    conv_stream<0>(sm, kimg, XIMG + cw * XS, sm + B_YIMG + cw * YS + 2 * lane, sm + FLAGS + 16 + 4 * w, lane STV_PASS);
    ROLE_END(0);
  }
}

extern "C" void kernel_launch(void* const* d_in, const int* in_sizes, int n_in,
                              void* d_out, int out_size, void* d_ws, size_t ws_size,
                              hipStream_t stream) {
  Params P;
  P.q = (const u16*)d_in[0];
  P.k = (const u16*)d_in[1];
  P.v = (const u16*)d_in[2];
  P.shift_k = (const u16*)d_in[3];
  P.ssm_k = (const u16*)d_in[4];
  P.ssm_D = (const u16*)d_in[5];
  P.u = (const u16*)d_in[6];
  P.A_re = (const u16*)d_in[7];
  P.A_im = (const u16*)d_in[8];
  P.B_re = (const u16*)d_in[9];
  P.B_im = (const u16*)d_in[10];
  P.C_re = (const u16*)d_in[11];
  P.C_im = (const u16*)d_in[12];
  P.log_dt = (const u16*)d_in[13];
  P.Dskip = (const u16*)d_in[14];
  P.out = (u16*)d_out;
  P.ws = (unsigned long long*)d_ws;
  P.ex = (char*)d_ws + (16 << 20);
  (void)hipFuncSetAttribute((const void*)fused_kernel, hipFuncAttributeMaxDynamicSharedMemorySize, LDS_BYTES);
  fused_kernel<<<256, 768, LDS_BYTES, stream>>>(P);
}
```

```cpp
#include <hip/hip_runtime.h>
#include <hip/hip_bf16.h>
#include <stdint.h>

typedef __attribute__((ext_vector_type(4))) short s16x4;
typedef __attribute__((ext_vector_type(8))) _Float16 f16x8;
typedef __attribute__((ext_vector_type(8))) __bf16 bf16x8;
typedef __attribute__((ext_vector_type(2))) __bf16 bf16x2;
typedef __attribute__((ext_vector_type(2))) _Float16 f16x2;
typedef __attribute__((ext_vector_type(2))) float f32x2;
typedef __attribute__((ext_vector_type(4))) float f32x4;
typedef __attribute__((ext_vector_type(2))) unsigned int u32x2;
typedef __attribute__((ext_vector_type(4))) unsigned int u32x4;
typedef unsigned int u32;
typedef unsigned short u16;
#define LDSP __attribute__((address_space(3)))
#define DEV __device__ __forceinline__

constexpr int SEQ = 2048;
constexpr int DCH = 640;
constexpr int NST = 128;
constexpr int ROWB = DCH * 2;

constexpr int XS = 4272;
constexpr int XPADB = 160;
constexpr int YS = 4160;
constexpr int KS = 4224;
constexpr int KPADB = 96;
constexpr int FLAGS = 0;
constexpr int ZROW = 64;
constexpr int FLAG2 = 192;
constexpr int FLAG3 = 256;
constexpr int PFD = 320;
constexpr int SHK = 1024;
constexpr int XIMG = 2048;
constexpr int PUNIT = 8704 + 8192 + 8192;
constexpr int YIMG = 36224;
constexpr int KIMG = 69504;
constexpr int A_PRAW = 103296;
constexpr int A_PSTG = YIMG;
constexpr int STS = 8704;
constexpr int STG = 19200;
constexpr int H_XIMG = STG + 4 * STS;
constexpr int H_KIMG = H_XIMG + 4 * XS;
constexpr int B_KIMG = 88832;
constexpr int B_YIMG = 105728;
constexpr int HYS = 1344;
constexpr int H_YIMG = 122368;
constexpr int RAWU = 127744;
constexpr int B_PRAW = RAWU + 8192;
constexpr int B_PSTG = XIMG + XPADB;
constexpr int LDS_BYTES = 161024;

constexpr size_t WS_EPOCH = 0;
constexpr size_t WS_FLAG = 4096;
constexpr size_t WS_EXX = 1 << 20;
constexpr size_t WS_EXQ = 4 << 20;

DEV float bf2f(u16 v) { return __uint_as_float(((u32)v) << 16); }
DEV float bflo(u32 v) { return __uint_as_float(v << 16); }
DEV float bfhi(u32 v) { return __uint_as_float(v & 0xffff0000u); }
DEV u32 pkbf(float a, float b) {
  f32x2 t = {a, b};
  bf16x2 r = __builtin_convertvector(t, bf16x2);
  return __builtin_bit_cast(u32, r);
}
DEV u32 pkh(float a, float b) {
  f32x2 t = {a, b};
  f16x2 r = __builtin_convertvector(t, f16x2);
  return __builtin_bit_cast(u32, r);
}
DEV float rbf(float a) { return bflo(pkbf(a, a)); }
DEV float dot2(u32 a, u32 b, float c) {
  return __builtin_amdgcn_fdot2_f32_bf16(__builtin_bit_cast(bf16x2, a), __builtin_bit_cast(bf16x2, b), c, false);
}
DEV float vfma(float a, float b, float c) { float d; asm("v_fma_f32 %0, %1, %2, %3" : "=v"(d) : "v"(a), "v"(b), "v"(c)); return d; }
DEV float vmul(float a, float b) { float d; asm("v_mul_f32 %0, %1, %2" : "=v"(d) : "v"(a), "v"(b)); return d; }
typedef __attribute__((address_space(1))) unsigned long long gu64;
#define RLX_AGENT __ATOMIC_RELAXED, __HIP_MEMORY_SCOPE_AGENT
typedef LDSP char* lptr;
DEV u32x2 trread(lptr p) {
  s16x4 r = __builtin_amdgcn_ds_read_tr16_b64_v4i16((LDSP s16x4*)p);
  return __builtin_bit_cast(u32x2, r);
}
DEV bf16x8 frag(u32x2 a, u32x2 b) {
  u32x4 f = {a.x, a.y, b.x, b.y};
  return __builtin_bit_cast(bf16x8, f);
}
DEV float ror1(float v) {
  int r = __builtin_amdgcn_update_dpp(0, __float_as_int(v), 0x13C, 0xf, 0xf, false);
  return __int_as_float(r);
}
DEV void wavefence() { __builtin_amdgcn_fence(__ATOMIC_ACQ_REL, "wavefront"); __builtin_amdgcn_wave_barrier(); }
DEV void wgbarrier() {
  asm volatile("s_waitcnt lgkmcnt(0)" ::: "memory");
  __builtin_amdgcn_s_barrier();
  asm volatile("" ::: "memory");
}
DEV void dma16(const void* base, u32 voff, u32 ldsaddr) {
  u32 keep;
  ldsaddr = __builtin_amdgcn_readfirstlane(ldsaddr);
  const unsigned long long b64 = (unsigned long long)(uintptr_t)base;
  const unsigned long long sb = ((unsigned long long)(u32)__builtin_amdgcn_readfirstlane((u32)(b64 >> 32)) << 32) |
                                (u32)__builtin_amdgcn_readfirstlane((u32)b64);
  asm volatile("s_mov_b32 %0, m0\n\ts_mov_b32 m0, %3\n\ts_nop 2\n\tglobal_load_lds_dwordx4 %1, %2\n\ts_mov_b32 m0, %0"
               : "=&s"(keep) : "v"(voff), "s"(sb), "s"(ldsaddr) : "memory");
}
DEV void touch64(const void* p, u32 ldsaddr) {
  u32 keep;
  asm volatile("s_mov_b32 %0, m0\n\ts_mov_b32 m0, %2\n\ts_nop 0\n\tglobal_load_lds_dword %1, off\n\ts_mov_b32 m0, %0"
               : "=&s"(keep) : "v"(p), "s"(ldsaddr) : "memory");
}
template <int N> DEV void vmcnt() { asm volatile("s_waitcnt vmcnt(%0)" :: "i"(N) : "memory"); }
#define SCHED0() __builtin_amdgcn_sched_barrier(0)

template <int V> struct IC { static constexpr int value = V; };
template <int I, int N, class F> DEV void static_for(F&& f) {
  if constexpr (I < N) { f(IC<I>{}); static_for<I + 1, N>(f); }
}

struct cf { float r, i; };
DEV cf cmul(cf a, cf b) {
  cf o;
  o.r = fmaf(a.r, b.r, -(a.i * b.i));
  o.i = fmaf(a.r, b.i, a.i * b.r);
  return o;
}
DEV cf cexp_pow2(float zr, float zi, float x) {
  float mag = __builtin_amdgcn_exp2f((zr * x) * 1.44269504088896340736f);
  float th = zi * x;
  const float C_HI = 0.15915494f;
  const float C_LO = 6.4206382e-09f;
  float f = fmaf(th, C_LO, fmaf(th, C_HI, -rintf(th * C_HI)));
  cf o;
  o.r = mag * __builtin_amdgcn_cosf(f);
  o.i = mag * __builtin_amdgcn_sinf(f);
  return o;
}

#define STAMP(n)
#define CSTAMP(n)
#define STAMP_OUT()

struct Params {
  const u16 *q, *k, *v, *shift_k, *ssm_k, *ssm_D;
  const u16 *u, *A_re, *A_im, *B_re, *B_im, *C_re, *C_im, *log_dt, *Dskip;
  u16* out;
  unsigned long long* ws;
  char* ex;
};

DEV float ld_bf_uniform(const u16* p, int idx) {
  u32 d = *(const __attribute__((address_space(4))) u32*)(uintptr_t)(p + (idx & ~1));
  return (idx & 1) ? bfhi(d) : bflo(d);
}

DEV u32 prefetch_off(int half, int slice, int xcd, int lane) {
  const u32 row = (u32)(half * 4 + (lane >> 4)) * 256u + (u32)slice * 8u + ((lane >> 1) & 7);
  return row * ROWB + (u32)(((160 * xcd) >> 7) + (lane & 1)) * 128u;
}
DEV void wait_words(lptr p, int lane, int n, u32 v) {
#pragma unroll 1
  for (int spin = 0; spin < (1 << 16); ++spin) {
    u32 x = *(volatile LDSP u32*)(p + 4 * (lane & (n - 1)));
    if (__all(x >= v)) break;
    __builtin_amdgcn_s_sleep(1);
  }
  asm volatile("" ::: "memory");
}
#define PFS 3
#define PFP 3
#define PFU 8
#define PFH 4
#define FSP 16
#define PSL 6
DEV void delay(int n) {
  for (int i = 0; i < n; ++i) __builtin_amdgcn_s_sleep(PFS);
}

DEV void set_word(lptr p, u32 v) {
  asm volatile("" ::: "memory");
  *(volatile LDSP u32*)p = v;
  asm volatile("" ::: "memory");
}

struct Sched {
  int n;
  int p[272], par[272], d[272], ld[272];
  bool first[272];
  int base[16];
  int cm[16], om[16];
  int ldm[40], ldu[40], ldp[40];
  int ku[16];
  int nld, nst;
};
constexpr void add_unit(Sched& S, int p, int d, int ld, int pm, bool first) {
  for (int par = 0; par < 2; ++par)
    if ((pm >> par) & 1) {
      S.p[S.n] = p; S.par[S.n] = par; S.d[S.n] = d; S.ld[S.n] = ld; S.first[S.n] = first;
      if (S.ldu[ld] < 0) S.ldu[ld] = S.n;
      S.ldp[ld] |= 1 << par;
      if (S.ku[d] < 0) S.ku[d] = S.n;
      ++S.n;
    }
}
constexpr Sched make_sched(int mode) {
  Sched S{};
  const int last = (mode == 1) ? 11 : 15;
  S.nst = last + 1;
  int nl = 16;
  for (int l = 0; l < 40; ++l) { S.ldu[l] = -1; S.ldm[l] = l; }
  for (int d = 0; d < 16; ++d) S.ku[d] = -1;
  for (int s = 0; s <= last; ++s) {
    S.base[s] = S.n;
    S.cm[s] = (mode == 2 && s < 11) ? 0 : ((mode == 1 && s == 11) ? 1 : 3);
    S.om[s] = (mode == 2 && s == 11) ? 2 : S.cm[s];
    if (mode == 2) {
      for (int p = (s > 11 ? s : 11); p <= 15; ++p) add_unit(S, p, p - s, s, 3, s == 0);
      continue;
    }
    for (int k = 0; k < 8 && s + k <= last; ++k)
      add_unit(S, s + k, k, s, (mode == 1 && s + k == 11) ? 1 : 3, (s == 0) || (k == 7));
    int lo = -1, hi = -1, cnt = 0, half = 0;
    if (s == 6) { lo = 8; hi = 8; cnt = 1; half = 2; }
    else if (s >= 7 && s <= last - 3) { lo = (s & 1) ? s + 2 : s + 1; hi = lo + 1; cnt = hi - 7; half = (s & 1) ? 0 : 1; }
    else if (s == last - 2 || s == last - 1) { lo = last; hi = last; cnt = last - 7; half = (s == last - 2) ? 0 : 1; }
    if (cnt) {
      const int split = (cnt + 1) / 2;
      const int j0 = (half == 1) ? split : 0, j1 = (half == 0) ? split : cnt;
      for (int j = j0; j < j1; ++j) {
        const int mm = cnt - 1 - j;
        S.ldm[nl] = mm;
        for (int t = hi; t >= lo; --t)
          if (t - mm >= 8) add_unit(S, t, t - mm, nl, (mode == 1 && t == 11) ? 1 : 3, false);
        ++nl;
      }
    }
  }
  S.nld = nl;
  return S;
}
constexpr int NGAP = 272 + 40, CAP = 2, KPRE = 16;
enum { A_NONE = 0, A_X, A_K, A_Q, A_PXR, A_PXC, A_EP };
struct Acts { int n[NGAP]; int a[NGAP][4]; int end; };
constexpr int place_back(Acts& T, int latest, int code, int cap) {
  int g = latest;
  while (g > 0 && T.n[g] >= cap) --g;
  T.a[g][T.n[g]++] = code;
  return g;
}
constexpr int place_fwd(Acts& T, int earliest, int code, int cap, int nm) {
  int g = earliest;
  while (T.n[g] >= (g < nm ? cap : 4)) ++g;
  T.a[g][T.n[g]++] = code;
  if (g + 1 > T.end) T.end = g + 1;
  return g;
}
constexpr Acts make_acts(const Sched& S, int mode) {
  Acts T{};
  T.end = S.n;
  const int dense = (mode == 0) ? 200 : (mode == 1 ? 90 : 100);
  for (int l = S.nst - 1; l >= 1; --l) {
    int g = S.ldu[l] - 5;
    const int cap = S.ldu[l] >= dense && mode ? 2 : 1;
    for (int i = 3; i >= 0; --i) if ((S.ldp[l] >> (i >> 1)) & 1) g = place_back(T, g - 1, A_X | (l << 4) | (i << 12), cap);
    for (int o = 1; o >= 0; --o) if ((S.om[l] >> o) & 1) g = place_back(T, g - 1, A_Q | (l << 4) | (o << 12), cap);
    if ((mode == 0 && !(l & 1)) || (mode == 1 && !(l & 3))) {
      g = place_back(T, g - 1, A_PXC | (l << 4), 1);
      place_back(T, g - 6, A_PXR, 1);
    }
  }
  for (int l = S.nld - 1; l >= 16; --l) {
    int g = S.ldu[l] - 5;
    for (int i = 3; i >= 0; --i) if ((S.ldp[l] >> (i >> 1)) & 1) g = place_back(T, g - 1, A_X | (l << 4) | (i << 12), S.ldu[l] >= dense ? 2 : 1);
  }
  for (int d = 0; d < 16; ++d) {
    if (S.ku[d] < KPRE) continue;
    int g = S.ku[d] - 5;
    g = place_back(T, g - 1, A_K | (d << 4) | (1 << 12), 2);
    place_back(T, g - 1, A_K | (d << 4), 2);
  }
  int prev[18] = {};
  for (int s = 0; s < S.nst; ++s) {
    if (!S.cm[s]) continue;
    int g = S.base[s] + 4;
    const int p16 = prev[16];
    for (int e = 0; e < ((s & 1) ? 18 : 17); ++e) {
      if (e < 14 && !(((e >= 8 ? S.om[s] : S.cm[s]) >> (e & 1)) & 1)) continue;
      if ((e == 15 && !(S.om[s] & 1)) || (e == 16 && !(S.om[s] & 2))) continue;
      int lo = g + (((e & 1) && e < 14) ? 0 : 1);
      if (lo <= prev[e | 1]) lo = prev[e | 1] + 1;
      if (e == 17 && lo <= p16) lo = p16 + 1;
      g = place_fwd(T, lo, A_EP | (s << 4) | (e << 12), S.base[s] >= dense ? 3 : CAP, S.n);
      prev[e] = g;
    }
  }
  return T;
}
template <int MODE> struct Tab {
  static constexpr Sched S = make_sched(MODE);
  static constexpr Acts A = make_acts(S, MODE);
};
static_assert(Tab<0>::S.n == 272 && Tab<1>::S.n == 144 && Tab<2>::S.n == 140, "schedule");

#define MFMA(a, b, c) __builtin_amdgcn_mfma_f32_16x16x32_bf16((a), (b), (c), 0, 0, 0)
#define STV_ARG
#define STV_PASS

template <int MODE>
DEV void conv_stream(lptr sm, u32 kimg, u32 ximg, lptr ywp, lptr pyp, int lane STV_ARG) {
  using T = Tab<MODE>;
  const int g = lane >> 4, lq = (lane & 15) >> 2, lp = lane & 3;
  const lptr kb = sm + kimg + 8 * (4 * lp + 8 * g + lq);
  const lptr xb = sm + ximg + 224 + 8 * (lp - 8 * g - lq);
  const bool l63 = (lane == 63);
  const lptr pxp = sm + FLAGS + 4 * (lane & 3);
  const u32 qa = (u32)(uintptr_t)ywp;
  u32x2 kr[16][2], xr[40][4];
  f32x4 ac[16][2];
  const f32x4 zero = {0.f, 0.f, 0.f, 0.f};
  float ts[16][2], u2[16][2], u1[16][2], yy[16][2];
  u32 pk[16];
  constexpr int NQ = MODE == 0 ? 3 : 5;
  u32 qv[NQ][2], px = 0;
#pragma unroll
  for (int i = 0; i < NQ; ++i) { qv[i][0] = 0; qv[i][1] = 0; }
#define RDQ(s_, i_) asm volatile("ds_read_u16_d16_hi %0, %1 offset:%2" : "+v"(qv[(s_) % NQ][i_]) : "v"(qa), "i"((s_) * 256 + 128 * (i_)) : "memory")
#define RDXP(l_, i_) xr[l_][i_] = trread(xb + 256 * T::S.ldm[l_] + ((i_) >> 1) * 128 + (((i_) & 1) ? 0 : 32))
  static_for<0, 16>([&](auto D_) {
    constexpr int d = decltype(D_)::value;
    if constexpr (T::S.ku[d] >= 0 && T::S.ku[d] < KPRE) { kr[d][0] = trread(kb + 256 * d); kr[d][1] = trread(kb + 256 * d + 32); }
  });
  if constexpr (MODE != 2) wait_words(sm + FLAGS, lane, 4, 1);
  STAMP(4);
  if constexpr (MODE != 2) { RDQ(0, 0); RDQ(0, 1); }
  RDXP(0, 0); RDXP(0, 1); RDXP(0, 2); RDXP(0, 3);
  SCHED0();
  static_for<0, T::A.end>([&](auto G_) {
    constexpr int G = decltype(G_)::value;
    if constexpr (G < T::S.n) {
      constexpr int p = T::S.p[G], par = T::S.par[G], d = T::S.d[G], l = T::S.ld[G];
      ac[p][par] = MFMA(frag(kr[d][0], kr[d][1]), frag(xr[l][2 * par], xr[l][2 * par + 1]), T::S.first[G] ? zero : ac[p][par]);
      SCHED0();
    }
    static_for<0, T::A.n[G]>([&](auto I_) {
      constexpr int code = T::A.a[G][decltype(I_)::value];
      constexpr int kind = code & 15, a0 = (code >> 4) & 255, a1 = code >> 12;
      if constexpr (kind == A_X) RDXP(a0, a1);
      if constexpr (kind == A_K) kr[a0][a1] = trread(kb + 256 * a0 + 32 * a1);
      if constexpr (kind == A_Q) RDQ(a0, a1);
      if constexpr (kind == A_PXR) px = *(volatile LDSP u32*)pxp;
      if constexpr (kind == A_PXC) {
        if (__builtin_expect(!__all(px >= (u32)(a0 / 2 + 1)), 0)) wait_words(sm + FLAGS, lane, 4, (u32)(a0 / 2 + 1));
        asm volatile("" ::: "memory");
      }
      if constexpr (kind == A_EP) {
        constexpr int s = a0, e = a1 >> 1, o = a1 & 1;
        constexpr bool hp = s > 0 && T::S.cm[s > 0 ? s - 1 : 0] != 0;
        if constexpr (a1 < 14) {
          if constexpr (e == 0) {
            float cur = ac[s][o][3], prev = 0.f;
            if constexpr (o) prev = ac[s][0][3]; else if constexpr (hp) prev = ac[s - 1][1][3];
            ts[s][o] = l63 ? prev : cur;
          }
          if constexpr (e == 1) { float a = ac[s][o][2]; u2[s][o] = a + ror1(ts[s][o]); }
          if constexpr (e == 2) {
            float prev = 0.f;
            if constexpr (o) prev = u2[s][0]; else if constexpr (hp) prev = u2[s - 1][1];
            ts[s][o] = l63 ? prev : u2[s][o];
          }
          if constexpr (e == 3) { float a = ac[s][o][1]; u1[s][o] = a + ror1(ts[s][o]); }
          if constexpr (e == 4) {
            float prev = 0.f;
            if constexpr (o) prev = u1[s][0]; else if constexpr (hp) prev = u1[s - 1][1];
            ts[s][o] = l63 ? prev : u1[s][o];
          }
          if constexpr (e == 5) { float a = ac[s][o][0]; yy[s][o] = a + ror1(ts[s][o]); asm volatile("" : "+v"(yy[s][o])); }
          if constexpr (e == 6) {
            asm volatile("" : "+v"(qv[s % NQ][o]));
            yy[s][o] *= __uint_as_float(qv[s % NQ][o]);
            asm volatile("" : "+v"(yy[s][o]));
          }
        }
        if constexpr (a1 == 14) pk[s] = pkbf(yy[s][(T::S.om[s] & 1) ? 0 : 1], yy[s][(T::S.om[s] & 2) ? 1 : 0]);
        if constexpr (a1 == 15) *(LDSP u16*)(ywp + s * 256) = (u16)pk[s];
        if constexpr (a1 == 16) *(LDSP u16*)(ywp + s * 256 + 128) = (u16)(pk[s] >> 16);
        if constexpr (a1 == 17) set_word(pyp, (u32)(s / 2 + 1));
      }
      SCHED0();
    });
  });
  STAMP(8);
#undef RDQ
#undef RDXP
}

#define ROLE_END(n) do { STAMP_OUT(); asm volatile(".Lfk_end" #n ":"); return; } while (0)

__global__ __launch_bounds__(768, 3) void fused_kernel(Params P) {
  extern __shared__ __attribute__((aligned(16))) char smem[];
  const lptr sm = (lptr)smem;
  const u32 smb = (u32)(uintptr_t)sm;
  const int tid = threadIdx.x;
  const int w = __builtin_amdgcn_readfirstlane(tid >> 6);
  const int lane = tid & 63;
  const int wg = blockIdx.x;
  const int xcd = wg & 7, idx = wg >> 3;
  const bool partA = idx < 10;
  const int g = lane >> 4, li = lane & 15, lq = li >> 2, lp = li & 3;
  STAMP(0);
  asm volatile("" :: "s"(P.q), "s"(P.k), "s"(P.v), "s"(P.shift_k), "s"(P.ssm_k), "s"(P.ssm_D), "s"(P.u), "s"(P.A_re), "s"(P.A_im));
  asm volatile("" :: "s"(P.B_re), "s"(P.B_im), "s"(P.C_re), "s"(P.C_im), "s"(P.log_dt), "s"(P.Dskip), "s"(P.out), "s"(P.ex));
  if (w == 11) {
    unsigned long long pc;
    u32 len;
    asm volatile("s_getpc_b64 %0\n.Lfk_code_begin:\n\ts_mov_b32 %1, .Lfk_end0-.Lfk_code_begin"
                 "\n\ts_max_i32 %1, %1, .Lfk_end1-.Lfk_code_begin\n\ts_max_i32 %1, %1, .Lfk_end2-.Lfk_code_begin"
                 "\n\ts_max_i32 %1, %1, .Lfk_end3-.Lfk_code_begin\n\ts_max_i32 %1, %1, .Lfk_end4-.Lfk_code_begin"
                 "\n\ts_max_i32 %1, %1, .Lfk_end5-.Lfk_code_begin" : "=s"(pc), "=&s"(len) :: "scc");
    const u32 off = min((u32)(lane + 64 * (idx & 3)) * 128u, len & ~3u);
    touch64((const char*)pc + off, smb + PFD);
  }
  gu64* const gflag = (gu64*)(uintptr_t)(P.ex + WS_FLAG);
  gu64* const gepoch = (gu64*)(uintptr_t)(P.ex + WS_EPOCH);
  if (idx >= 30) {
    if (w < 2) { delay(PFU); touch64((const char*)P.u + prefetch_off(w, idx, xcd, lane), smb + PFD); }
    if (w == 8 && wg == 240) {
      const unsigned long long tag = __hip_atomic_load(gepoch, RLX_AGENT) + 1;
      delay(40);
#pragma unroll 1
      for (int spin = 0; spin < (1 << 16); ++spin) {
        bool ok = true;
#pragma unroll
        for (int j = 0; j < 5; ++j) { const int f = lane + 64 * j; ok &= (__hip_atomic_load(gflag + FSP * ((f >> 5) * 128 + (f & 31)), RLX_AGENT) == tag); }
        if (__all(ok)) break;
        __builtin_amdgcn_s_sleep(64);
      }
      if (lane == 0) __hip_atomic_store(gepoch, tag, RLX_AGENT);
    }
    vmcnt<0>();
    asm volatile(".Lfk_end1:");
    return;
  }
  if (tid < 80) *(LDSP u32*)(sm + FLAGS + 4 * tid) = 0;

  const int gi = partA ? xcd * 10 + idx : xcd * 20 + (idx - 10);
  const auto rx = __builtin_amdgcn_make_buffer_rsrc(P.ex + WS_EXX, 0, 6u << 20, 0x00020000);

  auto poll_exchange = [&](int col, int first, int nst) {
    wait_words(sm + FLAG3, lane, 1, 1);
    const unsigned long long tag = *(volatile LDSP unsigned long long*)(sm + FLAG3 + 8);
    bool ok = lane >= 8 * nst || lane < 8 * first;
#pragma unroll 1
    for (int spin = 0; spin < (1 << 16); ++spin) {
      if (!ok) ok = (__hip_atomic_load(gflag + FSP * (col * 32 + lane), RLX_AGENT) == tag);
      const unsigned long long miss = __ballot(!ok);
      const int done = miss ? (__builtin_ctzll(miss) >> 3) : nst;
      if (done > first) set_word(sm + FLAG3 + 4, (u32)done);
      if (!miss) break;
      __builtin_amdgcn_s_sleep(PSL);
    }
  };

  if (w >= (partA ? 4 : 8)) {
    const int h = w & 3, pg = (w < 8) ? 1 : 0;
    const int un = partA ? wg * 2 + pg : wg - 80;
    const int pcol = un % 10, pchunk = un / 10 + (partA ? 16 : 0);
    const u32 praw = partA ? A_PRAW + pg * PUNIT : B_PRAW;
    const u32 shk = SHK + pg * 512;
    const lptr fw = sm + FLAG2 + pg * 32;
    __builtin_amdgcn_s_setprio(3);
    unsigned long long tag = 0;
    {
      const u32 po = (u32)(64 * pchunk + 16 * h + (lane >> 3)) * ROWB + (u32)pcol * 128 + 16 * (lane & 7);
#pragma unroll
      for (int j = 0; j < 2; ++j) {
        dma16(P.k, po + j * 8 * ROWB, smb + praw + 512 + h * 2048 + j * 1024);
        dma16(P.v, po + j * 8 * ROWB, smb + praw + 8704 + h * 2048 + j * 1024);
        dma16(P.q, po + j * 8 * ROWB, smb + praw + 16896 + h * 2048 + j * 1024);
      }
      if (h == 0 && lane < 32) {
        if (pchunk) dma16(P.k, po - 4 * ROWB, smb + praw);
        else *(LDSP u32x4*)(sm + praw + 16 * lane) = u32x4{0, 0, 0, 0};
      }
      if (h == 1 && lane < 32) dma16(P.shift_k, (u32)pcol * 512 + 16 * lane, smb + shk);
    }
    if (w == 8) tag = __hip_atomic_load(gepoch, RLX_AGENT);
    STAMP(7);
    wgbarrier();
    if (!partA) {
      const u32x4 ones = {0x3F803F80u, 0x3F803F80u, 0x3F803F80u, 0x3F803F80u};
#pragma unroll
      for (int i = 0; i < 4; ++i) *(LDSP u32x4*)(sm + B_YIMG + h * YS + 1024 * i + 16 * lane) = ones;
      if (lane < 10) *(LDSP u32x4*)(sm + XIMG + h * XS + 16 * lane) = u32x4{0, 0, 0, 0};
    }
    vmcnt<0>();
    if (w == 8) {
      *(LDSP unsigned long long*)(sm + FLAG3 + 8) = tag + 1;
      set_word(sm + FLAG3, 1);
    }
    set_word(fw + 4 * h, 1);
    if (w >= 10) touch64((const char*)P.u + prefetch_off(w - 10, idx, xcd, lane), smb + PFD);
    wait_words(fw, lane, 4, 1);
    STAMP(12);
    {
      const u32x2 sw = *(LDSP u32x2*)(sm + shk + 8 * (16 * h + li));
      const float w0 = bflo(sw.x), w1 = bfhi(sw.x), w2 = bflo(sw.y), w3 = bfhi(sw.y);
      const u32 co = praw + 32 * h + 8 * lp;
      const u32 stg = partA ? A_PSTG + (w - 4) * YS : B_PSTG + h * XS;
      const u32 sw_ = stg + li * 128 + 8 * (g & 1);
      const u32 sr_ = stg + (lane >> 3) * 128 + 16 * ((lane ^ (lane >> 3)) & 7);
      const u32 eo = (u32)(pcol * 64 + 16 * h + (lane >> 3)) * 4096 + 128 * pchunk + 16 * (lane & 7);
      const u32 zst = (lq & 1) ? 256u : 0u;
      const u32 ka = (lq & 1) ? co + 128 * (4 * g + (lq >> 1)) : (u32)ZROW;
      const u32 va = (lq & 1) ? co + 8704 + 128 * (4 * g + (lq >> 1)) : (u32)ZROW;
      u32x2 qq[4], kr_[4][4], vr_[4][2];
#pragma unroll
      for (int u = 0; u < 4; ++u) {
#pragma unroll
        for (int j = 0; j < 4; ++j) kr_[u][j] = trread(sm + ka + zst * (8 * u + j));
#pragma unroll
        for (int j = 0; j < 2; ++j) vr_[u][j] = trread(sm + va + zst * (8 * u + j));
        qq[u] = trread(sm + co + 16896 + 128 * (16 * u + 4 * g + lq));
      }
#pragma unroll
      for (int u = 0; u < 4; ++u) {
        float kx[8], vx[4], kv[4];
#pragma unroll
        for (int j = 0; j < 4; ++j) { kx[2 * j] = __uint_as_float(kr_[u][j].x); kx[2 * j + 1] = __uint_as_float(kr_[u][j].y); }
#pragma unroll
        for (int j = 0; j < 2; ++j) { vx[2 * j] = __uint_as_float(vr_[u][j].x); vx[2 * j + 1] = __uint_as_float(vr_[u][j].y); }
#pragma unroll
        for (int j = 0; j < 4; ++j)
          kv[j] = vmul(vfma(kx[j + 4], w0, vfma(kx[j + 3], w1, vfma(kx[j + 2], w2, vmul(kx[j + 1], w3)))), vx[j]);
        const u32 so = sw_ + 16 * (((2 * u + (g >> 1)) ^ li) & 7);
        *(LDSP u32x2*)(sm + so) = u32x2{pkbf(kv[0], kv[1]), pkbf(kv[2], kv[3])};
        *(LDSP u32x2*)(sm + so + 2048) = qq[u];
      }
      wavefence();
      STAMP(6);
#pragma unroll
      for (int j = 0; j < 4; ++j)
        __builtin_amdgcn_raw_buffer_store_b128(*(const LDSP u32x4*)(sm + sr_ + 1024 * j), rx, eo + (j & 1) * 8 * 4096 + (j >> 1) * (u32)(WS_EXQ - WS_EXX), 0, 16);
    }
    STAMP(13);
    const u32 urow = (u32)(64 * h + lane) * ROWB + (u32)((gi * 4) & ~7) * 2;
    set_word(fw + 16 + 4 * h, 1);
    wait_words(sm + FLAG3, lane, 1, 1);
    tag = *(volatile LDSP unsigned long long*)(sm + FLAG3 + 8);
    vmcnt<0>();
    if (lane == 0) __hip_atomic_store(gflag + FSP * ((pcol * 4 + h) * 32 + pchunk), tag, RLX_AGENT);
    if (!partA) {
      dma16(P.u, urow, smb + RAWU + 1024 * h);
      dma16(P.u, urow + 256 * ROWB, smb + RAWU + 4096 + 1024 * h);
      wait_words(fw + 16, lane, 4, 1);
    }
    STAMP(9);
    if (w >= 8) {
      if (partA) {
        wait_words(sm + FLAG2 + 16, lane, 4, 1);
        wait_words(sm + FLAG2 + 48, lane, 4, 1);
#pragma unroll 1
        for (int st = 0; st < 3; ++st) {
          if (h == 0) poll_exchange(gi >> 1, st, st + 1); else wait_words(sm + FLAG3 + 4, lane, 1, (u32)(st + 1));
          __builtin_amdgcn_fence(__ATOMIC_ACQUIRE, "wavefront");
          u32x4 d[4];
#pragma unroll
          for (int j = 0; j < 4; ++j)
            d[j] = __builtin_amdgcn_raw_buffer_load_b128(rx, (u32)(gi * 8 + 2 * h + (j & 1)) * 4096 + 1024 * st + 16 * lane + (j >> 1) * (u32)(WS_EXQ - WS_EXX), 0, 16);
#pragma unroll
          for (int j = 0; j < 4; ++j)
            *(LDSP u32x4*)(sm + ((j >> 1) ? YIMG + (2 * h + (j & 1)) * YS : XIMG + (2 * h + (j & 1)) * XS + XPADB) + 1024 * st + 16 * lane) = d[j];
          set_word(sm + FLAGS + 4 * h, (u32)(2 * st + 2));
          if (st == 0) STAMP(5); if (st == 2) STAMP(14);
        }
        const u32 yrd = YIMG + lq * YS + 2 * (64 * h + 16 * g + 4 * lp);
        u16* const outp = P.out + gi * 8 + (size_t)(64 * h + lane) * (2 * DCH);
#pragma unroll 1
        for (int b = 0; b < (h < 3 ? 6 : 5); ++b) {
          wait_words(sm + FLAGS + 16, lane, 8, (u32)(b + 1));
          u32x2 a = trread(sm + yrd + 512 * b);
          u32x2 c = trread(sm + yrd + 512 * b + 4 * YS);
          *(u32x4*)(outp + (size_t)b * 256 * (2 * DCH)) = u32x4{a.x, a.y, c.x, c.y};
        }
        ROLE_END(2);
      }
      const u32 ua = RAWU + 1024 * h + 16 * (16 * g + 4 * lp + lq) + 2 * ((gi * 4) & 4);
      const u32 xw = XIMG + (lane & 3) * XS + XPADB + 8 * (lane >> 2) + 128 * h;
      const u32 yrd = B_YIMG + lq * YS + 2 * (64 * h + 16 * g + 4 * lp);
      const u32 hrd = H_YIMG + lq * HYS + 2 * (64 * h + 16 * g + 4 * lp - 1408);
      u16* const outp = P.out + gi * 4 + (size_t)(64 * h + lane) * (2 * DCH);
#pragma unroll 1
      for (int it = 0; it < 8; ++it) {
        int i = it;
        asm volatile("" : "+s"(i));
        if (i + 2 < 8) dma16(P.u, urow + (u32)(i + 2) * (256 * ROWB), smb + RAWU + 4096 * (i + 2) + 1024 * h);
        if (i < 6) vmcnt<2>(); else if (i == 6) vmcnt<1>(); else vmcnt<0>();
        *(LDSP u32x2*)(sm + xw + 512 * i) = trread(sm + ua + 4096 * i);
        set_word(sm + FLAGS + 4 * h, (u32)(i + 1));
      }
      STAMP(14);
#pragma unroll 1
      for (int i = 0; i < 8; ++i) {
        wait_words(sm + FLAGS + 16, lane, 4, (u32)(i + 1));
        *(u32x2*)(outp + DCH + (size_t)i * 256 * (2 * DCH)) = trread(sm + yrd + 512 * i);
      }
#pragma unroll 1
      for (int i = (h == 3 ? 5 : 6); i < 8; ++i) {
        wait_words(sm + FLAGS + 32, lane, 4, (u32)(i + 1));
        *(u32x2*)(outp + (size_t)i * 256 * (2 * DCH)) = trread(sm + hrd + 512 * i);
      }
      ROLE_END(3);
    }
    __builtin_amdgcn_s_setprio(0);
  } else if (partA) {
    wgbarrier();
  }

  if (partA) {
    const int ch = gi * 8 + w;
    const u32 kimg = KIMG + w * KS, ximg = XIMG + w * XS, yimg = YIMG + w * YS;
    if (lane < 10) *(LDSP u32x4*)(sm + ximg + 16 * lane) = u32x4{0, 0, 0, 0};
    if (lane < 6) *(LDSP u32x4*)(sm + kimg + 16 * lane) = u32x4{0, 0, 0, 0};
    const float dsk = ld_bf_uniform(P.ssm_D, ch);
    wait_words(sm + FLAG2, lane, 4, 1);
#pragma unroll
    for (int j = 0; j < 3; ++j) dma16(P.ssm_k, (u32)ch * (SEQ * 2) + 1024 * j + 16 * lane, smb + kimg + KPADB + 1024 * j);
    vmcnt<0>();
    if (lane == 0) {
      LDSP u16* k0 = (LDSP u16*)(sm + kimg + KPADB);
      *k0 = (u16)pkbf(bf2f(*k0) + dsk, 0.f);
    }
    STAMP(2);
    wavefence();
    conv_stream<1>(sm, kimg, ximg, sm + yimg + 2 * lane, sm + FLAGS + 16 + 4 * w, lane STV_PASS);
    ROLE_END(4);
  }

  {
    const int ch0 = gi * 4;
    const int cw = w & 3, ps = w >> 2;
    const int ch = ch0 + cw;
    const size_t o = (size_t)ch * NST + 64 * ps + lane;
    const u16 i_ar = P.A_re[o], i_ai = P.A_im[o], i_br = P.B_re[o], i_bi = P.B_im[o], i_cr = P.C_re[o], i_ci = P.C_im[o];
    const float ldt = ld_bf_uniform(P.log_dt, ch);
    const float dsk = ld_bf_uniform(P.Dskip, ch);
    wgbarrier();
    const float dt = rbf(expf(ldt));
    const float zr = dt * bf2f(i_ar), zi = dt * bf2f(i_ai);
    const cf dB = {dt * bf2f(i_br), dt * bf2f(i_bi)};
    const cf C = {bf2f(i_cr), bf2f(i_ci)};
    const cf W = cmul(C, dB);
    float mx = fmaxf(fabsf(W.r), fabsf(W.i));
    STAMP(1);
    {
      int m = __float_as_int(mx);
      m = max(m, __builtin_amdgcn_update_dpp(0, m, 0x111, 0xf, 0xf, false));
      m = max(m, __builtin_amdgcn_update_dpp(0, m, 0x112, 0xf, 0xf, false));
      m = max(m, __builtin_amdgcn_update_dpp(0, m, 0x114, 0xf, 0xf, false));
      m = max(m, __builtin_amdgcn_update_dpp(0, m, 0x118, 0xf, 0xf, false));
      int a0 = __builtin_amdgcn_readlane(m, 15), a1 = __builtin_amdgcn_readlane(m, 31);
      int a2 = __builtin_amdgcn_readlane(m, 47), a3 = __builtin_amdgcn_readlane(m, 63);
      mx = __int_as_float(max(max(a0, a1), max(a2, a3)));
    }
    int ex = (int)((__float_as_uint(mx) >> 23) & 0xff);
    ex = min(max(ex, 30), 240);
    const float sY = __uint_as_float((u32)(267 - ex) << 23);
    const float so = __uint_as_float((u32)(ex - 27) << 23);
    const float SX = 16384.f;

    f32x4 ak[3];
    const u32 stg = STG + w * STS;
    const u32 rb = stg + li * 272 + g * 16;
    const u32 m0v = smb + w * STS;
#define STGW(row, val_r, val_i) \
    asm volatile("ds_write_addtid_b32 %0 offset:%1" :: "v"(pkh((val_r), (val_i))), "i"(STG + (row) * 272) : "memory")
    {
      const cf E1 = cexp_pow2(zr, zi, 1.f);
      const cf E8c = cexp_pow2(zr, -zi, 8.f);
      const cf E128 = cexp_pow2(zr, zi, 128.f);
      const cf E1024 = cexp_pow2(zr, zi, 1024.f);
      const cf E2 = cmul(E1, E1);
      const cf E4 = cmul(E2, E2);
      u32 keep;
      asm volatile("s_mov_b32 %0, m0\n\ts_mov_b32 m0, %1\n\ts_nop 0" : "=&s"(keep) : "s"(m0v) : "memory");
      cf y = {W.r * sY, -(W.i * sY)};
      cf P1 = {E128.r * SX, E128.i * SX};
      cf v0 = {SX, 0.f}, v1 = P1;
#pragma unroll
      for (int b = 0; b < 8; ++b) {
        STGW(2 * b, y.r, y.i); y = cmul(y, E8c);
        STGW(16 + b, v0.r, v0.i); if (b < 7) v0 = cmul(v0, E1);
        STGW(2 * b + 1, y.r, y.i); if (b < 7) y = cmul(y, E8c);
        STGW(24 + b, v1.r, v1.i); if (b < 7) v1 = cmul(v1, E1);
      }
      f16x8 yf[4];
      const f32x4 zero = {0.f, 0.f, 0.f, 0.f};
#pragma unroll
      for (int s = 0; s < 4; ++s) {
        yf[s] = *(const LDSP f16x8*)(sm + rb + 64 * s);
        f16x8 xf = *(const LDSP f16x8*)(sm + rb + 16 * 272 + 64 * s);
        ak[0] = __builtin_amdgcn_mfma_f32_16x16x32_f16(xf, yf[s], s ? ak[0] : zero, 0, 0, 0);
      }
      asm volatile("" ::: "memory");
      cf Pa = cmul(P1, E128);
      cf Pb = {E1024.r * SX, E1024.i * SX};
#pragma unroll
      for (int m = 0; m < 2; ++m) {
        cf v = Pa;
#pragma unroll
        for (int r = 0; r < 4; ++r) {
          STGW(16 + 4 * m + r, v.r, v.i); if (r < 3) v = cmul(v, E2);
          if (r & 1) { STGW(4 * (2 * m + (r >> 1)) + 2, Pb.r, Pb.i); Pb = cmul(Pb, E128); }
        }
        Pa = cmul(Pa, E128);
      }
#pragma unroll
      for (int m = 0; m < 4; ++m) {
        STGW(4 * m, Pa.r, Pa.i);
        cf v = cmul(Pa, E4);
        STGW(4 * m + 3, Pb.r, Pb.i); Pb = cmul(Pb, E128);
        STGW(4 * m + 1, v.r, v.i);
        if (m < 3) Pa = cmul(Pa, E128);
      }
      STGW(24, Pb.r, Pb.i);
      asm volatile("s_mov_b32 m0, %0" :: "s"(keep) : "memory");
#pragma unroll
      for (int s = 0; s < 4; ++s) {
        f16x8 x1 = *(const LDSP f16x8*)(sm + rb + 64 * s);
        f16x8 x2 = *(const LDSP f16x8*)(sm + rb + 16 * 272 + 64 * s);
        ak[1] = __builtin_amdgcn_mfma_f32_16x16x32_f16(x1, yf[s], s ? ak[1] : zero, 0, 0, 0);
        ak[2] = __builtin_amdgcn_mfma_f32_16x16x32_f16(x2, yf[s], s ? ak[2] : zero, 0, 0, 0);
      }
    }
#undef STGW
#pragma unroll
    for (int i = 0; i < 3; ++i) ak[i] *= so;
    STAMP(2);
    asm volatile("" ::: "memory");
    const u32 kimg = B_KIMG + cw * KS;
    if (ps) {
#pragma unroll
      for (int i = 0; i < 3; ++i) *(LDSP f32x4*)(sm + kimg + 1024 * i + 16 * lane) = ak[i];
      set_word(sm + FLAGS + 48 + 4 * cw, 1);
      const u32 hx = stg, hk = stg + XS, hy = H_YIMG + cw * HYS;
      asm volatile("s_waitcnt lgkmcnt(0)" ::: "memory");
#pragma unroll
      for (int j = 0; j < 4; ++j) dma16(P.ssm_k, (u32)ch * (SEQ * 2) + 1024 * j + 16 * lane, smb + hk + KPADB + 1024 * j);
      if (lane < 10) *(LDSP u32x4*)(sm + hx + 16 * lane) = u32x4{0, 0, 0, 0};
      if (lane < 6) *(LDSP u32x4*)(sm + hk + 16 * lane) = u32x4{0, 0, 0, 0};
      const float dh = ld_bf_uniform(P.ssm_D, ch);
      if (w == 4) poll_exchange(gi >> 2, 0, 4); else wait_words(sm + FLAG3 + 4, lane, 1, 4);
      __builtin_amdgcn_fence(__ATOMIC_ACQUIRE, "wavefront");
      STAMP(5);
      {
        u32x4 d[6];
#pragma unroll
        for (int j = 0; j < 4; ++j) d[j] = __builtin_amdgcn_raw_buffer_load_b128(rx, (u32)ch * 4096 + 1024 * j + 16 * lane, 0, 16);
#pragma unroll
        for (int j = 0; j < 2; ++j)
          d[4 + j] = __builtin_amdgcn_raw_buffer_load_b128(rx, (u32)ch * 4096 + 2816 + 256 * j + 16 * lane + (u32)(WS_EXQ - WS_EXX), 0, 16);
        vmcnt<0>();
        if (lane == 0) {
          LDSP u16* k0 = (LDSP u16*)(sm + hk + KPADB);
          *k0 = (u16)pkbf(bf2f(*k0) + dh, 0.f);
        }
#pragma unroll
        for (int j = 0; j < 4; ++j) *(LDSP u32x4*)(sm + hx + XPADB + 1024 * j + 16 * lane) = d[j];
#pragma unroll
        for (int j = 0; j < 2; ++j) *(LDSP u32x4*)(sm + hy + 256 * j + 16 * lane) = d[4 + j];
      }
      wavefence();
      conv_stream<2>(sm, hk, hx, sm + hy + 2 * lane - 11 * 256, sm + FLAGS + 16 + 4 * w, lane STV_PASS);
      ROLE_END(5);
    }
    wait_words(sm + FLAGS + 48 + 4 * cw, lane, 1, 1);
    {
#pragma unroll
      for (int i = 0; i < 3; ++i) ak[i] += *(const LDSP f32x4*)(sm + kimg + 1024 * i + 16 * lane);
      wavefence();
      auto nxt = [&](float x, float e, bool lastl) {
        float r = __int_as_float(__builtin_amdgcn_update_dpp(0, __float_as_int(x), 0x130, 0xf, 0xf, true));
        return lastl ? e : r;
      };
      auto lane_of = [&](float x, int l) { return __int_as_float(__builtin_amdgcn_readlane(__float_as_int(x), l)); };
      const lptr kd = sm + kimg + KPADB;
      if (lane < 6) *(LDSP u32x4*)(sm + kimg + 16 * lane) = u32x4{0, 0, 0, 0};
      {
        const float a0 = ak[0][0] + (lane == 0 ? dsk : 0.f), a1 = ak[0][1], a2 = ak[0][2], a3 = ak[0][3];
        *(LDSP u32x2*)(kd + 2 * (128 * (g >> 1) + 8 * li + 4 * (g & 1))) = u32x2{pkbf(a0, a1), pkbf(a2, a3)};
      }
      const float s20 = ak[1][0], s21 = ak[1][1], s3a = ak[1][2], s3b = ak[1][3];
      const float c0 = ak[2][0], c1 = ak[2][1], c2 = ak[2][2], c3 = ak[2][3];
      const bool l63 = lane == 63;
      {
        const float n = nxt(c0, lane_of(s20, 0), lane == 31);
        if (lane < 32) *(LDSP u32x4*)(kd + 16 * (32 + lane)) = u32x4{pkbf(c0, c0), pkbf(c1, c2), pkbf(c2, c2), pkbf(c3, n)};
      }
      {
        const float n = nxt(s20, lane_of(s3a, 0), l63);
        *(LDSP u32x4*)(kd + 16 * (64 + lane)) = u32x4{pkbf(s20, s20), pkbf(s20, s21), pkbf(s21, s21), pkbf(n, n)};
      }
      {
        const float na = nxt(s3a, lane_of(s3b, 0), l63), nb = nxt(s3b, lane_of(c0, 32), l63);
        const u32 a = pkbf(s3a, s3a), b = pkbf(s3b, s3b);
        *(LDSP u32x4*)(kd + 16 * (128 + lane)) = u32x4{a, a, pkbf((lane & 1) ? na : s3a, na), pkbf(na, na)};
        *(LDSP u32x4*)(kd + 16 * (192 + lane)) = u32x4{b, b, pkbf((lane & 1) ? nb : s3b, nb), pkbf(nb, nb)};
      }
    }
    wavefence();
    STAMP(3);
    conv_stream<0>(sm, kimg, XIMG + cw * XS, sm + B_YIMG + cw * YS + 2 * lane, sm + FLAGS + 16 + 4 * w, lane STV_PASS);
    ROLE_END(0);
  }
}

extern "C" void kernel_launch(void* const* d_in, const int* in_sizes, int n_in,
                              void* d_out, int out_size, void* d_ws, size_t ws_size,
                              hipStream_t stream) {
  Params P;
  P.q = (const u16*)d_in[0];
  P.k = (const u16*)d_in[1];
  P.v = (const u16*)d_in[2];
  P.shift_k = (const u16*)d_in[3];
  P.ssm_k = (const u16*)d_in[4];
  P.ssm_D = (const u16*)d_in[5];
  P.u = (const u16*)d_in[6];
  P.A_re = (const u16*)d_in[7];
  P.A_im = (const u16*)d_in[8];
  P.B_re = (const u16*)d_in[9];
  P.B_im = (const u16*)d_in[10];
  P.C_re = (const u16*)d_in[11];
  P.C_im = (const u16*)d_in[12];
  P.log_dt = (const u16*)d_in[13];
  P.Dskip = (const u16*)d_in[14];
  P.out = (u16*)d_out;
  P.ws = (unsigned long long*)d_ws;
  P.ex = (char*)d_ws + (16 << 20);
  (void)hipFuncSetAttribute((const void*)fused_kernel, hipFuncAttributeMaxDynamicSharedMemorySize, LDS_BYTES);
  fused_kernel<<<256, 768, LDS_BYTES, stream>>>(P);
}
```

```cpp
#include <hip/hip_runtime.h>
#include <hip/hip_bf16.h>
#include <stdint.h>

typedef __attribute__((ext_vector_type(4))) short s16x4;
typedef __attribute__((ext_vector_type(8))) _Float16 f16x8;
typedef __attribute__((ext_vector_type(8))) __bf16 bf16x8;
typedef __attribute__((ext_vector_type(2))) __bf16 bf16x2;
typedef __attribute__((ext_vector_type(2))) _Float16 f16x2;
typedef __attribute__((ext_vector_type(2))) float f32x2;
typedef __attribute__((ext_vector_type(4))) float f32x4;
typedef __attribute__((ext_vector_type(2))) unsigned int u32x2;
typedef __attribute__((ext_vector_type(4))) unsigned int u32x4;
typedef unsigned int u32;
typedef unsigned short u16;
#define LDSP __attribute__((address_space(3)))
#define DEV __device__ __forceinline__

constexpr int SEQ = 2048;
constexpr int DCH = 640;
constexpr int NST = 128;
constexpr int ROWB = DCH * 2;

constexpr int XS = 4272;
constexpr int XPADB = 160;
constexpr int YS = 4160;
constexpr int KS = 4224;
constexpr int KPADB = 96;
constexpr int FLAGS = 0;
constexpr int ZROW = 64;
constexpr int FLAG2 = 192;
constexpr int FLAG3 = 256;
constexpr int PFD = 320;
constexpr int SHK = 1024;
constexpr int XIMG = 2048;
constexpr int PUNIT = 8704 + 8192 + 8192;
constexpr int YIMG = 36224;
constexpr int KIMG = 69504;
constexpr int A_PRAW = 103296;
constexpr int A_PSTG = YIMG;
constexpr int STS = 8704;
constexpr int STG = 19200;
constexpr int H_XIMG = STG + 4 * STS;
constexpr int H_KIMG = H_XIMG + 4 * XS;
constexpr int B_KIMG = 88832;
constexpr int B_YIMG = 105728;
constexpr int HYS = 1344;
constexpr int H_YIMG = 122368;
constexpr int RAWU = 127744;
constexpr int B_PRAW = RAWU + 8192;
constexpr int B_PSTG = XIMG + XPADB;
constexpr int LDS_BYTES = 161024;

constexpr size_t WS_EPOCH = 0;
constexpr size_t WS_FLAG = 4096;
constexpr size_t WS_EXX = 1 << 20;
constexpr size_t WS_EXQ = 4 << 20;

DEV float bf2f(u16 v) { return __uint_as_float(((u32)v) << 16); }
DEV float bflo(u32 v) { return __uint_as_float(v << 16); }
DEV float bfhi(u32 v) { return __uint_as_float(v & 0xffff0000u); }
DEV u32 pkbf(float a, float b) {
  f32x2 t = {a, b};
  bf16x2 r = __builtin_convertvector(t, bf16x2);
  return __builtin_bit_cast(u32, r);
}
DEV u32 pkh(float a, float b) {
  f32x2 t = {a, b};
  f16x2 r = __builtin_convertvector(t, f16x2);
  return __builtin_bit_cast(u32, r);
}
DEV float rbf(float a) { return bflo(pkbf(a, a)); }
DEV float dot2(u32 a, u32 b, float c) {
  return __builtin_amdgcn_fdot2_f32_bf16(__builtin_bit_cast(bf16x2, a), __builtin_bit_cast(bf16x2, b), c, false);
}
DEV float vfma(float a, float b, float c) { float d; asm("v_fma_f32 %0, %1, %2, %3" : "=v"(d) : "v"(a), "v"(b), "v"(c)); return d; }
DEV float vmul(float a, float b) { float d; asm("v_mul_f32 %0, %1, %2" : "=v"(d) : "v"(a), "v"(b)); return d; }
typedef __attribute__((address_space(1))) unsigned long long gu64;
#define RLX_AGENT __ATOMIC_RELAXED, __HIP_MEMORY_SCOPE_AGENT
typedef LDSP char* lptr;
DEV u32x2 trread(lptr p) {
  s16x4 r = __builtin_amdgcn_ds_read_tr16_b64_v4i16((LDSP s16x4*)p);
  return __builtin_bit_cast(u32x2, r);
}
DEV bf16x8 frag(u32x2 a, u32x2 b) {
  u32x4 f = {a.x, a.y, b.x, b.y};
  return __builtin_bit_cast(bf16x8, f);
}
DEV float ror1(float v) {
  int r = __builtin_amdgcn_update_dpp(0, __float_as_int(v), 0x13C, 0xf, 0xf, false);
  return __int_as_float(r);
}
DEV void wavefence() { __builtin_amdgcn_fence(__ATOMIC_ACQ_REL, "wavefront"); __builtin_amdgcn_wave_barrier(); }
DEV void wgbarrier() {
  asm volatile("s_waitcnt lgkmcnt(0)" ::: "memory");
  __builtin_amdgcn_s_barrier();
  asm volatile("" ::: "memory");
}
DEV void dma16(const void* base, u32 voff, u32 ldsaddr) {
  u32 keep;
  ldsaddr = __builtin_amdgcn_readfirstlane(ldsaddr);
  const unsigned long long b64 = (unsigned long long)(uintptr_t)base;
  const unsigned long long sb = ((unsigned long long)(u32)__builtin_amdgcn_readfirstlane((u32)(b64 >> 32)) << 32) |
                                (u32)__builtin_amdgcn_readfirstlane((u32)b64);
  asm volatile("s_mov_b32 %0, m0\n\ts_mov_b32 m0, %3\n\ts_nop 2\n\tglobal_load_lds_dwordx4 %1, %2\n\ts_mov_b32 m0, %0"
               : "=&s"(keep) : "v"(voff), "s"(sb), "s"(ldsaddr) : "memory");
}
DEV void touch64(const void* p, u32 ldsaddr) {
  u32 keep;
  asm volatile("s_mov_b32 %0, m0\n\ts_mov_b32 m0, %2\n\ts_nop 0\n\tglobal_load_lds_dword %1, off\n\ts_mov_b32 m0, %0"
               : "=&s"(keep) : "v"(p), "s"(ldsaddr) : "memory");
}
template <int N> DEV void vmcnt() { asm volatile("s_waitcnt vmcnt(%0)" :: "i"(N) : "memory"); }
#define SCHED0() __builtin_amdgcn_sched_barrier(0)

template <int V> struct IC { static constexpr int value = V; };
template <int I, int N, class F> DEV void static_for(F&& f) {
  if constexpr (I < N) { f(IC<I>{}); static_for<I + 1, N>(f); }
}

struct cf { float r, i; };
DEV cf cmul(cf a, cf b) {
  cf o;
  o.r = fmaf(a.r, b.r, -(a.i * b.i));
  o.i = fmaf(a.r, b.i, a.i * b.r);
  return o;
}
DEV cf cexp_pow2(float zr, float zi, float x) {
  float mag = __builtin_amdgcn_exp2f((zr * x) * 1.44269504088896340736f);
  float th = zi * x;
  const float C_HI = 0.15915494f;
  const float C_LO = 6.4206382e-09f;
  float f = fmaf(th, C_LO, fmaf(th, C_HI, -rintf(th * C_HI)));
  cf o;
  o.r = mag * __builtin_amdgcn_cosf(f);
  o.i = mag * __builtin_amdgcn_sinf(f);
  return o;
}

#define STAMP(n)
#define CSTAMP(n)
#define STAMP_OUT()

struct Params {
  const u16 *k, *v, *q, *shift_k; char* ex; const u16 *u, *ssm_k; u16* out;
  const u16 *A_re, *A_im, *B_re, *B_im, *C_re, *C_im, *log_dt, *Dskip;
  const u16* ssm_D;
  unsigned long long* ws;
};

DEV float ld_bf_uniform(const u16* p, int idx) {
  u32 d = *(const __attribute__((address_space(4))) u32*)(uintptr_t)(p + (idx & ~1));
  return (idx & 1) ? bfhi(d) : bflo(d);
}

DEV u32 prefetch_off(int half, int slice, int xcd, int lane) {
  const u32 row = (u32)(half * 4 + (lane >> 4)) * 256u + (u32)slice * 8u + ((lane >> 1) & 7);
  return row * ROWB + (u32)(((160 * xcd) >> 7) + (lane & 1)) * 128u;
}
DEV void wait_words(lptr p, int lane, int n, u32 v) {
#pragma unroll 1
  for (int spin = 0; spin < (1 << 16); ++spin) {
    u32 x = *(volatile LDSP u32*)(p + 4 * (lane & (n - 1)));
    if (__all(x >= v)) break;
    __builtin_amdgcn_s_sleep(1);
  }
  asm volatile("" ::: "memory");
}
#define PFS 3
#define PFP 3
#define PFU 8
#define PFH 4
#define FSP 16
#define PSL 6
DEV void delay(int n) {
  for (int i = 0; i < n; ++i) __builtin_amdgcn_s_sleep(PFS);
}

DEV void set_word(lptr p, u32 v) {
  asm volatile("" ::: "memory");
  *(volatile LDSP u32*)p = v;
  asm volatile("" ::: "memory");
}

struct Sched {
  int n;
  int p[272], par[272], d[272], ld[272];
  bool first[272];
  int base[16];
  int cm[16], om[16];
  int ldm[40], ldu[40], ldp[40];
  int ku[16];
  int nld, nst;
};
constexpr void add_unit(Sched& S, int p, int d, int ld, int pm, bool first) {
  for (int par = 0; par < 2; ++par)
    if ((pm >> par) & 1) {
      S.p[S.n] = p; S.par[S.n] = par; S.d[S.n] = d; S.ld[S.n] = ld; S.first[S.n] = first;
      if (S.ldu[ld] < 0) S.ldu[ld] = S.n;
      S.ldp[ld] |= 1 << par;
      if (S.ku[d] < 0) S.ku[d] = S.n;
      ++S.n;
    }
}
constexpr Sched make_sched(int mode) {
  Sched S{};
  const int last = (mode == 1) ? 11 : 15;
  S.nst = last + 1;
  int nl = 16;
  for (int l = 0; l < 40; ++l) { S.ldu[l] = -1; S.ldm[l] = l; }
  for (int d = 0; d < 16; ++d) S.ku[d] = -1;
  for (int s = 0; s <= last; ++s) {
    S.base[s] = S.n;
    S.cm[s] = (mode == 2 && s < 11) ? 0 : ((mode == 1 && s == 11) ? 1 : 3);
    S.om[s] = (mode == 2 && s == 11) ? 2 : S.cm[s];
    if (mode == 2) {
      for (int p = (s > 11 ? s : 11); p <= 15; ++p) add_unit(S, p, p - s, s, 3, s == 0);
      continue;
    }
    for (int k = 0; k < 8 && s + k <= last; ++k)
      add_unit(S, s + k, k, s, (mode == 1 && s + k == 11) ? 1 : 3, (s == 0) || (k == 7));
    int lo = -1, hi = -1, cnt = 0, half = 0;
    if (s == 6) { lo = 8; hi = 8; cnt = 1; half = 2; }
    else if (s >= 7 && s <= last - 3) { lo = (s & 1) ? s + 2 : s + 1; hi = lo + 1; cnt = hi - 7; half = (s & 1) ? 0 : 1; }
    else if (s == last - 2 || s == last - 1) { lo = last; hi = last; cnt = last - 7; half = (s == last - 2) ? 0 : 1; }
    if (cnt) {
      const int split = (cnt + 1) / 2;
      const int j0 = (half == 1) ? split : 0, j1 = (half == 0) ? split : cnt;
      for (int j = j0; j < j1; ++j) {
        const int mm = cnt - 1 - j;
        S.ldm[nl] = mm;
        for (int t = hi; t >= lo; --t)
          if (t - mm >= 8) add_unit(S, t, t - mm, nl, (mode == 1 && t == 11) ? 1 : 3, false);
        ++nl;
      }
    }
  }
  S.nld = nl;
  return S;
}
constexpr int NGAP = 272 + 40, CAP = 2, KPRE = 16;
enum { A_NONE = 0, A_X, A_K, A_Q, A_PXR, A_PXC, A_EP };
struct Acts { int n[NGAP]; int a[NGAP][4]; int end; };
constexpr int place_back(Acts& T, int latest, int code, int cap) {
  int g = latest;
  while (g > 0 && T.n[g] >= cap) --g;
  T.a[g][T.n[g]++] = code;
  return g;
}
constexpr int place_fwd(Acts& T, int earliest, int code, int cap, int nm) {
  int g = earliest;
  while (T.n[g] >= (g < nm ? cap : 4)) ++g;
  T.a[g][T.n[g]++] = code;
  if (g + 1 > T.end) T.end = g + 1;
  return g;
}
constexpr Acts make_acts(const Sched& S, int mode) {
  Acts T{};
  T.end = S.n;
  const int dense = (mode == 0) ? 200 : (mode == 1 ? 90 : 100);
  for (int l = S.nst - 1; l >= 1; --l) {
    int g = S.ldu[l] - 5;
    const int cap = S.ldu[l] >= dense && mode ? 2 : 1;
    for (int i = 3; i >= 0; --i) if ((S.ldp[l] >> (i >> 1)) & 1) g = place_back(T, g - 1, A_X | (l << 4) | (i << 12), cap);
    for (int o = 1; o >= 0; --o) if ((S.om[l] >> o) & 1) g = place_back(T, g - 1, A_Q | (l << 4) | (o << 12), cap);
    if ((mode == 0 && !(l & 1)) || (mode == 1 && !(l & 3))) {
      g = place_back(T, g - 1, A_PXC | (l << 4), 1);
      place_back(T, g - 6, A_PXR, 1);
    }
  }
  for (int l = S.nld - 1; l >= 16; --l) {
    int g = S.ldu[l] - 5;
    for (int i = 3; i >= 0; --i) if ((S.ldp[l] >> (i >> 1)) & 1) g = place_back(T, g - 1, A_X | (l << 4) | (i << 12), S.ldu[l] >= dense ? 2 : 1);
  }
  for (int d = 0; d < 16; ++d) {
    if (S.ku[d] < KPRE) continue;
    int g = S.ku[d] - 5;
    g = place_back(T, g - 1, A_K | (d << 4) | (1 << 12), 2);
    place_back(T, g - 1, A_K | (d << 4), 2);
  }
  int prev[18] = {};
  for (int s = 0; s < S.nst; ++s) {
    if (!S.cm[s]) continue;
    int g = S.base[s] + 4;
    const int p16 = prev[16];
    for (int e = 0; e < ((s & 1) ? 18 : 17); ++e) {
      if (e < 14 && !(((e >= 8 ? S.om[s] : S.cm[s]) >> (e & 1)) & 1)) continue;
      if ((e == 15 && !(S.om[s] & 1)) || (e == 16 && !(S.om[s] & 2))) continue;
      int lo = g + (((e & 1) && e < 14) ? 0 : 1);
      if (lo <= prev[e | 1]) lo = prev[e | 1] + 1;
      if (e == 17 && lo <= p16) lo = p16 + 1;
      g = place_fwd(T, lo, A_EP | (s << 4) | (e << 12), S.base[s] >= dense ? 3 : CAP, S.n);
      prev[e] = g;
    }
  }
  return T;
}
template <int MODE> struct Tab {
  static constexpr Sched S = make_sched(MODE);
  static constexpr Acts A = make_acts(S, MODE);
};
static_assert(Tab<0>::S.n == 272 && Tab<1>::S.n == 144 && Tab<2>::S.n == 140, "schedule");

#define MFMA(a, b, c) __builtin_amdgcn_mfma_f32_16x16x32_bf16((a), (b), (c), 0, 0, 0)
#define STV_ARG
#define STV_PASS

template <int MODE>
DEV void conv_stream(lptr sm, u32 kimg, u32 ximg, lptr ywp, lptr pyp, int lane STV_ARG) {
  using T = Tab<MODE>;
  const int g = lane >> 4, lq = (lane & 15) >> 2, lp = lane & 3;
  const lptr kb = sm + kimg + 8 * (4 * lp + 8 * g + lq);
  const lptr xb = sm + ximg + 224 + 8 * (lp - 8 * g - lq);
  const bool l63 = (lane == 63);
  const lptr pxp = sm + FLAGS + 4 * (lane & 3);
  const u32 qa = (u32)(uintptr_t)ywp;
  u32x2 kr[16][2], xr[40][4];
  f32x4 ac[16][2];
  const f32x4 zero = {0.f, 0.f, 0.f, 0.f};
  float ts[16][2], u2[16][2], u1[16][2], yy[16][2];
  u32 pk[16];
  constexpr int NQ = MODE == 0 ? 3 : 5;
  u32 qv[NQ][2], px = 0;
#pragma unroll
  for (int i = 0; i < NQ; ++i) { qv[i][0] = 0; qv[i][1] = 0; }
#define RDQ(s_, i_) asm volatile("ds_read_u16_d16_hi %0, %1 offset:%2" : "+v"(qv[(s_) % NQ][i_]) : "v"(qa), "i"((s_) * 256 + 128 * (i_)) : "memory")
#define RDXP(l_, i_) xr[l_][i_] = trread(xb + 256 * T::S.ldm[l_] + ((i_) >> 1) * 128 + (((i_) & 1) ? 0 : 32))
  static_for<0, 16>([&](auto D_) {
    constexpr int d = decltype(D_)::value;
    if constexpr (T::S.ku[d] >= 0 && T::S.ku[d] < KPRE) { kr[d][0] = trread(kb + 256 * d); kr[d][1] = trread(kb + 256 * d + 32); }
  });
  if constexpr (MODE != 2) wait_words(sm + FLAGS, lane, 4, 1);
  STAMP(4);
  if constexpr (MODE != 2) { RDQ(0, 0); RDQ(0, 1); }
  RDXP(0, 0); RDXP(0, 1); RDXP(0, 2); RDXP(0, 3);
  SCHED0();
  static_for<0, T::A.end>([&](auto G_) {
    constexpr int G = decltype(G_)::value;
    if constexpr (G < T::S.n) {
      constexpr int p = T::S.p[G], par = T::S.par[G], d = T::S.d[G], l = T::S.ld[G];
      ac[p][par] = MFMA(frag(kr[d][0], kr[d][1]), frag(xr[l][2 * par], xr[l][2 * par + 1]), T::S.first[G] ? zero : ac[p][par]);
      SCHED0();
    }
    static_for<0, T::A.n[G]>([&](auto I_) {
      constexpr int code = T::A.a[G][decltype(I_)::value];
      constexpr int kind = code & 15, a0 = (code >> 4) & 255, a1 = code >> 12;
      if constexpr (kind == A_X) RDXP(a0, a1);
      if constexpr (kind == A_K) kr[a0][a1] = trread(kb + 256 * a0 + 32 * a1);
      if constexpr (kind == A_Q) RDQ(a0, a1);
      if constexpr (kind == A_PXR) px = *(volatile LDSP u32*)pxp;
      if constexpr (kind == A_PXC) {
        if (__builtin_expect(!__all(px >= (u32)(a0 / 2 + 1)), 0)) wait_words(sm + FLAGS, lane, 4, (u32)(a0 / 2 + 1));
        asm volatile("" ::: "memory");
      }
      if constexpr (kind == A_EP) {
        constexpr int s = a0, e = a1 >> 1, o = a1 & 1;
        constexpr bool hp = s > 0 && T::S.cm[s > 0 ? s - 1 : 0] != 0;
        if constexpr (a1 < 14) {
          if constexpr (e == 0) {
            float cur = ac[s][o][3], prev = 0.f;
            if constexpr (o) prev = ac[s][0][3]; else if constexpr (hp) prev = ac[s - 1][1][3];
            ts[s][o] = l63 ? prev : cur;
          }
          if constexpr (e == 1) { float a = ac[s][o][2]; u2[s][o] = a + ror1(ts[s][o]); }
          if constexpr (e == 2) {
            float prev = 0.f;
            if constexpr (o) prev = u2[s][0]; else if constexpr (hp) prev = u2[s - 1][1];
            ts[s][o] = l63 ? prev : u2[s][o];
          }
          if constexpr (e == 3) { float a = ac[s][o][1]; u1[s][o] = a + ror1(ts[s][o]); }
          if constexpr (e == 4) {
            float prev = 0.f;
            if constexpr (o) prev = u1[s][0]; else if constexpr (hp) prev = u1[s - 1][1];
            ts[s][o] = l63 ? prev : u1[s][o];
          }
          if constexpr (e == 5) { float a = ac[s][o][0]; yy[s][o] = a + ror1(ts[s][o]); asm volatile("" : "+v"(yy[s][o])); }
          if constexpr (e == 6) {
            asm volatile("" : "+v"(qv[s % NQ][o]));
            yy[s][o] *= __uint_as_float(qv[s % NQ][o]);
            asm volatile("" : "+v"(yy[s][o]));
          }
        }
        if constexpr (a1 == 14) pk[s] = pkbf(yy[s][(T::S.om[s] & 1) ? 0 : 1], yy[s][(T::S.om[s] & 2) ? 1 : 0]);
        if constexpr (a1 == 15) *(LDSP u16*)(ywp + s * 256) = (u16)pk[s];
        if constexpr (a1 == 16) *(LDSP u16*)(ywp + s * 256 + 128) = (u16)(pk[s] >> 16);
        if constexpr (a1 == 17) set_word(pyp, (u32)(s / 2 + 1));
      }
      SCHED0();
    });
  });
  STAMP(8);
#undef RDQ
#undef RDXP
}

#define ROLE_END(n) do { STAMP_OUT(); asm volatile(".Lfk_end" #n ":"); return; } while (0)

__global__ __launch_bounds__(768, 3) void fused_kernel(Params P) {
  extern __shared__ __attribute__((aligned(16))) char smem[];
  const lptr sm = (lptr)smem;
  const u32 smb = (u32)(uintptr_t)sm;
  const int tid = threadIdx.x;
  const int w = __builtin_amdgcn_readfirstlane(tid >> 6);
  const int lane = tid & 63;
  const int wg = blockIdx.x;
  const int xcd = wg & 7, idx = wg >> 3;
  const bool partA = idx < 10;
  const int g = lane >> 4, li = lane & 15, lq = li >> 2, lp = li & 3;
  STAMP(0);
  if (w == 11) {
    unsigned long long pc;
    u32 len;
    asm volatile("s_getpc_b64 %0\n.Lfk_code_begin:\n\ts_mov_b32 %1, .Lfk_end0-.Lfk_code_begin"
                 "\n\ts_max_i32 %1, %1, .Lfk_end1-.Lfk_code_begin\n\ts_max_i32 %1, %1, .Lfk_end2-.Lfk_code_begin"
                 "\n\ts_max_i32 %1, %1, .Lfk_end3-.Lfk_code_begin\n\ts_max_i32 %1, %1, .Lfk_end4-.Lfk_code_begin"
                 "\n\ts_max_i32 %1, %1, .Lfk_end5-.Lfk_code_begin" : "=s"(pc), "=&s"(len) :: "scc");
    const u32 off = min((u32)(lane + 64 * (idx & 3)) * 128u, len & ~3u);
    touch64((const char*)pc + off, smb + PFD);
  }
#define gflag ((gu64*)(uintptr_t)(P.ex + WS_FLAG))
#define gepoch ((gu64*)(uintptr_t)(P.ex + WS_EPOCH))
#define rx __builtin_amdgcn_make_buffer_rsrc(P.ex + WS_EXX, 0, 6u << 20, 0x00020000)
  if (idx >= 30) {
    if (w < 2) { delay(PFU); touch64((const char*)P.u + prefetch_off(w, idx, xcd, lane), smb + PFD); }
    if (w == 8 && wg == 240) {
      const unsigned long long tag = __hip_atomic_load(gepoch, RLX_AGENT) + 1;
      delay(40);
#pragma unroll 1
      for (int spin = 0; spin < (1 << 16); ++spin) {
        bool ok = true;
#pragma unroll
        for (int j = 0; j < 5; ++j) { const int f = lane + 64 * j; ok &= (__hip_atomic_load(gflag + FSP * ((f >> 5) * 128 + (f & 31)), RLX_AGENT) == tag); }
        if (__all(ok)) break;
        __builtin_amdgcn_s_sleep(64);
      }
      if (lane == 0) __hip_atomic_store(gepoch, tag, RLX_AGENT);
    }
    vmcnt<0>();
    asm volatile(".Lfk_end1:");
    return;
  }
  if (tid < 80) *(LDSP u32*)(sm + FLAGS + 4 * tid) = 0;

  const int gi = partA ? xcd * 10 + idx : xcd * 20 + (idx - 10);

  auto poll_exchange = [&](int col, int first, int nst) {
    wait_words(sm + FLAG3, lane, 1, 1);
    const unsigned long long tag = *(volatile LDSP unsigned long long*)(sm + FLAG3 + 8);
    bool ok = lane >= 8 * nst || lane < 8 * first;
#pragma unroll 1
    for (int spin = 0; spin < (1 << 16); ++spin) {
      if (!ok) ok = (__hip_atomic_load(gflag + FSP * (col * 32 + lane), RLX_AGENT) == tag);
      const unsigned long long miss = __ballot(!ok);
      const int done = miss ? (__builtin_ctzll(miss) >> 3) : nst;
      if (done > first) set_word(sm + FLAG3 + 4, (u32)done);
      if (!miss) break;
      __builtin_amdgcn_s_sleep(PSL);
    }
  };

  if (w >= (partA ? 4 : 8)) {
    const int h = w & 3, pg = (w < 8) ? 1 : 0;
    const int un = partA ? wg * 2 + pg : wg - 80;
    const int pcol = un % 10, pchunk = un / 10 + (partA ? 16 : 0);
    const u32 praw = partA ? A_PRAW + pg * PUNIT : B_PRAW;
    const u32 shk = SHK + pg * 512;
    const lptr fw = sm + FLAG2 + pg * 32;
    asm volatile("" :: "s"(P.k), "s"(P.v), "s"(P.q), "s"(P.shift_k), "s"(P.ex), "s"(P.u), "s"(P.ssm_k), "s"(P.out));
    __builtin_amdgcn_s_setprio(3);
    unsigned long long tag = 0;
    {
      const u32 po = (u32)(64 * pchunk + 16 * h + (lane >> 3)) * ROWB + (u32)pcol * 128 + 16 * (lane & 7);
#pragma unroll
      for (int j = 0; j < 2; ++j) {
        dma16(P.k, po + j * 8 * ROWB, smb + praw + 512 + h * 2048 + j * 1024);
        dma16(P.v, po + j * 8 * ROWB, smb + praw + 8704 + h * 2048 + j * 1024);
        dma16(P.q, po + j * 8 * ROWB, smb + praw + 16896 + h * 2048 + j * 1024);
      }
      if (h == 0 && lane < 32) {
        if (pchunk) dma16(P.k, po - 4 * ROWB, smb + praw);
        else *(LDSP u32x4*)(sm + praw + 16 * lane) = u32x4{0, 0, 0, 0};
      }
      if (h == 1 && lane < 32) dma16(P.shift_k, (u32)pcol * 512 + 16 * lane, smb + shk);
    }
    STAMP(7);
    wgbarrier();
    if (!partA) {
      const u32x4 ones = {0x3F803F80u, 0x3F803F80u, 0x3F803F80u, 0x3F803F80u};
#pragma unroll
      for (int i = 0; i < 4; ++i) *(LDSP u32x4*)(sm + B_YIMG + h * YS + 1024 * i + 16 * lane) = ones;
      if (lane < 10) *(LDSP u32x4*)(sm + XIMG + h * XS + 16 * lane) = u32x4{0, 0, 0, 0};
    }
    if (w == 8) {
      *(LDSP unsigned long long*)(sm + FLAG3 + 8) = __hip_atomic_load(gepoch, RLX_AGENT) + 1;
      set_word(sm + FLAG3, 1);
    }
    vmcnt<0>();
    set_word(fw + 4 * h, 1);
    if (w >= 10) touch64((const char*)P.u + prefetch_off(w - 10, idx, xcd, lane), smb + PFD);
    wait_words(fw, lane, 4, 1);
    STAMP(12);
    {
      const u32x2 sw = *(LDSP u32x2*)(sm + shk + 8 * (16 * h + li));
      const float w0 = bflo(sw.x), w1 = bfhi(sw.x), w2 = bflo(sw.y), w3 = bfhi(sw.y);
      const u32 co = praw + 32 * h + 8 * lp;
      const u32 stg = partA ? A_PSTG + (w - 4) * YS : B_PSTG + h * XS;
      const u32 sw_ = stg + li * 128 + 8 * (g & 1);
      const u32 sr_ = stg + (lane >> 3) * 128 + 16 * ((lane ^ (lane >> 3)) & 7);
      const u32 eo = (u32)(pcol * 64 + 16 * h + (lane >> 3)) * 4096 + 128 * pchunk + 16 * (lane & 7);
      const u32 zst = (lq & 1) ? 256u : 0u;
      const u32 ka = (lq & 1) ? co + 128 * (4 * g + (lq >> 1)) : (u32)ZROW;
      const u32 va = (lq & 1) ? co + 8704 + 128 * (4 * g + (lq >> 1)) : (u32)ZROW;
      u32x2 qq[4], kr_[4][4], vr_[4][2];
#pragma unroll
      for (int u = 0; u < 4; ++u) {
#pragma unroll
        for (int j = 0; j < 4; ++j) kr_[u][j] = trread(sm + ka + zst * (8 * u + j));
#pragma unroll
        for (int j = 0; j < 2; ++j) vr_[u][j] = trread(sm + va + zst * (8 * u + j));
        qq[u] = trread(sm + co + 16896 + 128 * (16 * u + 4 * g + lq));
      }
#pragma unroll
      for (int u = 0; u < 4; ++u) {
        float kx[8], vx[4], kv[4];
#pragma unroll
        for (int j = 0; j < 4; ++j) { kx[2 * j] = __uint_as_float(kr_[u][j].x); kx[2 * j + 1] = __uint_as_float(kr_[u][j].y); }
#pragma unroll
        for (int j = 0; j < 2; ++j) { vx[2 * j] = __uint_as_float(vr_[u][j].x); vx[2 * j + 1] = __uint_as_float(vr_[u][j].y); }
#pragma unroll
        for (int j = 0; j < 4; ++j)
          kv[j] = vmul(vfma(kx[j + 4], w0, vfma(kx[j + 3], w1, vfma(kx[j + 2], w2, vmul(kx[j + 1], w3)))), vx[j]);
        const u32 so = sw_ + 16 * (((2 * u + (g >> 1)) ^ li) & 7);
        *(LDSP u32x2*)(sm + so) = u32x2{pkbf(kv[0], kv[1]), pkbf(kv[2], kv[3])};
        *(LDSP u32x2*)(sm + so + 2048) = qq[u];
      }
      wavefence();
      STAMP(6);
#pragma unroll
      for (int j = 0; j < 4; ++j)
        __builtin_amdgcn_raw_buffer_store_b128(*(const LDSP u32x4*)(sm + sr_ + 1024 * j), rx, eo + (j & 1) * 8 * 4096 + (j >> 1) * (u32)(WS_EXQ - WS_EXX), 0, 16);
    }
    STAMP(13);
    const u32 urow = (u32)(64 * h + lane) * ROWB + (u32)((gi * 4) & ~7) * 2;
    set_word(fw + 16 + 4 * h, 1);
    wait_words(sm + FLAG3, lane, 1, 1);
    tag = *(volatile LDSP unsigned long long*)(sm + FLAG3 + 8);
    vmcnt<0>();
    if (lane == 0) __hip_atomic_store(gflag + FSP * ((pcol * 4 + h) * 32 + pchunk), tag, RLX_AGENT);
    if (!partA) {
      dma16(P.u, urow, smb + RAWU + 1024 * h);
      dma16(P.u, urow + 256 * ROWB, smb + RAWU + 4096 + 1024 * h);
      wait_words(fw + 16, lane, 4, 1);
    }
    STAMP(9);
    if (w >= 8) {
      if (partA) {
        wait_words(sm + FLAG2 + 16, lane, 4, 1);
        wait_words(sm + FLAG2 + 48, lane, 4, 1);
#pragma unroll 1
        for (int st = 0; st < 3; ++st) {
          if (h == 0) poll_exchange(gi >> 1, st, st + 1); else wait_words(sm + FLAG3 + 4, lane, 1, (u32)(st + 1));
          __builtin_amdgcn_fence(__ATOMIC_ACQUIRE, "wavefront");
          u32x4 d[4];
#pragma unroll
          for (int j = 0; j < 4; ++j)
            d[j] = __builtin_amdgcn_raw_buffer_load_b128(rx, (u32)(gi * 8 + 2 * h + (j & 1)) * 4096 + 1024 * st + 16 * lane + (j >> 1) * (u32)(WS_EXQ - WS_EXX), 0, 16);
#pragma unroll
          for (int j = 0; j < 4; ++j)
            *(LDSP u32x4*)(sm + ((j >> 1) ? YIMG + (2 * h + (j & 1)) * YS : XIMG + (2 * h + (j & 1)) * XS + XPADB) + 1024 * st + 16 * lane) = d[j];
          set_word(sm + FLAGS + 4 * h, (u32)(2 * st + 2));
          if (st == 0) STAMP(5); if (st == 2) STAMP(14);
        }
        const u32 yrd = YIMG + lq * YS + 2 * (64 * h + 16 * g + 4 * lp);
        u16* const outp = P.out + gi * 8 + (size_t)(64 * h + lane) * (2 * DCH);
#pragma unroll 1
        for (int b = 0; b < (h < 3 ? 6 : 5); ++b) {
          wait_words(sm + FLAGS + 16, lane, 8, (u32)(b + 1));
          u32x2 a = trread(sm + yrd + 512 * b);
          u32x2 c = trread(sm + yrd + 512 * b + 4 * YS);
          *(u32x4*)(outp + (size_t)b * 256 * (2 * DCH)) = u32x4{a.x, a.y, c.x, c.y};
        }
        ROLE_END(2);
      }
      const u32 ua = RAWU + 1024 * h + 16 * (16 * g + 4 * lp + lq) + 2 * ((gi * 4) & 4);
      const u32 xw = XIMG + (lane & 3) * XS + XPADB + 8 * (lane >> 2) + 128 * h;
      const u32 yrd = B_YIMG + lq * YS + 2 * (64 * h + 16 * g + 4 * lp);
      const u32 hrd = H_YIMG + lq * HYS + 2 * (64 * h + 16 * g + 4 * lp - 1408);
      u16* const outp = P.out + gi * 4 + (size_t)(64 * h + lane) * (2 * DCH);
#pragma unroll 1
      for (int it = 0; it < 8; ++it) {
        int i = it;
        asm volatile("" : "+s"(i));
        if (i + 2 < 8) dma16(P.u, urow + (u32)(i + 2) * (256 * ROWB), smb + RAWU + 4096 * (i + 2) + 1024 * h);
        if (i < 6) vmcnt<2>(); else if (i == 6) vmcnt<1>(); else vmcnt<0>();
        *(LDSP u32x2*)(sm + xw + 512 * i) = trread(sm + ua + 4096 * i);
        set_word(sm + FLAGS + 4 * h, (u32)(i + 1));
      }
      STAMP(14);
#pragma unroll 1
      for (int i = 0; i < 8; ++i) {
        wait_words(sm + FLAGS + 16, lane, 4, (u32)(i + 1));
        *(u32x2*)(outp + DCH + (size_t)i * 256 * (2 * DCH)) = trread(sm + yrd + 512 * i);
      }
#pragma unroll 1
      for (int i = (h == 3 ? 5 : 6); i < 8; ++i) {
        wait_words(sm + FLAGS + 32, lane, 4, (u32)(i + 1));
        *(u32x2*)(outp + (size_t)i * 256 * (2 * DCH)) = trread(sm + hrd + 512 * i);
      }
      ROLE_END(3);
    }
    __builtin_amdgcn_s_setprio(0);
  } else if (partA) {
    wgbarrier();
  }

  if (partA) {
    const int ch = gi * 8 + w;
    const u32 kimg = KIMG + w * KS, ximg = XIMG + w * XS, yimg = YIMG + w * YS;
    if (lane < 10) *(LDSP u32x4*)(sm + ximg + 16 * lane) = u32x4{0, 0, 0, 0};
    if (lane < 6) *(LDSP u32x4*)(sm + kimg + 16 * lane) = u32x4{0, 0, 0, 0};
    const float dsk = ld_bf_uniform(P.ssm_D, ch);
    wait_words(sm + FLAG2, lane, 4, 1);
#pragma unroll
    for (int j = 0; j < 3; ++j) dma16(P.ssm_k, (u32)ch * (SEQ * 2) + 1024 * j + 16 * lane, smb + kimg + KPADB + 1024 * j);
    vmcnt<0>();
    if (lane == 0) {
      LDSP u16* k0 = (LDSP u16*)(sm + kimg + KPADB);
      *k0 = (u16)pkbf(bf2f(*k0) + dsk, 0.f);
    }
    STAMP(2);
    wavefence();
    conv_stream<1>(sm, kimg, ximg, sm + yimg + 2 * lane, sm + FLAGS + 16 + 4 * w, lane STV_PASS);
    ROLE_END(4);
  }

  {
    const int ch0 = gi * 4;
    const int cw = w & 3, ps = w >> 2;
    const int ch = ch0 + cw;
    const size_t o = (size_t)ch * NST + 64 * ps + lane;
    const u16 i_ar = P.A_re[o], i_ai = P.A_im[o], i_br = P.B_re[o], i_bi = P.B_im[o], i_cr = P.C_re[o], i_ci = P.C_im[o];
    const float ldt = ld_bf_uniform(P.log_dt, ch);
    const float dsk = ld_bf_uniform(P.Dskip, ch);
    wgbarrier();
    const float dt = rbf(expf(ldt));
    const float zr = dt * bf2f(i_ar), zi = dt * bf2f(i_ai);
    const cf dB = {dt * bf2f(i_br), dt * bf2f(i_bi)};
    const cf C = {bf2f(i_cr), bf2f(i_ci)};
    const cf W = cmul(C, dB);
    float mx = fmaxf(fabsf(W.r), fabsf(W.i));
    STAMP(1);
    {
      int m = __float_as_int(mx);
      m = max(m, __builtin_amdgcn_update_dpp(0, m, 0x111, 0xf, 0xf, false));
      m = max(m, __builtin_amdgcn_update_dpp(0, m, 0x112, 0xf, 0xf, false));
      m = max(m, __builtin_amdgcn_update_dpp(0, m, 0x114, 0xf, 0xf, false));
      m = max(m, __builtin_amdgcn_update_dpp(0, m, 0x118, 0xf, 0xf, false));
      int a0 = __builtin_amdgcn_readlane(m, 15), a1 = __builtin_amdgcn_readlane(m, 31);
      int a2 = __builtin_amdgcn_readlane(m, 47), a3 = __builtin_amdgcn_readlane(m, 63);
      mx = __int_as_float(max(max(a0, a1), max(a2, a3)));
    }
    int ex = (int)((__float_as_uint(mx) >> 23) & 0xff);
    ex = min(max(ex, 30), 240);
    const float sY = __uint_as_float((u32)(267 - ex) << 23);
    const float so = __uint_as_float((u32)(ex - 27) << 23);
    const float SX = 16384.f;

    f32x4 ak[3];
    const u32 stg = STG + w * STS;
    const u32 rb = stg + li * 272 + g * 16;
    const u32 m0v = smb + w * STS;
#define STGW(row, val_r, val_i) \
    asm volatile("ds_write_addtid_b32 %0 offset:%1" :: "v"(pkh((val_r), (val_i))), "i"(STG + (row) * 272) : "memory")
    {
      const cf E1 = cexp_pow2(zr, zi, 1.f);
      const cf E8c = cexp_pow2(zr, -zi, 8.f);
      const cf E128 = cexp_pow2(zr, zi, 128.f);
      const cf E1024 = cexp_pow2(zr, zi, 1024.f);
      const cf E2 = cmul(E1, E1);
      const cf E4 = cmul(E2, E2);
      u32 keep;
      asm volatile("s_mov_b32 %0, m0\n\ts_mov_b32 m0, %1\n\ts_nop 0" : "=&s"(keep) : "s"(m0v) : "memory");
      cf y = {W.r * sY, -(W.i * sY)};
      cf P1 = {E128.r * SX, E128.i * SX};
      cf v0 = {SX, 0.f}, v1 = P1;
#pragma unroll
      for (int b = 0; b < 8; ++b) {
        STGW(2 * b, y.r, y.i); y = cmul(y, E8c);
        STGW(16 + b, v0.r, v0.i); if (b < 7) v0 = cmul(v0, E1);
        STGW(2 * b + 1, y.r, y.i); if (b < 7) y = cmul(y, E8c);
        STGW(24 + b, v1.r, v1.i); if (b < 7) v1 = cmul(v1, E1);
      }
      f16x8 yf[4];
      const f32x4 zero = {0.f, 0.f, 0.f, 0.f};
#pragma unroll
      for (int s = 0; s < 4; ++s) {
        yf[s] = *(const LDSP f16x8*)(sm + rb + 64 * s);
        f16x8 xf = *(const LDSP f16x8*)(sm + rb + 16 * 272 + 64 * s);
        ak[0] = __builtin_amdgcn_mfma_f32_16x16x32_f16(xf, yf[s], s ? ak[0] : zero, 0, 0, 0);
      }
      asm volatile("" ::: "memory");
      cf Pa = cmul(P1, E128);
      cf Pb = {E1024.r * SX, E1024.i * SX};
#pragma unroll
      for (int m = 0; m < 2; ++m) {
        cf v = Pa;
#pragma unroll
        for (int r = 0; r < 4; ++r) {
          STGW(16 + 4 * m + r, v.r, v.i); if (r < 3) v = cmul(v, E2);
          if (r & 1) { STGW(4 * (2 * m + (r >> 1)) + 2, Pb.r, Pb.i); Pb = cmul(Pb, E128); }
        }
        Pa = cmul(Pa, E128);
      }
#pragma unroll
      for (int m = 0; m < 4; ++m) {
        STGW(4 * m, Pa.r, Pa.i);
        cf v = cmul(Pa, E4);
        STGW(4 * m + 3, Pb.r, Pb.i); Pb = cmul(Pb, E128);
        STGW(4 * m + 1, v.r, v.i);
        if (m < 3) Pa = cmul(Pa, E128);
      }
      STGW(24, Pb.r, Pb.i);
      asm volatile("s_mov_b32 m0, %0" :: "s"(keep) : "memory");
#pragma unroll
      for (int s = 0; s < 4; ++s) {
        f16x8 x1 = *(const LDSP f16x8*)(sm + rb + 64 * s);
        f16x8 x2 = *(const LDSP f16x8*)(sm + rb + 16 * 272 + 64 * s);
        ak[1] = __builtin_amdgcn_mfma_f32_16x16x32_f16(x1, yf[s], s ? ak[1] : zero, 0, 0, 0);
        ak[2] = __builtin_amdgcn_mfma_f32_16x16x32_f16(x2, yf[s], s ? ak[2] : zero, 0, 0, 0);
      }
    }
#undef STGW
#pragma unroll
    for (int i = 0; i < 3; ++i) ak[i] *= so;
    STAMP(2);
    asm volatile("" ::: "memory");
    const u32 kimg = B_KIMG + cw * KS;
    if (ps) {
#pragma unroll
      for (int i = 0; i < 3; ++i) *(LDSP f32x4*)(sm + kimg + 1024 * i + 16 * lane) = ak[i];
      set_word(sm + FLAGS + 48 + 4 * cw, 1);
      const u32 hx = stg, hk = stg + XS, hy = H_YIMG + cw * HYS;
      asm volatile("s_waitcnt lgkmcnt(0)" ::: "memory");
#pragma unroll
      for (int j = 0; j < 4; ++j) dma16(P.ssm_k, (u32)ch * (SEQ * 2) + 1024 * j + 16 * lane, smb + hk + KPADB + 1024 * j);
      if (lane < 10) *(LDSP u32x4*)(sm + hx + 16 * lane) = u32x4{0, 0, 0, 0};
      if (lane < 6) *(LDSP u32x4*)(sm + hk + 16 * lane) = u32x4{0, 0, 0, 0};
      const float dh = ld_bf_uniform(P.ssm_D, ch);
      if (w == 4) poll_exchange(gi >> 2, 0, 4); else wait_words(sm + FLAG3 + 4, lane, 1, 4);
      __builtin_amdgcn_fence(__ATOMIC_ACQUIRE, "wavefront");
      STAMP(5);
      {
        u32x4 d[6];
#pragma unroll
        for (int j = 0; j < 4; ++j) d[j] = __builtin_amdgcn_raw_buffer_load_b128(rx, (u32)ch * 4096 + 1024 * j + 16 * lane, 0, 16);
#pragma unroll
        for (int j = 0; j < 2; ++j)
          d[4 + j] = __builtin_amdgcn_raw_buffer_load_b128(rx, (u32)ch * 4096 + 2816 + 256 * j + 16 * lane + (u32)(WS_EXQ - WS_EXX), 0, 16);
        vmcnt<0>();
        if (lane == 0) {
          LDSP u16* k0 = (LDSP u16*)(sm + hk + KPADB);
          *k0 = (u16)pkbf(bf2f(*k0) + dh, 0.f);
        }
#pragma unroll
        for (int j = 0; j < 4; ++j) *(LDSP u32x4*)(sm + hx + XPADB + 1024 * j + 16 * lane) = d[j];
#pragma unroll
        for (int j = 0; j < 2; ++j) *(LDSP u32x4*)(sm + hy + 256 * j + 16 * lane) = d[4 + j];
      }
      wavefence();
      conv_stream<2>(sm, hk, hx, sm + hy + 2 * lane - 11 * 256, sm + FLAGS + 16 + 4 * w, lane STV_PASS);
      ROLE_END(5);
    }
    wait_words(sm + FLAGS + 48 + 4 * cw, lane, 1, 1);
    {
#pragma unroll
      for (int i = 0; i < 3; ++i) ak[i] += *(const LDSP f32x4*)(sm + kimg + 1024 * i + 16 * lane);
      wavefence();
      auto nxt = [&](float x, float e, bool lastl) {
        float r = __int_as_float(__builtin_amdgcn_update_dpp(0, __float_as_int(x), 0x130, 0xf, 0xf, true));
        return lastl ? e : r;
      };
      auto lane_of = [&](float x, int l) { return __int_as_float(__builtin_amdgcn_readlane(__float_as_int(x), l)); };
      const lptr kd = sm + kimg + KPADB;
      if (lane < 6) *(LDSP u32x4*)(sm + kimg + 16 * lane) = u32x4{0, 0, 0, 0};
      {
        const float a0 = ak[0][0] + (lane == 0 ? dsk : 0.f), a1 = ak[0][1], a2 = ak[0][2], a3 = ak[0][3];
        *(LDSP u32x2*)(kd + 2 * (128 * (g >> 1) + 8 * li + 4 * (g & 1))) = u32x2{pkbf(a0, a1), pkbf(a2, a3)};
      }
      const float s20 = ak[1][0], s21 = ak[1][1], s3a = ak[1][2], s3b = ak[1][3];
      const float c0 = ak[2][0], c1 = ak[2][1], c2 = ak[2][2], c3 = ak[2][3];
      const bool l63 = lane == 63;
      {
        const float n = nxt(c0, lane_of(s20, 0), lane == 31);
        if (lane < 32) *(LDSP u32x4*)(kd + 16 * (32 + lane)) = u32x4{pkbf(c0, c0), pkbf(c1, c2), pkbf(c2, c2), pkbf(c3, n)};
      }
      {
        const float n = nxt(s20, lane_of(s3a, 0), l63);
        *(LDSP u32x4*)(kd + 16 * (64 + lane)) = u32x4{pkbf(s20, s20), pkbf(s20, s21), pkbf(s21, s21), pkbf(n, n)};
      }
      {
        const float na = nxt(s3a, lane_of(s3b, 0), l63), nb = nxt(s3b, lane_of(c0, 32), l63);
        const u32 a = pkbf(s3a, s3a), b = pkbf(s3b, s3b);
        *(LDSP u32x4*)(kd + 16 * (128 + lane)) = u32x4{a, a, pkbf((lane & 1) ? na : s3a, na), pkbf(na, na)};
        *(LDSP u32x4*)(kd + 16 * (192 + lane)) = u32x4{b, b, pkbf((lane & 1) ? nb : s3b, nb), pkbf(nb, nb)};
      }
    }
    wavefence();
    STAMP(3);
    conv_stream<0>(sm, kimg, XIMG + cw * XS, sm + B_YIMG + cw * YS + 2 * lane, sm + FLAGS + 16 + 4 * w, lane STV_PASS);
    ROLE_END(0);
  }
}

extern "C" void kernel_launch(void* const* d_in, const int* in_sizes, int n_in,
                              void* d_out, int out_size, void* d_ws, size_t ws_size,
                              hipStream_t stream) {
  Params P;
  P.q = (const u16*)d_in[0];
  P.k = (const u16*)d_in[1];
  P.v = (const u16*)d_in[2];
  P.shift_k = (const u16*)d_in[3];
  P.ssm_k = (const u16*)d_in[4];
  P.ssm_D = (const u16*)d_in[5];
  P.u = (const u16*)d_in[6];
  P.A_re = (const u16*)d_in[7];
  P.A_im = (const u16*)d_in[8];
  P.B_re = (const u16*)d_in[9];
  P.B_im = (const u16*)d_in[10];
  P.C_re = (const u16*)d_in[11];
  P.C_im = (const u16*)d_in[12];
  P.log_dt = (const u16*)d_in[13];
  P.Dskip = (const u16*)d_in[14];
  P.out = (u16*)d_out;
  P.ws = (unsigned long long*)d_ws;
  P.ex = (char*)d_ws + (16 << 20);
  (void)hipFuncSetAttribute((const void*)fused_kernel, hipFuncAttributeMaxDynamicSharedMemorySize, LDS_BYTES);
  fused_kernel<<<256, 768, LDS_BYTES, stream>>>(P);
}
```

```cpp
#include <hip/hip_runtime.h>
#include <hip/hip_bf16.h>
#include <stdint.h>

typedef __attribute__((ext_vector_type(4))) short s16x4;
typedef __attribute__((ext_vector_type(8))) _Float16 f16x8;
typedef __attribute__((ext_vector_type(8))) __bf16 bf16x8;
typedef __attribute__((ext_vector_type(2))) __bf16 bf16x2;
typedef __attribute__((ext_vector_type(2))) _Float16 f16x2;
typedef __attribute__((ext_vector_type(2))) float f32x2;
typedef __attribute__((ext_vector_type(4))) float f32x4;
typedef __attribute__((ext_vector_type(2))) unsigned int u32x2;
typedef __attribute__((ext_vector_type(4))) unsigned int u32x4;
typedef unsigned int u32;
typedef unsigned short u16;
#define LDSP __attribute__((address_space(3)))
#define DEV __device__ __forceinline__

constexpr int SEQ = 2048;
constexpr int DCH = 640;
constexpr int NST = 128;
constexpr int ROWB = DCH * 2;

constexpr int XS = 4272;
constexpr int XPADB = 160;
constexpr int YS = 4160;
constexpr int KS = 4224;
constexpr int KPADB = 96;
constexpr int FLAGS = 0;
constexpr int ZROW = 64;
constexpr int FLAG2 = 192;
constexpr int FLAG3 = 256;
constexpr int PFD = 320;
constexpr int SHK = 1024;
constexpr int XIMG = 2048;
constexpr int PUNIT = 8704 + 8192 + 8192;
constexpr int YIMG = 36224;
constexpr int KIMG = 69504;
constexpr int A_PRAW = 103296;
constexpr int A_PSTG = YIMG;
constexpr int STS = 8704;
constexpr int STG = 19200;
constexpr int H_XIMG = STG + 4 * STS;
constexpr int H_KIMG = H_XIMG + 4 * XS;
constexpr int B_KIMG = 88832;
constexpr int B_YIMG = 105728;
constexpr int HYS = 1344;
constexpr int H_YIMG = 122368;
constexpr int RAWU = 127744;
constexpr int B_PRAW = RAWU + 8192;
constexpr int B_PSTG = XIMG + XPADB;
constexpr int LDS_BYTES = 161024;

constexpr size_t WS_EPOCH = 0;
constexpr size_t WS_FLAG = 4096;
constexpr size_t WS_EXX = 1 << 20;
constexpr size_t WS_EXQ = 4 << 20;

DEV float bf2f(u16 v) { return __uint_as_float(((u32)v) << 16); }
DEV float bflo(u32 v) { return __uint_as_float(v << 16); }
DEV float bfhi(u32 v) { return __uint_as_float(v & 0xffff0000u); }
DEV u32 pkbf(float a, float b) {
  f32x2 t = {a, b};
  bf16x2 r = __builtin_convertvector(t, bf16x2);
  return __builtin_bit_cast(u32, r);
}
DEV u32 pkh(float a, float b) {
  f32x2 t = {a, b};
  f16x2 r = __builtin_convertvector(t, f16x2);
  return __builtin_bit_cast(u32, r);
}
DEV float rbf(float a) { return bflo(pkbf(a, a)); }
DEV float dot2(u32 a, u32 b, float c) {
  return __builtin_amdgcn_fdot2_f32_bf16(__builtin_bit_cast(bf16x2, a), __builtin_bit_cast(bf16x2, b), c, false);
}
DEV float vfma(float a, float b, float c) { float d; asm("v_fma_f32 %0, %1, %2, %3" : "=v"(d) : "v"(a), "v"(b), "v"(c)); return d; }
DEV float vmul(float a, float b) { float d; asm("v_mul_f32 %0, %1, %2" : "=v"(d) : "v"(a), "v"(b)); return d; }
typedef __attribute__((address_space(1))) unsigned long long gu64;
#define RLX_AGENT __ATOMIC_RELAXED, __HIP_MEMORY_SCOPE_AGENT
typedef LDSP char* lptr;
DEV u32x2 trread(lptr p) {
  s16x4 r = __builtin_amdgcn_ds_read_tr16_b64_v4i16((LDSP s16x4*)p);
  return __builtin_bit_cast(u32x2, r);
}
DEV bf16x8 frag(u32x2 a, u32x2 b) {
  u32x4 f = {a.x, a.y, b.x, b.y};
  return __builtin_bit_cast(bf16x8, f);
}
DEV float ror1(float v) {
  int r = __builtin_amdgcn_update_dpp(0, __float_as_int(v), 0x13C, 0xf, 0xf, false);
  return __int_as_float(r);
}
DEV void wavefence() { __builtin_amdgcn_fence(__ATOMIC_ACQ_REL, "wavefront"); __builtin_amdgcn_wave_barrier(); }
DEV void wgbarrier() {
  asm volatile("s_waitcnt lgkmcnt(0)" ::: "memory");
  __builtin_amdgcn_s_barrier();
  asm volatile("" ::: "memory");
}
DEV void dma16(const void* base, u32 voff, u32 ldsaddr) {
  u32 keep;
  ldsaddr = __builtin_amdgcn_readfirstlane(ldsaddr);
  const unsigned long long b64 = (unsigned long long)(uintptr_t)base;
  const unsigned long long sb = ((unsigned long long)(u32)__builtin_amdgcn_readfirstlane((u32)(b64 >> 32)) << 32) |
                                (u32)__builtin_amdgcn_readfirstlane((u32)b64);
  asm volatile("s_mov_b32 %0, m0\n\ts_mov_b32 m0, %3\n\ts_nop 2\n\tglobal_load_lds_dwordx4 %1, %2\n\ts_mov_b32 m0, %0"
               : "=&s"(keep) : "v"(voff), "s"(sb), "s"(ldsaddr) : "memory");
}
DEV void touch64(const void* p, u32 ldsaddr) {
  u32 keep;
  asm volatile("s_mov_b32 %0, m0\n\ts_mov_b32 m0, %2\n\ts_nop 0\n\tglobal_load_lds_dword %1, off\n\ts_mov_b32 m0, %0"
               : "=&s"(keep) : "v"(p), "s"(ldsaddr) : "memory");
}
template <int N> DEV void vmcnt() { asm volatile("s_waitcnt vmcnt(%0)" :: "i"(N) : "memory"); }
#define SCHED0() __builtin_amdgcn_sched_barrier(0)

template <int V> struct IC { static constexpr int value = V; };
template <int I, int N, class F> DEV void static_for(F&& f) {
  if constexpr (I < N) { f(IC<I>{}); static_for<I + 1, N>(f); }
}

struct cf { float r, i; };
DEV cf cmul(cf a, cf b) {
  cf o;
  o.r = fmaf(a.r, b.r, -(a.i * b.i));
  o.i = fmaf(a.r, b.i, a.i * b.r);
  return o;
}
DEV cf cexp_pow2(float zr, float zi, float x) {
  float mag = __builtin_amdgcn_exp2f((zr * x) * 1.44269504088896340736f);
  float th = zi * x;
  const float C_HI = 0.15915494f;
  const float C_LO = 6.4206382e-09f;
  float f = fmaf(th, C_LO, fmaf(th, C_HI, -rintf(th * C_HI)));
  cf o;
  o.r = mag * __builtin_amdgcn_cosf(f);
  o.i = mag * __builtin_amdgcn_sinf(f);
  return o;
}

#define STAMP(n)
#define CSTAMP(n)
#define STAMP_OUT()

struct Params {
  const u16 *k, *v, *q, *shift_k; char* ex; const u16 *u, *ssm_k; u16* out;
  const u16 *A_re, *A_im, *B_re, *B_im, *C_re, *C_im, *log_dt, *Dskip;
  const u16* ssm_D;
  unsigned long long* ws;
};

DEV float ld_bf_uniform(const u16* p, int idx) {
  u32 d = *(const __attribute__((address_space(4))) u32*)(uintptr_t)(p + (idx & ~1));
  return (idx & 1) ? bfhi(d) : bflo(d);
}

DEV u32 prefetch_off(int half, int slice, int xcd, int lane) {
  const u32 row = (u32)(half * 4 + (lane >> 4)) * 256u + (u32)slice * 8u + ((lane >> 1) & 7);
  return row * ROWB + (u32)(((160 * xcd) >> 7) + (lane & 1)) * 128u;
}
DEV void wait_words(lptr p, int lane, int n, u32 v) {
#pragma unroll 1
  for (int spin = 0; spin < (1 << 16); ++spin) {
    u32 x = *(volatile LDSP u32*)(p + 4 * (lane & (n - 1)));
    if (__all(x >= v)) break;
    __builtin_amdgcn_s_sleep(1);
  }
  asm volatile("" ::: "memory");
}
#define PFS 3
#define PFP 3
#define PFU 8
#define PFH 4
#define FSP 16
#define PSL 6
DEV void delay(int n) {
  for (int i = 0; i < n; ++i) __builtin_amdgcn_s_sleep(PFS);
}

DEV void set_word(lptr p, u32 v) {
  asm volatile("" ::: "memory");
  *(volatile LDSP u32*)p = v;
  asm volatile("" ::: "memory");
}

struct Sched {
  int n;
  int p[272], par[272], d[272], ld[272];
  bool first[272];
  int base[16];
  int cm[16], om[16];
  int ldm[40], ldu[40], ldp[40];
  int ku[16];
  int nld, nst;
};
constexpr void add_unit(Sched& S, int p, int d, int ld, int pm, bool first) {
  for (int par = 0; par < 2; ++par)
    if ((pm >> par) & 1) {
      S.p[S.n] = p; S.par[S.n] = par; S.d[S.n] = d; S.ld[S.n] = ld; S.first[S.n] = first;
      if (S.ldu[ld] < 0) S.ldu[ld] = S.n;
      S.ldp[ld] |= 1 << par;
      if (S.ku[d] < 0) S.ku[d] = S.n;
      ++S.n;
    }
}
constexpr Sched make_sched(int mode) {
  Sched S{};
  const int last = (mode == 1) ? 11 : 15;
  S.nst = last + 1;
  int nl = 16;
  for (int l = 0; l < 40; ++l) { S.ldu[l] = -1; S.ldm[l] = l; }
  for (int d = 0; d < 16; ++d) S.ku[d] = -1;
  for (int s = 0; s <= last; ++s) {
    S.base[s] = S.n;
    S.cm[s] = (mode == 2 && s < 11) ? 0 : ((mode == 1 && s == 11) ? 1 : 3);
    S.om[s] = (mode == 2 && s == 11) ? 2 : S.cm[s];
    if (mode == 2) {
      for (int p = (s > 11 ? s : 11); p <= 15; ++p) add_unit(S, p, p - s, s, 3, s == 0);
      continue;
    }
    for (int k = 0; k < 8 && s + k <= last; ++k)
      add_unit(S, s + k, k, s, (mode == 1 && s + k == 11) ? 1 : 3, (s == 0) || (k == 7));
    int lo = -1, hi = -1, cnt = 0, half = 0;
    if (s == 6) { lo = 8; hi = 8; cnt = 1; half = 2; }
    else if (s >= 7 && s <= last - 3) { lo = (s & 1) ? s + 2 : s + 1; hi = lo + 1; cnt = hi - 7; half = (s & 1) ? 0 : 1; }
    else if (s == last - 2 || s == last - 1) { lo = last; hi = last; cnt = last - 7; half = (s == last - 2) ? 0 : 1; }
    if (cnt) {
      const int split = (cnt + 1) / 2;
      const int j0 = (half == 1) ? split : 0, j1 = (half == 0) ? split : cnt;
      for (int j = j0; j < j1; ++j) {
        const int mm = cnt - 1 - j;
        S.ldm[nl] = mm;
        for (int t = hi; t >= lo; --t)
          if (t - mm >= 8) add_unit(S, t, t - mm, nl, (mode == 1 && t == 11) ? 1 : 3, false);
        ++nl;
      }
    }
  }
  S.nld = nl;
  return S;
}
constexpr int NGAP = 272 + 40, CAP = 2, KPRE = 16;
enum { A_NONE = 0, A_X, A_K, A_Q, A_PXR, A_PXC, A_EP };
struct Acts { int n[NGAP]; int a[NGAP][4]; int end; };
constexpr int place_back(Acts& T, int latest, int code, int cap) {
  int g = latest;
  while (g > 0 && T.n[g] >= cap) --g;
  T.a[g][T.n[g]++] = code;
  return g;
}
constexpr int place_fwd(Acts& T, int earliest, int code, int cap, int nm) {
  int g = earliest;
  while (T.n[g] >= (g < nm ? cap : 4)) ++g;
  T.a[g][T.n[g]++] = code;
  if (g + 1 > T.end) T.end = g + 1;
  return g;
}
constexpr Acts make_acts(const Sched& S, int mode) {
  Acts T{};
  T.end = S.n;
  const int dense = (mode == 0) ? 200 : (mode == 1 ? 90 : 100);
  for (int l = S.nst - 1; l >= 1; --l) {
    int g = S.ldu[l] - 5;
    const int cap = S.ldu[l] >= dense && mode ? 2 : 1;
    for (int i = 3; i >= 0; --i) if ((S.ldp[l] >> (i >> 1)) & 1) g = place_back(T, g - 1, A_X | (l << 4) | (i << 12), cap);
    for (int o = 1; o >= 0; --o) if (mode && ((S.om[l] >> o) & 1)) g = place_back(T, g - 1, A_Q | (l << 4) | (o << 12), cap);
    if ((mode == 0 && !(l & 1)) || (mode == 1 && !(l & 3))) {
      g = place_back(T, g - 1, A_PXC | (l << 4), 1);
      place_back(T, g - 6, A_PXR, 1);
    }
  }
  for (int l = S.nld - 1; l >= 16; --l) {
    int g = S.ldu[l] - 5;
    for (int i = 3; i >= 0; --i) if ((S.ldp[l] >> (i >> 1)) & 1) g = place_back(T, g - 1, A_X | (l << 4) | (i << 12), S.ldu[l] >= dense ? 2 : 1);
  }
  for (int d = 0; d < 16; ++d) {
    if (S.ku[d] < KPRE) continue;
    int g = S.ku[d] - 5;
    g = place_back(T, g - 1, A_K | (d << 4) | (1 << 12), 2);
    place_back(T, g - 1, A_K | (d << 4), 2);
  }
  int prev[18] = {};
  for (int s = 0; s < S.nst; ++s) {
    if (!S.cm[s]) continue;
    int g = S.base[s] + 4;
    const int p16 = prev[16];
    for (int e = 0; e < ((s & 1) ? 18 : 17); ++e) {
      if (e < 14 && !(((e >= 8 ? S.om[s] : S.cm[s]) >> (e & 1)) & 1)) continue;
      if ((e == 15 && !(S.om[s] & 1)) || (e == 16 && !(S.om[s] & 2))) continue;
      if (mode == 0 && (e == 12 || e == 13)) continue;
      int lo = g + (((e & 1) && e < 14) ? 0 : 1);
      if (lo <= prev[e | 1]) lo = prev[e | 1] + 1;
      if (e == 17 && lo <= p16) lo = p16 + 1;
      g = place_fwd(T, lo, A_EP | (s << 4) | (e << 12), S.base[s] >= dense ? 3 : CAP, S.n);
      prev[e] = g;
    }
  }
  return T;
}
template <int MODE> struct Tab {
  static constexpr Sched S = make_sched(MODE);
  static constexpr Acts A = make_acts(S, MODE);
};
static_assert(Tab<0>::S.n == 272 && Tab<1>::S.n == 144 && Tab<2>::S.n == 140, "schedule");

#define MFMA(a, b, c) __builtin_amdgcn_mfma_f32_16x16x32_bf16((a), (b), (c), 0, 0, 0)
#define STV_ARG
#define STV_PASS

template <int MODE>
DEV void conv_stream(lptr sm, u32 kimg, u32 ximg, lptr ywp, lptr pyp, int lane STV_ARG) {
  using T = Tab<MODE>;
  const int g = lane >> 4, lq = (lane & 15) >> 2, lp = lane & 3;
  const lptr kb = sm + kimg + 8 * (4 * lp + 8 * g + lq);
  const lptr xb = sm + ximg + 224 + 8 * (lp - 8 * g - lq);
  const bool l63 = (lane == 63);
  const lptr pxp = sm + FLAGS + 4 * (lane & 3);
  const u32 qa = (u32)(uintptr_t)ywp;
  u32x2 kr[16][2], xr[40][4];
  f32x4 ac[16][2];
  const f32x4 zero = {0.f, 0.f, 0.f, 0.f};
  float ts[16][2], u2[16][2], u1[16][2], yy[16][2];
  u32 pk[16];
  constexpr int NQ = MODE == 0 ? 3 : 5;
  u32 qv[NQ][2], px = 0;
#pragma unroll
  for (int i = 0; i < NQ; ++i) { qv[i][0] = 0; qv[i][1] = 0; }
#define RDQ(s_, i_) asm volatile("ds_read_u16_d16_hi %0, %1 offset:%2" : "+v"(qv[(s_) % NQ][i_]) : "v"(qa), "i"((s_) * 256 + 128 * (i_)) : "memory")
#define RDXP(l_, i_) xr[l_][i_] = trread(xb + 256 * T::S.ldm[l_] + ((i_) >> 1) * 128 + (((i_) & 1) ? 0 : 32))
  static_for<0, 16>([&](auto D_) {
    constexpr int d = decltype(D_)::value;
    if constexpr (T::S.ku[d] >= 0 && T::S.ku[d] < KPRE) { kr[d][0] = trread(kb + 256 * d); kr[d][1] = trread(kb + 256 * d + 32); }
  });
  if constexpr (MODE == 0) wait_words(sm + FLAGS, lane, 4, 1);
  STAMP(4);
  if constexpr (MODE == 1) { RDQ(0, 0); RDQ(0, 1); }
  RDXP(0, 0); RDXP(0, 1); RDXP(0, 2); RDXP(0, 3);
  SCHED0();
  static_for<0, T::A.end>([&](auto G_) {
    constexpr int G = decltype(G_)::value;
    if constexpr (G < T::S.n) {
      constexpr int p = T::S.p[G], par = T::S.par[G], d = T::S.d[G], l = T::S.ld[G];
      ac[p][par] = MFMA(frag(kr[d][0], kr[d][1]), frag(xr[l][2 * par], xr[l][2 * par + 1]), T::S.first[G] ? zero : ac[p][par]);
      SCHED0();
    }
    static_for<0, T::A.n[G]>([&](auto I_) {
      constexpr int code = T::A.a[G][decltype(I_)::value];
      constexpr int kind = code & 15, a0 = (code >> 4) & 255, a1 = code >> 12;
      if constexpr (kind == A_X) RDXP(a0, a1);
      if constexpr (kind == A_K) kr[a0][a1] = trread(kb + 256 * a0 + 32 * a1);
      if constexpr (kind == A_Q) RDQ(a0, a1);
      if constexpr (kind == A_PXR) px = *(volatile LDSP u32*)pxp;
      if constexpr (kind == A_PXC) {
        if (__builtin_expect(!__all(px >= (u32)(a0 / 2 + 1)), 0)) wait_words(sm + FLAGS, lane, 4, (u32)(a0 / 2 + 1));
        asm volatile("" ::: "memory");
      }
      if constexpr (kind == A_EP) {
        constexpr int s = a0, e = a1 >> 1, o = a1 & 1;
        constexpr bool hp = s > 0 && T::S.cm[s > 0 ? s - 1 : 0] != 0;
        if constexpr (a1 < 14) {
          if constexpr (e == 0) {
            float cur = ac[s][o][3], prev = 0.f;
            if constexpr (o) prev = ac[s][0][3]; else if constexpr (hp) prev = ac[s - 1][1][3];
            ts[s][o] = l63 ? prev : cur;
          }
          if constexpr (e == 1) { float a = ac[s][o][2]; u2[s][o] = a + ror1(ts[s][o]); }
          if constexpr (e == 2) {
            float prev = 0.f;
            if constexpr (o) prev = u2[s][0]; else if constexpr (hp) prev = u2[s - 1][1];
            ts[s][o] = l63 ? prev : u2[s][o];
          }
          if constexpr (e == 3) { float a = ac[s][o][1]; u1[s][o] = a + ror1(ts[s][o]); }
          if constexpr (e == 4) {
            float prev = 0.f;
            if constexpr (o) prev = u1[s][0]; else if constexpr (hp) prev = u1[s - 1][1];
            ts[s][o] = l63 ? prev : u1[s][o];
          }
          if constexpr (e == 5) { float a = ac[s][o][0]; yy[s][o] = a + ror1(ts[s][o]); asm volatile("" : "+v"(yy[s][o])); }
          if constexpr (e == 6) {
            asm volatile("" : "+v"(qv[s % NQ][o]));
            yy[s][o] *= __uint_as_float(qv[s % NQ][o]);
            asm volatile("" : "+v"(yy[s][o]));
          }
        }
        if constexpr (a1 == 14) pk[s] = pkbf(yy[s][(T::S.om[s] & 1) ? 0 : 1], yy[s][(T::S.om[s] & 2) ? 1 : 0]);
        if constexpr (a1 == 15) *(LDSP u16*)(ywp + s * 256) = (u16)pk[s];
        if constexpr (a1 == 16) *(LDSP u16*)(ywp + s * 256 + 128) = (u16)(pk[s] >> 16);
        if constexpr (a1 == 17) set_word(pyp, (u32)(s / 2 + 1));
      }
      SCHED0();
    });
  });
  STAMP(8);
#undef RDQ
#undef RDXP
}

#define ROLE_END(n) do { STAMP_OUT(); asm volatile(".Lfk_end" #n ":"); return; } while (0)

__global__ __launch_bounds__(768, 3) void fused_kernel(Params P) {
  extern __shared__ __attribute__((aligned(16))) char smem[];
  const lptr sm = (lptr)smem;
  const u32 smb = (u32)(uintptr_t)sm;
  const int tid = threadIdx.x;
  const int w = __builtin_amdgcn_readfirstlane(tid >> 6);
  const int lane = tid & 63;
  const int wg = blockIdx.x;
  const int xcd = wg & 7, idx = wg >> 3;
  const bool partA = idx < 10;
  const int g = lane >> 4, li = lane & 15, lq = li >> 2, lp = li & 3;
  STAMP(0);
  if (w == 11) {
    unsigned long long pc;
    u32 len;
    asm volatile("s_getpc_b64 %0\n.Lfk_code_begin:\n\ts_mov_b32 %1, .Lfk_end0-.Lfk_code_begin"
                 "\n\ts_max_i32 %1, %1, .Lfk_end1-.Lfk_code_begin\n\ts_max_i32 %1, %1, .Lfk_end2-.Lfk_code_begin"
                 "\n\ts_max_i32 %1, %1, .Lfk_end3-.Lfk_code_begin\n\ts_max_i32 %1, %1, .Lfk_end4-.Lfk_code_begin"
                 "\n\ts_max_i32 %1, %1, .Lfk_end5-.Lfk_code_begin" : "=s"(pc), "=&s"(len) :: "scc");
    const u32 off = min((u32)(lane + 64 * (idx & 3)) * 128u, len & ~3u);
    touch64((const char*)pc + off, smb + PFD);
  }
#define gflag ((gu64*)(uintptr_t)(P.ex + WS_FLAG))
#define gepoch ((gu64*)(uintptr_t)(P.ex + WS_EPOCH))
#define rx __builtin_amdgcn_make_buffer_rsrc(P.ex + WS_EXX, 0, 6u << 20, 0x00020000)
  if (idx >= 30) {
    if (w < 2) { delay(PFP); touch64((const char*)P.u + prefetch_off(w, idx, xcd, lane), smb + PFD); }
    if (w == 8 && wg == 240) {
      const unsigned long long tag = __hip_atomic_load(gepoch, RLX_AGENT) + 1;
      delay(40);
#pragma unroll 1
      for (int spin = 0; spin < (1 << 16); ++spin) {
        bool ok = true;
#pragma unroll
        for (int j = 0; j < 5; ++j) { const int f = lane + 64 * j; ok &= (__hip_atomic_load(gflag + FSP * ((f >> 5) * 128 + (f & 31)), RLX_AGENT) == tag); }
        if (__all(ok)) break;
        __builtin_amdgcn_s_sleep(64);
      }
      if (lane == 0) __hip_atomic_store(gepoch, tag, RLX_AGENT);
    }
    vmcnt<0>();
    asm volatile(".Lfk_end1:");
    return;
  }
  if (tid < 80) *(LDSP u32*)(sm + FLAGS + 4 * tid) = 0;

  const int gi = partA ? xcd * 10 + idx : xcd * 20 + (idx - 10);

  auto poll_exchange = [&](int col, int first, int nst, int word) {
    wait_words(sm + FLAG3, lane, 1, 1);
    const unsigned long long tag = *(volatile LDSP unsigned long long*)(sm + FLAG3 + 8);
    bool ok = lane >= 8 * nst || lane < 8 * first;
#pragma unroll 1
    for (int spin = 0; spin < (1 << 16); ++spin) {
      if (!ok) ok = (__hip_atomic_load(gflag + FSP * (col * 32 + lane), RLX_AGENT) == tag);
      const unsigned long long miss = __ballot(!ok);
      const int done = miss ? (__builtin_ctzll(miss) >> 3) : nst;
      if (done > first) set_word(sm + FLAG3 + word, (u32)done);
      if (!miss) break;
      __builtin_amdgcn_s_sleep(PSL);
    }
  };

  if (w >= (partA ? 4 : 8)) {
    const int h = w & 3, pg = (w < 8) ? 1 : 0;
    const int un = partA ? wg * 2 + pg : wg - 80;
    const int pcol = un % 10, pchunk = un / 10 + (partA ? 16 : 0);
    const u32 praw = partA ? A_PRAW + pg * PUNIT : B_PRAW;
    const u32 shk = SHK + pg * 512;
    const lptr fw = sm + FLAG2 + pg * 32;
    asm volatile("" :: "s"(P.k), "s"(P.v), "s"(P.q), "s"(P.shift_k), "s"(P.ex), "s"(P.u), "s"(P.ssm_k), "s"(P.out));
    __builtin_amdgcn_s_setprio(3);
    unsigned long long tag = 0;
    {
      const u32 po = (u32)(64 * pchunk + 16 * h + (lane >> 3)) * ROWB + (u32)pcol * 128 + 16 * (lane & 7);
#pragma unroll
      for (int j = 0; j < 2; ++j) {
        dma16(P.k, po + j * 8 * ROWB, smb + praw + 512 + h * 2048 + j * 1024);
        dma16(P.v, po + j * 8 * ROWB, smb + praw + 8704 + h * 2048 + j * 1024);
        dma16(P.q, po + j * 8 * ROWB, smb + praw + 16896 + h * 2048 + j * 1024);
      }
      if (h == 0 && lane < 32) {
        if (pchunk) dma16(P.k, po - 4 * ROWB, smb + praw);
        else *(LDSP u32x4*)(sm + praw + 16 * lane) = u32x4{0, 0, 0, 0};
      }
      if (h == 1 && lane < 32) dma16(P.shift_k, (u32)pcol * 512 + 16 * lane, smb + shk);
    }
    STAMP(7);
    wgbarrier();
    if (!partA) {
      if (lane < 10) *(LDSP u32x4*)(sm + XIMG + h * XS + 16 * lane) = u32x4{0, 0, 0, 0};
    }
    if (w == 8) {
      *(LDSP unsigned long long*)(sm + FLAG3 + 8) = __hip_atomic_load(gepoch, RLX_AGENT) + 1;
      set_word(sm + FLAG3, 1);
    }
    const u32 co = praw + 32 * h + 8 * lp + 512 * g;
    const u32 stg = partA ? A_PSTG + (w - 4) * YS : B_PSTG + h * XS;
    u32 ka1 = co + 128 * (lq + 1);
    u32 ka2 = co + 128 * ((lq < 2) ? lq + 5 : lq);
    u32 ka3 = co + 128 * lq;
    u32 so[4];
#pragma unroll
    for (int u = 0; u < 4; ++u) so[u] = stg + li * 128 + 8 * (g & 1) + 16 * (((2 * u + (g >> 1)) ^ li) & 7);
    u32 sr_ = stg + (lane >> 3) * 128 + 16 * ((lane ^ (lane >> 3)) & 7);
    u32 eo = (u32)(pcol * 64 + 16 * h + (lane >> 3)) * 4096 + 128 * pchunk + 16 * (lane & 7);
    u32 swa = shk + 8 * (16 * h + li);
    asm volatile("" : "+v"(ka1), "+v"(ka2), "+v"(ka3), "+v"(so[0]), "+v"(so[1]), "+v"(so[2]), "+v"(so[3]), "+v"(sr_), "+v"(eo), "+v"(swa));
    vmcnt<0>();
    set_word(fw + 4 * h, 1);
    wait_words(fw, lane, 4, 1);
    STAMP(12);
    {
      const u32x2 sw = *(LDSP u32x2*)(sm + swa);
      u32x2 r1[4], r2[4], r3[4], vv[4], qq[4];
#pragma unroll
      for (int u = 0; u < 4; ++u) {
        r1[u] = trread(sm + ka1 + 2048 * u);
        r2[u] = trread(sm + ka2 + 2048 * u);
        r3[u] = trread(sm + ka3 + 2048 * u + 512);
        vv[u] = trread(sm + ka3 + 2048 * u + 8704);
        qq[u] = trread(sm + ka3 + 2048 * u + 16896);
      }
      const u32 W32 = (sw.y >> 16) | (sw.y << 16), W10 = (sw.x >> 16) | (sw.x << 16);
#pragma unroll
      for (int u = 0; u < 4; ++u) {
        const float s0 = dot2(r1[u].y, W10, dot2(r1[u].x, W32, 0.f));
        const float s1 = dot2(r3[u].x, W10, dot2(r2[u].y, W32, 0.f));
        const float s2 = dot2(r2[u].x, W10, dot2(r1[u].y, W32, 0.f));
        const float s3 = dot2(r3[u].y, W10, dot2(r3[u].x, W32, 0.f));
        *(LDSP u32x2*)(sm + so[u]) = u32x2{pkbf(s0 * bflo(vv[u].x), s1 * bfhi(vv[u].x)), pkbf(s2 * bflo(vv[u].y), s3 * bfhi(vv[u].y))};
        *(LDSP u32x2*)(sm + so[u] + 2048) = qq[u];
      }
      wavefence();
      STAMP(6);
#pragma unroll
      for (int j = 0; j < 4; ++j)
        __builtin_amdgcn_raw_buffer_store_b128(*(const LDSP u32x4*)(sm + sr_ + 1024 * j), rx, eo + (j & 1) * 8 * 4096 + (j >> 1) * (u32)(WS_EXQ - WS_EXX), 0, 16);
    }
    STAMP(13);
    const u32 urow = (u32)(64 * h + lane) * ROWB + (u32)((gi * 4) & ~7) * 2;
    set_word(fw + 16 + 4 * h, 1);
    wait_words(sm + FLAG3, lane, 1, 1);
    tag = *(volatile LDSP unsigned long long*)(sm + FLAG3 + 8);
    vmcnt<0>();
    if (lane == 0) __hip_atomic_store(gflag + FSP * ((pcol * 4 + h) * 32 + pchunk), tag, RLX_AGENT);
    if (!partA) {
      dma16(P.u, urow, smb + RAWU + 1024 * h);
      dma16(P.u, urow + 256 * ROWB, smb + RAWU + 4096 + 1024 * h);
      wait_words(fw + 16, lane, 4, 1);
    }
    STAMP(9);
    if (w >= 8) {
      if (partA) {
        wait_words(sm + FLAG2 + 16, lane, 4, 1);
        wait_words(sm + FLAG2 + 48, lane, 4, 1);
#pragma unroll 1
        for (int st = 1; st < 3; ++st) {
          if (h == 0) poll_exchange(gi >> 1, st, st + 1, 4); else wait_words(sm + FLAG3 + 4, lane, 1, (u32)(st + 1));
          __builtin_amdgcn_fence(__ATOMIC_ACQUIRE, "wavefront");
          u32x4 d[4];
#pragma unroll
          for (int j = 0; j < 4; ++j)
            d[j] = __builtin_amdgcn_raw_buffer_load_b128(rx, (u32)(gi * 8 + 2 * h + (j & 1)) * 4096 + 1024 * st + 16 * lane + (j >> 1) * (u32)(WS_EXQ - WS_EXX), 0, 16);
#pragma unroll
          for (int j = 0; j < 4; ++j)
            *(LDSP u32x4*)(sm + ((j >> 1) ? YIMG + (2 * h + (j & 1)) * YS : XIMG + (2 * h + (j & 1)) * XS + XPADB) + 1024 * st + 16 * lane) = d[j];
          set_word(sm + FLAGS + 4 * h, (u32)(2 * st + 2));
          if (st == 1) STAMP(5); if (st == 2) STAMP(14);
        }
        const u32 yrd = YIMG + lq * YS + 2 * (64 * h + 16 * g + 4 * lp);
        u16* const outp = P.out + gi * 8 + (size_t)(64 * h + lane) * (2 * DCH);
#pragma unroll 1
        for (int b = 0; b < (h < 3 ? 6 : 5); ++b) {
          wait_words(sm + FLAGS + 16, lane, 8, (u32)(b + 1));
          u32x2 a = trread(sm + yrd + 512 * b);
          u32x2 c = trread(sm + yrd + 512 * b + 4 * YS);
          *(u32x4*)(outp + (size_t)b * 256 * (2 * DCH)) = u32x4{a.x, a.y, c.x, c.y};
        }
        ROLE_END(2);
      }
      const u32 ua = RAWU + 1024 * h + 16 * (16 * g + 4 * lp + lq) + 2 * ((gi * 4) & 4);
      const u32 xw = XIMG + (lane & 3) * XS + XPADB + 8 * (lane >> 2) + 128 * h;
      const u32 yrd = B_YIMG + lq * YS + 2 * (64 * h + 16 * g + 4 * lp);
      const u32 hrd = H_YIMG + lq * HYS + 2 * (64 * h + 16 * g + 4 * lp - 1408);
      u16* const outp = P.out + gi * 4 + (size_t)(64 * h + lane) * (2 * DCH);
#pragma unroll 1
      for (int it = 0; it < 8; ++it) {
        int i = it;
        asm volatile("" : "+s"(i));
        if (i + 2 < 8) dma16(P.u, urow + (u32)(i + 2) * (256 * ROWB), smb + RAWU + 4096 * (i + 2) + 1024 * h);
        if (i < 6) vmcnt<2>(); else if (i == 6) vmcnt<1>(); else vmcnt<0>();
        *(LDSP u32x2*)(sm + xw + 512 * i) = trread(sm + ua + 4096 * i);
        set_word(sm + FLAGS + 4 * h, (u32)(i + 1));
      }
      STAMP(14);
#pragma unroll 1
      for (int i = 0; i < 8; ++i) {
        wait_words(sm + FLAGS + 16, lane, 4, (u32)(i + 1));
        *(u32x2*)(outp + DCH + (size_t)i * 256 * (2 * DCH)) = trread(sm + yrd + 512 * i);
      }
#pragma unroll 1
      for (int i = (h == 3 ? 5 : 6); i < 8; ++i) {
        wait_words(sm + FLAGS + 32, lane, 4, (u32)(i + 1));
        *(u32x2*)(outp + (size_t)i * 256 * (2 * DCH)) = trread(sm + hrd + 512 * i);
      }
      ROLE_END(3);
    }
    __builtin_amdgcn_s_setprio(0);
  } else if (partA) {
    wgbarrier();
    delay(PFP);
    const int pc = idx * 4 + w;
    touch64((const char*)P.u + prefetch_off(pc / 30, pc % 30, xcd, lane), smb + PFD);
    if (pc < 20) touch64((const char*)P.u + prefetch_off(1, 10 + pc, xcd, lane), smb + PFD);
  }

  if (partA) {
    const int ch = gi * 8 + w;
    const u32 kimg = KIMG + w * KS, ximg = XIMG + w * XS, yimg = YIMG + w * YS;
    if (lane < 10) *(LDSP u32x4*)(sm + ximg + 16 * lane) = u32x4{0, 0, 0, 0};
    if (lane < 6) *(LDSP u32x4*)(sm + kimg + 16 * lane) = u32x4{0, 0, 0, 0};
    const float dsk = ld_bf_uniform(P.ssm_D, ch);
    wait_words(sm + FLAG2, lane, 4, 1);
#pragma unroll
    for (int j = 0; j < 3; ++j) dma16(P.ssm_k, (u32)ch * (SEQ * 2) + 1024 * j + 16 * lane, smb + kimg + KPADB + 1024 * j);
    vmcnt<0>();
    if (lane == 0) {
      LDSP u16* k0 = (LDSP u16*)(sm + kimg + KPADB);
      *k0 = (u16)pkbf(bf2f(*k0) + dsk, 0.f);
    }
    if (w == 0) poll_exchange(gi >> 1, 0, 1, 16); else wait_words(sm + FLAG3 + 16, lane, 1, 1);
    __builtin_amdgcn_fence(__ATOMIC_ACQUIRE, "wavefront");
    STAMP(2);
    {
      const u32x4 dx = __builtin_amdgcn_raw_buffer_load_b128(rx, (u32)ch * 4096 + 16 * lane, 0, 16);
      const u32x4 dq = __builtin_amdgcn_raw_buffer_load_b128(rx, (u32)ch * 4096 + 16 * lane + (u32)(WS_EXQ - WS_EXX), 0, 16);
      wait_words(sm + FLAG2 + 16 + (w < 4 ? 32 : 0), lane, 4, 1);
      *(LDSP u32x4*)(sm + ximg + XPADB + 16 * lane) = dx;
      *(LDSP u32x4*)(sm + yimg + 16 * lane) = dq;
    }
    wavefence();
    conv_stream<1>(sm, kimg, ximg, sm + yimg + 2 * lane, sm + FLAGS + 16 + 4 * w, lane STV_PASS);
    ROLE_END(4);
  }

  {
    const int ch0 = gi * 4;
    const int cw = w & 3, ps = w >> 2;
    const int ch = ch0 + cw;
    const size_t o = (size_t)ch * NST + 64 * ps + lane;
    const u16 i_ar = P.A_re[o], i_ai = P.A_im[o], i_br = P.B_re[o], i_bi = P.B_im[o], i_cr = P.C_re[o], i_ci = P.C_im[o];
    const float ldt = ld_bf_uniform(P.log_dt, ch);
    const float dsk = ld_bf_uniform(P.Dskip, ch);
    wgbarrier();
    const float dt = rbf(expf(ldt));
    const float zr = dt * bf2f(i_ar), zi = dt * bf2f(i_ai);
    const cf dB = {dt * bf2f(i_br), dt * bf2f(i_bi)};
    const cf C = {bf2f(i_cr), bf2f(i_ci)};
    const cf W = cmul(C, dB);
    float mx = fmaxf(fabsf(W.r), fabsf(W.i));
    STAMP(1);
    {
      int m = __float_as_int(mx);
      m = max(m, __builtin_amdgcn_update_dpp(0, m, 0x111, 0xf, 0xf, false));
      m = max(m, __builtin_amdgcn_update_dpp(0, m, 0x112, 0xf, 0xf, false));
      m = max(m, __builtin_amdgcn_update_dpp(0, m, 0x114, 0xf, 0xf, false));
      m = max(m, __builtin_amdgcn_update_dpp(0, m, 0x118, 0xf, 0xf, false));
      int a0 = __builtin_amdgcn_readlane(m, 15), a1 = __builtin_amdgcn_readlane(m, 31);
      int a2 = __builtin_amdgcn_readlane(m, 47), a3 = __builtin_amdgcn_readlane(m, 63);
      mx = __int_as_float(max(max(a0, a1), max(a2, a3)));
    }
    int ex = (int)((__float_as_uint(mx) >> 23) & 0xff);
    ex = min(max(ex, 30), 240);
    const float sY = __uint_as_float((u32)(267 - ex) << 23);
    const float so = __uint_as_float((u32)(ex - 27) << 23);
    const float SX = 16384.f;

    f32x4 ak[3];
    const u32 stg = STG + w * STS;
    const u32 rb = stg + li * 272 + g * 16;
    const u32 m0v = smb + w * STS;
#define STGW(row, val_r, val_i) \
    asm volatile("ds_write_addtid_b32 %0 offset:%1" :: "v"(pkh((val_r), (val_i))), "i"(STG + (row) * 272) : "memory")
    {
      const cf E1 = cexp_pow2(zr, zi, 1.f);
      const cf E8c = cexp_pow2(zr, -zi, 8.f);
      const cf E128 = cexp_pow2(zr, zi, 128.f);
      const cf E1024 = cexp_pow2(zr, zi, 1024.f);
      const cf E2 = cmul(E1, E1);
      const cf E4 = cmul(E2, E2);
      u32 keep;
      asm volatile("s_mov_b32 %0, m0\n\ts_mov_b32 m0, %1\n\ts_nop 0" : "=&s"(keep) : "s"(m0v) : "memory");
      cf y = {W.r * sY, -(W.i * sY)};
      cf P1 = {E128.r * SX, E128.i * SX};
      cf v0 = {SX, 0.f}, v1 = P1;
#pragma unroll
      for (int b = 0; b < 8; ++b) {
        STGW(2 * b, y.r, y.i); y = cmul(y, E8c);
        STGW(16 + b, v0.r, v0.i); if (b < 7) v0 = cmul(v0, E1);
        STGW(2 * b + 1, y.r, y.i); if (b < 7) y = cmul(y, E8c);
        STGW(24 + b, v1.r, v1.i); if (b < 7) v1 = cmul(v1, E1);
      }
      f16x8 yf[4];
      const f32x4 zero = {0.f, 0.f, 0.f, 0.f};
#pragma unroll
      for (int s = 0; s < 4; ++s) {
        yf[s] = *(const LDSP f16x8*)(sm + rb + 64 * s);
        f16x8 xf = *(const LDSP f16x8*)(sm + rb + 16 * 272 + 64 * s);
        ak[0] = __builtin_amdgcn_mfma_f32_16x16x32_f16(xf, yf[s], s ? ak[0] : zero, 0, 0, 0);
      }
      asm volatile("" ::: "memory");
      cf Pa = cmul(P1, E128);
      cf Pb = {E1024.r * SX, E1024.i * SX};
#pragma unroll
      for (int m = 0; m < 2; ++m) {
        cf v = Pa;
#pragma unroll
        for (int r = 0; r < 4; ++r) {
          STGW(16 + 4 * m + r, v.r, v.i); if (r < 3) v = cmul(v, E2);
          if (r & 1) { STGW(4 * (2 * m + (r >> 1)) + 2, Pb.r, Pb.i); Pb = cmul(Pb, E128); }
        }
        Pa = cmul(Pa, E128);
      }
#pragma unroll
      for (int m = 0; m < 4; ++m) {
        STGW(4 * m, Pa.r, Pa.i);
        cf v = cmul(Pa, E4);
        STGW(4 * m + 3, Pb.r, Pb.i); Pb = cmul(Pb, E128);
        STGW(4 * m + 1, v.r, v.i);
        if (m < 3) Pa = cmul(Pa, E128);
      }
      STGW(24, Pb.r, Pb.i);
      asm volatile("s_mov_b32 m0, %0" :: "s"(keep) : "memory");
#pragma unroll
      for (int s = 0; s < 4; ++s) {
        f16x8 x1 = *(const LDSP f16x8*)(sm + rb + 64 * s);
        f16x8 x2 = *(const LDSP f16x8*)(sm + rb + 16 * 272 + 64 * s);
        ak[1] = __builtin_amdgcn_mfma_f32_16x16x32_f16(x1, yf[s], s ? ak[1] : zero, 0, 0, 0);
        ak[2] = __builtin_amdgcn_mfma_f32_16x16x32_f16(x2, yf[s], s ? ak[2] : zero, 0, 0, 0);
      }
    }
#undef STGW
#pragma unroll
    for (int i = 0; i < 3; ++i) ak[i] *= so;
    STAMP(2);
    asm volatile("" ::: "memory");
    const u32 kimg = B_KIMG + cw * KS;
    if (ps) {
#pragma unroll
      for (int i = 0; i < 3; ++i) *(LDSP f32x4*)(sm + kimg + 1024 * i + 16 * lane) = ak[i];
      set_word(sm + FLAGS + 48 + 4 * cw, 1);
      const u32 hx = stg, hk = stg + XS, hy = H_YIMG + cw * HYS;
      asm volatile("s_waitcnt lgkmcnt(0)" ::: "memory");
#pragma unroll
      for (int j = 0; j < 4; ++j) dma16(P.ssm_k, (u32)ch * (SEQ * 2) + 1024 * j + 16 * lane, smb + hk + KPADB + 1024 * j);
      if (lane < 10) *(LDSP u32x4*)(sm + hx + 16 * lane) = u32x4{0, 0, 0, 0};
      if (lane < 6) *(LDSP u32x4*)(sm + hk + 16 * lane) = u32x4{0, 0, 0, 0};
      const float dh = ld_bf_uniform(P.ssm_D, ch);
      if (w == 4) poll_exchange(gi >> 2, 0, 4, 4); else wait_words(sm + FLAG3 + 4, lane, 1, 4);
      __builtin_amdgcn_fence(__ATOMIC_ACQUIRE, "wavefront");
      STAMP(5);
      {
        u32x4 d[6];
#pragma unroll
        for (int j = 0; j < 4; ++j) d[j] = __builtin_amdgcn_raw_buffer_load_b128(rx, (u32)ch * 4096 + 1024 * j + 16 * lane, 0, 16);
#pragma unroll
        for (int j = 0; j < 2; ++j)
          d[4 + j] = __builtin_amdgcn_raw_buffer_load_b128(rx, (u32)ch * 4096 + 2816 + 256 * j + 16 * lane + (u32)(WS_EXQ - WS_EXX), 0, 16);
        vmcnt<0>();
        if (lane == 0) {
          LDSP u16* k0 = (LDSP u16*)(sm + hk + KPADB);
          *k0 = (u16)pkbf(bf2f(*k0) + dh, 0.f);
        }
#pragma unroll
        for (int j = 0; j < 4; ++j) *(LDSP u32x4*)(sm + hx + XPADB + 1024 * j + 16 * lane) = d[j];
#pragma unroll
        for (int j = 0; j < 2; ++j) *(LDSP u32x4*)(sm + hy + 256 * j + 16 * lane) = d[4 + j];
      }
      wavefence();
      conv_stream<2>(sm, hk, hx, sm + hy + 2 * lane - 11 * 256, sm + FLAGS + 16 + 4 * w, lane STV_PASS);
      ROLE_END(5);
    }
    wait_words(sm + FLAGS + 48 + 4 * cw, lane, 1, 1);
    {
#pragma unroll
      for (int i = 0; i < 3; ++i) ak[i] += *(const LDSP f32x4*)(sm + kimg + 1024 * i + 16 * lane);
      wavefence();
      auto nxt = [&](float x, float e, bool lastl) {
        float r = __int_as_float(__builtin_amdgcn_update_dpp(0, __float_as_int(x), 0x130, 0xf, 0xf, true));
        return lastl ? e : r;
      };
      auto lane_of = [&](float x, int l) { return __int_as_float(__builtin_amdgcn_readlane(__float_as_int(x), l)); };
      const lptr kd = sm + kimg + KPADB;
      if (lane < 6) *(LDSP u32x4*)(sm + kimg + 16 * lane) = u32x4{0, 0, 0, 0};
      {
        const float a0 = ak[0][0] + (lane == 0 ? dsk : 0.f), a1 = ak[0][1], a2 = ak[0][2], a3 = ak[0][3];
        *(LDSP u32x2*)(kd + 2 * (128 * (g >> 1) + 8 * li + 4 * (g & 1))) = u32x2{pkbf(a0, a1), pkbf(a2, a3)};
      }
      const float s20 = ak[1][0], s21 = ak[1][1], s3a = ak[1][2], s3b = ak[1][3];
      const float c0 = ak[2][0], c1 = ak[2][1], c2 = ak[2][2], c3 = ak[2][3];
      const bool l63 = lane == 63;
      {
        const float n = nxt(c0, lane_of(s20, 0), lane == 31);
        if (lane < 32) *(LDSP u32x4*)(kd + 16 * (32 + lane)) = u32x4{pkbf(c0, c0), pkbf(c1, c2), pkbf(c2, c2), pkbf(c3, n)};
      }
      {
        const float n = nxt(s20, lane_of(s3a, 0), l63);
        *(LDSP u32x4*)(kd + 16 * (64 + lane)) = u32x4{pkbf(s20, s20), pkbf(s20, s21), pkbf(s21, s21), pkbf(n, n)};
      }
      {
        const float na = nxt(s3a, lane_of(s3b, 0), l63), nb = nxt(s3b, lane_of(c0, 32), l63);
        const u32 a = pkbf(s3a, s3a), b = pkbf(s3b, s3b);
        *(LDSP u32x4*)(kd + 16 * (128 + lane)) = u32x4{a, a, pkbf((lane & 1) ? na : s3a, na), pkbf(na, na)};
        *(LDSP u32x4*)(kd + 16 * (192 + lane)) = u32x4{b, b, pkbf((lane & 1) ? nb : s3b, nb), pkbf(nb, nb)};
      }
    }
    wavefence();
    STAMP(3);
    conv_stream<0>(sm, kimg, XIMG + cw * XS, sm + B_YIMG + cw * YS + 2 * lane, sm + FLAGS + 16 + 4 * w, lane STV_PASS);
    ROLE_END(0);
  }
}

extern "C" void kernel_launch(void* const* d_in, const int* in_sizes, int n_in,
                              void* d_out, int out_size, void* d_ws, size_t ws_size,
                              hipStream_t stream) {
  Params P;
  P.q = (const u16*)d_in[0];
  P.k = (const u16*)d_in[1];
  P.v = (const u16*)d_in[2];
  P.shift_k = (const u16*)d_in[3];
  P.ssm_k = (const u16*)d_in[4];
  P.ssm_D = (const u16*)d_in[5];
  P.u = (const u16*)d_in[6];
  P.A_re = (const u16*)d_in[7];
  P.A_im = (const u16*)d_in[8];
  P.B_re = (const u16*)d_in[9];
  P.B_im = (const u16*)d_in[10];
  P.C_re = (const u16*)d_in[11];
  P.C_im = (const u16*)d_in[12];
  P.log_dt = (const u16*)d_in[13];
  P.Dskip = (const u16*)d_in[14];
  P.out = (u16*)d_out;
  P.ws = (unsigned long long*)d_ws;
  P.ex = (char*)d_ws + (16 << 20);
  (void)hipFuncSetAttribute((const void*)fused_kernel, hipFuncAttributeMaxDynamicSharedMemorySize, LDS_BYTES);
  fused_kernel<<<256, 768, LDS_BYTES, stream>>>(P);
}
```

```cpp
#include <hip/hip_runtime.h>
#include <hip/hip_bf16.h>
#include <stdint.h>

typedef __attribute__((ext_vector_type(4))) short s16x4;
typedef __attribute__((ext_vector_type(8))) _Float16 f16x8;
typedef __attribute__((ext_vector_type(8))) __bf16 bf16x8;
typedef __attribute__((ext_vector_type(2))) __bf16 bf16x2;
typedef __attribute__((ext_vector_type(2))) _Float16 f16x2;
typedef __attribute__((ext_vector_type(2))) float f32x2;
typedef __attribute__((ext_vector_type(4))) float f32x4;
typedef __attribute__((ext_vector_type(2))) unsigned int u32x2;
typedef __attribute__((ext_vector_type(4))) unsigned int u32x4;
typedef unsigned int u32;
typedef unsigned short u16;
#define LDSP __attribute__((address_space(3)))
#define DEV __device__ __forceinline__

constexpr int SEQ = 2048;
constexpr int DCH = 640;
constexpr int NST = 128;
constexpr int ROWB = DCH * 2;

constexpr int XS = 4272;
constexpr int XPADB = 160;
constexpr int YS = 4160;
constexpr int KS = 4224;
constexpr int KPADB = 96;
constexpr int FLAGS = 0;
constexpr int ZROW = 64;
constexpr int FLAG2 = 192;
constexpr int FLAG3 = 256;
constexpr int PFD = 320;
constexpr int SHK = 1024;
constexpr int XIMG = 2048;
constexpr int PUNIT = 8704 + 8192 + 8192;
constexpr int YIMG = 36224;
constexpr int KIMG = 69504;
constexpr int A_PRAW = 103296;
constexpr int A_PSTG = YIMG;
constexpr int STS = 8704;
constexpr int STG = 19200;
constexpr int H_XIMG = STG + 4 * STS;
constexpr int H_KIMG = H_XIMG + 4 * XS;
constexpr int B_KIMG = 88832;
constexpr int B_YIMG = 105728;
constexpr int HYS = 1344;
constexpr int H_YIMG = 122368;
constexpr int RAWU = 127744;
constexpr int B_PRAW = RAWU + 8192;
constexpr int B_PSTG = XIMG + XPADB;
constexpr int LDS_BYTES = 161024;

constexpr size_t WS_EPOCH = 0;
constexpr size_t WS_FLAG = 4096;
constexpr size_t WS_EXX = 1 << 20;
constexpr size_t WS_EXQ = 4 << 20;

DEV float bf2f(u16 v) { return __uint_as_float(((u32)v) << 16); }
DEV float bflo(u32 v) { return __uint_as_float(v << 16); }
DEV float bfhi(u32 v) { return __uint_as_float(v & 0xffff0000u); }
DEV u32 pkbf(float a, float b) {
  f32x2 t = {a, b};
  bf16x2 r = __builtin_convertvector(t, bf16x2);
  return __builtin_bit_cast(u32, r);
}
DEV u32 pkh(float a, float b) {
  f32x2 t = {a, b};
  f16x2 r = __builtin_convertvector(t, f16x2);
  return __builtin_bit_cast(u32, r);
}
DEV float rbf(float a) { return bflo(pkbf(a, a)); }
DEV float dot2(u32 a, u32 b, float c) {
  return __builtin_amdgcn_fdot2_f32_bf16(__builtin_bit_cast(bf16x2, a), __builtin_bit_cast(bf16x2, b), c, false);
}
DEV float vfma(float a, float b, float c) { float d; asm("v_fma_f32 %0, %1, %2, %3" : "=v"(d) : "v"(a), "v"(b), "v"(c)); return d; }
DEV float vmul(float a, float b) { float d; asm("v_mul_f32 %0, %1, %2" : "=v"(d) : "v"(a), "v"(b)); return d; }
typedef __attribute__((address_space(1))) unsigned long long gu64;
#define RLX_AGENT __ATOMIC_RELAXED, __HIP_MEMORY_SCOPE_AGENT
typedef LDSP char* lptr;
DEV u32x2 trread(lptr p) {
  s16x4 r = __builtin_amdgcn_ds_read_tr16_b64_v4i16((LDSP s16x4*)p);
  return __builtin_bit_cast(u32x2, r);
}
DEV bf16x8 frag(u32x2 a, u32x2 b) {
  u32x4 f = {a.x, a.y, b.x, b.y};
  return __builtin_bit_cast(bf16x8, f);
}
DEV float ror1(float v) {
  int r = __builtin_amdgcn_update_dpp(0, __float_as_int(v), 0x13C, 0xf, 0xf, false);
  return __int_as_float(r);
}
DEV void wavefence() { __builtin_amdgcn_fence(__ATOMIC_ACQ_REL, "wavefront"); __builtin_amdgcn_wave_barrier(); }
DEV void wgbarrier() {
  asm volatile("s_waitcnt lgkmcnt(0)" ::: "memory");
  __builtin_amdgcn_s_barrier();
  asm volatile("" ::: "memory");
}
DEV void dma16(const void* base, u32 voff, u32 ldsaddr) {
  u32 keep;
  ldsaddr = __builtin_amdgcn_readfirstlane(ldsaddr);
  const unsigned long long b64 = (unsigned long long)(uintptr_t)base;
  const unsigned long long sb = ((unsigned long long)(u32)__builtin_amdgcn_readfirstlane((u32)(b64 >> 32)) << 32) |
                                (u32)__builtin_amdgcn_readfirstlane((u32)b64);
  asm volatile("s_mov_b32 %0, m0\n\ts_mov_b32 m0, %3\n\ts_nop 2\n\tglobal_load_lds_dwordx4 %1, %2\n\ts_mov_b32 m0, %0"
               : "=&s"(keep) : "v"(voff), "s"(sb), "s"(ldsaddr) : "memory");
}
DEV void touch64(const void* p, u32 ldsaddr) {
  u32 keep;
  asm volatile("s_mov_b32 %0, m0\n\ts_mov_b32 m0, %2\n\ts_nop 0\n\tglobal_load_lds_dword %1, off\n\ts_mov_b32 m0, %0"
               : "=&s"(keep) : "v"(p), "s"(ldsaddr) : "memory");
}
template <int N> DEV void vmcnt() { asm volatile("s_waitcnt vmcnt(%0)" :: "i"(N) : "memory"); }
#define SCHED0() __builtin_amdgcn_sched_barrier(0)

template <int V> struct IC { static constexpr int value = V; };
template <int I, int N, class F> DEV void static_for(F&& f) {
  if constexpr (I < N) { f(IC<I>{}); static_for<I + 1, N>(f); }
}

struct cf { float r, i; };
DEV cf cmul(cf a, cf b) {
  cf o;
  o.r = fmaf(a.r, b.r, -(a.i * b.i));
  o.i = fmaf(a.r, b.i, a.i * b.r);
  return o;
}
DEV cf cexp_pow2(float zr, float zi, float x) {
  float mag = __builtin_amdgcn_exp2f((zr * x) * 1.44269504088896340736f);
  float th = zi * x;
  const float C_HI = 0.15915494f;
  const float C_LO = 6.4206382e-09f;
  float f = fmaf(th, C_LO, fmaf(th, C_HI, -rintf(th * C_HI)));
  cf o;
  o.r = mag * __builtin_amdgcn_cosf(f);
  o.i = mag * __builtin_amdgcn_sinf(f);
  return o;
}

#define STAMP(n)
#define CSTAMP(n)
#define STAMP_OUT()

struct Params {
  const u16 *k, *v, *q, *shift_k; char* ex; const u16 *u, *ssm_k; u16* out;
  const u16 *A_re, *A_im, *B_re, *B_im, *C_re, *C_im, *log_dt, *Dskip;
  const u16* ssm_D;
  unsigned long long* ws;
};

DEV float ld_bf_uniform(const u16* p, int idx) {
  u32 d = *(const __attribute__((address_space(4))) u32*)(uintptr_t)(p + (idx & ~1));
  return (idx & 1) ? bfhi(d) : bflo(d);
}

DEV u32 prefetch_off(int half, int slice, int xcd, int lane) {
  const u32 row = (u32)(half * 4 + (lane >> 4)) * 256u + (u32)slice * 8u + ((lane >> 1) & 7);
  return row * ROWB + (u32)(((160 * xcd) >> 7) + (lane & 1)) * 128u;
}
DEV void wait_words(lptr p, int lane, int n, u32 v) {
#pragma unroll 1
  for (int spin = 0; spin < (1 << 16); ++spin) {
    u32 x = *(volatile LDSP u32*)(p + 4 * (lane & (n - 1)));
    if (__all(x >= v)) break;
    __builtin_amdgcn_s_sleep(1);
  }
  asm volatile("" ::: "memory");
}
#define PFS 3
#define PFP 3
#define PFU 8
#define PFH 4
#define FSP 16
#define PSL 6
DEV void delay(int n) {
  for (int i = 0; i < n; ++i) __builtin_amdgcn_s_sleep(PFS);
}

DEV void set_word(lptr p, u32 v) {
  asm volatile("" ::: "memory");
  *(volatile LDSP u32*)p = v;
  asm volatile("" ::: "memory");
}

struct Sched {
  int n;
  int p[272], par[272], d[272], ld[272];
  bool first[272];
  int base[16];
  int cm[16], om[16];
  int ldm[40], ldu[40], ldp[40];
  int ku[16];
  int nld, nst;
};
constexpr void add_unit(Sched& S, int p, int d, int ld, int pm, bool first) {
  for (int par = 0; par < 2; ++par)
    if ((pm >> par) & 1) {
      S.p[S.n] = p; S.par[S.n] = par; S.d[S.n] = d; S.ld[S.n] = ld; S.first[S.n] = first;
      if (S.ldu[ld] < 0) S.ldu[ld] = S.n;
      S.ldp[ld] |= 1 << par;
      if (S.ku[d] < 0) S.ku[d] = S.n;
      ++S.n;
    }
}
constexpr Sched make_sched(int mode) {
  Sched S{};
  const int last = (mode == 1) ? 11 : 15;
  S.nst = last + 1;
  int nl = 16;
  for (int l = 0; l < 40; ++l) { S.ldu[l] = -1; S.ldm[l] = l; }
  for (int d = 0; d < 16; ++d) S.ku[d] = -1;
  for (int s = 0; s <= last; ++s) {
    S.base[s] = S.n;
    S.cm[s] = (mode == 2 && s < 11) ? 0 : ((mode == 1 && s == 11) ? 1 : 3);
    S.om[s] = (mode == 2 && s == 11) ? 2 : S.cm[s];
    if (mode == 2) {
      for (int p = (s > 11 ? s : 11); p <= 15; ++p) add_unit(S, p, p - s, s, 3, s == 0);
      continue;
    }
    for (int k = 0; k < 8 && s + k <= last; ++k)
      add_unit(S, s + k, k, s, (mode == 1 && s + k == 11) ? 1 : 3, (s == 0) || (k == 7));
    int lo = -1, hi = -1, cnt = 0, half = 0;
    if (s == 6) { lo = 8; hi = 8; cnt = 1; half = 2; }
    else if (s >= 7 && s <= last - 3) { lo = (s & 1) ? s + 2 : s + 1; hi = lo + 1; cnt = hi - 7; half = (s & 1) ? 0 : 1; }
    else if (s == last - 2 || s == last - 1) { lo = last; hi = last; cnt = last - 7; half = (s == last - 2) ? 0 : 1; }
    if (cnt) {
      const int split = (cnt + 1) / 2;
      const int j0 = (half == 1) ? split : 0, j1 = (half == 0) ? split : cnt;
      for (int j = j0; j < j1; ++j) {
        const int mm = cnt - 1 - j;
        S.ldm[nl] = mm;
        for (int t = hi; t >= lo; --t)
          if (t - mm >= 8) add_unit(S, t, t - mm, nl, (mode == 1 && t == 11) ? 1 : 3, false);
        ++nl;
      }
    }
  }
  S.nld = nl;
  return S;
}
constexpr int NGAP = 272 + 40, CAP = 2, KPRE = 16;
enum { A_NONE = 0, A_X, A_K, A_Q, A_PXR, A_PXC, A_EP };
struct Acts { int n[NGAP]; int a[NGAP][4]; int end; };
constexpr int place_back(Acts& T, int latest, int code, int cap) {
  int g = latest;
  while (g > 0 && T.n[g] >= cap) --g;
  T.a[g][T.n[g]++] = code;
  return g;
}
constexpr int place_fwd(Acts& T, int earliest, int code, int cap, int nm) {
  int g = earliest;
  while (T.n[g] >= (g < nm ? cap : 4)) ++g;
  T.a[g][T.n[g]++] = code;
  if (g + 1 > T.end) T.end = g + 1;
  return g;
}
constexpr Acts make_acts(const Sched& S, int mode) {
  Acts T{};
  T.end = S.n;
  const int dense = (mode == 0) ? 200 : (mode == 1 ? 90 : 100);
  for (int l = S.nst - 1; l >= 1; --l) {
    int g = S.ldu[l] - 5;
    const int cap = S.ldu[l] >= dense && mode ? 2 : 1;
    for (int i = 3; i >= 0; --i) if ((S.ldp[l] >> (i >> 1)) & 1) g = place_back(T, g - 1, A_X | (l << 4) | (i << 12), cap);
    for (int o = 1; o >= 0; --o) if (mode && ((S.om[l] >> o) & 1)) g = place_back(T, g - 1, A_Q | (l << 4) | (o << 12), cap);
    if ((mode == 0 && !(l & 1)) || (mode == 1 && !(l & 3))) {
      g = place_back(T, g - 1, A_PXC | (l << 4), 1);
      place_back(T, g - 6, A_PXR, 1);
    }
  }
  for (int l = S.nld - 1; l >= 16; --l) {
    int g = S.ldu[l] - 5;
    for (int i = 3; i >= 0; --i) if ((S.ldp[l] >> (i >> 1)) & 1) g = place_back(T, g - 1, A_X | (l << 4) | (i << 12), S.ldu[l] >= dense ? 2 : 1);
  }
  for (int d = 0; d < 16; ++d) {
    if (S.ku[d] < KPRE) continue;
    int g = S.ku[d] - 5;
    g = place_back(T, g - 1, A_K | (d << 4) | (1 << 12), 2);
    place_back(T, g - 1, A_K | (d << 4), 2);
  }
  int prev[18] = {};
  for (int s = 0; s < S.nst; ++s) {
    if (!S.cm[s]) continue;
    int g = S.base[s] + 4;
    const int p16 = prev[16];
    for (int e = 0; e < ((s & 1) ? 18 : 17); ++e) {
      if (e < 14 && !(((e >= 8 ? S.om[s] : S.cm[s]) >> (e & 1)) & 1)) continue;
      if ((e == 15 && !(S.om[s] & 1)) || (e == 16 && !(S.om[s] & 2))) continue;
      if (mode == 0 && (e == 12 || e == 13)) continue;
      int lo = g + (((e & 1) && e < 14) ? 0 : 1);
      if (lo <= prev[e | 1]) lo = prev[e | 1] + 1;
      if (e == 17 && lo <= p16) lo = p16 + 1;
      g = place_fwd(T, lo, A_EP | (s << 4) | (e << 12), S.base[s] >= dense ? 3 : CAP, S.n);
      prev[e] = g;
    }
  }
  return T;
}
template <int MODE> struct Tab {
  static constexpr Sched S = make_sched(MODE);
  static constexpr Acts A = make_acts(S, MODE);
};
static_assert(Tab<0>::S.n == 272 && Tab<1>::S.n == 144 && Tab<2>::S.n == 140, "schedule");

#define MFMA(a, b, c) __builtin_amdgcn_mfma_f32_16x16x32_bf16((a), (b), (c), 0, 0, 0)
#define STV_ARG
#define STV_PASS

template <int MODE>
DEV void conv_stream(lptr sm, u32 kimg, u32 ximg, lptr ywp, lptr pyp, int lane STV_ARG) {
  using T = Tab<MODE>;
  const int g = lane >> 4, lq = (lane & 15) >> 2, lp = lane & 3;
  const lptr kb = sm + kimg + 8 * (4 * lp + 8 * g + lq);
  const lptr xb = sm + ximg + 224 + 8 * (lp - 8 * g - lq);
  const bool l63 = (lane == 63);
  const lptr pxp = sm + FLAGS + 4 * (lane & 3);
  const u32 qa = (u32)(uintptr_t)ywp;
  u32x2 kr[16][2], xr[40][4];
  f32x4 ac[16][2];
  const f32x4 zero = {0.f, 0.f, 0.f, 0.f};
  float ts[16][2], u2[16][2], u1[16][2], yy[16][2];
  u32 pk[16];
  constexpr int NQ = MODE == 0 ? 3 : 5;
  u32 qv[NQ][2], px = 0;
#pragma unroll
  for (int i = 0; i < NQ; ++i) { qv[i][0] = 0; qv[i][1] = 0; }
#define RDQ(s_, i_) asm volatile("ds_read_u16_d16_hi %0, %1 offset:%2" : "+v"(qv[(s_) % NQ][i_]) : "v"(qa), "i"((s_) * 256 + 128 * (i_)) : "memory")
#define RDXP(l_, i_) xr[l_][i_] = trread(xb + 256 * T::S.ldm[l_] + ((i_) >> 1) * 128 + (((i_) & 1) ? 0 : 32))
  static_for<0, 16>([&](auto D_) {
    constexpr int d = decltype(D_)::value;
    if constexpr (T::S.ku[d] >= 0 && T::S.ku[d] < KPRE) { kr[d][0] = trread(kb + 256 * d); kr[d][1] = trread(kb + 256 * d + 32); }
  });
  if constexpr (MODE == 0) wait_words(sm + FLAGS, lane, 4, 1);
  STAMP(4);
  if constexpr (MODE == 1) { RDQ(0, 0); RDQ(0, 1); }
  RDXP(0, 0); RDXP(0, 1); RDXP(0, 2); RDXP(0, 3);
  SCHED0();
  static_for<0, T::A.end>([&](auto G_) {
    constexpr int G = decltype(G_)::value;
    if constexpr (G < T::S.n) {
      constexpr int p = T::S.p[G], par = T::S.par[G], d = T::S.d[G], l = T::S.ld[G];
      ac[p][par] = MFMA(frag(kr[d][0], kr[d][1]), frag(xr[l][2 * par], xr[l][2 * par + 1]), T::S.first[G] ? zero : ac[p][par]);
      SCHED0();
    }
    static_for<0, T::A.n[G]>([&](auto I_) {
      constexpr int code = T::A.a[G][decltype(I_)::value];
      constexpr int kind = code & 15, a0 = (code >> 4) & 255, a1 = code >> 12;
      if constexpr (kind == A_X) RDXP(a0, a1);
      if constexpr (kind == A_K) kr[a0][a1] = trread(kb + 256 * a0 + 32 * a1);
      if constexpr (kind == A_Q) RDQ(a0, a1);
      if constexpr (kind == A_PXR) px = *(volatile LDSP u32*)pxp;
      if constexpr (kind == A_PXC) {
        if (__builtin_expect(!__all(px >= (u32)(a0 / 2 + 1)), 0)) wait_words(sm + FLAGS, lane, 4, (u32)(a0 / 2 + 1));
        asm volatile("" ::: "memory");
      }
      if constexpr (kind == A_EP) {
        constexpr int s = a0, e = a1 >> 1, o = a1 & 1;
        constexpr bool hp = s > 0 && T::S.cm[s > 0 ? s - 1 : 0] != 0;
        if constexpr (a1 < 14) {
          if constexpr (e == 0) {
            float cur = ac[s][o][3], prev = 0.f;
            if constexpr (o) prev = ac[s][0][3]; else if constexpr (hp) prev = ac[s - 1][1][3];
            ts[s][o] = l63 ? prev : cur;
          }
          if constexpr (e == 1) { float a = ac[s][o][2]; u2[s][o] = a + ror1(ts[s][o]); }
          if constexpr (e == 2) {
            float prev = 0.f;
            if constexpr (o) prev = u2[s][0]; else if constexpr (hp) prev = u2[s - 1][1];
            ts[s][o] = l63 ? prev : u2[s][o];
          }
          if constexpr (e == 3) { float a = ac[s][o][1]; u1[s][o] = a + ror1(ts[s][o]); }
          if constexpr (e == 4) {
            float prev = 0.f;
            if constexpr (o) prev = u1[s][0]; else if constexpr (hp) prev = u1[s - 1][1];
            ts[s][o] = l63 ? prev : u1[s][o];
          }
          if constexpr (e == 5) { float a = ac[s][o][0]; yy[s][o] = a + ror1(ts[s][o]); asm volatile("" : "+v"(yy[s][o])); }
          if constexpr (e == 6) {
            asm volatile("" : "+v"(qv[s % NQ][o]));
            yy[s][o] *= __uint_as_float(qv[s % NQ][o]);
            asm volatile("" : "+v"(yy[s][o]));
          }
        }
        if constexpr (a1 == 14) pk[s] = pkbf(yy[s][(T::S.om[s] & 1) ? 0 : 1], yy[s][(T::S.om[s] & 2) ? 1 : 0]);
        if constexpr (a1 == 15) *(LDSP u16*)(ywp + s * 256) = (u16)pk[s];
        if constexpr (a1 == 16) *(LDSP u16*)(ywp + s * 256 + 128) = (u16)(pk[s] >> 16);
        if constexpr (a1 == 17) set_word(pyp, (u32)(s / 2 + 1));
      }
      SCHED0();
    });
  });
  STAMP(8);
#undef RDQ
#undef RDXP
}

#define ROLE_END(n) do { STAMP_OUT(); asm volatile(".Lfk_end" #n ":"); return; } while (0)

__global__ __launch_bounds__(768, 3) void fused_kernel(Params P) {
  extern __shared__ __attribute__((aligned(16))) char smem[];
  const lptr sm = (lptr)smem;
  const u32 smb = (u32)(uintptr_t)sm;
  const int tid = threadIdx.x;
  const int w = __builtin_amdgcn_readfirstlane(tid >> 6);
  const int lane = tid & 63;
  const int wg = blockIdx.x;
  const int xcd = wg & 7, idx = wg >> 3;
  const bool partA = idx >= 20;
  const int g = lane >> 4, li = lane & 15, lq = li >> 2, lp = li & 3;
  STAMP(0);
  if (w == 11) {
    unsigned long long pc;
    u32 len;
    asm volatile("s_getpc_b64 %0\n.Lfk_code_begin:\n\ts_mov_b32 %1, .Lfk_end0-.Lfk_code_begin"
                 "\n\ts_max_i32 %1, %1, .Lfk_end1-.Lfk_code_begin\n\ts_max_i32 %1, %1, .Lfk_end2-.Lfk_code_begin"
                 "\n\ts_max_i32 %1, %1, .Lfk_end3-.Lfk_code_begin\n\ts_max_i32 %1, %1, .Lfk_end4-.Lfk_code_begin"
                 "\n\ts_max_i32 %1, %1, .Lfk_end5-.Lfk_code_begin" : "=s"(pc), "=&s"(len) :: "scc");
    const u32 off = min((u32)(lane + 64 * (idx & 3)) * 128u, len & ~3u);
    touch64((const char*)pc + off, smb + PFD);
  }
#define gflag ((gu64*)(uintptr_t)(P.ex + WS_FLAG))
#define gepoch ((gu64*)(uintptr_t)(P.ex + WS_EPOCH))
#define rx __builtin_amdgcn_make_buffer_rsrc(P.ex + WS_EXX, 0, 6u << 20, 0x00020000)
  if (idx >= 30) {
    if (w < 2) { delay(PFP); touch64((const char*)P.u + prefetch_off(w, idx, xcd, lane), smb + PFD); }
    if (w == 8 && wg == 240) {
      const unsigned long long tag = __hip_atomic_load(gepoch, RLX_AGENT) + 1;
      delay(40);
#pragma unroll 1
      for (int spin = 0; spin < (1 << 16); ++spin) {
        bool ok = true;
#pragma unroll
        for (int j = 0; j < 5; ++j) { const int f = lane + 64 * j; ok &= (__hip_atomic_load(gflag + FSP * ((f >> 5) * 128 + (f & 31)), RLX_AGENT) == tag); }
        if (__all(ok)) break;
        __builtin_amdgcn_s_sleep(64);
      }
      if (lane == 0) __hip_atomic_store(gepoch, tag, RLX_AGENT);
    }
    vmcnt<0>();
    asm volatile(".Lfk_end1:");
    return;
  }
  if (tid < 80) *(LDSP u32*)(sm + FLAGS + 4 * tid) = 0;

  const int gi = partA ? xcd * 10 + (idx - 20) : xcd * 20 + idx;

  auto poll_exchange = [&](int col, int first, int nst, int word) {
    wait_words(sm + FLAG3, lane, 1, 1);
    const unsigned long long tag = *(volatile LDSP unsigned long long*)(sm + FLAG3 + 8);
    bool ok = lane >= 8 * nst || lane < 8 * first;
#pragma unroll 1
    for (int spin = 0; spin < (1 << 16); ++spin) {
      if (!ok) ok = (__hip_atomic_load(gflag + FSP * (col * 32 + lane), RLX_AGENT) == tag);
      const unsigned long long miss = __ballot(!ok);
      const int done = miss ? (__builtin_ctzll(miss) >> 3) : nst;
      if (done > first) set_word(sm + FLAG3 + word, (u32)done);
      if (!miss) break;
      __builtin_amdgcn_s_sleep(PSL);
    }
  };

  if (w >= (partA ? 4 : 8)) {
    const int h = w & 3, pg = (w < 8) ? 1 : 0;
    const int un = partA ? (wg - 160) * 2 + pg : wg;
    const int pcol = un % 10, pchunk = un / 10 + (partA ? 16 : 0);
    const u32 praw = partA ? A_PRAW + pg * PUNIT : B_PRAW;
    const u32 shk = SHK + pg * 512;
    const lptr fw = sm + FLAG2 + pg * 32;
    asm volatile("" :: "s"(P.k), "s"(P.v), "s"(P.q), "s"(P.shift_k), "s"(P.ex), "s"(P.u), "s"(P.ssm_k), "s"(P.out));
    __builtin_amdgcn_s_setprio(3);
    unsigned long long tag = 0;
    {
      const u32 po = (u32)(64 * pchunk + 16 * h + (lane >> 3)) * ROWB + (u32)pcol * 128 + 16 * (lane & 7);
#pragma unroll
      for (int j = 0; j < 2; ++j) {
        dma16(P.k, po + j * 8 * ROWB, smb + praw + 512 + h * 2048 + j * 1024);
        dma16(P.v, po + j * 8 * ROWB, smb + praw + 8704 + h * 2048 + j * 1024);
        dma16(P.q, po + j * 8 * ROWB, smb + praw + 16896 + h * 2048 + j * 1024);
      }
      if (h == 0 && lane < 32) {
        if (pchunk) dma16(P.k, po - 4 * ROWB, smb + praw);
        else *(LDSP u32x4*)(sm + praw + 16 * lane) = u32x4{0, 0, 0, 0};
      }
      if (h == 1 && lane < 32) dma16(P.shift_k, (u32)pcol * 512 + 16 * lane, smb + shk);
    }
    STAMP(7);
    wgbarrier();
    if (!partA) {
      if (lane < 10) *(LDSP u32x4*)(sm + XIMG + h * XS + 16 * lane) = u32x4{0, 0, 0, 0};
    }
    if (w == 8) {
      *(LDSP unsigned long long*)(sm + FLAG3 + 8) = __hip_atomic_load(gepoch, RLX_AGENT) + 1;
      set_word(sm + FLAG3, 1);
    }
    const u32 co = praw + 32 * h + 8 * lp + 512 * g;
    const u32 stg = partA ? A_PSTG + (w - 4) * YS : B_PSTG + h * XS;
    u32 ka1 = co + 128 * (lq + 1);
    u32 ka2 = co + 128 * ((lq < 2) ? lq + 5 : lq);
    u32 ka3 = co + 128 * lq;
    u32 so[4];
#pragma unroll
    for (int u = 0; u < 4; ++u) so[u] = stg + li * 128 + 8 * (g & 1) + 16 * (((2 * u + (g >> 1)) ^ li) & 7);
    u32 sr_ = stg + (lane >> 3) * 128 + 16 * ((lane ^ (lane >> 3)) & 7);
    u32 eo = (u32)(pcol * 64 + 16 * h + (lane >> 3)) * 4096 + 128 * pchunk + 16 * (lane & 7);
    u32 swa = shk + 8 * (16 * h + li);
    asm volatile("" : "+v"(ka1), "+v"(ka2), "+v"(ka3), "+v"(so[0]), "+v"(so[1]), "+v"(so[2]), "+v"(so[3]), "+v"(sr_), "+v"(eo), "+v"(swa));
    vmcnt<0>();
    set_word(fw + 4 * h, 1);
    wait_words(fw, lane, 4, 1);
    STAMP(12);
    {
      const u32x2 sw = *(LDSP u32x2*)(sm + swa);
      u32x2 r1[4], r2[4], r3[4], vv[4], qq[4];
#pragma unroll
      for (int u = 0; u < 4; ++u) {
        r1[u] = trread(sm + ka1 + 2048 * u);
        r2[u] = trread(sm + ka2 + 2048 * u);
        r3[u] = trread(sm + ka3 + 2048 * u + 512);
        vv[u] = trread(sm + ka3 + 2048 * u + 8704);
        qq[u] = trread(sm + ka3 + 2048 * u + 16896);
      }
      const u32 W32 = (sw.y >> 16) | (sw.y << 16), W10 = (sw.x >> 16) | (sw.x << 16);
#pragma unroll
      for (int u = 0; u < 4; ++u) {
        const float s0 = dot2(r1[u].y, W10, dot2(r1[u].x, W32, 0.f));
        const float s1 = dot2(r3[u].x, W10, dot2(r2[u].y, W32, 0.f));
        const float s2 = dot2(r2[u].x, W10, dot2(r1[u].y, W32, 0.f));
        const float s3 = dot2(r3[u].y, W10, dot2(r3[u].x, W32, 0.f));
        *(LDSP u32x2*)(sm + so[u]) = u32x2{pkbf(s0 * bflo(vv[u].x), s1 * bfhi(vv[u].x)), pkbf(s2 * bflo(vv[u].y), s3 * bfhi(vv[u].y))};
        *(LDSP u32x2*)(sm + so[u] + 2048) = qq[u];
      }
      wavefence();
      STAMP(6);
#pragma unroll
      for (int j = 0; j < 4; ++j)
        __builtin_amdgcn_raw_buffer_store_b128(*(const LDSP u32x4*)(sm + sr_ + 1024 * j), rx, eo + (j & 1) * 8 * 4096 + (j >> 1) * (u32)(WS_EXQ - WS_EXX), 0, 16);
    }
    STAMP(13);
    const u32 urow = (u32)(64 * h + lane) * ROWB + (u32)((gi * 4) & ~7) * 2;
    set_word(fw + 16 + 4 * h, 1);
    wait_words(sm + FLAG3, lane, 1, 1);
    tag = *(volatile LDSP unsigned long long*)(sm + FLAG3 + 8);
    vmcnt<0>();
    if (lane == 0) __hip_atomic_store(gflag + FSP * ((pcol * 4 + h) * 32 + pchunk), tag, RLX_AGENT);
    if (!partA) {
      dma16(P.u, urow, smb + RAWU + 1024 * h);
      dma16(P.u, urow + 256 * ROWB, smb + RAWU + 4096 + 1024 * h);
      wait_words(fw + 16, lane, 4, 1);
    }
    STAMP(9);
    if (w >= 8) {
      if (partA) {
        wait_words(sm + FLAG2 + 16, lane, 4, 1);
        wait_words(sm + FLAG2 + 48, lane, 4, 1);
#pragma unroll 1
        for (int st = 1; st < 3; ++st) {
          if (h == 0) poll_exchange(gi >> 1, st, st + 1, 4); else wait_words(sm + FLAG3 + 4, lane, 1, (u32)(st + 1));
          __builtin_amdgcn_fence(__ATOMIC_ACQUIRE, "wavefront");
          u32x4 d[4];
#pragma unroll
          for (int j = 0; j < 4; ++j)
            d[j] = __builtin_amdgcn_raw_buffer_load_b128(rx, (u32)(gi * 8 + 2 * h + (j & 1)) * 4096 + 1024 * st + 16 * lane + (j >> 1) * (u32)(WS_EXQ - WS_EXX), 0, 16);
#pragma unroll
          for (int j = 0; j < 4; ++j)
            *(LDSP u32x4*)(sm + ((j >> 1) ? YIMG + (2 * h + (j & 1)) * YS : XIMG + (2 * h + (j & 1)) * XS + XPADB) + 1024 * st + 16 * lane) = d[j];
          set_word(sm + FLAGS + 4 * h, (u32)(2 * st + 2));
          if (st == 1) STAMP(5); if (st == 2) STAMP(14);
        }
        const u32 yrd = YIMG + lq * YS + 2 * (64 * h + 16 * g + 4 * lp);
        u16* const outp = P.out + gi * 8 + (size_t)(64 * h + lane) * (2 * DCH);
#pragma unroll 1
        for (int b = 0; b < (h < 3 ? 6 : 5); ++b) {
          wait_words(sm + FLAGS + 16, lane, 8, (u32)(b + 1));
          u32x2 a = trread(sm + yrd + 512 * b);
          u32x2 c = trread(sm + yrd + 512 * b + 4 * YS);
          *(u32x4*)(outp + (size_t)b * 256 * (2 * DCH)) = u32x4{a.x, a.y, c.x, c.y};
        }
        ROLE_END(2);
      }
      const u32 ua = RAWU + 1024 * h + 16 * (16 * g + 4 * lp + lq) + 2 * ((gi * 4) & 4);
      const u32 xw = XIMG + (lane & 3) * XS + XPADB + 8 * (lane >> 2) + 128 * h;
      const u32 yrd = B_YIMG + lq * YS + 2 * (64 * h + 16 * g + 4 * lp);
      const u32 hrd = H_YIMG + lq * HYS + 2 * (64 * h + 16 * g + 4 * lp - 1408);
      u16* const outp = P.out + gi * 4 + (size_t)(64 * h + lane) * (2 * DCH);
#pragma unroll 1
      for (int it = 0; it < 8; ++it) {
        int i = it;
        asm volatile("" : "+s"(i));
        if (i + 2 < 8) dma16(P.u, urow + (u32)(i + 2) * (256 * ROWB), smb + RAWU + 4096 * (i + 2) + 1024 * h);
        if (i < 6) vmcnt<2>(); else if (i == 6) vmcnt<1>(); else vmcnt<0>();
        *(LDSP u32x2*)(sm + xw + 512 * i) = trread(sm + ua + 4096 * i);
        set_word(sm + FLAGS + 4 * h, (u32)(i + 1));
      }
      STAMP(14);
#pragma unroll 1
      for (int i = 0; i < 8; ++i) {
        wait_words(sm + FLAGS + 16, lane, 4, (u32)(i + 1));
        *(u32x2*)(outp + DCH + (size_t)i * 256 * (2 * DCH)) = trread(sm + yrd + 512 * i);
      }
#pragma unroll 1
      for (int i = (h == 3 ? 5 : 6); i < 8; ++i) {
        wait_words(sm + FLAGS + 32, lane, 4, (u32)(i + 1));
        *(u32x2*)(outp + (size_t)i * 256 * (2 * DCH)) = trread(sm + hrd + 512 * i);
      }
      ROLE_END(3);
    }
    __builtin_amdgcn_s_setprio(0);
  } else if (partA) {
    wgbarrier();
    delay(PFP);
    const int pc = (idx - 20) * 4 + w;
    touch64((const char*)P.u + prefetch_off(pc / 30, pc % 30, xcd, lane), smb + PFD);
    if (pc < 20) touch64((const char*)P.u + prefetch_off(1, 10 + pc, xcd, lane), smb + PFD);
  }

  if (partA) {
    const int ch = gi * 8 + w;
    const u32 kimg = KIMG + w * KS, ximg = XIMG + w * XS, yimg = YIMG + w * YS;
    if (lane < 10) *(LDSP u32x4*)(sm + ximg + 16 * lane) = u32x4{0, 0, 0, 0};
    if (lane < 6) *(LDSP u32x4*)(sm + kimg + 16 * lane) = u32x4{0, 0, 0, 0};
    const float dsk = ld_bf_uniform(P.ssm_D, ch);
    wait_words(sm + FLAG2, lane, 4, 1);
#pragma unroll
    for (int j = 0; j < 3; ++j) dma16(P.ssm_k, (u32)ch * (SEQ * 2) + 1024 * j + 16 * lane, smb + kimg + KPADB + 1024 * j);
    vmcnt<0>();
    if (lane == 0) {
      LDSP u16* k0 = (LDSP u16*)(sm + kimg + KPADB);
      *k0 = (u16)pkbf(bf2f(*k0) + dsk, 0.f);
    }
    if (w == 0) poll_exchange(gi >> 1, 0, 1, 16); else wait_words(sm + FLAG3 + 16, lane, 1, 1);
    __builtin_amdgcn_fence(__ATOMIC_ACQUIRE, "wavefront");
    STAMP(2);
    {
      const u32x4 dx = __builtin_amdgcn_raw_buffer_load_b128(rx, (u32)ch * 4096 + 16 * lane, 0, 16);
      const u32x4 dq = __builtin_amdgcn_raw_buffer_load_b128(rx, (u32)ch * 4096 + 16 * lane + (u32)(WS_EXQ - WS_EXX), 0, 16);
      wait_words(sm + FLAG2 + 16 + (w < 4 ? 32 : 0), lane, 4, 1);
      *(LDSP u32x4*)(sm + ximg + XPADB + 16 * lane) = dx;
      *(LDSP u32x4*)(sm + yimg + 16 * lane) = dq;
    }
    wavefence();
    conv_stream<1>(sm, kimg, ximg, sm + yimg + 2 * lane, sm + FLAGS + 16 + 4 * w, lane STV_PASS);
    ROLE_END(4);
  }

  {
    const int ch0 = gi * 4;
    const int cw = w & 3, ps = w >> 2;
    const int ch = ch0 + cw;
    const size_t o = (size_t)ch * NST + 64 * ps + lane;
    const u16 i_ar = P.A_re[o], i_ai = P.A_im[o], i_br = P.B_re[o], i_bi = P.B_im[o], i_cr = P.C_re[o], i_ci = P.C_im[o];
    const float ldt = ld_bf_uniform(P.log_dt, ch);
    const float dsk = ld_bf_uniform(P.Dskip, ch);
    wgbarrier();
    const float dt = rbf(expf(ldt));
    const float zr = dt * bf2f(i_ar), zi = dt * bf2f(i_ai);
    const cf dB = {dt * bf2f(i_br), dt * bf2f(i_bi)};
    const cf C = {bf2f(i_cr), bf2f(i_ci)};
    const cf W = cmul(C, dB);
    float mx = fmaxf(fabsf(W.r), fabsf(W.i));
    STAMP(1);
    {
      int m = __float_as_int(mx);
      m = max(m, __builtin_amdgcn_update_dpp(0, m, 0x111, 0xf, 0xf, false));
      m = max(m, __builtin_amdgcn_update_dpp(0, m, 0x112, 0xf, 0xf, false));
      m = max(m, __builtin_amdgcn_update_dpp(0, m, 0x114, 0xf, 0xf, false));
      m = max(m, __builtin_amdgcn_update_dpp(0, m, 0x118, 0xf, 0xf, false));
      int a0 = __builtin_amdgcn_readlane(m, 15), a1 = __builtin_amdgcn_readlane(m, 31);
      int a2 = __builtin_amdgcn_readlane(m, 47), a3 = __builtin_amdgcn_readlane(m, 63);
      mx = __int_as_float(max(max(a0, a1), max(a2, a3)));
    }
    int ex = (int)((__float_as_uint(mx) >> 23) & 0xff);
    ex = min(max(ex, 30), 240);
    const float sY = __uint_as_float((u32)(267 - ex) << 23);
    const float so = __uint_as_float((u32)(ex - 27) << 23);
    const float SX = 16384.f;

    f32x4 ak[3];
    const u32 stg = STG + w * STS;
    const u32 rb = stg + li * 272 + g * 16;
    const u32 m0v = smb + w * STS;
#define STGW(row, val_r, val_i) \
    asm volatile("ds_write_addtid_b32 %0 offset:%1" :: "v"(pkh((val_r), (val_i))), "i"(STG + (row) * 272) : "memory")
    {
      const cf E1 = cexp_pow2(zr, zi, 1.f);
      const cf E8c = cexp_pow2(zr, -zi, 8.f);
      const cf E128 = cexp_pow2(zr, zi, 128.f);
      const cf E1024 = cexp_pow2(zr, zi, 1024.f);
      const cf E2 = cmul(E1, E1);
      const cf E4 = cmul(E2, E2);
      u32 keep;
      asm volatile("s_mov_b32 %0, m0\n\ts_mov_b32 m0, %1\n\ts_nop 0" : "=&s"(keep) : "s"(m0v) : "memory");
      cf y = {W.r * sY, -(W.i * sY)};
      cf P1 = {E128.r * SX, E128.i * SX};
      cf v0 = {SX, 0.f}, v1 = P1;
#pragma unroll
      for (int b = 0; b < 8; ++b) {
        STGW(2 * b, y.r, y.i); y = cmul(y, E8c);
        STGW(16 + b, v0.r, v0.i); if (b < 7) v0 = cmul(v0, E1);
        STGW(2 * b + 1, y.r, y.i); if (b < 7) y = cmul(y, E8c);
        STGW(24 + b, v1.r, v1.i); if (b < 7) v1 = cmul(v1, E1);
      }
      f16x8 yf[4];
      const f32x4 zero = {0.f, 0.f, 0.f, 0.f};
#pragma unroll
      for (int s = 0; s < 4; ++s) {
        yf[s] = *(const LDSP f16x8*)(sm + rb + 64 * s);
        f16x8 xf = *(const LDSP f16x8*)(sm + rb + 16 * 272 + 64 * s);
        ak[0] = __builtin_amdgcn_mfma_f32_16x16x32_f16(xf, yf[s], s ? ak[0] : zero, 0, 0, 0);
      }
      asm volatile("" ::: "memory");
      cf Pa = cmul(P1, E128);
      cf Pb = {E1024.r * SX, E1024.i * SX};
#pragma unroll
      for (int m = 0; m < 2; ++m) {
        cf v = Pa;
#pragma unroll
        for (int r = 0; r < 4; ++r) {
          STGW(16 + 4 * m + r, v.r, v.i); if (r < 3) v = cmul(v, E2);
          if (r & 1) { STGW(4 * (2 * m + (r >> 1)) + 2, Pb.r, Pb.i); Pb = cmul(Pb, E128); }
        }
        Pa = cmul(Pa, E128);
      }
#pragma unroll
      for (int m = 0; m < 4; ++m) {
        STGW(4 * m, Pa.r, Pa.i);
        cf v = cmul(Pa, E4);
        STGW(4 * m + 3, Pb.r, Pb.i); Pb = cmul(Pb, E128);
        STGW(4 * m + 1, v.r, v.i);
        if (m < 3) Pa = cmul(Pa, E128);
      }
      STGW(24, Pb.r, Pb.i);
      asm volatile("s_mov_b32 m0, %0" :: "s"(keep) : "memory");
#pragma unroll
      for (int s = 0; s < 4; ++s) {
        f16x8 x1 = *(const LDSP f16x8*)(sm + rb + 64 * s);
        f16x8 x2 = *(const LDSP f16x8*)(sm + rb + 16 * 272 + 64 * s);
        ak[1] = __builtin_amdgcn_mfma_f32_16x16x32_f16(x1, yf[s], s ? ak[1] : zero, 0, 0, 0);
        ak[2] = __builtin_amdgcn_mfma_f32_16x16x32_f16(x2, yf[s], s ? ak[2] : zero, 0, 0, 0);
      }
    }
#undef STGW
#pragma unroll
    for (int i = 0; i < 3; ++i) ak[i] *= so;
    STAMP(2);
    asm volatile("" ::: "memory");
    const u32 kimg = B_KIMG + cw * KS;
    if (ps) {
#pragma unroll
      for (int i = 0; i < 3; ++i) *(LDSP f32x4*)(sm + kimg + 1024 * i + 16 * lane) = ak[i];
      set_word(sm + FLAGS + 48 + 4 * cw, 1);
      const u32 hx = stg, hk = stg + XS, hy = H_YIMG + cw * HYS;
      asm volatile("s_waitcnt lgkmcnt(0)" ::: "memory");
#pragma unroll
      for (int j = 0; j < 4; ++j) dma16(P.ssm_k, (u32)ch * (SEQ * 2) + 1024 * j + 16 * lane, smb + hk + KPADB + 1024 * j);
      if (lane < 10) *(LDSP u32x4*)(sm + hx + 16 * lane) = u32x4{0, 0, 0, 0};
      if (lane < 6) *(LDSP u32x4*)(sm + hk + 16 * lane) = u32x4{0, 0, 0, 0};
      const float dh = ld_bf_uniform(P.ssm_D, ch);
      if (w == 4) poll_exchange(gi >> 2, 0, 4, 4); else wait_words(sm + FLAG3 + 4, lane, 1, 4);
      __builtin_amdgcn_fence(__ATOMIC_ACQUIRE, "wavefront");
      STAMP(5);
      {
        u32x4 d[6];
#pragma unroll
        for (int j = 0; j < 4; ++j) d[j] = __builtin_amdgcn_raw_buffer_load_b128(rx, (u32)ch * 4096 + 1024 * j + 16 * lane, 0, 16);
#pragma unroll
        for (int j = 0; j < 2; ++j)
          d[4 + j] = __builtin_amdgcn_raw_buffer_load_b128(rx, (u32)ch * 4096 + 2816 + 256 * j + 16 * lane + (u32)(WS_EXQ - WS_EXX), 0, 16);
        vmcnt<0>();
        if (lane == 0) {
          LDSP u16* k0 = (LDSP u16*)(sm + hk + KPADB);
          *k0 = (u16)pkbf(bf2f(*k0) + dh, 0.f);
        }
#pragma unroll
        for (int j = 0; j < 4; ++j) *(LDSP u32x4*)(sm + hx + XPADB + 1024 * j + 16 * lane) = d[j];
#pragma unroll
        for (int j = 0; j < 2; ++j) *(LDSP u32x4*)(sm + hy + 256 * j + 16 * lane) = d[4 + j];
      }
      wavefence();
      conv_stream<2>(sm, hk, hx, sm + hy + 2 * lane - 11 * 256, sm + FLAGS + 16 + 4 * w, lane STV_PASS);
      ROLE_END(5);
    }
    wait_words(sm + FLAGS + 48 + 4 * cw, lane, 1, 1);
    {
#pragma unroll
      for (int i = 0; i < 3; ++i) ak[i] += *(const LDSP f32x4*)(sm + kimg + 1024 * i + 16 * lane);
      wavefence();
      auto nxt = [&](float x, float e, bool lastl) {
        float r = __int_as_float(__builtin_amdgcn_update_dpp(0, __float_as_int(x), 0x130, 0xf, 0xf, true));
        return lastl ? e : r;
      };
      auto lane_of = [&](float x, int l) { return __int_as_float(__builtin_amdgcn_readlane(__float_as_int(x), l)); };
      const lptr kd = sm + kimg + KPADB;
      if (lane < 6) *(LDSP u32x4*)(sm + kimg + 16 * lane) = u32x4{0, 0, 0, 0};
      {
        const float a0 = ak[0][0] + (lane == 0 ? dsk : 0.f), a1 = ak[0][1], a2 = ak[0][2], a3 = ak[0][3];
        *(LDSP u32x2*)(kd + 2 * (128 * (g >> 1) + 8 * li + 4 * (g & 1))) = u32x2{pkbf(a0, a1), pkbf(a2, a3)};
      }
      const float s20 = ak[1][0], s21 = ak[1][1], s3a = ak[1][2], s3b = ak[1][3];
      const float c0 = ak[2][0], c1 = ak[2][1], c2 = ak[2][2], c3 = ak[2][3];
      const bool l63 = lane == 63;
      {
        const float n = nxt(c0, lane_of(s20, 0), lane == 31);
        if (lane < 32) *(LDSP u32x4*)(kd + 16 * (32 + lane)) = u32x4{pkbf(c0, c0), pkbf(c1, c2), pkbf(c2, c2), pkbf(c3, n)};
      }
      {
        const float n = nxt(s20, lane_of(s3a, 0), l63);
        *(LDSP u32x4*)(kd + 16 * (64 + lane)) = u32x4{pkbf(s20, s20), pkbf(s20, s21), pkbf(s21, s21), pkbf(n, n)};
      }
      {
        const float na = nxt(s3a, lane_of(s3b, 0), l63), nb = nxt(s3b, lane_of(c0, 32), l63);
        const u32 a = pkbf(s3a, s3a), b = pkbf(s3b, s3b);
        *(LDSP u32x4*)(kd + 16 * (128 + lane)) = u32x4{a, a, pkbf((lane & 1) ? na : s3a, na), pkbf(na, na)};
        *(LDSP u32x4*)(kd + 16 * (192 + lane)) = u32x4{b, b, pkbf((lane & 1) ? nb : s3b, nb), pkbf(nb, nb)};
      }
    }
    wavefence();
    STAMP(3);
    conv_stream<0>(sm, kimg, XIMG + cw * XS, sm + B_YIMG + cw * YS + 2 * lane, sm + FLAGS + 16 + 4 * w, lane STV_PASS);
    ROLE_END(0);
  }
}

extern "C" void kernel_launch(void* const* d_in, const int* in_sizes, int n_in,
                              void* d_out, int out_size, void* d_ws, size_t ws_size,
                              hipStream_t stream) {
  Params P;
  P.q = (const u16*)d_in[0];
  P.k = (const u16*)d_in[1];
  P.v = (const u16*)d_in[2];
  P.shift_k = (const u16*)d_in[3];
  P.ssm_k = (const u16*)d_in[4];
  P.ssm_D = (const u16*)d_in[5];
  P.u = (const u16*)d_in[6];
  P.A_re = (const u16*)d_in[7];
  P.A_im = (const u16*)d_in[8];
  P.B_re = (const u16*)d_in[9];
  P.B_im = (const u16*)d_in[10];
  P.C_re = (const u16*)d_in[11];
  P.C_im = (const u16*)d_in[12];
  P.log_dt = (const u16*)d_in[13];
  P.Dskip = (const u16*)d_in[14];
  P.out = (u16*)d_out;
  P.ws = (unsigned long long*)d_ws;
  P.ex = (char*)d_ws + (16 << 20);
  (void)hipFuncSetAttribute((const void*)fused_kernel, hipFuncAttributeMaxDynamicSharedMemorySize, LDS_BYTES);
  fused_kernel<<<256, 768, LDS_BYTES, stream>>>(P);
}
```

```cpp
#include <hip/hip_runtime.h>
#include <hip/hip_bf16.h>
#include <stdint.h>

typedef __attribute__((ext_vector_type(4))) short s16x4;
typedef __attribute__((ext_vector_type(8))) _Float16 f16x8;
typedef __attribute__((ext_vector_type(8))) __bf16 bf16x8;
typedef __attribute__((ext_vector_type(2))) __bf16 bf16x2;
typedef __attribute__((ext_vector_type(2))) _Float16 f16x2;
typedef __attribute__((ext_vector_type(2))) float f32x2;
typedef __attribute__((ext_vector_type(4))) float f32x4;
typedef __attribute__((ext_vector_type(2))) unsigned int u32x2;
typedef __attribute__((ext_vector_type(4))) unsigned int u32x4;
typedef unsigned int u32;
typedef unsigned short u16;
#define LDSP __attribute__((address_space(3)))
#define DEV __device__ __forceinline__

constexpr int SEQ = 2048;
constexpr int DCH = 640;
constexpr int NST = 128;
constexpr int ROWB = DCH * 2;

constexpr int XS = 4272;
constexpr int XPADB = 160;
constexpr int YS = 4160;
constexpr int KS = 4224;
constexpr int KPADB = 96;
constexpr int FLAGS = 0;
constexpr int FLAG2 = 192;
constexpr int FLAG3 = 256;
constexpr int PFD = 320;
constexpr int SHK = 1024;
constexpr int XIMG = 2048;
constexpr int PUNIT = 8704 + 8192 + 8192;
constexpr int YIMG = 36224;
constexpr int KIMG = 69504;
constexpr int A_PRAW = 103296;
constexpr int A_PSTG = YIMG;
constexpr int STS = 8704;
constexpr int STG = 19200;
constexpr int B_KIMG = 88832;
constexpr int B_YIMG = 105728;
constexpr int HYS = 1344;
constexpr int H_YIMG = 122368;
constexpr int RAWU = 127744;
constexpr int B_PRAW = RAWU + 8192;
constexpr int B_PSTG = XIMG + XPADB;
constexpr int LDS_BYTES = 161024;

constexpr size_t WS_EPOCH = 0;
constexpr size_t WS_FLAG = 4096;
constexpr size_t WS_EXX = 1 << 20;
constexpr size_t WS_EXQ = 4 << 20;

DEV float bf2f(u16 v) { return __uint_as_float(((u32)v) << 16); }
DEV float bflo(u32 v) { return __uint_as_float(v << 16); }
DEV float bfhi(u32 v) { return __uint_as_float(v & 0xffff0000u); }
DEV u32 pkbf(float a, float b) {
  f32x2 t = {a, b};
  bf16x2 r = __builtin_convertvector(t, bf16x2);
  return __builtin_bit_cast(u32, r);
}
DEV u32 pkh(float a, float b) {
  f32x2 t = {a, b};
  f16x2 r = __builtin_convertvector(t, f16x2);
  return __builtin_bit_cast(u32, r);
}
DEV float rbf(float a) { return bflo(pkbf(a, a)); }
DEV float dot2(u32 a, u32 b, float c) {
  return __builtin_amdgcn_fdot2_f32_bf16(__builtin_bit_cast(bf16x2, a), __builtin_bit_cast(bf16x2, b), c, false);
}
typedef __attribute__((address_space(1))) unsigned long long gu64;
#define RLX_AGENT __ATOMIC_RELAXED, __HIP_MEMORY_SCOPE_AGENT
typedef LDSP char* lptr;
DEV u32x2 trread(lptr p) {
  s16x4 r = __builtin_amdgcn_ds_read_tr16_b64_v4i16((LDSP s16x4*)p);
  return __builtin_bit_cast(u32x2, r);
}
DEV bf16x8 frag(u32x2 a, u32x2 b) {
  u32x4 f = {a.x, a.y, b.x, b.y};
  return __builtin_bit_cast(bf16x8, f);
}
DEV float ror1(float v) {
  int r = __builtin_amdgcn_update_dpp(0, __float_as_int(v), 0x13C, 0xf, 0xf, false);
  return __int_as_float(r);
}
DEV void wavefence() { __builtin_amdgcn_fence(__ATOMIC_ACQ_REL, "wavefront"); __builtin_amdgcn_wave_barrier(); }
DEV void wgbarrier() {
  asm volatile("s_waitcnt lgkmcnt(0)" ::: "memory");
  __builtin_amdgcn_s_barrier();
  asm volatile("" ::: "memory");
}
DEV void dma16(const void* base, u32 voff, u32 ldsaddr) {
  u32 keep;
  ldsaddr = __builtin_amdgcn_readfirstlane(ldsaddr);
  const unsigned long long b64 = (unsigned long long)(uintptr_t)base;
  const unsigned long long sb = ((unsigned long long)(u32)__builtin_amdgcn_readfirstlane((u32)(b64 >> 32)) << 32) |
                                (u32)__builtin_amdgcn_readfirstlane((u32)b64);
  asm volatile("s_mov_b32 %0, m0\n\ts_mov_b32 m0, %3\n\ts_nop 2\n\tglobal_load_lds_dwordx4 %1, %2\n\ts_mov_b32 m0, %0"
               : "=&s"(keep) : "v"(voff), "s"(sb), "s"(ldsaddr) : "memory");
}
DEV void touch64(const void* p, u32 ldsaddr) {
  u32 keep;
  asm volatile("s_mov_b32 %0, m0\n\ts_mov_b32 m0, %2\n\ts_nop 0\n\tglobal_load_lds_dword %1, off\n\ts_mov_b32 m0, %0"
               : "=&s"(keep) : "v"(p), "s"(ldsaddr) : "memory");
}
template <int N> DEV void vmcnt() { asm volatile("s_waitcnt vmcnt(%0)" :: "i"(N) : "memory"); }
#define SCHED0() __builtin_amdgcn_sched_barrier(0)

template <int V> struct IC { static constexpr int value = V; };
template <int I, int N, class F> DEV void static_for(F&& f) {
  if constexpr (I < N) { f(IC<I>{}); static_for<I + 1, N>(f); }
}

struct cf { float r, i; };
DEV cf cmul(cf a, cf b) {
  cf o;
  o.r = fmaf(a.r, b.r, -(a.i * b.i));
  o.i = fmaf(a.r, b.i, a.i * b.r);
  return o;
}
DEV cf cexp_pow2(float zr, float zi, float x) {
  float mag = __builtin_amdgcn_exp2f((zr * x) * 1.44269504088896340736f);
  float th = zi * x;
  const float C_HI = 0.15915494f;
  const float C_LO = 6.4206382e-09f;
  float f = fmaf(th, C_LO, fmaf(th, C_HI, -rintf(th * C_HI)));
  cf o;
  o.r = mag * __builtin_amdgcn_cosf(f);
  o.i = mag * __builtin_amdgcn_sinf(f);
  return o;
}

struct Params {
  const u16 *k, *v, *q, *shift_k; char* ex; const u16 *u, *ssm_k; u16* out;
  const u16 *A_re, *A_im, *B_re, *B_im, *C_re, *C_im, *log_dt, *Dskip;
  const u16* ssm_D;
};

DEV float ld_bf_uniform(const u16* p, int idx) {
  u32 d = *(const __attribute__((address_space(4))) u32*)(uintptr_t)(p + (idx & ~1));
  return (idx & 1) ? bfhi(d) : bflo(d);
}

DEV u32 prefetch_off(int half, int slice, int xcd, int lane) {
  const u32 row = (u32)(half * 4 + (lane >> 4)) * 256u + (u32)slice * 8u + ((lane >> 1) & 7);
  return row * ROWB + (u32)(((160 * xcd) >> 7) + (lane & 1)) * 128u;
}
DEV void wait_words(lptr p, int lane, int n, u32 v) {
#pragma unroll 1
  for (int spin = 0; spin < (1 << 22); ++spin) {
    u32 x = *(volatile LDSP u32*)(p + 4 * (lane & (n - 1)));
    if (__all(x >= v)) break;
    __builtin_amdgcn_s_sleep(1);
  }
  asm volatile("" ::: "memory");
}
constexpr int PFP = 3;
constexpr int FSP = 16;
constexpr int PSL = 6;
DEV void delay(int n) {
  for (int i = 0; i < n; ++i) __builtin_amdgcn_s_sleep(3);
}

DEV void set_word(lptr p, u32 v) {
  asm volatile("" ::: "memory");
  *(volatile LDSP u32*)p = v;
  asm volatile("" ::: "memory");
}

struct Sched {
  int n;
  int p[272], par[272], d[272], ld[272];
  bool first[272];
  int base[16];
  int cm[16], om[16];
  int ldm[40], ldu[40], ldp[40];
  int ku[16];
  int nld, nst;
};
constexpr void add_unit(Sched& S, int p, int d, int ld, int pm, bool first) {
  for (int par = 0; par < 2; ++par)
    if ((pm >> par) & 1) {
      S.p[S.n] = p; S.par[S.n] = par; S.d[S.n] = d; S.ld[S.n] = ld; S.first[S.n] = first;
      if (S.ldu[ld] < 0) S.ldu[ld] = S.n;
      S.ldp[ld] |= 1 << par;
      if (S.ku[d] < 0) S.ku[d] = S.n;
      ++S.n;
    }
}
constexpr Sched make_sched(int mode) {
  Sched S{};
  const int last = (mode == 1) ? 11 : 15;
  S.nst = last + 1;
  int nl = 16;
  for (int l = 0; l < 40; ++l) { S.ldu[l] = -1; S.ldm[l] = l; }
  for (int d = 0; d < 16; ++d) S.ku[d] = -1;
  for (int s = 0; s <= last; ++s) {
    S.base[s] = S.n;
    S.cm[s] = (mode == 2 && s < 11) ? 0 : ((mode == 1 && s == 11) ? 1 : 3);
    S.om[s] = (mode == 2 && s == 11) ? 2 : S.cm[s];
    if (mode == 2) {
      for (int p = (s > 11 ? s : 11); p <= 15; ++p) add_unit(S, p, p - s, s, 3, s == 0);
      continue;
    }
    for (int k = 0; k < 8 && s + k <= last; ++k)
      add_unit(S, s + k, k, s, (mode == 1 && s + k == 11) ? 1 : 3, (s == 0) || (k == 7));
    int lo = -1, hi = -1, cnt = 0, half = 0;
    if (s == 6) { lo = 8; hi = 8; cnt = 1; half = 2; }
    else if (s >= 7 && s <= last - 3) { lo = (s & 1) ? s + 2 : s + 1; hi = lo + 1; cnt = hi - 7; half = (s & 1) ? 0 : 1; }
    else if (s == last - 2 || s == last - 1) { lo = last; hi = last; cnt = last - 7; half = (s == last - 2) ? 0 : 1; }
    if (cnt) {
      const int split = (cnt + 1) / 2;
      const int j0 = (half == 1) ? split : 0, j1 = (half == 0) ? split : cnt;
      for (int j = j0; j < j1; ++j) {
        const int mm = cnt - 1 - j;
        S.ldm[nl] = mm;
        for (int t = hi; t >= lo; --t)
          if (t - mm >= 8) add_unit(S, t, t - mm, nl, (mode == 1 && t == 11) ? 1 : 3, false);
        ++nl;
      }
    }
  }
  S.nld = nl;
  return S;
}
constexpr int NGAP = 272 + 40, CAP = 2, KPRE = 16;
enum { A_NONE = 0, A_X, A_K, A_Q, A_PXR, A_PXC, A_EP };
struct Acts { int n[NGAP]; int a[NGAP][4]; int end; };
constexpr int place_back(Acts& T, int latest, int code, int cap) {
  int g = latest;
  while (g > 0 && T.n[g] >= cap) --g;
  T.a[g][T.n[g]++] = code;
  return g;
}
constexpr int place_fwd(Acts& T, int earliest, int code, int cap, int nm) {
  int g = earliest;
  while (T.n[g] >= (g < nm ? cap : 4)) ++g;
  T.a[g][T.n[g]++] = code;
  if (g + 1 > T.end) T.end = g + 1;
  return g;
}
constexpr Acts make_acts(const Sched& S, int mode) {
  Acts T{};
  T.end = S.n;
  const int dense = (mode == 0) ? 200 : (mode == 1 ? 90 : 100);
  for (int l = S.nst - 1; l >= 1; --l) {
    int g = S.ldu[l] - 5;
    const int cap = S.ldu[l] >= dense && mode ? 2 : 1;
    for (int i = 3; i >= 0; --i) if ((S.ldp[l] >> (i >> 1)) & 1) g = place_back(T, g - 1, A_X | (l << 4) | (i << 12), cap);
    for (int o = 1; o >= 0; --o) if (mode && ((S.om[l] >> o) & 1)) g = place_back(T, g - 1, A_Q | (l << 4) | (o << 12), cap);
    if ((mode == 0 && !(l & 1)) || (mode == 1 && !(l & 3))) {
      g = place_back(T, g - 1, A_PXC | (l << 4), 1);
      place_back(T, g - 6, A_PXR, 1);
    }
  }
  for (int l = S.nld - 1; l >= 16; --l) {
    int g = S.ldu[l] - 5;
    for (int i = 3; i >= 0; --i) if ((S.ldp[l] >> (i >> 1)) & 1) g = place_back(T, g - 1, A_X | (l << 4) | (i << 12), S.ldu[l] >= dense ? 2 : 1);
  }
  for (int d = 0; d < 16; ++d) {
    if (S.ku[d] < KPRE) continue;
    int g = S.ku[d] - 5;
    g = place_back(T, g - 1, A_K | (d << 4) | (1 << 12), 2);
    place_back(T, g - 1, A_K | (d << 4), 2);
  }
  int prev[18] = {};
  for (int s = 0; s < S.nst; ++s) {
    if (!S.cm[s]) continue;
    int g = S.base[s] + 4;
    const int p16 = prev[16];
    for (int e = 0; e < ((s & 1) ? 18 : 17); ++e) {
      if (e < 14 && !(((e >= 8 ? S.om[s] : S.cm[s]) >> (e & 1)) & 1)) continue;
      if ((e == 15 && !(S.om[s] & 1)) || (e == 16 && !(S.om[s] & 2))) continue;
      if (mode == 0 && (e == 12 || e == 13)) continue;
      int lo = g + (((e & 1) && e < 14) ? 0 : 1);
      if (lo <= prev[e | 1]) lo = prev[e | 1] + 1;
      if (e == 17 && lo <= p16) lo = p16 + 1;
      g = place_fwd(T, lo, A_EP | (s << 4) | (e << 12), S.base[s] >= dense ? 3 : CAP, S.n);
      prev[e] = g;
    }
  }
  return T;
}
template <int MODE> struct Tab {
  static constexpr Sched S = make_sched(MODE);
  static constexpr Acts A = make_acts(S, MODE);
};
static_assert(Tab<0>::S.n == 272 && Tab<1>::S.n == 144 && Tab<2>::S.n == 140, "schedule");

#define MFMA(a, b, c) __builtin_amdgcn_mfma_f32_16x16x32_bf16((a), (b), (c), 0, 0, 0)

template <int MODE>
DEV void conv_stream(lptr sm, u32 kimg, u32 ximg, lptr ywp, lptr pyp, int lane) {
  using T = Tab<MODE>;
  const int g = lane >> 4, lq = (lane & 15) >> 2, lp = lane & 3;
  const lptr kb = sm + kimg + 8 * (4 * lp + 8 * g + lq);
  const lptr xb = sm + ximg + 224 + 8 * (lp - 8 * g - lq);
  const bool l63 = (lane == 63);
  const lptr pxp = sm + FLAGS + 4 * (lane & 3);
  const u32 qa = (u32)(uintptr_t)ywp;
  u32x2 kr[16][2], xr[40][4];
  f32x4 ac[16][2];
  const f32x4 zero = {0.f, 0.f, 0.f, 0.f};
  float ts[16][2], u2[16][2], u1[16][2], yy[16][2];
  u32 pk[16];
  constexpr int NQ = MODE == 0 ? 3 : 5;
  u32 qv[NQ][2], px = 0;
#pragma unroll
  for (int i = 0; i < NQ; ++i) { qv[i][0] = 0; qv[i][1] = 0; }
#define RDQ(s_, i_) asm volatile("ds_read_u16_d16_hi %0, %1 offset:%2" : "+v"(qv[(s_) % NQ][i_]) : "v"(qa), "i"((s_) * 256 + 128 * (i_)) : "memory")
#define RDXP(l_, i_) xr[l_][i_] = trread(xb + 256 * T::S.ldm[l_] + ((i_) >> 1) * 128 + (((i_) & 1) ? 0 : 32))
  static_for<0, 16>([&](auto D_) {
    constexpr int d = decltype(D_)::value;
    if constexpr (T::S.ku[d] >= 0 && T::S.ku[d] < KPRE) { kr[d][0] = trread(kb + 256 * d); kr[d][1] = trread(kb + 256 * d + 32); }
  });
  if constexpr (MODE == 0) wait_words(sm + FLAGS, lane, 4, 1);
  if constexpr (MODE == 1) { RDQ(0, 0); RDQ(0, 1); }
  RDXP(0, 0); RDXP(0, 1); RDXP(0, 2); RDXP(0, 3);
  SCHED0();
  static_for<0, T::A.end>([&](auto G_) {
    constexpr int G = decltype(G_)::value;
    if constexpr (G < T::S.n) {
      constexpr int p = T::S.p[G], par = T::S.par[G], d = T::S.d[G], l = T::S.ld[G];
      ac[p][par] = MFMA(frag(kr[d][0], kr[d][1]), frag(xr[l][2 * par], xr[l][2 * par + 1]), T::S.first[G] ? zero : ac[p][par]);
      SCHED0();
    }
    static_for<0, T::A.n[G]>([&](auto I_) {
      constexpr int code = T::A.a[G][decltype(I_)::value];
      constexpr int kind = code & 15, a0 = (code >> 4) & 255, a1 = code >> 12;
      if constexpr (kind == A_X) RDXP(a0, a1);
      if constexpr (kind == A_K) kr[a0][a1] = trread(kb + 256 * a0 + 32 * a1);
      if constexpr (kind == A_Q) RDQ(a0, a1);
      if constexpr (kind == A_PXR) px = *(volatile LDSP u32*)pxp;
      if constexpr (kind == A_PXC) {
        if (__builtin_expect(!__all(px >= (u32)(a0 / 2 + 1)), 0)) wait_words(sm + FLAGS, lane, 4, (u32)(a0 / 2 + 1));
        asm volatile("" ::: "memory");
      }
      if constexpr (kind == A_EP) {
        constexpr int s = a0, e = a1 >> 1, o = a1 & 1;
        constexpr bool hp = s > 0 && T::S.cm[s > 0 ? s - 1 : 0] != 0;
        if constexpr (a1 < 14) {
          if constexpr (e == 0) {
            float cur = ac[s][o][3], prev = 0.f;
            if constexpr (o) prev = ac[s][0][3]; else if constexpr (hp) prev = ac[s - 1][1][3];
            ts[s][o] = l63 ? prev : cur;
          }
          if constexpr (e == 1) { float a = ac[s][o][2]; u2[s][o] = a + ror1(ts[s][o]); }
          if constexpr (e == 2) {
            float prev = 0.f;
            if constexpr (o) prev = u2[s][0]; else if constexpr (hp) prev = u2[s - 1][1];
            ts[s][o] = l63 ? prev : u2[s][o];
          }
          if constexpr (e == 3) { float a = ac[s][o][1]; u1[s][o] = a + ror1(ts[s][o]); }
          if constexpr (e == 4) {
            float prev = 0.f;
            if constexpr (o) prev = u1[s][0]; else if constexpr (hp) prev = u1[s - 1][1];
            ts[s][o] = l63 ? prev : u1[s][o];
          }
          if constexpr (e == 5) { float a = ac[s][o][0]; yy[s][o] = a + ror1(ts[s][o]); asm volatile("" : "+v"(yy[s][o])); }
          if constexpr (e == 6) {
            asm volatile("" : "+v"(qv[s % NQ][o]));
            yy[s][o] *= __uint_as_float(qv[s % NQ][o]);
            asm volatile("" : "+v"(yy[s][o]));
          }
        }
        if constexpr (a1 == 14) pk[s] = pkbf(yy[s][(T::S.om[s] & 1) ? 0 : 1], yy[s][(T::S.om[s] & 2) ? 1 : 0]);
        if constexpr (a1 == 15) *(LDSP u16*)(ywp + s * 256) = (u16)pk[s];
        if constexpr (a1 == 16) *(LDSP u16*)(ywp + s * 256 + 128) = (u16)(pk[s] >> 16);
        if constexpr (a1 == 17) set_word(pyp, (u32)(s / 2 + 1));
      }
      SCHED0();
    });
  });
#undef RDQ
#undef RDXP
}

#define ROLE_END(n) do { asm volatile(".Lfk_end" #n ":"); return; } while (0)

__global__ __launch_bounds__(768, 3) void fused_kernel(Params P) {
  extern __shared__ __attribute__((aligned(16))) char smem[];
  const lptr sm = (lptr)smem;
  const u32 smb = (u32)(uintptr_t)sm;
  const int tid = threadIdx.x;
  const int w = __builtin_amdgcn_readfirstlane(tid >> 6);
  const int lane = tid & 63;
  const int wg = blockIdx.x;
  const int xcd = wg & 7, idx = wg >> 3;
  const bool partA = idx >= 20;
  const int g = lane >> 4, li = lane & 15, lq = li >> 2, lp = li & 3;
  if (w == 11) {
    unsigned long long pc;
    u32 len;
    asm volatile("s_getpc_b64 %0\n.Lfk_code_begin:\n\ts_mov_b32 %1, .Lfk_end0-.Lfk_code_begin"
                 "\n\ts_max_i32 %1, %1, .Lfk_end1-.Lfk_code_begin\n\ts_max_i32 %1, %1, .Lfk_end2-.Lfk_code_begin"
                 "\n\ts_max_i32 %1, %1, .Lfk_end3-.Lfk_code_begin\n\ts_max_i32 %1, %1, .Lfk_end4-.Lfk_code_begin"
                 "\n\ts_max_i32 %1, %1, .Lfk_end5-.Lfk_code_begin" : "=s"(pc), "=&s"(len) :: "scc");
    const u32 off = min((u32)(lane + 64 * (idx & 3)) * 128u, len & ~3u);
    touch64((const char*)pc + off, smb + PFD);
  }
#define gflag ((gu64*)(uintptr_t)(P.ex + WS_FLAG))
#define gepoch ((gu64*)(uintptr_t)(P.ex + WS_EPOCH))
#define rx __builtin_amdgcn_make_buffer_rsrc(P.ex + WS_EXX, 0, 6u << 20, 0x00020000)
  if (idx >= 30) {
    if (w < 2) { delay(PFP); touch64((const char*)P.u + prefetch_off(w, idx, xcd, lane), smb + PFD); }
    if (w == 8 && wg == 240) {
      const unsigned long long tag = __hip_atomic_load(gepoch, RLX_AGENT) + 1;
      delay(40);
#pragma unroll 1
      for (int spin = 0; spin < (1 << 16); ++spin) {
        bool ok = true;
#pragma unroll
        for (int j = 0; j < 5; ++j) { const int f = lane + 64 * j; ok &= (__hip_atomic_load(gflag + FSP * ((f >> 5) * 128 + (f & 31)), RLX_AGENT) == tag); }
        if (__all(ok)) break;
        __builtin_amdgcn_s_sleep(64);
      }
      if (lane == 0) __hip_atomic_store(gepoch, tag, RLX_AGENT);
    }
    vmcnt<0>();
    asm volatile(".Lfk_end1:");
    return;
  }
  if (tid < 80) *(LDSP u32*)(sm + FLAGS + 4 * tid) = 0;

  const int gi = partA ? xcd * 10 + (idx - 20) : xcd * 20 + idx;

  auto poll_exchange = [&](int col, int first, int nst, int word) {
    wait_words(sm + FLAG3, lane, 1, 1);
    const unsigned long long tag = *(volatile LDSP unsigned long long*)(sm + FLAG3 + 8);
    bool ok = lane >= 8 * nst || lane < 8 * first;
#pragma unroll 1
    for (int spin = 0; spin < (1 << 17); ++spin) {
      if (!ok) ok = (__hip_atomic_load(gflag + FSP * (col * 32 + lane), RLX_AGENT) == tag);
      const unsigned long long miss = __ballot(!ok);
      const int done = miss ? (__builtin_ctzll(miss) >> 3) : nst;
      if (done > first) set_word(sm + FLAG3 + word, (u32)done);
      if (!miss) break;
      __builtin_amdgcn_s_sleep(PSL);
    }
  };

  if (w >= (partA ? 4 : 8)) {
    const int h = w & 3, pg = (w < 8) ? 1 : 0;
    const int un = partA ? (wg - 160) * 2 + pg : wg;
    const int pcol = un % 10, pchunk = un / 10 + (partA ? 16 : 0);
    const u32 praw = partA ? A_PRAW + pg * PUNIT : B_PRAW;
    const u32 shk = SHK + pg * 512;
    const lptr fw = sm + FLAG2 + pg * 32;
    asm volatile("" :: "s"(P.k), "s"(P.v), "s"(P.q), "s"(P.shift_k), "s"(P.ex), "s"(P.u), "s"(P.ssm_k), "s"(P.out));
    __builtin_amdgcn_s_setprio(3);
    unsigned long long tag = 0;
    {
      const u32 po = (u32)(64 * pchunk + 16 * h + (lane >> 3)) * ROWB + (u32)pcol * 128 + 16 * (lane & 7);
#pragma unroll
      for (int j = 0; j < 2; ++j) {
        dma16(P.k, po + j * 8 * ROWB, smb + praw + 512 + h * 2048 + j * 1024);
        dma16(P.v, po + j * 8 * ROWB, smb + praw + 8704 + h * 2048 + j * 1024);
        dma16(P.q, po + j * 8 * ROWB, smb + praw + 16896 + h * 2048 + j * 1024);
      }
      if (h == 0 && lane < 32) {
        if (pchunk) dma16(P.k, po - 4 * ROWB, smb + praw);
        else *(LDSP u32x4*)(sm + praw + 16 * lane) = u32x4{0, 0, 0, 0};
      }
      if (h == 1 && lane < 32) dma16(P.shift_k, (u32)pcol * 512 + 16 * lane, smb + shk);
    }
    wgbarrier();
    if (!partA) {
      if (lane < 10) *(LDSP u32x4*)(sm + XIMG + h * XS + 16 * lane) = u32x4{0, 0, 0, 0};
    }
    const u32 co = praw + 32 * h + 8 * lp + 512 * g;
    const u32 stg = partA ? A_PSTG + (w - 4) * YS : B_PSTG + h * XS;
    u32 ka1 = co + 128 * (lq + 1);
    u32 ka2 = co + 128 * ((lq < 2) ? lq + 5 : lq);
    u32 ka3 = co + 128 * lq;
    u32 so[4];
#pragma unroll
    for (int u = 0; u < 4; ++u) so[u] = stg + li * 128 + 8 * (g & 1) + 16 * (((2 * u + (g >> 1)) ^ li) & 7);
    u32 sr_ = stg + (lane >> 3) * 128 + 16 * ((lane ^ (lane >> 3)) & 7);
    u32 eo = (u32)(pcol * 64 + 16 * h + (lane >> 3)) * 4096 + 128 * pchunk + 16 * (lane & 7);
    u32 swa = shk + 8 * (16 * h + li);
    asm volatile("" : "+v"(ka1), "+v"(ka2), "+v"(ka3), "+v"(so[0]), "+v"(so[1]), "+v"(so[2]), "+v"(so[3]), "+v"(sr_), "+v"(eo), "+v"(swa));
    vmcnt<0>();
    set_word(fw + 4 * h, 1);
    wait_words(fw, lane, 4, 1);
    {
      const u32x2 sw = *(LDSP u32x2*)(sm + swa);
      u32x2 r1[4], r2[4], r3[4], vv[4], qq[4];
#pragma unroll
      for (int u = 0; u < 4; ++u) {
        r1[u] = trread(sm + ka1 + 2048 * u);
        r2[u] = trread(sm + ka2 + 2048 * u);
        r3[u] = trread(sm + ka3 + 2048 * u + 512);
        vv[u] = trread(sm + ka3 + 2048 * u + 8704);
        qq[u] = trread(sm + ka3 + 2048 * u + 16896);
      }
      const u32 W32 = (sw.y >> 16) | (sw.y << 16), W10 = (sw.x >> 16) | (sw.x << 16);
#pragma unroll
      for (int u = 0; u < 4; ++u) {
        const float s0 = dot2(r1[u].y, W10, dot2(r1[u].x, W32, 0.f));
        const float s1 = dot2(r3[u].x, W10, dot2(r2[u].y, W32, 0.f));
        const float s2 = dot2(r2[u].x, W10, dot2(r1[u].y, W32, 0.f));
        const float s3 = dot2(r3[u].y, W10, dot2(r3[u].x, W32, 0.f));
        *(LDSP u32x2*)(sm + so[u]) = u32x2{pkbf(s0 * bflo(vv[u].x), s1 * bfhi(vv[u].x)), pkbf(s2 * bflo(vv[u].y), s3 * bfhi(vv[u].y))};
        *(LDSP u32x2*)(sm + so[u] + 2048) = qq[u];
      }
      wavefence();
#pragma unroll
      for (int j = 0; j < 4; ++j)
        __builtin_amdgcn_raw_buffer_store_b128(*(const LDSP u32x4*)(sm + sr_ + 1024 * j), rx, eo + (j & 1) * 8 * 4096 + (j >> 1) * (u32)(WS_EXQ - WS_EXX), 0, 16);
    }
    const u32 urow = (u32)(64 * h + lane) * ROWB + (u32)((gi * 4) & ~7) * 2;
    set_word(fw + 16 + 4 * h, 1);
    wait_words(sm + FLAG3, lane, 1, 1);
    tag = *(volatile LDSP unsigned long long*)(sm + FLAG3 + 8);
    vmcnt<0>();
    if (lane == 0) __hip_atomic_store(gflag + FSP * ((pcol * 4 + h) * 32 + pchunk), tag, RLX_AGENT);
    if (!partA) {
      dma16(P.u, urow, smb + RAWU + 1024 * h);
      dma16(P.u, urow + 256 * ROWB, smb + RAWU + 4096 + 1024 * h);
      wait_words(fw + 16, lane, 4, 1);
    }
    if (w >= 8) {
      if (partA) {
        wait_words(sm + FLAG2 + 16, lane, 4, 1);
        wait_words(sm + FLAG2 + 48, lane, 4, 1);
#pragma unroll 1
        for (int st = 1; st < 3; ++st) {
          if (h == 0) poll_exchange(gi >> 1, st, st + 1, 4); else wait_words(sm + FLAG3 + 4, lane, 1, (u32)(st + 1));
          __builtin_amdgcn_fence(__ATOMIC_ACQUIRE, "wavefront");
          u32x4 d[4];
#pragma unroll
          for (int j = 0; j < 4; ++j)
            d[j] = __builtin_amdgcn_raw_buffer_load_b128(rx, (u32)(gi * 8 + 2 * h + (j & 1)) * 4096 + 1024 * st + 16 * lane + (j >> 1) * (u32)(WS_EXQ - WS_EXX), 0, 16);
#pragma unroll
          for (int j = 0; j < 4; ++j)
            *(LDSP u32x4*)(sm + ((j >> 1) ? YIMG + (2 * h + (j & 1)) * YS : XIMG + (2 * h + (j & 1)) * XS + XPADB) + 1024 * st + 16 * lane) = d[j];
          set_word(sm + FLAGS + 4 * h, (u32)(2 * st + 2));
        }
        const u32 yrd = YIMG + lq * YS + 2 * (64 * h + 16 * g + 4 * lp);
        u16* const outp = P.out + gi * 8 + (size_t)(64 * h + lane) * (2 * DCH);
#pragma unroll 1
        for (int b = 0; b < (h < 3 ? 6 : 5); ++b) {
          wait_words(sm + FLAGS + 16, lane, 8, (u32)(b + 1));
          u32x2 a = trread(sm + yrd + 512 * b);
          u32x2 c = trread(sm + yrd + 512 * b + 4 * YS);
          *(u32x4*)(outp + (size_t)b * 256 * (2 * DCH)) = u32x4{a.x, a.y, c.x, c.y};
        }
        ROLE_END(2);
      }
      const u32 ua = RAWU + 1024 * h + 16 * (16 * g + 4 * lp + lq) + 2 * ((gi * 4) & 4);
      const u32 xw = XIMG + (lane & 3) * XS + XPADB + 8 * (lane >> 2) + 128 * h;
      const u32 yrd = B_YIMG + lq * YS + 2 * (64 * h + 16 * g + 4 * lp);
      const u32 hrd = H_YIMG + lq * HYS + 2 * (64 * h + 16 * g + 4 * lp - 1408);
      u16* const outp = P.out + gi * 4 + (size_t)(64 * h + lane) * (2 * DCH);
#pragma unroll 1
      for (int it = 0; it < 8; ++it) {
        int i = it;
        asm volatile("" : "+s"(i));
        if (i + 2 < 8) dma16(P.u, urow + (u32)(i + 2) * (256 * ROWB), smb + RAWU + 4096 * (i + 2) + 1024 * h);
        if (i < 6) vmcnt<2>(); else if (i == 6) vmcnt<1>(); else vmcnt<0>();
        *(LDSP u32x2*)(sm + xw + 512 * i) = trread(sm + ua + 4096 * i);
        set_word(sm + FLAGS + 4 * h, (u32)(i + 1));
      }
#pragma unroll 1
      for (int i = 0; i < 8; ++i) {
        wait_words(sm + FLAGS + 16, lane, 4, (u32)(i + 1));
        *(u32x2*)(outp + DCH + (size_t)i * 256 * (2 * DCH)) = trread(sm + yrd + 512 * i);
      }
#pragma unroll 1
      for (int i = (h == 3 ? 5 : 6); i < 8; ++i) {
        wait_words(sm + FLAGS + 32, lane, 4, (u32)(i + 1));
        *(u32x2*)(outp + (size_t)i * 256 * (2 * DCH)) = trread(sm + hrd + 512 * i);
      }
      ROLE_END(3);
    }
    __builtin_amdgcn_s_setprio(0);
  } else if (partA) {
    wgbarrier();
    if (w == 0) {
      *(LDSP unsigned long long*)(sm + FLAG3 + 8) = __hip_atomic_load(gepoch, RLX_AGENT) + 1;
      set_word(sm + FLAG3, 1);
    }
    delay(PFP);
    const int pc = (idx - 20) * 4 + w;
    touch64((const char*)P.u + prefetch_off(pc / 30, pc % 30, xcd, lane), smb + PFD);
    if (pc < 20) touch64((const char*)P.u + prefetch_off(1, 10 + pc, xcd, lane), smb + PFD);
  }

  if (partA) {
    const int ch = gi * 8 + w;
    const u32 kimg = KIMG + w * KS, ximg = XIMG + w * XS, yimg = YIMG + w * YS;
    if (lane < 10) *(LDSP u32x4*)(sm + ximg + 16 * lane) = u32x4{0, 0, 0, 0};
    if (lane < 6) *(LDSP u32x4*)(sm + kimg + 16 * lane) = u32x4{0, 0, 0, 0};
    const float dsk = ld_bf_uniform(P.ssm_D, ch);
    wait_words(sm + FLAG2, lane, 4, 1);
#pragma unroll
    for (int j = 0; j < 3; ++j) dma16(P.ssm_k, (u32)ch * (SEQ * 2) + 1024 * j + 16 * lane, smb + kimg + KPADB + 1024 * j);
    vmcnt<0>();
    if (lane == 0) {
      LDSP u16* k0 = (LDSP u16*)(sm + kimg + KPADB);
      *k0 = (u16)pkbf(bf2f(*k0) + dsk, 0.f);
    }
    if (w == 0) poll_exchange(gi >> 1, 0, 1, 16); else wait_words(sm + FLAG3 + 16, lane, 1, 1);
    __builtin_amdgcn_fence(__ATOMIC_ACQUIRE, "wavefront");
    {
      const u32x4 dx = __builtin_amdgcn_raw_buffer_load_b128(rx, (u32)ch * 4096 + 16 * lane, 0, 16);
      const u32x4 dq = __builtin_amdgcn_raw_buffer_load_b128(rx, (u32)ch * 4096 + 16 * lane + (u32)(WS_EXQ - WS_EXX), 0, 16);
      wait_words(sm + FLAG2 + 16 + (w < 4 ? 32 : 0), lane, 4, 1);
      *(LDSP u32x4*)(sm + ximg + XPADB + 16 * lane) = dx;
      *(LDSP u32x4*)(sm + yimg + 16 * lane) = dq;
    }
    wavefence();
    conv_stream<1>(sm, kimg, ximg, sm + yimg + 2 * lane, sm + FLAGS + 16 + 4 * w, lane);
    ROLE_END(4);
  }

  {
    const int ch0 = gi * 4;
    const int cw = w & 3, ps = w >> 2;
    const int ch = ch0 + cw;
    const size_t o = (size_t)ch * NST + 64 * ps + lane;
    const u16 i_ar = P.A_re[o], i_ai = P.A_im[o], i_br = P.B_re[o], i_bi = P.B_im[o], i_cr = P.C_re[o], i_ci = P.C_im[o];
    const float ldt = ld_bf_uniform(P.log_dt, ch);
    const float dsk = ld_bf_uniform(P.Dskip, ch);
    const unsigned long long ep = __hip_atomic_load(w == 0 ? gepoch : (gu64*)(uintptr_t)(P.A_re + (size_t)ch * NST), RLX_AGENT);
    wgbarrier();
    const float dt = rbf(expf(ldt));
    const float zr = dt * bf2f(i_ar), zi = dt * bf2f(i_ai);
    const cf dB = {dt * bf2f(i_br), dt * bf2f(i_bi)};
    const cf C = {bf2f(i_cr), bf2f(i_ci)};
    const cf W = cmul(C, dB);
    float mx = fmaxf(fabsf(W.r), fabsf(W.i));
    {
      int m = __float_as_int(mx);
      m = max(m, __builtin_amdgcn_update_dpp(0, m, 0x111, 0xf, 0xf, false));
      m = max(m, __builtin_amdgcn_update_dpp(0, m, 0x112, 0xf, 0xf, false));
      m = max(m, __builtin_amdgcn_update_dpp(0, m, 0x114, 0xf, 0xf, false));
      m = max(m, __builtin_amdgcn_update_dpp(0, m, 0x118, 0xf, 0xf, false));
      int a0 = __builtin_amdgcn_readlane(m, 15), a1 = __builtin_amdgcn_readlane(m, 31);
      int a2 = __builtin_amdgcn_readlane(m, 47), a3 = __builtin_amdgcn_readlane(m, 63);
      mx = __int_as_float(max(max(a0, a1), max(a2, a3)));
    }
    int ex = (int)((__float_as_uint(mx) >> 23) & 0xff);
    ex = min(max(ex, 30), 240);
    const float sY = __uint_as_float((u32)(267 - ex) << 23);
    const float so = __uint_as_float((u32)(ex - 27) << 23);
    const float SX = 16384.f;

    f32x4 ak[3];
    const u32 stg = STG + w * STS;
    const u32 rb = stg + li * 272 + g * 16;
    const u32 m0v = smb + w * STS;
#define STGW(row, val_r, val_i) \
    asm volatile("ds_write_addtid_b32 %0 offset:%1" :: "v"(pkh((val_r), (val_i))), "i"(STG + (row) * 272) : "memory")
    {
      const cf E1 = cexp_pow2(zr, zi, 1.f);
      const cf E8c = cexp_pow2(zr, -zi, 8.f);
      const cf E128 = cexp_pow2(zr, zi, 128.f);
      const cf E1024 = cexp_pow2(zr, zi, 1024.f);
      if (w == 0) {
        *(LDSP unsigned long long*)(sm + FLAG3 + 8) = ep + 1;
        set_word(sm + FLAG3, 1);
      }
      const cf E2 = cmul(E1, E1);
      const cf E4 = cmul(E2, E2);
      u32 keep;
      asm volatile("s_mov_b32 %0, m0\n\ts_mov_b32 m0, %1\n\ts_nop 0" : "=&s"(keep) : "s"(m0v) : "memory");
      cf y = {W.r * sY, -(W.i * sY)};
      cf P1 = {E128.r * SX, E128.i * SX};
      cf v0 = {SX, 0.f}, v1 = P1;
#pragma unroll
      for (int b = 0; b < 8; ++b) {
        STGW(2 * b, y.r, y.i); y = cmul(y, E8c);
        STGW(16 + b, v0.r, v0.i); if (b < 7) v0 = cmul(v0, E1);
        STGW(2 * b + 1, y.r, y.i); if (b < 7) y = cmul(y, E8c);
        STGW(24 + b, v1.r, v1.i); if (b < 7) v1 = cmul(v1, E1);
      }
      f16x8 yf[4];
      const f32x4 zero = {0.f, 0.f, 0.f, 0.f};
#pragma unroll
      for (int s = 0; s < 4; ++s) {
        yf[s] = *(const LDSP f16x8*)(sm + rb + 64 * s);
        f16x8 xf = *(const LDSP f16x8*)(sm + rb + 16 * 272 + 64 * s);
        ak[0] = __builtin_amdgcn_mfma_f32_16x16x32_f16(xf, yf[s], s ? ak[0] : zero, 0, 0, 0);
      }
      asm volatile("" ::: "memory");
      cf Pa = cmul(P1, E128);
      cf Pb = {E1024.r * SX, E1024.i * SX};
#pragma unroll
      for (int m = 0; m < 2; ++m) {
        cf v = Pa;
#pragma unroll
        for (int r = 0; r < 4; ++r) {
          STGW(16 + 4 * m + r, v.r, v.i); if (r < 3) v = cmul(v, E2);
          if (r & 1) { STGW(4 * (2 * m + (r >> 1)) + 2, Pb.r, Pb.i); Pb = cmul(Pb, E128); }
        }
        Pa = cmul(Pa, E128);
      }
#pragma unroll
      for (int m = 0; m < 4; ++m) {
        STGW(4 * m, Pa.r, Pa.i);
        cf v = cmul(Pa, E4);
        STGW(4 * m + 3, Pb.r, Pb.i); Pb = cmul(Pb, E128);
        STGW(4 * m + 1, v.r, v.i);
        if (m < 3) Pa = cmul(Pa, E128);
      }
      STGW(24, Pb.r, Pb.i);
      asm volatile("s_mov_b32 m0, %0" :: "s"(keep) : "memory");
#pragma unroll
      for (int s = 0; s < 4; ++s) {
        f16x8 x1 = *(const LDSP f16x8*)(sm + rb + 64 * s);
        f16x8 x2 = *(const LDSP f16x8*)(sm + rb + 16 * 272 + 64 * s);
        ak[1] = __builtin_amdgcn_mfma_f32_16x16x32_f16(x1, yf[s], s ? ak[1] : zero, 0, 0, 0);
        ak[2] = __builtin_amdgcn_mfma_f32_16x16x32_f16(x2, yf[s], s ? ak[2] : zero, 0, 0, 0);
      }
    }
#undef STGW
#pragma unroll
    for (int i = 0; i < 3; ++i) ak[i] *= so;
    asm volatile("" ::: "memory");
    const u32 kimg = B_KIMG + cw * KS;
    if (ps) {
#pragma unroll
      for (int i = 0; i < 3; ++i) *(LDSP f32x4*)(sm + kimg + 1024 * i + 16 * lane) = ak[i];
      set_word(sm + FLAGS + 48 + 4 * cw, 1);
      const u32 hx = stg, hk = stg + XS, hy = H_YIMG + cw * HYS;
      asm volatile("s_waitcnt lgkmcnt(0)" ::: "memory");
#pragma unroll
      for (int j = 0; j < 4; ++j) dma16(P.ssm_k, (u32)ch * (SEQ * 2) + 1024 * j + 16 * lane, smb + hk + KPADB + 1024 * j);
      if (lane < 10) *(LDSP u32x4*)(sm + hx + 16 * lane) = u32x4{0, 0, 0, 0};
      if (lane < 6) *(LDSP u32x4*)(sm + hk + 16 * lane) = u32x4{0, 0, 0, 0};
      const float dh = ld_bf_uniform(P.ssm_D, ch);
      if (w == 4) poll_exchange(gi >> 2, 0, 4, 4); else wait_words(sm + FLAG3 + 4, lane, 1, 4);
      __builtin_amdgcn_fence(__ATOMIC_ACQUIRE, "wavefront");
      {
        u32x4 d[6];
#pragma unroll
        for (int j = 0; j < 4; ++j) d[j] = __builtin_amdgcn_raw_buffer_load_b128(rx, (u32)ch * 4096 + 1024 * j + 16 * lane, 0, 16);
#pragma unroll
        for (int j = 0; j < 2; ++j)
          d[4 + j] = __builtin_amdgcn_raw_buffer_load_b128(rx, (u32)ch * 4096 + 2816 + 256 * j + 16 * lane + (u32)(WS_EXQ - WS_EXX), 0, 16);
        vmcnt<0>();
        if (lane == 0) {
          LDSP u16* k0 = (LDSP u16*)(sm + hk + KPADB);
          *k0 = (u16)pkbf(bf2f(*k0) + dh, 0.f);
        }
#pragma unroll
        for (int j = 0; j < 4; ++j) *(LDSP u32x4*)(sm + hx + XPADB + 1024 * j + 16 * lane) = d[j];
#pragma unroll
        for (int j = 0; j < 2; ++j) *(LDSP u32x4*)(sm + hy + 256 * j + 16 * lane) = d[4 + j];
      }
      wavefence();
      conv_stream<2>(sm, hk, hx, sm + hy + 2 * lane - 11 * 256, sm + FLAGS + 16 + 4 * w, lane);
      ROLE_END(5);
    }
    wait_words(sm + FLAGS + 48 + 4 * cw, lane, 1, 1);
    {
#pragma unroll
      for (int i = 0; i < 3; ++i) ak[i] += *(const LDSP f32x4*)(sm + kimg + 1024 * i + 16 * lane);
      wavefence();
      auto nxt = [&](float x, float e, bool lastl) {
        float r = __int_as_float(__builtin_amdgcn_update_dpp(0, __float_as_int(x), 0x130, 0xf, 0xf, true));
        return lastl ? e : r;
      };
      auto lane_of = [&](float x, int l) { return __int_as_float(__builtin_amdgcn_readlane(__float_as_int(x), l)); };
      const lptr kd = sm + kimg + KPADB;
      if (lane < 6) *(LDSP u32x4*)(sm + kimg + 16 * lane) = u32x4{0, 0, 0, 0};
      {
        const float a0 = ak[0][0] + (lane == 0 ? dsk : 0.f), a1 = ak[0][1], a2 = ak[0][2], a3 = ak[0][3];
        *(LDSP u32x2*)(kd + 2 * (128 * (g >> 1) + 8 * li + 4 * (g & 1))) = u32x2{pkbf(a0, a1), pkbf(a2, a3)};
      }
      const float s20 = ak[1][0], s21 = ak[1][1], s3a = ak[1][2], s3b = ak[1][3];
      const float c0 = ak[2][0], c1 = ak[2][1], c2 = ak[2][2], c3 = ak[2][3];
      const bool l63 = lane == 63;
      {
        const float n = nxt(c0, lane_of(s20, 0), lane == 31);
        if (lane < 32) *(LDSP u32x4*)(kd + 16 * (32 + lane)) = u32x4{pkbf(c0, c0), pkbf(c1, c2), pkbf(c2, c2), pkbf(c3, n)};
      }
      {
        const float n = nxt(s20, lane_of(s3a, 0), l63);
        *(LDSP u32x4*)(kd + 16 * (64 + lane)) = u32x4{pkbf(s20, s20), pkbf(s20, s21), pkbf(s21, s21), pkbf(n, n)};
      }
      {
        const float na = nxt(s3a, lane_of(s3b, 0), l63), nb = nxt(s3b, lane_of(c0, 32), l63);
        const u32 a = pkbf(s3a, s3a), b = pkbf(s3b, s3b);
        *(LDSP u32x4*)(kd + 16 * (128 + lane)) = u32x4{a, a, pkbf((lane & 1) ? na : s3a, na), pkbf(na, na)};
        *(LDSP u32x4*)(kd + 16 * (192 + lane)) = u32x4{b, b, pkbf((lane & 1) ? nb : s3b, nb), pkbf(nb, nb)};
      }
    }
    wavefence();
    conv_stream<0>(sm, kimg, XIMG + cw * XS, sm + B_YIMG + cw * YS + 2 * lane, sm + FLAGS + 16 + 4 * w, lane);
    ROLE_END(0);
  }
}

extern "C" void kernel_launch(void* const* d_in, const int* in_sizes, int n_in,
                              void* d_out, int out_size, void* d_ws, size_t ws_size,
                              hipStream_t stream) {
  Params P;
  P.q = (const u16*)d_in[0];
  P.k = (const u16*)d_in[1];
  P.v = (const u16*)d_in[2];
  P.shift_k = (const u16*)d_in[3];
  P.ssm_k = (const u16*)d_in[4];
  P.ssm_D = (const u16*)d_in[5];
  P.u = (const u16*)d_in[6];
  P.A_re = (const u16*)d_in[7];
  P.A_im = (const u16*)d_in[8];
  P.B_re = (const u16*)d_in[9];
  P.B_im = (const u16*)d_in[10];
  P.C_re = (const u16*)d_in[11];
  P.C_im = (const u16*)d_in[12];
  P.log_dt = (const u16*)d_in[13];
  P.Dskip = (const u16*)d_in[14];
  P.out = (u16*)d_out;
  P.ex = (char*)d_ws;
  (void)hipFuncSetAttribute((const void*)fused_kernel, hipFuncAttributeMaxDynamicSharedMemorySize, LDS_BYTES);
  fused_kernel<<<256, 768, LDS_BYTES, stream>>>(P);
}
```

```cpp
#include <hip/hip_runtime.h>
#include <hip/hip_bf16.h>
#include <stdint.h>

typedef __attribute__((ext_vector_type(4))) short s16x4;
typedef __attribute__((ext_vector_type(8))) _Float16 f16x8;
typedef __attribute__((ext_vector_type(8))) __bf16 bf16x8;
typedef __attribute__((ext_vector_type(2))) __bf16 bf16x2;
typedef __attribute__((ext_vector_type(2))) _Float16 f16x2;
typedef __attribute__((ext_vector_type(2))) float f32x2;
typedef __attribute__((ext_vector_type(4))) float f32x4;
typedef __attribute__((ext_vector_type(2))) unsigned int u32x2;
typedef __attribute__((ext_vector_type(4))) unsigned int u32x4;
typedef unsigned int u32;
typedef unsigned short u16;
#define LDSP __attribute__((address_space(3)))
#define DEV __device__ __forceinline__

constexpr int SEQ = 2048;
constexpr int DCH = 640;
constexpr int NST = 128;
constexpr int ROWB = DCH * 2;

constexpr int XS = 4272;
constexpr int XPADB = 160;
constexpr int YS = 4160;
constexpr int KS = 4224;
constexpr int KPADB = 96;
constexpr int FLAGS = 0;
constexpr int FLAG2 = 192;
constexpr int FLAG3 = 256;
constexpr int PFD = 320;
constexpr int SHK = 1024;
constexpr int XIMG = 2048;
constexpr int PUNIT = 8704 + 8192 + 8192;
constexpr int YIMG = 36224;
constexpr int KIMG = 69504;
constexpr int A_PRAW = 103296;
constexpr int A_PSTG = YIMG;
constexpr int STS = 8704;
constexpr int STG = 19200;
constexpr int B_KIMG = 88832;
constexpr int B_YIMG = 105728;
constexpr int HYS = 1344;
constexpr int H_YIMG = 122368;
constexpr int RAWU = 127744;
constexpr int B_PRAW = RAWU + 8192;
constexpr int B_PSTG = XIMG + XPADB;
constexpr int LDS_BYTES = 161024;

constexpr size_t WS_EPOCH = 0;
constexpr size_t WS_FLAG = 4096;
constexpr size_t WS_EXX = 1 << 20;
constexpr size_t WS_EXQ = 4 << 20;

DEV float bf2f(u16 v) { return __uint_as_float(((u32)v) << 16); }
DEV float bflo(u32 v) { return __uint_as_float(v << 16); }
DEV float bfhi(u32 v) { return __uint_as_float(v & 0xffff0000u); }
DEV u32 pkbf(float a, float b) {
  f32x2 t = {a, b};
  bf16x2 r = __builtin_convertvector(t, bf16x2);
  return __builtin_bit_cast(u32, r);
}
DEV u32 pkh(float a, float b) {
  f32x2 t = {a, b};
  f16x2 r = __builtin_convertvector(t, f16x2);
  return __builtin_bit_cast(u32, r);
}
DEV float rbf(float a) { return bflo(pkbf(a, a)); }
DEV float dot2(u32 a, u32 b, float c) {
  return __builtin_amdgcn_fdot2_f32_bf16(__builtin_bit_cast(bf16x2, a), __builtin_bit_cast(bf16x2, b), c, false);
}
typedef __attribute__((address_space(1))) unsigned long long gu64;
#define RLX_AGENT __ATOMIC_RELAXED, __HIP_MEMORY_SCOPE_AGENT
typedef LDSP char* lptr;
DEV u32x2 trread(lptr p) {
  s16x4 r = __builtin_amdgcn_ds_read_tr16_b64_v4i16((LDSP s16x4*)p);
  return __builtin_bit_cast(u32x2, r);
}
DEV bf16x8 frag(u32x2 a, u32x2 b) {
  u32x4 f = {a.x, a.y, b.x, b.y};
  return __builtin_bit_cast(bf16x8, f);
}
DEV float ror1(float v) {
  int r = __builtin_amdgcn_update_dpp(0, __float_as_int(v), 0x13C, 0xf, 0xf, false);
  return __int_as_float(r);
}
DEV void wavefence() { __builtin_amdgcn_fence(__ATOMIC_ACQ_REL, "wavefront"); __builtin_amdgcn_wave_barrier(); }
DEV void wgbarrier() {
  asm volatile("s_waitcnt lgkmcnt(0)" ::: "memory");
  __builtin_amdgcn_s_barrier();
  asm volatile("" ::: "memory");
}
DEV void dma16(const void* base, u32 voff, u32 ldsaddr) {
  u32 keep;
  ldsaddr = __builtin_amdgcn_readfirstlane(ldsaddr);
  const unsigned long long b64 = (unsigned long long)(uintptr_t)base;
  const unsigned long long sb = ((unsigned long long)(u32)__builtin_amdgcn_readfirstlane((u32)(b64 >> 32)) << 32) |
                                (u32)__builtin_amdgcn_readfirstlane((u32)b64);
  asm volatile("s_mov_b32 %0, m0\n\ts_mov_b32 m0, %3\n\ts_nop 2\n\tglobal_load_lds_dwordx4 %1, %2\n\ts_mov_b32 m0, %0"
               : "=&s"(keep) : "v"(voff), "s"(sb), "s"(ldsaddr) : "memory");
}
DEV void touch64(const void* p, u32 ldsaddr) {
  u32 keep;
  asm volatile("s_mov_b32 %0, m0\n\ts_mov_b32 m0, %2\n\ts_nop 0\n\tglobal_load_lds_dword %1, off\n\ts_mov_b32 m0, %0"
               : "=&s"(keep) : "v"(p), "s"(ldsaddr) : "memory");
}
template <int N> DEV void vmcnt() { asm volatile("s_waitcnt vmcnt(%0)" :: "i"(N) : "memory"); }
#define SCHED0() __builtin_amdgcn_sched_barrier(0)

template <int V> struct IC { static constexpr int value = V; };
template <int I, int N, class F> DEV void static_for(F&& f) {
  if constexpr (I < N) { f(IC<I>{}); static_for<I + 1, N>(f); }
}

struct cf { float r, i; };
DEV cf cmul(cf a, cf b) {
  cf o;
  o.r = fmaf(a.r, b.r, -(a.i * b.i));
  o.i = fmaf(a.r, b.i, a.i * b.r);
  return o;
}
DEV cf cexp_pow2(float zr, float zi, float x) {
  float mag = __builtin_amdgcn_exp2f((zr * x) * 1.44269504088896340736f);
  float th = zi * x;
  const float C_HI = 0.15915494f;
  const float C_LO = 6.4206382e-09f;
  float f = fmaf(th, C_LO, fmaf(th, C_HI, -rintf(th * C_HI)));
  cf o;
  o.r = mag * __builtin_amdgcn_cosf(f);
  o.i = mag * __builtin_amdgcn_sinf(f);
  return o;
}

struct Params {
  const u16 *k, *v, *q, *shift_k; char* ex; const u16 *u, *ssm_k; u16* out;
  const u16 *A_re, *A_im, *B_re, *B_im, *C_re, *C_im, *log_dt, *Dskip;
  const u16* ssm_D;
};

DEV float ld_bf_uniform(const u16* p, int idx) {
  u32 d = *(const __attribute__((address_space(4))) u32*)(uintptr_t)(p + (idx & ~1));
  return (idx & 1) ? bfhi(d) : bflo(d);
}

DEV u32 prefetch_off(int half, int slice, int xcd, int lane) {
  const u32 row = (u32)(half * 4 + (lane >> 4)) * 256u + (u32)slice * 8u + ((lane >> 1) & 7);
  return row * ROWB + (u32)(((160 * xcd) >> 7) + (lane & 1)) * 128u;
}
DEV void wait_words(lptr p, int lane, int n, u32 v) {
#pragma unroll 1
  for (int spin = 0; spin < (1 << 22); ++spin) {
    u32 x = *(volatile LDSP u32*)(p + 4 * (lane & (n - 1)));
    if (__all(x >= v)) break;
    __builtin_amdgcn_s_sleep(1);
  }
  asm volatile("" ::: "memory");
}
constexpr int PFP = 3;
constexpr int FSP = 16;
constexpr int PSL = 6;
DEV void delay(int n) {
  for (int i = 0; i < n; ++i) __builtin_amdgcn_s_sleep(3);
}

DEV void set_word(lptr p, u32 v) {
  asm volatile("" ::: "memory");
  *(volatile LDSP u32*)p = v;
  asm volatile("" ::: "memory");
}

struct Sched {
  int n;
  int p[272], par[272], d[272], ld[272];
  bool first[272];
  int base[16];
  int cm[16], om[16];
  int ldm[40], ldu[40], ldp[40];
  int ku[16];
  int nld, nst;
};
constexpr void add_unit(Sched& S, int p, int d, int ld, int pm, bool first) {
  for (int par = 0; par < 2; ++par)
    if ((pm >> par) & 1) {
      S.p[S.n] = p; S.par[S.n] = par; S.d[S.n] = d; S.ld[S.n] = ld; S.first[S.n] = first;
      if (S.ldu[ld] < 0) S.ldu[ld] = S.n;
      S.ldp[ld] |= 1 << par;
      if (S.ku[d] < 0) S.ku[d] = S.n;
      ++S.n;
    }
}
constexpr Sched make_sched(int mode) {
  Sched S{};
  const int last = (mode == 1) ? 11 : 15;
  S.nst = last + 1;
  int nl = 16;
  for (int l = 0; l < 40; ++l) { S.ldu[l] = -1; S.ldm[l] = l; }
  for (int d = 0; d < 16; ++d) S.ku[d] = -1;
  for (int s = 0; s <= last; ++s) {
    S.base[s] = S.n;
    S.cm[s] = (mode == 2 && s < 11) ? 0 : ((mode == 1 && s == 11) ? 1 : 3);
    S.om[s] = (mode == 2 && s == 11) ? 2 : S.cm[s];
    if (mode == 2) {
      for (int p = (s > 11 ? s : 11); p <= 15; ++p) add_unit(S, p, p - s, s, 3, s == 0);
      continue;
    }
    for (int k = 0; k < 8 && s + k <= last; ++k)
      add_unit(S, s + k, k, s, (mode == 1 && s + k == 11) ? 1 : 3, (s == 0) || (k == 7));
    int lo = -1, hi = -1, cnt = 0, half = 0;
    if (s == 6) { lo = 8; hi = 8; cnt = 1; half = 2; }
    else if (s >= 7 && s <= last - 3) { lo = (s & 1) ? s + 2 : s + 1; hi = lo + 1; cnt = hi - 7; half = (s & 1) ? 0 : 1; }
    else if (s == last - 2 || s == last - 1) { lo = last; hi = last; cnt = last - 7; half = (s == last - 2) ? 0 : 1; }
    if (cnt) {
      const int split = (cnt + 1) / 2;
      const int j0 = (half == 1) ? split : 0, j1 = (half == 0) ? split : cnt;
      for (int j = j0; j < j1; ++j) {
        const int mm = cnt - 1 - j;
        S.ldm[nl] = mm;
        for (int t = hi; t >= lo; --t)
          if (t - mm >= 8) add_unit(S, t, t - mm, nl, (mode == 1 && t == 11) ? 1 : 3, false);
        ++nl;
      }
    }
  }
  S.nld = nl;
  return S;
}
constexpr int NGAP = 272 + 40, CAP = 2, KPRE = 16;
enum { A_NONE = 0, A_X, A_K, A_Q, A_PXR, A_PXC, A_EP };
struct Acts { int n[NGAP]; int a[NGAP][4]; int end; };
constexpr int place_back(Acts& T, int latest, int code, int cap) {
  int g = latest;
  while (g > 0 && T.n[g] >= cap) --g;
  T.a[g][T.n[g]++] = code;
  return g;
}
constexpr int place_fwd(Acts& T, int earliest, int code, int cap, int nm) {
  int g = earliest;
  while (T.n[g] >= (g < nm ? cap : 4)) ++g;
  T.a[g][T.n[g]++] = code;
  if (g + 1 > T.end) T.end = g + 1;
  return g;
}
constexpr Acts make_acts(const Sched& S, int mode) {
  Acts T{};
  T.end = S.n;
  const int dense = (mode == 0) ? 200 : (mode == 1 ? 90 : 100);
  for (int l = S.nst - 1; l >= 1; --l) {
    int g = S.ldu[l] - 5;
    const int cap = S.ldu[l] >= dense && mode ? 2 : 1;
    for (int i = 3; i >= 0; --i) if ((S.ldp[l] >> (i >> 1)) & 1) g = place_back(T, g - 1, A_X | (l << 4) | (i << 12), cap);
    for (int o = 1; o >= 0; --o) if (mode && ((S.om[l] >> o) & 1)) g = place_back(T, g - 1, A_Q | (l << 4) | (o << 12), cap);
    if ((mode == 0 && !(l & 1)) || (mode == 1 && !(l & 3))) {
      g = place_back(T, g - 1, A_PXC | (l << 4), 1);
      place_back(T, g - 6, A_PXR, 1);
    }
  }
  for (int l = S.nld - 1; l >= 16; --l) {
    int g = S.ldu[l] - 5;
    for (int i = 3; i >= 0; --i) if ((S.ldp[l] >> (i >> 1)) & 1) g = place_back(T, g - 1, A_X | (l << 4) | (i << 12), S.ldu[l] >= dense ? 2 : 1);
  }
  for (int d = 0; d < 16; ++d) {
    if (S.ku[d] < KPRE) continue;
    int g = S.ku[d] - 5;
    g = place_back(T, g - 1, A_K | (d << 4) | (1 << 12), 2);
    place_back(T, g - 1, A_K | (d << 4), 2);
  }
  int prev[18] = {};
  for (int s = 0; s < S.nst; ++s) {
    if (!S.cm[s]) continue;
    int g = S.base[s] + 4;
    const int p16 = prev[16];
    for (int e = 0; e < ((s & 1) ? 18 : 17); ++e) {
      if (e < 14 && !(((e >= 8 ? S.om[s] : S.cm[s]) >> (e & 1)) & 1)) continue;
      if ((e == 15 && !(S.om[s] & 1)) || (e == 16 && !(S.om[s] & 2))) continue;
      if (mode == 0 && (e == 12 || e == 13)) continue;
      int lo = g + (((e & 1) && e < 14) ? 0 : 1);
      if (lo <= prev[e | 1]) lo = prev[e | 1] + 1;
      if (e == 17 && lo <= p16) lo = p16 + 1;
      g = place_fwd(T, lo, A_EP | (s << 4) | (e << 12), S.base[s] >= dense ? 3 : CAP, S.n);
      prev[e] = g;
    }
  }
  return T;
}
template <int MODE> struct Tab {
  static constexpr Sched S = make_sched(MODE);
  static constexpr Acts A = make_acts(S, MODE);
};
static_assert(Tab<0>::S.n == 272 && Tab<1>::S.n == 144 && Tab<2>::S.n == 140, "schedule");

#define MFMA(a, b, c) __builtin_amdgcn_mfma_f32_16x16x32_bf16((a), (b), (c), 0, 0, 0)

template <int MODE>
DEV void conv_stream(lptr sm, u32 kimg, u32 ximg, lptr ywp, lptr pyp, int lane) {
  using T = Tab<MODE>;
  const int g = lane >> 4, lq = (lane & 15) >> 2, lp = lane & 3;
  const lptr kb = sm + kimg + 8 * (4 * lp + 8 * g + lq);
  const lptr xb = sm + ximg + 224 + 8 * (lp - 8 * g - lq);
  const bool l63 = (lane == 63);
  const lptr pxp = sm + FLAGS + 4 * (lane & 3);
  const u32 qa = (u32)(uintptr_t)ywp;
  u32x2 kr[16][2], xr[40][4];
  f32x4 ac[16][2];
  const f32x4 zero = {0.f, 0.f, 0.f, 0.f};
  float ts[16][2], u2[16][2], u1[16][2], yy[16][2];
  u32 pk[16];
  constexpr int NQ = MODE == 0 ? 3 : 5;
  u32 qv[NQ][2], px = 0;
#pragma unroll
  for (int i = 0; i < NQ; ++i) { qv[i][0] = 0; qv[i][1] = 0; }
#define RDQ(s_, i_) asm volatile("ds_read_u16_d16_hi %0, %1 offset:%2" : "+v"(qv[(s_) % NQ][i_]) : "v"(qa), "i"((s_) * 256 + 128 * (i_)) : "memory")
#define RDXP(l_, i_) xr[l_][i_] = trread(xb + 256 * T::S.ldm[l_] + ((i_) >> 1) * 128 + (((i_) & 1) ? 0 : 32))
  static_for<0, 16>([&](auto D_) {
    constexpr int d = decltype(D_)::value;
    if constexpr (T::S.ku[d] >= 0 && T::S.ku[d] < KPRE) { kr[d][0] = trread(kb + 256 * d); kr[d][1] = trread(kb + 256 * d + 32); }
  });
  if constexpr (MODE == 0) wait_words(sm + FLAGS, lane, 4, 1);
  if constexpr (MODE == 1) { RDQ(0, 0); RDQ(0, 1); }
  RDXP(0, 0); RDXP(0, 1); RDXP(0, 2); RDXP(0, 3);
  SCHED0();
  static_for<0, T::A.end>([&](auto G_) {
    constexpr int G = decltype(G_)::value;
    if constexpr (G < T::S.n) {
      constexpr int p = T::S.p[G], par = T::S.par[G], d = T::S.d[G], l = T::S.ld[G];
      ac[p][par] = MFMA(frag(kr[d][0], kr[d][1]), frag(xr[l][2 * par], xr[l][2 * par + 1]), T::S.first[G] ? zero : ac[p][par]);
      SCHED0();
    }
    static_for<0, T::A.n[G]>([&](auto I_) {
      constexpr int code = T::A.a[G][decltype(I_)::value];
      constexpr int kind = code & 15, a0 = (code >> 4) & 255, a1 = code >> 12;
      if constexpr (kind == A_X) RDXP(a0, a1);
      if constexpr (kind == A_K) kr[a0][a1] = trread(kb + 256 * a0 + 32 * a1);
      if constexpr (kind == A_Q) RDQ(a0, a1);
      if constexpr (kind == A_PXR) px = *(volatile LDSP u32*)pxp;
      if constexpr (kind == A_PXC) {
        if (__builtin_expect(!__all(px >= (u32)(a0 / 2 + 1)), 0)) wait_words(sm + FLAGS, lane, 4, (u32)(a0 / 2 + 1));
        asm volatile("" ::: "memory");
      }
      if constexpr (kind == A_EP) {
        constexpr int s = a0, e = a1 >> 1, o = a1 & 1;
        constexpr bool hp = s > 0 && T::S.cm[s > 0 ? s - 1 : 0] != 0;
        if constexpr (a1 < 14) {
          if constexpr (e == 0) {
            float cur = ac[s][o][3], prev = 0.f;
            if constexpr (o) prev = ac[s][0][3]; else if constexpr (hp) prev = ac[s - 1][1][3];
            ts[s][o] = l63 ? prev : cur;
          }
          if constexpr (e == 1) { float a = ac[s][o][2]; u2[s][o] = a + ror1(ts[s][o]); }
          if constexpr (e == 2) {
            float prev = 0.f;
            if constexpr (o) prev = u2[s][0]; else if constexpr (hp) prev = u2[s - 1][1];
            ts[s][o] = l63 ? prev : u2[s][o];
          }
          if constexpr (e == 3) { float a = ac[s][o][1]; u1[s][o] = a + ror1(ts[s][o]); }
          if constexpr (e == 4) {
            float prev = 0.f;
            if constexpr (o) prev = u1[s][0]; else if constexpr (hp) prev = u1[s - 1][1];
            ts[s][o] = l63 ? prev : u1[s][o];
          }
          if constexpr (e == 5) { float a = ac[s][o][0]; yy[s][o] = a + ror1(ts[s][o]); asm volatile("" : "+v"(yy[s][o])); }
          if constexpr (e == 6) {
            asm volatile("" : "+v"(qv[s % NQ][o]));
            yy[s][o] *= __uint_as_float(qv[s % NQ][o]);
            asm volatile("" : "+v"(yy[s][o]));
          }
        }
        if constexpr (a1 == 14) pk[s] = pkbf(yy[s][(T::S.om[s] & 1) ? 0 : 1], yy[s][(T::S.om[s] & 2) ? 1 : 0]);
        if constexpr (a1 == 15) *(LDSP u16*)(ywp + s * 256) = (u16)pk[s];
        if constexpr (a1 == 16) *(LDSP u16*)(ywp + s * 256 + 128) = (u16)(pk[s] >> 16);
        if constexpr (a1 == 17) set_word(pyp, (u32)(s / 2 + 1));
      }
      SCHED0();
    });
  });
#undef RDQ
#undef RDXP
}

#define ROLE_END(n) do { asm volatile(".Lfk_end" #n ":"); return; } while (0)

__global__ __launch_bounds__(768, 3) void fused_kernel(Params P) {
  extern __shared__ __attribute__((aligned(16))) char smem[];
  const lptr sm = (lptr)smem;
  const u32 smb = (u32)(uintptr_t)sm;
  const int tid = threadIdx.x;
  const int w = __builtin_amdgcn_readfirstlane(tid >> 6);
  const int lane = tid & 63;
  const int wg = blockIdx.x;
  const int xcd = wg & 7, idx = wg >> 3;
  const bool partA = idx >= 20;
  const int g = lane >> 4, li = lane & 15, lq = li >> 2, lp = li & 3;
  if (w == 11) {
    unsigned long long pc;
    u32 len;
    asm volatile("s_getpc_b64 %0\n.Lfk_code_begin:\n\ts_mov_b32 %1, .Lfk_end0-.Lfk_code_begin"
                 "\n\ts_max_i32 %1, %1, .Lfk_end1-.Lfk_code_begin\n\ts_max_i32 %1, %1, .Lfk_end2-.Lfk_code_begin"
                 "\n\ts_max_i32 %1, %1, .Lfk_end3-.Lfk_code_begin\n\ts_max_i32 %1, %1, .Lfk_end4-.Lfk_code_begin"
                 "\n\ts_max_i32 %1, %1, .Lfk_end5-.Lfk_code_begin" : "=s"(pc), "=&s"(len) :: "scc");
    const u32 off = min((u32)(lane + 64 * (idx & 3)) * 128u, len & ~3u);
    touch64((const char*)pc + off, smb + PFD);
  }
#define gflag ((gu64*)(uintptr_t)(P.ex + WS_FLAG))
#define gepoch ((gu64*)(uintptr_t)(P.ex + WS_EPOCH))
#define rx __builtin_amdgcn_make_buffer_rsrc(P.ex + WS_EXX, 0, 6u << 20, 0x00020000)
  if (__builtin_expect(idx >= 30, 0)) {
    if (w < 2) { delay(PFP); touch64((const char*)P.u + prefetch_off(w, idx, xcd, lane), smb + PFD); }
    if (w == 8 && wg == 240) {
      const unsigned long long tag = __hip_atomic_load(gepoch, RLX_AGENT) + 1;
      delay(40);
#pragma unroll 1
      for (int spin = 0; spin < (1 << 16); ++spin) {
        bool ok = true;
#pragma unroll
        for (int j = 0; j < 5; ++j) { const int f = lane + 64 * j; ok &= (__hip_atomic_load(gflag + FSP * ((f >> 5) * 128 + (f & 31)), RLX_AGENT) == tag); }
        if (__all(ok)) break;
        __builtin_amdgcn_s_sleep(64);
      }
      if (lane == 0) __hip_atomic_store(gepoch, tag, RLX_AGENT);
    }
    vmcnt<0>();
    asm volatile(".Lfk_end1:");
    return;
  }
  if (tid < 80) *(LDSP u32*)(sm + FLAGS + 4 * tid) = 0;
  const bool producer = w >= (partA ? 4 : 8);
  if (!producer) wgbarrier();

  const int gi = partA ? xcd * 10 + (idx - 20) : xcd * 20 + idx;

  auto poll_exchange = [&](int col, int first, int nst, int word) {
    wait_words(sm + FLAG3, lane, 1, 1);
    const unsigned long long tag = *(volatile LDSP unsigned long long*)(sm + FLAG3 + 8);
    bool ok = lane >= 8 * nst || lane < 8 * first;
#pragma unroll 1
    for (int spin = 0; spin < (1 << 17); ++spin) {
      if (!ok) ok = (__hip_atomic_load(gflag + FSP * (col * 32 + lane), RLX_AGENT) == tag);
      const unsigned long long miss = __ballot(!ok);
      const int done = miss ? (__builtin_ctzll(miss) >> 3) : nst;
      if (done > first) set_word(sm + FLAG3 + word, (u32)done);
      if (!miss) break;
      __builtin_amdgcn_s_sleep(PSL);
    }
  };

  if (producer) {
    const int h = w & 3, pg = (w < 8) ? 1 : 0;
    const int un = partA ? (wg - 160) * 2 + pg : wg;
    const int pcol = un % 10, pchunk = un / 10 + (partA ? 16 : 0);
    const u32 praw = partA ? A_PRAW + pg * PUNIT : B_PRAW;
    const u32 shk = SHK + pg * 512;
    const lptr fw = sm + FLAG2 + pg * 32;
    asm volatile("" :: "s"(P.k), "s"(P.v), "s"(P.q), "s"(P.shift_k), "s"(P.ex), "s"(P.u), "s"(P.ssm_k), "s"(P.out));
    __builtin_amdgcn_s_setprio(3);
    unsigned long long tag = 0;
    {
      const u32 po = (u32)(64 * pchunk + 16 * h + (lane >> 3)) * ROWB + (u32)pcol * 128 + 16 * (lane & 7);
#pragma unroll
      for (int j = 0; j < 2; ++j) {
        dma16(P.k, po + j * 8 * ROWB, smb + praw + 512 + h * 2048 + j * 1024);
        dma16(P.v, po + j * 8 * ROWB, smb + praw + 8704 + h * 2048 + j * 1024);
        dma16(P.q, po + j * 8 * ROWB, smb + praw + 16896 + h * 2048 + j * 1024);
      }
      if (h == 0 && lane < 32) {
        if (pchunk) dma16(P.k, po - 4 * ROWB, smb + praw);
        else *(LDSP u32x4*)(sm + praw + 16 * lane) = u32x4{0, 0, 0, 0};
      }
      if (h == 1 && lane < 32) dma16(P.shift_k, (u32)pcol * 512 + 16 * lane, smb + shk);
    }
    wgbarrier();
    if (!partA) {
      if (lane < 10) *(LDSP u32x4*)(sm + XIMG + h * XS + 16 * lane) = u32x4{0, 0, 0, 0};
    }
    if (w == 8) {
      *(LDSP unsigned long long*)(sm + FLAG3 + 8) = __hip_atomic_load(gepoch, RLX_AGENT) + 1;
      set_word(sm + FLAG3, 1);
    }
    const u32 co = praw + 32 * h + 8 * lp + 512 * g;
    const u32 stg = partA ? A_PSTG + (w - 4) * YS : B_PSTG + h * XS;
    u32 ka1 = co + 128 * (lq + 1);
    u32 ka2 = co + 128 * ((lq < 2) ? lq + 5 : lq);
    u32 ka3 = co + 128 * lq;
    u32 so[4];
#pragma unroll
    for (int u = 0; u < 4; ++u) so[u] = stg + li * 128 + 8 * (g & 1) + 16 * (((2 * u + (g >> 1)) ^ li) & 7);
    u32 sr_ = stg + (lane >> 3) * 128 + 16 * ((lane ^ (lane >> 3)) & 7);
    u32 eo = (u32)(pcol * 64 + 16 * h + (lane >> 3)) * 4096 + 128 * pchunk + 16 * (lane & 7);
    u32 swa = shk + 8 * (16 * h + li);
    asm volatile("" : "+v"(ka1), "+v"(ka2), "+v"(ka3), "+v"(so[0]), "+v"(so[1]), "+v"(so[2]), "+v"(so[3]), "+v"(sr_), "+v"(eo), "+v"(swa));
    vmcnt<0>();
    set_word(fw + 4 * h, 1);
    wait_words(fw, lane, 4, 1);
    {
      const u32x2 sw = *(LDSP u32x2*)(sm + swa);
      u32x2 r1[4], r2[4], r3[4], vv[4], qq[4];
#pragma unroll
      for (int u = 0; u < 4; ++u) {
        r1[u] = trread(sm + ka1 + 2048 * u);
        r2[u] = trread(sm + ka2 + 2048 * u);
        r3[u] = trread(sm + ka3 + 2048 * u + 512);
        vv[u] = trread(sm + ka3 + 2048 * u + 8704);
        qq[u] = trread(sm + ka3 + 2048 * u + 16896);
      }
      const u32 W32 = (sw.y >> 16) | (sw.y << 16), W10 = (sw.x >> 16) | (sw.x << 16);
#pragma unroll
      for (int u = 0; u < 4; ++u) {
        const float s0 = dot2(r1[u].y, W10, dot2(r1[u].x, W32, 0.f));
        const float s1 = dot2(r3[u].x, W10, dot2(r2[u].y, W32, 0.f));
        const float s2 = dot2(r2[u].x, W10, dot2(r1[u].y, W32, 0.f));
        const float s3 = dot2(r3[u].y, W10, dot2(r3[u].x, W32, 0.f));
        *(LDSP u32x2*)(sm + so[u]) = u32x2{pkbf(s0 * bflo(vv[u].x), s1 * bfhi(vv[u].x)), pkbf(s2 * bflo(vv[u].y), s3 * bfhi(vv[u].y))};
        *(LDSP u32x2*)(sm + so[u] + 2048) = qq[u];
      }
      wavefence();
#pragma unroll
      for (int j = 0; j < 4; ++j)
        __builtin_amdgcn_raw_buffer_store_b128(*(const LDSP u32x4*)(sm + sr_ + 1024 * j), rx, eo + (j & 1) * 8 * 4096 + (j >> 1) * (u32)(WS_EXQ - WS_EXX), 0, 16);
    }
    const u32 urow = (u32)(64 * h + lane) * ROWB + (u32)((gi * 4) & ~7) * 2;
    set_word(fw + 16 + 4 * h, 1);
    wait_words(sm + FLAG3, lane, 1, 1);
    tag = *(volatile LDSP unsigned long long*)(sm + FLAG3 + 8);
    vmcnt<0>();
    if (lane == 0) __hip_atomic_store(gflag + FSP * ((pcol * 4 + h) * 32 + pchunk), tag, RLX_AGENT);
    if (!partA) {
      dma16(P.u, urow, smb + RAWU + 1024 * h);
      dma16(P.u, urow + 256 * ROWB, smb + RAWU + 4096 + 1024 * h);
      wait_words(fw + 16, lane, 4, 1);
    }
    if (w >= 8) {
      if (partA) {
        wait_words(sm + FLAG2 + 16, lane, 4, 1);
        wait_words(sm + FLAG2 + 48, lane, 4, 1);
#pragma unroll 1
        for (int st = 1; st < 3; ++st) {
          if (h == 0) poll_exchange(gi >> 1, st, st + 1, 4); else wait_words(sm + FLAG3 + 4, lane, 1, (u32)(st + 1));
          __builtin_amdgcn_fence(__ATOMIC_ACQUIRE, "wavefront");
          u32x4 d[4];
#pragma unroll
          for (int j = 0; j < 4; ++j)
            d[j] = __builtin_amdgcn_raw_buffer_load_b128(rx, (u32)(gi * 8 + 2 * h + (j & 1)) * 4096 + 1024 * st + 16 * lane + (j >> 1) * (u32)(WS_EXQ - WS_EXX), 0, 16);
#pragma unroll
          for (int j = 0; j < 4; ++j)
            *(LDSP u32x4*)(sm + ((j >> 1) ? YIMG + (2 * h + (j & 1)) * YS : XIMG + (2 * h + (j & 1)) * XS + XPADB) + 1024 * st + 16 * lane) = d[j];
          set_word(sm + FLAGS + 4 * h, (u32)(2 * st + 2));
        }
        const u32 yrd = YIMG + lq * YS + 2 * (64 * h + 16 * g + 4 * lp);
        u16* const outp = P.out + gi * 8 + (size_t)(64 * h + lane) * (2 * DCH);
#pragma unroll 1
        for (int b = 0; b < (h < 3 ? 6 : 5); ++b) {
          wait_words(sm + FLAGS + 16, lane, 8, (u32)(b + 1));
          u32x2 a = trread(sm + yrd + 512 * b);
          u32x2 c = trread(sm + yrd + 512 * b + 4 * YS);
          *(u32x4*)(outp + (size_t)b * 256 * (2 * DCH)) = u32x4{a.x, a.y, c.x, c.y};
        }
        ROLE_END(2);
      }
      const u32 ua = RAWU + 1024 * h + 16 * (16 * g + 4 * lp + lq) + 2 * ((gi * 4) & 4);
      const u32 xw = XIMG + (lane & 3) * XS + XPADB + 8 * (lane >> 2) + 128 * h;
      const u32 yrd = B_YIMG + lq * YS + 2 * (64 * h + 16 * g + 4 * lp);
      const u32 hrd = H_YIMG + lq * HYS + 2 * (64 * h + 16 * g + 4 * lp - 1408);
      u16* const outp = P.out + gi * 4 + (size_t)(64 * h + lane) * (2 * DCH);
#pragma unroll 1
      for (int it = 0; it < 8; ++it) {
        int i = it;
        asm volatile("" : "+s"(i));
        if (i + 2 < 8) dma16(P.u, urow + (u32)(i + 2) * (256 * ROWB), smb + RAWU + 4096 * (i + 2) + 1024 * h);
        if (i < 6) vmcnt<2>(); else if (i == 6) vmcnt<1>(); else vmcnt<0>();
        *(LDSP u32x2*)(sm + xw + 512 * i) = trread(sm + ua + 4096 * i);
        set_word(sm + FLAGS + 4 * h, (u32)(i + 1));
      }
#pragma unroll 1
      for (int i = 0; i < 8; ++i) {
        wait_words(sm + FLAGS + 16, lane, 4, (u32)(i + 1));
        *(u32x2*)(outp + DCH + (size_t)i * 256 * (2 * DCH)) = trread(sm + yrd + 512 * i);
      }
#pragma unroll 1
      for (int i = (h == 3 ? 5 : 6); i < 8; ++i) {
        wait_words(sm + FLAGS + 32, lane, 4, (u32)(i + 1));
        *(u32x2*)(outp + (size_t)i * 256 * (2 * DCH)) = trread(sm + hrd + 512 * i);
      }
      ROLE_END(3);
    }
    __builtin_amdgcn_s_setprio(0);
  } else if (partA) {
    delay(PFP);
    const int pc = (idx - 20) * 4 + w;
    touch64((const char*)P.u + prefetch_off(pc / 30, pc % 30, xcd, lane), smb + PFD);
    if (pc < 20) touch64((const char*)P.u + prefetch_off(1, 10 + pc, xcd, lane), smb + PFD);
  }

  if (partA) {
    const int ch = gi * 8 + w;
    const u32 kimg = KIMG + w * KS, ximg = XIMG + w * XS, yimg = YIMG + w * YS;
    if (lane < 10) *(LDSP u32x4*)(sm + ximg + 16 * lane) = u32x4{0, 0, 0, 0};
    if (lane < 6) *(LDSP u32x4*)(sm + kimg + 16 * lane) = u32x4{0, 0, 0, 0};
    const float dsk = ld_bf_uniform(P.ssm_D, ch);
    wait_words(sm + FLAG2, lane, 4, 1);
#pragma unroll
    for (int j = 0; j < 3; ++j) dma16(P.ssm_k, (u32)ch * (SEQ * 2) + 1024 * j + 16 * lane, smb + kimg + KPADB + 1024 * j);
    vmcnt<0>();
    if (lane == 0) {
      LDSP u16* k0 = (LDSP u16*)(sm + kimg + KPADB);
      *k0 = (u16)pkbf(bf2f(*k0) + dsk, 0.f);
    }
    if (w == 0) poll_exchange(gi >> 1, 0, 1, 16); else wait_words(sm + FLAG3 + 16, lane, 1, 1);
    __builtin_amdgcn_fence(__ATOMIC_ACQUIRE, "wavefront");
    {
      const u32x4 dx = __builtin_amdgcn_raw_buffer_load_b128(rx, (u32)ch * 4096 + 16 * lane, 0, 16);
      const u32x4 dq = __builtin_amdgcn_raw_buffer_load_b128(rx, (u32)ch * 4096 + 16 * lane + (u32)(WS_EXQ - WS_EXX), 0, 16);
      wait_words(sm + FLAG2 + 16 + (w < 4 ? 32 : 0), lane, 4, 1);
      *(LDSP u32x4*)(sm + ximg + XPADB + 16 * lane) = dx;
      *(LDSP u32x4*)(sm + yimg + 16 * lane) = dq;
    }
    wavefence();
    conv_stream<1>(sm, kimg, ximg, sm + yimg + 2 * lane, sm + FLAGS + 16 + 4 * w, lane);
    ROLE_END(4);
  }

  {
    const int ch0 = gi * 4;
    const int cw = w & 3, ps = w >> 2;
    const int ch = ch0 + cw;
    const size_t o = (size_t)ch * NST + 64 * ps + lane;
    const u16 i_ar = P.A_re[o], i_ai = P.A_im[o], i_br = P.B_re[o], i_bi = P.B_im[o], i_cr = P.C_re[o], i_ci = P.C_im[o];
    const float ldt = ld_bf_uniform(P.log_dt, ch);
    const float dsk = ld_bf_uniform(P.Dskip, ch);
    const float dt = rbf(expf(ldt));
    const float zr = dt * bf2f(i_ar), zi = dt * bf2f(i_ai);
    const cf dB = {dt * bf2f(i_br), dt * bf2f(i_bi)};
    const cf C = {bf2f(i_cr), bf2f(i_ci)};
    const cf W = cmul(C, dB);
    float mx = fmaxf(fabsf(W.r), fabsf(W.i));
    {
      int m = __float_as_int(mx);
      m = max(m, __builtin_amdgcn_update_dpp(0, m, 0x111, 0xf, 0xf, false));
      m = max(m, __builtin_amdgcn_update_dpp(0, m, 0x112, 0xf, 0xf, false));
      m = max(m, __builtin_amdgcn_update_dpp(0, m, 0x114, 0xf, 0xf, false));
      m = max(m, __builtin_amdgcn_update_dpp(0, m, 0x118, 0xf, 0xf, false));
      int a0 = __builtin_amdgcn_readlane(m, 15), a1 = __builtin_amdgcn_readlane(m, 31);
      int a2 = __builtin_amdgcn_readlane(m, 47), a3 = __builtin_amdgcn_readlane(m, 63);
      mx = __int_as_float(max(max(a0, a1), max(a2, a3)));
    }
    int ex = (int)((__float_as_uint(mx) >> 23) & 0xff);
    ex = min(max(ex, 30), 240);
    const float sY = __uint_as_float((u32)(267 - ex) << 23);
    const float so = __uint_as_float((u32)(ex - 27) << 23);
    const float SX = 16384.f;

    f32x4 ak[3];
    const u32 stg = STG + w * STS;
    const u32 rb = stg + li * 272 + g * 16;
    const u32 m0v = smb + w * STS;
#define STGW(row, val_r, val_i) \
    asm volatile("ds_write_addtid_b32 %0 offset:%1" :: "v"(pkh((val_r), (val_i))), "i"(STG + (row) * 272) : "memory")
    {
      const cf E1 = cexp_pow2(zr, zi, 1.f);
      const cf E8c = cexp_pow2(zr, -zi, 8.f);
      const cf E128 = cexp_pow2(zr, zi, 128.f);
      const cf E1024 = cexp_pow2(zr, zi, 1024.f);
      const cf E2 = cmul(E1, E1);
      const cf E4 = cmul(E2, E2);
      u32 keep;
      asm volatile("s_mov_b32 %0, m0\n\ts_mov_b32 m0, %1\n\ts_nop 0" : "=&s"(keep) : "s"(m0v) : "memory");
      cf y = {W.r * sY, -(W.i * sY)};
      cf P1 = {E128.r * SX, E128.i * SX};
      cf v0 = {SX, 0.f}, v1 = P1;
#pragma unroll
      for (int b = 0; b < 8; ++b) {
        STGW(2 * b, y.r, y.i); y = cmul(y, E8c);
        STGW(16 + b, v0.r, v0.i); if (b < 7) v0 = cmul(v0, E1);
        STGW(2 * b + 1, y.r, y.i); if (b < 7) y = cmul(y, E8c);
        STGW(24 + b, v1.r, v1.i); if (b < 7) v1 = cmul(v1, E1);
      }
      f16x8 yf[4];
      const f32x4 zero = {0.f, 0.f, 0.f, 0.f};
#pragma unroll
      for (int s = 0; s < 4; ++s) {
        yf[s] = *(const LDSP f16x8*)(sm + rb + 64 * s);
        f16x8 xf = *(const LDSP f16x8*)(sm + rb + 16 * 272 + 64 * s);
        ak[0] = __builtin_amdgcn_mfma_f32_16x16x32_f16(xf, yf[s], s ? ak[0] : zero, 0, 0, 0);
      }
      asm volatile("" ::: "memory");
      cf Pa = cmul(P1, E128);
      cf Pb = {E1024.r * SX, E1024.i * SX};
#pragma unroll
      for (int m = 0; m < 2; ++m) {
        cf v = Pa;
#pragma unroll
        for (int r = 0; r < 4; ++r) {
          STGW(16 + 4 * m + r, v.r, v.i); if (r < 3) v = cmul(v, E2);
          if (r & 1) { STGW(4 * (2 * m + (r >> 1)) + 2, Pb.r, Pb.i); Pb = cmul(Pb, E128); }
        }
        Pa = cmul(Pa, E128);
      }
#pragma unroll
      for (int m = 0; m < 4; ++m) {
        STGW(4 * m, Pa.r, Pa.i);
        cf v = cmul(Pa, E4);
        STGW(4 * m + 3, Pb.r, Pb.i); Pb = cmul(Pb, E128);
        STGW(4 * m + 1, v.r, v.i);
        if (m < 3) Pa = cmul(Pa, E128);
      }
      STGW(24, Pb.r, Pb.i);
      asm volatile("s_mov_b32 m0, %0" :: "s"(keep) : "memory");
#pragma unroll
      for (int s = 0; s < 4; ++s) {
        f16x8 x1 = *(const LDSP f16x8*)(sm + rb + 64 * s);
        f16x8 x2 = *(const LDSP f16x8*)(sm + rb + 16 * 272 + 64 * s);
        ak[1] = __builtin_amdgcn_mfma_f32_16x16x32_f16(x1, yf[s], s ? ak[1] : zero, 0, 0, 0);
        ak[2] = __builtin_amdgcn_mfma_f32_16x16x32_f16(x2, yf[s], s ? ak[2] : zero, 0, 0, 0);
      }
    }
#undef STGW
#pragma unroll
    for (int i = 0; i < 3; ++i) ak[i] *= so;
    asm volatile("" ::: "memory");
    const u32 kimg = B_KIMG + cw * KS;
    if (ps) {
#pragma unroll
      for (int i = 0; i < 3; ++i) *(LDSP f32x4*)(sm + kimg + 1024 * i + 16 * lane) = ak[i];
      set_word(sm + FLAGS + 48 + 4 * cw, 1);
      const u32 hx = stg, hk = stg + XS, hy = H_YIMG + cw * HYS;
      asm volatile("s_waitcnt lgkmcnt(0)" ::: "memory");
#pragma unroll
      for (int j = 0; j < 4; ++j) dma16(P.ssm_k, (u32)ch * (SEQ * 2) + 1024 * j + 16 * lane, smb + hk + KPADB + 1024 * j);
      if (lane < 10) *(LDSP u32x4*)(sm + hx + 16 * lane) = u32x4{0, 0, 0, 0};
      if (lane < 6) *(LDSP u32x4*)(sm + hk + 16 * lane) = u32x4{0, 0, 0, 0};
      const float dh = ld_bf_uniform(P.ssm_D, ch);
      if (w == 4) poll_exchange(gi >> 2, 0, 4, 4); else wait_words(sm + FLAG3 + 4, lane, 1, 4);
      __builtin_amdgcn_fence(__ATOMIC_ACQUIRE, "wavefront");
      {
        u32x4 d[6];
#pragma unroll
        for (int j = 0; j < 4; ++j) d[j] = __builtin_amdgcn_raw_buffer_load_b128(rx, (u32)ch * 4096 + 1024 * j + 16 * lane, 0, 16);
#pragma unroll
        for (int j = 0; j < 2; ++j)
          d[4 + j] = __builtin_amdgcn_raw_buffer_load_b128(rx, (u32)ch * 4096 + 2816 + 256 * j + 16 * lane + (u32)(WS_EXQ - WS_EXX), 0, 16);
        vmcnt<0>();
        if (lane == 0) {
          LDSP u16* k0 = (LDSP u16*)(sm + hk + KPADB);
          *k0 = (u16)pkbf(bf2f(*k0) + dh, 0.f);
        }
#pragma unroll
        for (int j = 0; j < 4; ++j) *(LDSP u32x4*)(sm + hx + XPADB + 1024 * j + 16 * lane) = d[j];
#pragma unroll
        for (int j = 0; j < 2; ++j) *(LDSP u32x4*)(sm + hy + 256 * j + 16 * lane) = d[4 + j];
      }
      wavefence();
      conv_stream<2>(sm, hk, hx, sm + hy + 2 * lane - 11 * 256, sm + FLAGS + 16 + 4 * w, lane);
      ROLE_END(5);
    }
    wait_words(sm + FLAGS + 48 + 4 * cw, lane, 1, 1);
    {
#pragma unroll
      for (int i = 0; i < 3; ++i) ak[i] += *(const LDSP f32x4*)(sm + kimg + 1024 * i + 16 * lane);
      wavefence();
      auto nxt = [&](float x, float e, bool lastl) {
        float r = __int_as_float(__builtin_amdgcn_update_dpp(0, __float_as_int(x), 0x130, 0xf, 0xf, true));
        return lastl ? e : r;
      };
      auto lane_of = [&](float x, int l) { return __int_as_float(__builtin_amdgcn_readlane(__float_as_int(x), l)); };
      const lptr kd = sm + kimg + KPADB;
      if (lane < 6) *(LDSP u32x4*)(sm + kimg + 16 * lane) = u32x4{0, 0, 0, 0};
      {
        const float a0 = ak[0][0] + (lane == 0 ? dsk : 0.f), a1 = ak[0][1], a2 = ak[0][2], a3 = ak[0][3];
        *(LDSP u32x2*)(kd + 2 * (128 * (g >> 1) + 8 * li + 4 * (g & 1))) = u32x2{pkbf(a0, a1), pkbf(a2, a3)};
      }
      const float s20 = ak[1][0], s21 = ak[1][1], s3a = ak[1][2], s3b = ak[1][3];
      const float c0 = ak[2][0], c1 = ak[2][1], c2 = ak[2][2], c3 = ak[2][3];
      const bool l63 = lane == 63;
      {
        const float n = nxt(c0, lane_of(s20, 0), lane == 31);
        if (lane < 32) *(LDSP u32x4*)(kd + 16 * (32 + lane)) = u32x4{pkbf(c0, c0), pkbf(c1, c2), pkbf(c2, c2), pkbf(c3, n)};
      }
      {
        const float n = nxt(s20, lane_of(s3a, 0), l63);
        *(LDSP u32x4*)(kd + 16 * (64 + lane)) = u32x4{pkbf(s20, s20), pkbf(s20, s21), pkbf(s21, s21), pkbf(n, n)};
      }
      {
        const float na = nxt(s3a, lane_of(s3b, 0), l63), nb = nxt(s3b, lane_of(c0, 32), l63);
        const u32 a = pkbf(s3a, s3a), b = pkbf(s3b, s3b);
        *(LDSP u32x4*)(kd + 16 * (128 + lane)) = u32x4{a, a, pkbf((lane & 1) ? na : s3a, na), pkbf(na, na)};
        *(LDSP u32x4*)(kd + 16 * (192 + lane)) = u32x4{b, b, pkbf((lane & 1) ? nb : s3b, nb), pkbf(nb, nb)};
      }
    }
    wavefence();
    conv_stream<0>(sm, kimg, XIMG + cw * XS, sm + B_YIMG + cw * YS + 2 * lane, sm + FLAGS + 16 + 4 * w, lane);
    ROLE_END(0);
  }
}

extern "C" void kernel_launch(void* const* d_in, const int* in_sizes, int n_in,
                              void* d_out, int out_size, void* d_ws, size_t ws_size,
                              hipStream_t stream) {
  Params P;
  P.q = (const u16*)d_in[0];
  P.k = (const u16*)d_in[1];
  P.v = (const u16*)d_in[2];
  P.shift_k = (const u16*)d_in[3];
  P.ssm_k = (const u16*)d_in[4];
  P.ssm_D = (const u16*)d_in[5];
  P.u = (const u16*)d_in[6];
  P.A_re = (const u16*)d_in[7];
  P.A_im = (const u16*)d_in[8];
  P.B_re = (const u16*)d_in[9];
  P.B_im = (const u16*)d_in[10];
  P.C_re = (const u16*)d_in[11];
  P.C_im = (const u16*)d_in[12];
  P.log_dt = (const u16*)d_in[13];
  P.Dskip = (const u16*)d_in[14];
  P.out = (u16*)d_out;
  P.ex = (char*)d_ws;
  (void)hipFuncSetAttribute((const void*)fused_kernel, hipFuncAttributeMaxDynamicSharedMemorySize, LDS_BYTES);
  fused_kernel<<<256, 768, LDS_BYTES, stream>>>(P);
}
```

```cpp
#include <hip/hip_runtime.h>
#include <hip/hip_bf16.h>
#include <stdint.h>

typedef __attribute__((ext_vector_type(4))) short s16x4;
typedef __attribute__((ext_vector_type(8))) _Float16 f16x8;
typedef __attribute__((ext_vector_type(8))) __bf16 bf16x8;
typedef __attribute__((ext_vector_type(2))) __bf16 bf16x2;
typedef __attribute__((ext_vector_type(2))) _Float16 f16x2;
typedef __attribute__((ext_vector_type(2))) float f32x2;
typedef __attribute__((ext_vector_type(4))) float f32x4;
typedef __attribute__((ext_vector_type(2))) unsigned int u32x2;
typedef __attribute__((ext_vector_type(4))) unsigned int u32x4;
typedef unsigned int u32;
typedef unsigned short u16;
#define LDSP __attribute__((address_space(3)))
#define DEV __device__ __forceinline__

constexpr int SEQ = 2048;
constexpr int DCH = 640;
constexpr int NST = 128;
constexpr int ROWB = DCH * 2;

constexpr int XS = 4272;
constexpr int XPADB = 160;
constexpr int YS = 4160;
constexpr int KS = 4224;
constexpr int KPADB = 96;
constexpr int FLAGS = 0;
constexpr int FLAG2 = 192;
constexpr int FLAG3 = 256;
constexpr int EPO = 576;
constexpr int PFD = 320;
constexpr int SHK = 1024;
constexpr int XIMG = 2048;
constexpr int PUNIT = 8704 + 8192 + 8192;
constexpr int YIMG = 36224;
constexpr int KIMG = 69504;
constexpr int A_PRAW = 103296;
constexpr int A_PSTG = YIMG;
constexpr int STS = 8704;
constexpr int STG = 19200;
constexpr int B_KIMG = 88832;
constexpr int B_YIMG = 105728;
constexpr int HYS = 1344;
constexpr int H_YIMG = 122368;
constexpr int RAWU = 127744;
constexpr int B_PRAW = RAWU + 8192;
constexpr int B_PSTG = XIMG + XPADB;
constexpr int LDS_BYTES = 161024;

constexpr size_t WS_EPOCH = 0;
constexpr size_t WS_FLAG = 4096;
constexpr size_t WS_EXX = 1 << 20;
constexpr size_t WS_EXQ = 4 << 20;

DEV float bf2f(u16 v) { return __uint_as_float(((u32)v) << 16); }
DEV float bflo(u32 v) { return __uint_as_float(v << 16); }
DEV float bfhi(u32 v) { return __uint_as_float(v & 0xffff0000u); }
DEV u32 pkbf(float a, float b) {
  f32x2 t = {a, b};
  bf16x2 r = __builtin_convertvector(t, bf16x2);
  return __builtin_bit_cast(u32, r);
}
DEV u32 pkh(float a, float b) {
  f32x2 t = {a, b};
  f16x2 r = __builtin_convertvector(t, f16x2);
  return __builtin_bit_cast(u32, r);
}
DEV float rbf(float a) { return bflo(pkbf(a, a)); }
DEV float dot2(u32 a, u32 b, float c) {
  return __builtin_amdgcn_fdot2_f32_bf16(__builtin_bit_cast(bf16x2, a), __builtin_bit_cast(bf16x2, b), c, false);
}
typedef __attribute__((address_space(1))) unsigned long long gu64;
#define RLX_AGENT __ATOMIC_RELAXED, __HIP_MEMORY_SCOPE_AGENT
typedef LDSP char* lptr;
DEV u32x2 trread(lptr p) {
  s16x4 r = __builtin_amdgcn_ds_read_tr16_b64_v4i16((LDSP s16x4*)p);
  return __builtin_bit_cast(u32x2, r);
}
DEV bf16x8 frag(u32x2 a, u32x2 b) {
  u32x4 f = {a.x, a.y, b.x, b.y};
  return __builtin_bit_cast(bf16x8, f);
}
DEV float ror1(float v) {
  int r = __builtin_amdgcn_update_dpp(0, __float_as_int(v), 0x13C, 0xf, 0xf, false);
  return __int_as_float(r);
}
DEV void wavefence() { __builtin_amdgcn_fence(__ATOMIC_ACQ_REL, "wavefront"); __builtin_amdgcn_wave_barrier(); }
DEV void wgbarrier() {
  asm volatile("s_waitcnt lgkmcnt(0)" ::: "memory");
  __builtin_amdgcn_s_barrier();
  asm volatile("" ::: "memory");
}
DEV void dma16(const void* base, u32 voff, u32 ldsaddr) {
  u32 keep;
  ldsaddr = __builtin_amdgcn_readfirstlane(ldsaddr);
  const unsigned long long b64 = (unsigned long long)(uintptr_t)base;
  const unsigned long long sb = ((unsigned long long)(u32)__builtin_amdgcn_readfirstlane((u32)(b64 >> 32)) << 32) |
                                (u32)__builtin_amdgcn_readfirstlane((u32)b64);
  asm volatile("s_mov_b32 %0, m0\n\ts_mov_b32 m0, %3\n\ts_nop 2\n\tglobal_load_lds_dwordx4 %1, %2\n\ts_mov_b32 m0, %0"
               : "=&s"(keep) : "v"(voff), "s"(sb), "s"(ldsaddr) : "memory");
}
DEV void touch64(const void* p, u32 ldsaddr) {
  u32 keep;
  asm volatile("s_mov_b32 %0, m0\n\ts_mov_b32 m0, %2\n\ts_nop 0\n\tglobal_load_lds_dword %1, off\n\ts_mov_b32 m0, %0"
               : "=&s"(keep) : "v"(p), "s"(ldsaddr) : "memory");
}
template <int N> DEV void vmcnt() { asm volatile("s_waitcnt vmcnt(%0)" :: "i"(N) : "memory"); }
#define SCHED0() __builtin_amdgcn_sched_barrier(0)

template <int V> struct IC { static constexpr int value = V; };
template <int I, int N, class F> DEV void static_for(F&& f) {
  if constexpr (I < N) { f(IC<I>{}); static_for<I + 1, N>(f); }
}

struct cf { float r, i; };
DEV cf cmul(cf a, cf b) {
  cf o;
  o.r = fmaf(a.r, b.r, -(a.i * b.i));
  o.i = fmaf(a.r, b.i, a.i * b.r);
  return o;
}
DEV cf cexp_pow2(float zr, float zi, float x) {
  float mag = __builtin_amdgcn_exp2f((zr * x) * 1.44269504088896340736f);
  float th = zi * x;
  const float C_HI = 0.15915494f;
  const float C_LO = 6.4206382e-09f;
  float f = fmaf(th, C_LO, fmaf(th, C_HI, -rintf(th * C_HI)));
  cf o;
  o.r = mag * __builtin_amdgcn_cosf(f);
  o.i = mag * __builtin_amdgcn_sinf(f);
  return o;
}

struct Params {
  const u16 *k, *v, *q, *shift_k; char* ex; const u16 *u, *ssm_k; u16* out;
  const u16 *A_re, *A_im, *B_re, *B_im, *C_re, *C_im, *log_dt, *Dskip;
  const u16* ssm_D;
};

DEV float ld_bf_uniform(const u16* p, int idx) {
  u32 d = *(const __attribute__((address_space(4))) u32*)(uintptr_t)(p + (idx & ~1));
  return (idx & 1) ? bfhi(d) : bflo(d);
}

DEV u32 prefetch_off(int half, int slice, int xcd, int lane) {
  const u32 row = (u32)(half * 4 + (lane >> 4)) * 256u + (u32)slice * 8u + ((lane >> 1) & 7);
  return row * ROWB + (u32)(((160 * xcd) >> 7) + (lane & 1)) * 128u;
}
DEV void wait_words(lptr p, int lane, int n, u32 v) {
#pragma unroll 1
  for (int spin = 0; spin < (1 << 22); ++spin) {
    u32 x = *(volatile LDSP u32*)(p + 4 * (lane & (n - 1)));
    if (__all(x >= v)) break;
    __builtin_amdgcn_s_sleep(1);
  }
  asm volatile("" ::: "memory");
}
constexpr int PFP = 3;
constexpr int FSP = 16;
constexpr int PSL = 6;
DEV void delay(int n) {
  for (int i = 0; i < n; ++i) __builtin_amdgcn_s_sleep(3);
}

DEV void set_word(lptr p, u32 v) {
  asm volatile("" ::: "memory");
  *(volatile LDSP u32*)p = v;
  asm volatile("" ::: "memory");
}

struct Sched {
  int n;
  int p[272], par[272], d[272], ld[272];
  bool first[272];
  int base[16];
  int cm[16], om[16];
  int ldm[40], ldu[40], ldp[40];
  int ku[16];
  int nld, nst;
};
constexpr void add_unit(Sched& S, int p, int d, int ld, int pm, bool first) {
  for (int par = 0; par < 2; ++par)
    if ((pm >> par) & 1) {
      S.p[S.n] = p; S.par[S.n] = par; S.d[S.n] = d; S.ld[S.n] = ld; S.first[S.n] = first;
      if (S.ldu[ld] < 0) S.ldu[ld] = S.n;
      S.ldp[ld] |= 1 << par;
      if (S.ku[d] < 0) S.ku[d] = S.n;
      ++S.n;
    }
}
constexpr Sched make_sched(int mode) {
  Sched S{};
  const int last = (mode == 1) ? 11 : 15;
  S.nst = last + 1;
  int nl = 16;
  for (int l = 0; l < 40; ++l) { S.ldu[l] = -1; S.ldm[l] = l; }
  for (int d = 0; d < 16; ++d) S.ku[d] = -1;
  for (int s = 0; s <= last; ++s) {
    S.base[s] = S.n;
    S.cm[s] = (mode == 2 && s < 11) ? 0 : ((mode == 1 && s == 11) ? 1 : 3);
    S.om[s] = (mode == 2 && s == 11) ? 2 : S.cm[s];
    if (mode == 2) {
      for (int p = (s > 11 ? s : 11); p <= 15; ++p) add_unit(S, p, p - s, s, 3, s == 0);
      continue;
    }
    for (int k = 0; k < 8 && s + k <= last; ++k)
      add_unit(S, s + k, k, s, (mode == 1 && s + k == 11) ? 1 : 3, (s == 0) || (k == 7));
    int lo = -1, hi = -1, cnt = 0, half = 0;
    if (s == 6) { lo = 8; hi = 8; cnt = 1; half = 2; }
    else if (s >= 7 && s <= last - 3) { lo = (s & 1) ? s + 2 : s + 1; hi = lo + 1; cnt = hi - 7; half = (s & 1) ? 0 : 1; }
    else if (s == last - 2 || s == last - 1) { lo = last; hi = last; cnt = last - 7; half = (s == last - 2) ? 0 : 1; }
    if (cnt) {
      const int split = (cnt + 1) / 2;
      const int j0 = (half == 1) ? split : 0, j1 = (half == 0) ? split : cnt;
      for (int j = j0; j < j1; ++j) {
        const int mm = cnt - 1 - j;
        S.ldm[nl] = mm;
        for (int t = hi; t >= lo; --t)
          if (t - mm >= 8) add_unit(S, t, t - mm, nl, (mode == 1 && t == 11) ? 1 : 3, false);
        ++nl;
      }
    }
  }
  S.nld = nl;
  return S;
}
constexpr int NGAP = 272 + 40, CAP = 2, KPRE = 16;
enum { A_NONE = 0, A_X, A_K, A_Q, A_PXR, A_PXC, A_EP };
struct Acts { int n[NGAP]; int a[NGAP][4]; int end; };
constexpr int place_back(Acts& T, int latest, int code, int cap) {
  int g = latest;
  while (g > 0 && T.n[g] >= cap) --g;
  T.a[g][T.n[g]++] = code;
  return g;
}
constexpr int place_fwd(Acts& T, int earliest, int code, int cap, int nm) {
  int g = earliest;
  while (T.n[g] >= (g < nm ? cap : 4)) ++g;
  T.a[g][T.n[g]++] = code;
  if (g + 1 > T.end) T.end = g + 1;
  return g;
}
constexpr Acts make_acts(const Sched& S, int mode) {
  Acts T{};
  T.end = S.n;
  const int dense = (mode == 0) ? 200 : (mode == 1 ? 90 : 100);
  for (int l = S.nst - 1; l >= 1; --l) {
    int g = S.ldu[l] - 5;
    const int cap = S.ldu[l] >= dense && mode ? 2 : 1;
    for (int i = 3; i >= 0; --i) if ((S.ldp[l] >> (i >> 1)) & 1) g = place_back(T, g - 1, A_X | (l << 4) | (i << 12), cap);
    for (int o = 1; o >= 0; --o) if (mode && ((S.om[l] >> o) & 1)) g = place_back(T, g - 1, A_Q | (l << 4) | (o << 12), cap);
    if ((mode == 0 && !(l & 1)) || (mode == 1 && !(l & 3))) {
      g = place_back(T, g - 1, A_PXC | (l << 4), 1);
      place_back(T, g - 6, A_PXR, 1);
    }
  }
  for (int l = S.nld - 1; l >= 16; --l) {
    int g = S.ldu[l] - 5;
    for (int i = 3; i >= 0; --i) if ((S.ldp[l] >> (i >> 1)) & 1) g = place_back(T, g - 1, A_X | (l << 4) | (i << 12), S.ldu[l] >= dense ? 2 : 1);
  }
  for (int d = 0; d < 16; ++d) {
    if (S.ku[d] < KPRE) continue;
    int g = S.ku[d] - 5;
    g = place_back(T, g - 1, A_K | (d << 4) | (1 << 12), 2);
    place_back(T, g - 1, A_K | (d << 4), 2);
  }
  int prev[18] = {};
  for (int s = 0; s < S.nst; ++s) {
    if (!S.cm[s]) continue;
    int g = S.base[s] + 4;
    const int p16 = prev[16];
    for (int e = 0; e < ((s & 1) ? 18 : 17); ++e) {
      if (e < 14 && !(((e >= 8 ? S.om[s] : S.cm[s]) >> (e & 1)) & 1)) continue;
      if ((e == 15 && !(S.om[s] & 1)) || (e == 16 && !(S.om[s] & 2))) continue;
      if (mode == 0 && (e == 12 || e == 13)) continue;
      int lo = g + (((e & 1) && e < 14) ? 0 : 1);
      if (lo <= prev[e | 1]) lo = prev[e | 1] + 1;
      if (e == 17 && lo <= p16) lo = p16 + 1;
      g = place_fwd(T, lo, A_EP | (s << 4) | (e << 12), S.base[s] >= dense ? 3 : CAP, S.n);
      prev[e] = g;
    }
  }
  return T;
}
template <int MODE> struct Tab {
  static constexpr Sched S = make_sched(MODE);
  static constexpr Acts A = make_acts(S, MODE);
};
static_assert(Tab<0>::S.n == 272 && Tab<1>::S.n == 144 && Tab<2>::S.n == 140, "schedule");

#define MFMA(a, b, c) __builtin_amdgcn_mfma_f32_16x16x32_bf16((a), (b), (c), 0, 0, 0)

template <int MODE>
DEV void conv_stream(lptr sm, u32 kimg, u32 ximg, lptr ywp, lptr pyp, int lane) {
  using T = Tab<MODE>;
  const int g = lane >> 4, lq = (lane & 15) >> 2, lp = lane & 3;
  const lptr kb = sm + kimg + 8 * (4 * lp + 8 * g + lq);
  const lptr xb = sm + ximg + 224 + 8 * (lp - 8 * g - lq);
  const bool l63 = (lane == 63);
  const lptr pxp = sm + FLAGS + 4 * (lane & 3);
  const u32 qa = (u32)(uintptr_t)ywp;
  u32x2 kr[16][2], xr[40][4];
  f32x4 ac[16][2];
  const f32x4 zero = {0.f, 0.f, 0.f, 0.f};
  float ts[16][2], u2[16][2], u1[16][2], yy[16][2];
  u32 pk[16];
  constexpr int NQ = MODE == 0 ? 3 : 5;
  u32 qv[NQ][2], px = 0;
#pragma unroll
  for (int i = 0; i < NQ; ++i) { qv[i][0] = 0; qv[i][1] = 0; }
#define RDQ(s_, i_) asm volatile("ds_read_u16_d16_hi %0, %1 offset:%2" : "+v"(qv[(s_) % NQ][i_]) : "v"(qa), "i"((s_) * 256 + 128 * (i_)) : "memory")
#define RDXP(l_, i_) xr[l_][i_] = trread(xb + 256 * T::S.ldm[l_] + ((i_) >> 1) * 128 + (((i_) & 1) ? 0 : 32))
  static_for<0, 16>([&](auto D_) {
    constexpr int d = decltype(D_)::value;
    if constexpr (T::S.ku[d] >= 0 && T::S.ku[d] < KPRE) { kr[d][0] = trread(kb + 256 * d); kr[d][1] = trread(kb + 256 * d + 32); }
  });
  if constexpr (MODE == 0) wait_words(sm + FLAGS, lane, 4, 1);
  if constexpr (MODE == 1) { RDQ(0, 0); RDQ(0, 1); }
  RDXP(0, 0); RDXP(0, 1); RDXP(0, 2); RDXP(0, 3);
  SCHED0();
  static_for<0, T::A.end>([&](auto G_) {
    constexpr int G = decltype(G_)::value;
    if constexpr (G < T::S.n) {
      constexpr int p = T::S.p[G], par = T::S.par[G], d = T::S.d[G], l = T::S.ld[G];
      ac[p][par] = MFMA(frag(kr[d][0], kr[d][1]), frag(xr[l][2 * par], xr[l][2 * par + 1]), T::S.first[G] ? zero : ac[p][par]);
      SCHED0();
    }
    static_for<0, T::A.n[G]>([&](auto I_) {
      constexpr int code = T::A.a[G][decltype(I_)::value];
      constexpr int kind = code & 15, a0 = (code >> 4) & 255, a1 = code >> 12;
      if constexpr (kind == A_X) RDXP(a0, a1);
      if constexpr (kind == A_K) kr[a0][a1] = trread(kb + 256 * a0 + 32 * a1);
      if constexpr (kind == A_Q) RDQ(a0, a1);
      if constexpr (kind == A_PXR) px = *(volatile LDSP u32*)pxp;
      if constexpr (kind == A_PXC) {
        if (__builtin_expect(!__all(px >= (u32)(a0 / 2 + 1)), 0)) wait_words(sm + FLAGS, lane, 4, (u32)(a0 / 2 + 1));
        asm volatile("" ::: "memory");
      }
      if constexpr (kind == A_EP) {
        constexpr int s = a0, e = a1 >> 1, o = a1 & 1;
        constexpr bool hp = s > 0 && T::S.cm[s > 0 ? s - 1 : 0] != 0;
        if constexpr (a1 < 14) {
          if constexpr (e == 0) {
            float cur = ac[s][o][3], prev = 0.f;
            if constexpr (o) prev = ac[s][0][3]; else if constexpr (hp) prev = ac[s - 1][1][3];
            ts[s][o] = l63 ? prev : cur;
          }
          if constexpr (e == 1) { float a = ac[s][o][2]; u2[s][o] = a + ror1(ts[s][o]); }
          if constexpr (e == 2) {
            float prev = 0.f;
            if constexpr (o) prev = u2[s][0]; else if constexpr (hp) prev = u2[s - 1][1];
            ts[s][o] = l63 ? prev : u2[s][o];
          }
          if constexpr (e == 3) { float a = ac[s][o][1]; u1[s][o] = a + ror1(ts[s][o]); }
          if constexpr (e == 4) {
            float prev = 0.f;
            if constexpr (o) prev = u1[s][0]; else if constexpr (hp) prev = u1[s - 1][1];
            ts[s][o] = l63 ? prev : u1[s][o];
          }
          if constexpr (e == 5) { float a = ac[s][o][0]; yy[s][o] = a + ror1(ts[s][o]); asm volatile("" : "+v"(yy[s][o])); }
          if constexpr (e == 6) {
            asm volatile("" : "+v"(qv[s % NQ][o]));
            yy[s][o] *= __uint_as_float(qv[s % NQ][o]);
            asm volatile("" : "+v"(yy[s][o]));
          }
        }
        if constexpr (a1 == 14) pk[s] = pkbf(yy[s][(T::S.om[s] & 1) ? 0 : 1], yy[s][(T::S.om[s] & 2) ? 1 : 0]);
        if constexpr (a1 == 15) *(LDSP u16*)(ywp + s * 256) = (u16)pk[s];
        if constexpr (a1 == 16) *(LDSP u16*)(ywp + s * 256 + 128) = (u16)(pk[s] >> 16);
        if constexpr (a1 == 17) set_word(pyp, (u32)(s / 2 + 1));
      }
      SCHED0();
    });
  });
#undef RDQ
#undef RDXP
}

#define ROLE_END(n) do { asm volatile(".Lfk_end" #n ":"); return; } while (0)

__global__ __launch_bounds__(768, 3) void fused_kernel(Params P) {
  extern __shared__ __attribute__((aligned(16))) char smem[];
  const lptr sm = (lptr)smem;
  const u32 smb = (u32)(uintptr_t)sm;
  const int tid = threadIdx.x;
  const int w = __builtin_amdgcn_readfirstlane(tid >> 6);
  const int lane = tid & 63;
  const int wg = blockIdx.x;
  const int xcd = wg & 7, idx = wg >> 3;
  const bool partA = idx >= 20;
  const int g = lane >> 4, li = lane & 15, lq = li >> 2, lp = li & 3;
  if (w == 11) {
    unsigned long long pc;
    u32 len;
    asm volatile("s_getpc_b64 %0\n.Lfk_code_begin:\n\ts_mov_b32 %1, .Lfk_end0-.Lfk_code_begin"
                 "\n\ts_max_i32 %1, %1, .Lfk_end1-.Lfk_code_begin\n\ts_max_i32 %1, %1, .Lfk_end2-.Lfk_code_begin"
                 "\n\ts_max_i32 %1, %1, .Lfk_end3-.Lfk_code_begin\n\ts_max_i32 %1, %1, .Lfk_end4-.Lfk_code_begin"
                 "\n\ts_max_i32 %1, %1, .Lfk_end5-.Lfk_code_begin" : "=s"(pc), "=&s"(len) :: "scc");
    const u32 off = min((u32)(lane + 64 * (idx & 3)) * 128u, len & ~3u);
    touch64((const char*)pc + off, smb + PFD);
  }
#define gflag ((gu64*)(uintptr_t)(P.ex + WS_FLAG))
#define gepoch ((gu64*)(uintptr_t)(P.ex + WS_EPOCH))
#define rx __builtin_amdgcn_make_buffer_rsrc(P.ex + WS_EXX, 0, 6u << 20, 0x00020000)
  if (__builtin_expect(idx >= 30, 0)) {
    if (w < 2) { delay(PFP); touch64((const char*)P.u + prefetch_off(w, idx, xcd, lane), smb + PFD); }
    if (w == 8 && wg == 240) {
      const unsigned long long tag = __hip_atomic_load(gepoch, RLX_AGENT) + 1;
      delay(40);
#pragma unroll 1
      for (int spin = 0; spin < (1 << 16); ++spin) {
        bool ok = true;
#pragma unroll
        for (int j = 0; j < 5; ++j) { const int f = lane + 64 * j; ok &= (__hip_atomic_load(gflag + FSP * ((f >> 5) * 128 + (f & 31)), RLX_AGENT) == tag); }
        if (__all(ok)) break;
        __builtin_amdgcn_s_sleep(64);
      }
      if (lane == 0) __hip_atomic_store(gepoch, tag, RLX_AGENT);
    }
    vmcnt<0>();
    asm volatile(".Lfk_end1:");
    return;
  }
  if (tid < 80) *(LDSP u32*)(sm + FLAGS + 4 * tid) = 0;
  const bool producer = w >= (partA ? 4 : 8);
  if (!producer) wgbarrier();

  const int gi = partA ? xcd * 10 + (idx - 20) : xcd * 20 + idx;

  auto poll_exchange = [&](int col, int first, int nst, int word) {
    wait_words(sm + FLAG2, lane, 1, 1);
    const unsigned long long tag = *(volatile LDSP unsigned long long*)(sm + EPO) + 1;
    bool ok = lane >= 8 * nst || lane < 8 * first;
#pragma unroll 1
    for (int spin = 0; spin < (1 << 17); ++spin) {
      if (!ok) ok = (__hip_atomic_load(gflag + FSP * (col * 32 + lane), RLX_AGENT) == tag);
      const unsigned long long miss = __ballot(!ok);
      const int done = miss ? (__builtin_ctzll(miss) >> 3) : nst;
      if (done > first) set_word(sm + FLAG3 + word, (u32)done);
      if (!miss) break;
      __builtin_amdgcn_s_sleep(PSL);
    }
  };

  if (producer) {
    const int h = w & 3, pg = (w < 8) ? 1 : 0;
    const int un = partA ? (wg - 160) * 2 + pg : wg;
    const int pcol = un % 10, pchunk = un / 10 + (partA ? 16 : 0);
    const u32 praw = partA ? A_PRAW + pg * PUNIT : B_PRAW;
    const u32 shk = SHK + pg * 512;
    const lptr fw = sm + FLAG2 + pg * 32;
    asm volatile("" :: "s"(P.k), "s"(P.v), "s"(P.q), "s"(P.shift_k), "s"(P.ex), "s"(P.u), "s"(P.ssm_k), "s"(P.out));
    __builtin_amdgcn_s_setprio(3);
    unsigned long long tag = 0;
    {
      const u32 po = (u32)(64 * pchunk + 16 * h + (lane >> 3)) * ROWB + (u32)pcol * 128 + 16 * (lane & 7);
#pragma unroll
      for (int j = 0; j < 2; ++j) {
        dma16(P.k, po + j * 8 * ROWB, smb + praw + 512 + h * 2048 + j * 1024);
        dma16(P.v, po + j * 8 * ROWB, smb + praw + 8704 + h * 2048 + j * 1024);
        dma16(P.q, po + j * 8 * ROWB, smb + praw + 16896 + h * 2048 + j * 1024);
      }
      if (h == 0 && lane < 32) {
        if (pchunk) dma16(P.k, po - 4 * ROWB, smb + praw);
        else *(LDSP u32x4*)(sm + praw + 16 * lane) = u32x4{0, 0, 0, 0};
      }
      if (h == 1 && lane < 32) dma16(P.shift_k, (u32)pcol * 512 + 16 * lane, smb + shk);
      if (w == 8 && lane < 2) {
        u32 keep;
        asm volatile("s_mov_b32 %0, m0\n\ts_mov_b32 m0, %3\n\ts_nop 2\n\tglobal_load_lds_dword %1, %2 sc1\n\ts_mov_b32 m0, %0"
                     : "=&s"(keep) : "v"(4 * lane), "s"(P.ex + WS_EPOCH), "s"(smb + EPO) : "memory");
      }
    }
    wgbarrier();
    if (!partA) {
      if (lane < 10) *(LDSP u32x4*)(sm + XIMG + h * XS + 16 * lane) = u32x4{0, 0, 0, 0};
    }
    const u32 co = praw + 32 * h + 8 * lp + 512 * g;
    const u32 stg = partA ? A_PSTG + (w - 4) * YS : B_PSTG + h * XS;
    u32 ka1 = co + 128 * (lq + 1);
    u32 ka2 = co + 128 * ((lq < 2) ? lq + 5 : lq);
    u32 ka3 = co + 128 * lq;
    u32 so[4];
#pragma unroll
    for (int u = 0; u < 4; ++u) so[u] = stg + li * 128 + 8 * (g & 1) + 16 * (((2 * u + (g >> 1)) ^ li) & 7);
    u32 sr_ = stg + (lane >> 3) * 128 + 16 * ((lane ^ (lane >> 3)) & 7);
    u32 eo = (u32)(pcol * 64 + 16 * h + (lane >> 3)) * 4096 + 128 * pchunk + 16 * (lane & 7);
    u32 swa = shk + 8 * (16 * h + li);
    asm volatile("" : "+v"(ka1), "+v"(ka2), "+v"(ka3), "+v"(so[0]), "+v"(so[1]), "+v"(so[2]), "+v"(so[3]), "+v"(sr_), "+v"(eo), "+v"(swa));
    vmcnt<0>();
    set_word(fw + 4 * h, 1);
    wait_words(fw, lane, 4, 1);
    {
      const u32x2 sw = *(LDSP u32x2*)(sm + swa);
      u32x2 r1[4], r2[4], r3[4], vv[4], qq[4];
#pragma unroll
      for (int u = 0; u < 4; ++u) {
        r1[u] = trread(sm + ka1 + 2048 * u);
        r2[u] = trread(sm + ka2 + 2048 * u);
        r3[u] = trread(sm + ka3 + 2048 * u + 512);
        vv[u] = trread(sm + ka3 + 2048 * u + 8704);
        qq[u] = trread(sm + ka3 + 2048 * u + 16896);
      }
      const u32 W32 = (sw.y >> 16) | (sw.y << 16), W10 = (sw.x >> 16) | (sw.x << 16);
#pragma unroll
      for (int u = 0; u < 4; ++u) {
        const float s0 = dot2(r1[u].y, W10, dot2(r1[u].x, W32, 0.f));
        const float s1 = dot2(r3[u].x, W10, dot2(r2[u].y, W32, 0.f));
        const float s2 = dot2(r2[u].x, W10, dot2(r1[u].y, W32, 0.f));
        const float s3 = dot2(r3[u].y, W10, dot2(r3[u].x, W32, 0.f));
        *(LDSP u32x2*)(sm + so[u]) = u32x2{pkbf(s0 * bflo(vv[u].x), s1 * bfhi(vv[u].x)), pkbf(s2 * bflo(vv[u].y), s3 * bfhi(vv[u].y))};
        *(LDSP u32x2*)(sm + so[u] + 2048) = qq[u];
      }
      wavefence();
#pragma unroll
      for (int j = 0; j < 4; ++j)
        __builtin_amdgcn_raw_buffer_store_b128(*(const LDSP u32x4*)(sm + sr_ + 1024 * j), rx, eo + (j & 1) * 8 * 4096 + (j >> 1) * (u32)(WS_EXQ - WS_EXX), 0, 16);
    }
    const u32 urow = (u32)(64 * h + lane) * ROWB + (u32)((gi * 4) & ~7) * 2;
    set_word(fw + 16 + 4 * h, 1);
    wait_words(sm + FLAG2, lane, 1, 1);
    tag = *(volatile LDSP unsigned long long*)(sm + EPO) + 1;
    vmcnt<0>();
    if (lane == 0) __hip_atomic_store(gflag + FSP * ((pcol * 4 + h) * 32 + pchunk), tag, RLX_AGENT);
    if (!partA) {
      dma16(P.u, urow, smb + RAWU + 1024 * h);
      dma16(P.u, urow + 256 * ROWB, smb + RAWU + 4096 + 1024 * h);
      wait_words(fw + 16, lane, 4, 1);
    }
    if (w >= 8) {
      if (partA) {
        wait_words(sm + FLAG2 + 16, lane, 4, 1);
        wait_words(sm + FLAG2 + 48, lane, 4, 1);
#pragma unroll 1
        for (int st = 1; st < 3; ++st) {
          if (h == 0) poll_exchange(gi >> 1, st, st + 1, 4); else wait_words(sm + FLAG3 + 4, lane, 1, (u32)(st + 1));
          __builtin_amdgcn_fence(__ATOMIC_ACQUIRE, "wavefront");
          u32x4 d[4];
#pragma unroll
          for (int j = 0; j < 4; ++j)
            d[j] = __builtin_amdgcn_raw_buffer_load_b128(rx, (u32)(gi * 8 + 2 * h + (j & 1)) * 4096 + 1024 * st + 16 * lane + (j >> 1) * (u32)(WS_EXQ - WS_EXX), 0, 16);
#pragma unroll
          for (int j = 0; j < 4; ++j)
            *(LDSP u32x4*)(sm + ((j >> 1) ? YIMG + (2 * h + (j & 1)) * YS : XIMG + (2 * h + (j & 1)) * XS + XPADB) + 1024 * st + 16 * lane) = d[j];
          set_word(sm + FLAGS + 4 * h, (u32)(2 * st + 2));
        }
        const u32 yrd = YIMG + lq * YS + 2 * (64 * h + 16 * g + 4 * lp);
        u16* const outp = P.out + gi * 8 + (size_t)(64 * h + lane) * (2 * DCH);
#pragma unroll 1
        for (int b = 0; b < (h < 3 ? 6 : 5); ++b) {
          wait_words(sm + FLAGS + 16, lane, 8, (u32)(b + 1));
          u32x2 a = trread(sm + yrd + 512 * b);
          u32x2 c = trread(sm + yrd + 512 * b + 4 * YS);
          *(u32x4*)(outp + (size_t)b * 256 * (2 * DCH)) = u32x4{a.x, a.y, c.x, c.y};
        }
        ROLE_END(2);
      }
      const u32 ua = RAWU + 1024 * h + 16 * (16 * g + 4 * lp + lq) + 2 * ((gi * 4) & 4);
      const u32 xw = XIMG + (lane & 3) * XS + XPADB + 8 * (lane >> 2) + 128 * h;
      const u32 yrd = B_YIMG + lq * YS + 2 * (64 * h + 16 * g + 4 * lp);
      const u32 hrd = H_YIMG + lq * HYS + 2 * (64 * h + 16 * g + 4 * lp - 1408);
      u16* const outp = P.out + gi * 4 + (size_t)(64 * h + lane) * (2 * DCH);
#pragma unroll 1
      for (int it = 0; it < 8; ++it) {
        int i = it;
        asm volatile("" : "+s"(i));
        if (i + 2 < 8) dma16(P.u, urow + (u32)(i + 2) * (256 * ROWB), smb + RAWU + 4096 * (i + 2) + 1024 * h);
        if (i < 6) vmcnt<2>(); else if (i == 6) vmcnt<1>(); else vmcnt<0>();
        *(LDSP u32x2*)(sm + xw + 512 * i) = trread(sm + ua + 4096 * i);
        set_word(sm + FLAGS + 4 * h, (u32)(i + 1));
      }
#pragma unroll 1
      for (int i = 0; i < 8; ++i) {
        wait_words(sm + FLAGS + 16, lane, 4, (u32)(i + 1));
        *(u32x2*)(outp + DCH + (size_t)i * 256 * (2 * DCH)) = trread(sm + yrd + 512 * i);
      }
#pragma unroll 1
      for (int i = (h == 3 ? 5 : 6); i < 8; ++i) {
        wait_words(sm + FLAGS + 32, lane, 4, (u32)(i + 1));
        *(u32x2*)(outp + (size_t)i * 256 * (2 * DCH)) = trread(sm + hrd + 512 * i);
      }
      ROLE_END(3);
    }
    __builtin_amdgcn_s_setprio(0);
  } else if (partA) {
    delay(PFP);
    const int pc = (idx - 20) * 4 + w;
    touch64((const char*)P.u + prefetch_off(pc / 30, pc % 30, xcd, lane), smb + PFD);
    if (pc < 20) touch64((const char*)P.u + prefetch_off(1, 10 + pc, xcd, lane), smb + PFD);
  }

  if (partA) {
    const int ch = gi * 8 + w;
    const u32 kimg = KIMG + w * KS, ximg = XIMG + w * XS, yimg = YIMG + w * YS;
    if (lane < 10) *(LDSP u32x4*)(sm + ximg + 16 * lane) = u32x4{0, 0, 0, 0};
    if (lane < 6) *(LDSP u32x4*)(sm + kimg + 16 * lane) = u32x4{0, 0, 0, 0};
    const float dsk = ld_bf_uniform(P.ssm_D, ch);
    wait_words(sm + FLAG2, lane, 4, 1);
#pragma unroll
    for (int j = 0; j < 3; ++j) dma16(P.ssm_k, (u32)ch * (SEQ * 2) + 1024 * j + 16 * lane, smb + kimg + KPADB + 1024 * j);
    vmcnt<0>();
    if (lane == 0) {
      LDSP u16* k0 = (LDSP u16*)(sm + kimg + KPADB);
      *k0 = (u16)pkbf(bf2f(*k0) + dsk, 0.f);
    }
    if (w == 0) poll_exchange(gi >> 1, 0, 1, 16); else wait_words(sm + FLAG3 + 16, lane, 1, 1);
    __builtin_amdgcn_fence(__ATOMIC_ACQUIRE, "wavefront");
    {
      const u32x4 dx = __builtin_amdgcn_raw_buffer_load_b128(rx, (u32)ch * 4096 + 16 * lane, 0, 16);
      const u32x4 dq = __builtin_amdgcn_raw_buffer_load_b128(rx, (u32)ch * 4096 + 16 * lane + (u32)(WS_EXQ - WS_EXX), 0, 16);
      wait_words(sm + FLAG2 + 16 + (w < 4 ? 32 : 0), lane, 4, 1);
      *(LDSP u32x4*)(sm + ximg + XPADB + 16 * lane) = dx;
      *(LDSP u32x4*)(sm + yimg + 16 * lane) = dq;
    }
    wavefence();
    conv_stream<1>(sm, kimg, ximg, sm + yimg + 2 * lane, sm + FLAGS + 16 + 4 * w, lane);
    ROLE_END(4);
  }

  {
    const int ch0 = gi * 4;
    const int cw = w & 3, ps = w >> 2;
    const int ch = ch0 + cw;
    const size_t o = (size_t)ch * NST + 64 * ps + lane;
    const u16 i_ar = P.A_re[o], i_ai = P.A_im[o], i_br = P.B_re[o], i_bi = P.B_im[o], i_cr = P.C_re[o], i_ci = P.C_im[o];
    const float ldt = ld_bf_uniform(P.log_dt, ch);
    const float dsk = ld_bf_uniform(P.Dskip, ch);
    const float dt = rbf(expf(ldt));
    const float zr = dt * bf2f(i_ar), zi = dt * bf2f(i_ai);
    const cf dB = {dt * bf2f(i_br), dt * bf2f(i_bi)};
    const cf C = {bf2f(i_cr), bf2f(i_ci)};
    const cf W = cmul(C, dB);
    float mx = fmaxf(fabsf(W.r), fabsf(W.i));
    {
      int m = __float_as_int(mx);
      m = max(m, __builtin_amdgcn_update_dpp(0, m, 0x111, 0xf, 0xf, false));
      m = max(m, __builtin_amdgcn_update_dpp(0, m, 0x112, 0xf, 0xf, false));
      m = max(m, __builtin_amdgcn_update_dpp(0, m, 0x114, 0xf, 0xf, false));
      m = max(m, __builtin_amdgcn_update_dpp(0, m, 0x118, 0xf, 0xf, false));
      int a0 = __builtin_amdgcn_readlane(m, 15), a1 = __builtin_amdgcn_readlane(m, 31);
      int a2 = __builtin_amdgcn_readlane(m, 47), a3 = __builtin_amdgcn_readlane(m, 63);
      mx = __int_as_float(max(max(a0, a1), max(a2, a3)));
    }
    int ex = (int)((__float_as_uint(mx) >> 23) & 0xff);
    ex = min(max(ex, 30), 240);
    const float sY = __uint_as_float((u32)(267 - ex) << 23);
    const float so = __uint_as_float((u32)(ex - 27) << 23);
    const float SX = 16384.f;

    f32x4 ak[3];
    const u32 stg = STG + w * STS;
    const u32 rb = stg + li * 272 + g * 16;
    const u32 m0v = smb + w * STS;
#define STGW(row, val_r, val_i) \
    asm volatile("ds_write_addtid_b32 %0 offset:%1" :: "v"(pkh((val_r), (val_i))), "i"(STG + (row) * 272) : "memory")
    {
      const cf E1 = cexp_pow2(zr, zi, 1.f);
      const cf E8c = cexp_pow2(zr, -zi, 8.f);
      const cf E128 = cexp_pow2(zr, zi, 128.f);
      const cf E1024 = cexp_pow2(zr, zi, 1024.f);
      const cf E2 = cmul(E1, E1);
      const cf E4 = cmul(E2, E2);
      u32 keep;
      asm volatile("s_mov_b32 %0, m0\n\ts_mov_b32 m0, %1\n\ts_nop 0" : "=&s"(keep) : "s"(m0v) : "memory");
      cf y = {W.r * sY, -(W.i * sY)};
      cf P1 = {E128.r * SX, E128.i * SX};
      cf v0 = {SX, 0.f}, v1 = P1;
#pragma unroll
      for (int b = 0; b < 8; ++b) {
        STGW(2 * b, y.r, y.i); y = cmul(y, E8c);
        STGW(16 + b, v0.r, v0.i); if (b < 7) v0 = cmul(v0, E1);
        STGW(2 * b + 1, y.r, y.i); if (b < 7) y = cmul(y, E8c);
        STGW(24 + b, v1.r, v1.i); if (b < 7) v1 = cmul(v1, E1);
      }
      f16x8 yf[4];
      const f32x4 zero = {0.f, 0.f, 0.f, 0.f};
#pragma unroll
      for (int s = 0; s < 4; ++s) {
        yf[s] = *(const LDSP f16x8*)(sm + rb + 64 * s);
        f16x8 xf = *(const LDSP f16x8*)(sm + rb + 16 * 272 + 64 * s);
        ak[0] = __builtin_amdgcn_mfma_f32_16x16x32_f16(xf, yf[s], s ? ak[0] : zero, 0, 0, 0);
      }
      asm volatile("" ::: "memory");
      cf Pa = cmul(P1, E128);
      cf Pb = {E1024.r * SX, E1024.i * SX};
#pragma unroll
      for (int m = 0; m < 2; ++m) {
        cf v = Pa;
#pragma unroll
        for (int r = 0; r < 4; ++r) {
          STGW(16 + 4 * m + r, v.r, v.i); if (r < 3) v = cmul(v, E2);
          if (r & 1) { STGW(4 * (2 * m + (r >> 1)) + 2, Pb.r, Pb.i); Pb = cmul(Pb, E128); }
        }
        Pa = cmul(Pa, E128);
      }
#pragma unroll
      for (int m = 0; m < 4; ++m) {
        STGW(4 * m, Pa.r, Pa.i);
        cf v = cmul(Pa, E4);
        STGW(4 * m + 3, Pb.r, Pb.i); Pb = cmul(Pb, E128);
        STGW(4 * m + 1, v.r, v.i);
        if (m < 3) Pa = cmul(Pa, E128);
      }
      STGW(24, Pb.r, Pb.i);
      asm volatile("s_mov_b32 m0, %0" :: "s"(keep) : "memory");
#pragma unroll
      for (int s = 0; s < 4; ++s) {
        f16x8 x1 = *(const LDSP f16x8*)(sm + rb + 64 * s);
        f16x8 x2 = *(const LDSP f16x8*)(sm + rb + 16 * 272 + 64 * s);
        ak[1] = __builtin_amdgcn_mfma_f32_16x16x32_f16(x1, yf[s], s ? ak[1] : zero, 0, 0, 0);
        ak[2] = __builtin_amdgcn_mfma_f32_16x16x32_f16(x2, yf[s], s ? ak[2] : zero, 0, 0, 0);
      }
    }
#undef STGW
#pragma unroll
    for (int i = 0; i < 3; ++i) ak[i] *= so;
    asm volatile("" ::: "memory");
    const u32 kimg = B_KIMG + cw * KS;
    if (ps) {
#pragma unroll
      for (int i = 0; i < 3; ++i) *(LDSP f32x4*)(sm + kimg + 1024 * i + 16 * lane) = ak[i];
      set_word(sm + FLAGS + 48 + 4 * cw, 1);
      const u32 hx = stg, hk = stg + XS, hy = H_YIMG + cw * HYS;
      asm volatile("s_waitcnt lgkmcnt(0)" ::: "memory");
#pragma unroll
      for (int j = 0; j < 4; ++j) dma16(P.ssm_k, (u32)ch * (SEQ * 2) + 1024 * j + 16 * lane, smb + hk + KPADB + 1024 * j);
      if (lane < 10) *(LDSP u32x4*)(sm + hx + 16 * lane) = u32x4{0, 0, 0, 0};
      if (lane < 6) *(LDSP u32x4*)(sm + hk + 16 * lane) = u32x4{0, 0, 0, 0};
      const float dh = ld_bf_uniform(P.ssm_D, ch);
      if (w == 4) poll_exchange(gi >> 2, 0, 4, 4); else wait_words(sm + FLAG3 + 4, lane, 1, 4);
      __builtin_amdgcn_fence(__ATOMIC_ACQUIRE, "wavefront");
      {
        u32x4 d[6];
#pragma unroll
        for (int j = 0; j < 4; ++j) d[j] = __builtin_amdgcn_raw_buffer_load_b128(rx, (u32)ch * 4096 + 1024 * j + 16 * lane, 0, 16);
#pragma unroll
        for (int j = 0; j < 2; ++j)
          d[4 + j] = __builtin_amdgcn_raw_buffer_load_b128(rx, (u32)ch * 4096 + 2816 + 256 * j + 16 * lane + (u32)(WS_EXQ - WS_EXX), 0, 16);
        vmcnt<0>();
        if (lane == 0) {
          LDSP u16* k0 = (LDSP u16*)(sm + hk + KPADB);
          *k0 = (u16)pkbf(bf2f(*k0) + dh, 0.f);
        }
#pragma unroll
        for (int j = 0; j < 4; ++j) *(LDSP u32x4*)(sm + hx + XPADB + 1024 * j + 16 * lane) = d[j];
#pragma unroll
        for (int j = 0; j < 2; ++j) *(LDSP u32x4*)(sm + hy + 256 * j + 16 * lane) = d[4 + j];
      }
      wavefence();
      conv_stream<2>(sm, hk, hx, sm + hy + 2 * lane - 11 * 256, sm + FLAGS + 16 + 4 * w, lane);
      ROLE_END(5);
    }
    wait_words(sm + FLAGS + 48 + 4 * cw, lane, 1, 1);
    {
#pragma unroll
      for (int i = 0; i < 3; ++i) ak[i] += *(const LDSP f32x4*)(sm + kimg + 1024 * i + 16 * lane);
      wavefence();
      auto nxt = [&](float x, float e, bool lastl) {
        float r = __int_as_float(__builtin_amdgcn_update_dpp(0, __float_as_int(x), 0x130, 0xf, 0xf, true));
        return lastl ? e : r;
      };
      auto lane_of = [&](float x, int l) { return __int_as_float(__builtin_amdgcn_readlane(__float_as_int(x), l)); };
      const lptr kd = sm + kimg + KPADB;
      if (lane < 6) *(LDSP u32x4*)(sm + kimg + 16 * lane) = u32x4{0, 0, 0, 0};
      {
        const float a0 = ak[0][0] + (lane == 0 ? dsk : 0.f), a1 = ak[0][1], a2 = ak[0][2], a3 = ak[0][3];
        *(LDSP u32x2*)(kd + 2 * (128 * (g >> 1) + 8 * li + 4 * (g & 1))) = u32x2{pkbf(a0, a1), pkbf(a2, a3)};
      }
      const float s20 = ak[1][0], s21 = ak[1][1], s3a = ak[1][2], s3b = ak[1][3];
      const float c0 = ak[2][0], c1 = ak[2][1], c2 = ak[2][2], c3 = ak[2][3];
      const bool l63 = lane == 63;
      {
        const float n = nxt(c0, lane_of(s20, 0), lane == 31);
        if (lane < 32) *(LDSP u32x4*)(kd + 16 * (32 + lane)) = u32x4{pkbf(c0, c0), pkbf(c1, c2), pkbf(c2, c2), pkbf(c3, n)};
      }
      {
        const float n = nxt(s20, lane_of(s3a, 0), l63);
        *(LDSP u32x4*)(kd + 16 * (64 + lane)) = u32x4{pkbf(s20, s20), pkbf(s20, s21), pkbf(s21, s21), pkbf(n, n)};
      }
      {
        const float na = nxt(s3a, lane_of(s3b, 0), l63), nb = nxt(s3b, lane_of(c0, 32), l63);
        const u32 a = pkbf(s3a, s3a), b = pkbf(s3b, s3b);
        *(LDSP u32x4*)(kd + 16 * (128 + lane)) = u32x4{a, a, pkbf((lane & 1) ? na : s3a, na), pkbf(na, na)};
        *(LDSP u32x4*)(kd + 16 * (192 + lane)) = u32x4{b, b, pkbf((lane & 1) ? nb : s3b, nb), pkbf(nb, nb)};
      }
    }
    wavefence();
    conv_stream<0>(sm, kimg, XIMG + cw * XS, sm + B_YIMG + cw * YS + 2 * lane, sm + FLAGS + 16 + 4 * w, lane);
    ROLE_END(0);
  }
}

extern "C" void kernel_launch(void* const* d_in, const int* in_sizes, int n_in,
                              void* d_out, int out_size, void* d_ws, size_t ws_size,
                              hipStream_t stream) {
  Params P;
  P.q = (const u16*)d_in[0];
  P.k = (const u16*)d_in[1];
  P.v = (const u16*)d_in[2];
  P.shift_k = (const u16*)d_in[3];
  P.ssm_k = (const u16*)d_in[4];
  P.ssm_D = (const u16*)d_in[5];
  P.u = (const u16*)d_in[6];
  P.A_re = (const u16*)d_in[7];
  P.A_im = (const u16*)d_in[8];
  P.B_re = (const u16*)d_in[9];
  P.B_im = (const u16*)d_in[10];
  P.C_re = (const u16*)d_in[11];
  P.C_im = (const u16*)d_in[12];
  P.log_dt = (const u16*)d_in[13];
  P.Dskip = (const u16*)d_in[14];
  P.out = (u16*)d_out;
  P.ex = (char*)d_ws;
  (void)hipFuncSetAttribute((const void*)fused_kernel, hipFuncAttributeMaxDynamicSharedMemorySize, LDS_BYTES);
  fused_kernel<<<256, 768, LDS_BYTES, stream>>>(P);
}
```
